```python
import torch

def _softmax(x, axis=-1):
    return torch.softmax(x, dim=axis)
def _gelu(x):
    return torch.nn.functional.gelu(x, approximate="tanh")
def _silu(x):
    return torch.nn.functional.silu(x)
def _sigmoid(x):
    return torch.sigmoid(x)
def _layernorm(x, axis=-1, eps=1e-5):
    m = x.mean(dim=axis, keepdim=True)
    v = x.var(dim=axis, keepdim=True, unbiased=False)
    return (x - m) / torch.sqrt(v + eps)
def _scatter_add(tgt, idx, src, axis=0):
    return tgt.clone().index_add_(axis, idx, src)
def _segment_sum(src, seg, n):
    out = torch.zeros((n,) + src.shape[1:], dtype=src.dtype)
    return out.index_add_(0, seg, src)
def _pad(x, pw):
    flat = tuple(v for pair in reversed(pw) for v in pair)
    return torch.nn.functional.pad(x, flat)

kernel_name = 'standalone__mha_block_cached__near_ridge'

def setup_inputs():

    _w = (lambda a: (lambda t: t.to(torch.bfloat16) if t.is_floating_point() else t)(torch.as_tensor(a)))
    B, Sq, D = 16, 240, 448
    H, Sk, Dh = 16, 512, 256
    return {
        "x": _w((lambda *s: torch.randn(*s, dtype=torch.bfloat16) * 0.8)(B, Sq, D)),
        "k_cache": _w((lambda *s: torch.randn(*s, dtype=torch.bfloat16) * 0.8)(B, H, Sk, Dh)),
        "v_cache": _w((lambda *s: torch.randn(*s, dtype=torch.bfloat16) * 0.05)(B, H, Sk, Dh)),
        "wq": _w((lambda *s: torch.randn(*s, dtype=torch.bfloat16) * 0.8)(D, H * Dh) * (D ** -0.5)),
        "wo": _w((lambda *s: torch.randn(*s, dtype=torch.bfloat16) * 0.05)(H * Dh, D) * ((H * Dh) ** -0.5)),
    }

def reference(x, k_cache, v_cache, wq, wo):
    B, Sq, D = x.shape
    H, Dh = 16, 256
    q = (lambda x, *d: x.permute(*d))((x @ wq).reshape(B, Sq, H, Dh), 0, 2, 1, 3)
    scores = torch.einsum("bhqd,bhkd->bhqk", q, k_cache) * (Dh ** -0.5)
    p = _softmax(scores, axis=-1)
    o = torch.einsum("bhqk,bhkd->bhqd", p, v_cache)
    return (lambda x, *d: x.permute(*d))(o, 0, 2, 1, 3).reshape(B, Sq, H * Dh) @ wo
```

```cpp
#include <hip/hip_runtime.h>
#include <hip/hip_bf16.h>
typedef unsigned short u16;
__device__ __forceinline__ float bf2f(u16 v){ return __uint_as_float(((unsigned)v)<<16); }
__device__ __forceinline__ u16 f2bf(float f){ __bf16 b=(__bf16)f; return __builtin_bit_cast(u16,b); }
constexpr int B=16,SQ=240,D=448,H=16,SK=512,DH=256;
__global__ void k_q(const u16* x,const u16* wq,u16* q){
  int n=blockIdx.x*256+threadIdx.x; int m=blockIdx.y;
  float a=0; for(int c=0;c<D;++c) a+=bf2f(x[m*D+c])*bf2f(wq[(size_t)c*4096+n]);
  q[(size_t)m*4096+n]=f2bf(a);
}
__global__ void k_att(const u16* q,const u16* k,const u16* v,u16* o){
  __shared__ float s[SK]; __shared__ float qs[DH]; __shared__ float red[256];
  int qi=blockIdx.x,h=blockIdx.y,b=blockIdx.z,t=threadIdx.x;
  size_t row=(size_t)(b*SQ+qi)*4096+h*DH;
  qs[t]=bf2f(q[row+t]); __syncthreads();
  const u16* kb=k+(size_t)(b*H+h)*SK*DH; const u16* vb=v+(size_t)(b*H+h)*SK*DH;
  float mx=-1e30f;
  for(int j=t;j<SK;j+=256){ float a=0; for(int d=0;d<DH;++d) a+=qs[d]*bf2f(kb[j*DH+d]); a*=0.0625f; s[j]=a; mx=fmaxf(mx,a);}
  red[t]=mx; __syncthreads();
  for(int o2=128;o2;o2>>=1){ if(t<o2) red[t]=fmaxf(red[t],red[t+o2]); __syncthreads(); }
  mx=red[0]; __syncthreads();
  float sm=0; for(int j=t;j<SK;j+=256){ float e=__expf(s[j]-mx); s[j]=e; sm+=e; }
  red[t]=sm; __syncthreads();
  for(int o2=128;o2;o2>>=1){ if(t<o2) red[t]+=red[t+o2]; __syncthreads(); }
  sm=red[0];
  float a=0; for(int j=0;j<SK;++j) a+=s[j]*bf2f(vb[j*DH+t]);
  o[row+t]=f2bf(a/sm);
}
__global__ void k_o(const u16* o,const u16* wo,u16* out){
  int n=threadIdx.x; int m=blockIdx.x;
  float a=0; for(int c=0;c<4096;++c) a+=bf2f(o[(size_t)m*4096+c])*bf2f(wo[(size_t)c*D+n]);
  out[(size_t)m*D+n]=f2bf(a);
}
extern "C" void kernel_launch(void* const* d_in, const int* in_sizes, int n_in, void* d_out, int out_size, void* d_ws, size_t ws_size, hipStream_t stream) {
  const u16* x=(const u16*)d_in[0]; const u16* k=(const u16*)d_in[1]; const u16* v=(const u16*)d_in[2];
  const u16* wq=(const u16*)d_in[3]; const u16* wo=(const u16*)d_in[4];
  u16* q=(u16*)d_ws; u16* o=q+(size_t)3840*4096;
  k_q<<<dim3(16,3840),256,0,stream>>>(x,wq,q);
  k_att<<<dim3(SQ,H,B),256,0,stream>>>(q,k,v,o);
  k_o<<<3840,448,0,stream>>>(o,wo,(u16*)d_out);
}
```

```cpp
#include <hip/hip_runtime.h>
#include <hip/hip_bf16.h>
typedef unsigned short u16;
typedef float f32x2 __attribute__((ext_vector_type(2)));
typedef __bf16 bf16x2 __attribute__((ext_vector_type(2)));
typedef const __attribute__((address_space(4))) unsigned long long* kptr;
typedef __attribute__((address_space(1))) unsigned long long gu64;
#define DI __device__ __forceinline__
DI float bf2f(u16 v) { return __uint_as_float(((unsigned)v) << 16); }
DI u16 f2bf(float f) { __bf16 b = (__bf16)f; return __builtin_bit_cast(u16, b); }
DI unsigned cvtpk(float lo, float hi) { f32x2 v = {lo, hi}; bf16x2 b = __builtin_convertvector(v, bf16x2); return __builtin_bit_cast(unsigned, b); }

constexpr size_t WS_O = 0;
constexpr int LDS_K1 = 163840;
DI unsigned item_of(unsigned blk) {
  const unsigned x = blk & 7, idx = blk >> 3;
  return ((x & 1) * 8 + (idx & 7)) * 16 + (x >> 1) * 4 + (idx >> 3);
}

DI void slow_rows(const u16* x, const u16* kc, const u16* vc, const u16* wq, char* ows, float* sm, int b, int h, int w) {
  int lane; asm volatile("v_mbcnt_lo_u32_b32 %0, -1, 0\n\tv_mbcnt_hi_u32_b32 %0, -1, %0" : "=v"(lane));
  float* qs = sm; float* ps = sm + 256;
  const u16* kb = kc + (size_t)(b * 16 + h) * 131072; const u16* vb = vc + (size_t)(b * 16 + h) * 131072;
  for (int r = 64 * w; r < 64 * w + 64 && r < 240; ++r) {
    const u16* xr = x + (size_t)(b * 240 + r) * 448;
    for (int i = 0; i < 4; ++i) { const int d = lane + 64 * i; float a = 0.f; for (int c = 0; c < 448; ++c) a += bf2f(xr[c]) * bf2f(wq[(size_t)c * 4096 + h * 256 + d]); qs[d] = bf2f(f2bf(a)); }
    __builtin_amdgcn_fence(__ATOMIC_RELEASE, "wavefront"); __builtin_amdgcn_wave_barrier();
    float mx = -__builtin_inff();
    for (int i = 0; i < 8; ++i) { const int key = lane + 64 * i; float a = 0.f; for (int d = 0; d < 256; ++d) a += qs[d] * bf2f(kb[key * 256 + d]); a *= 0.0625f; ps[key] = a; mx = fmaxf(mx, a); }
    for (int o = 32; o; o >>= 1) mx = fmaxf(mx, __shfl_xor(mx, o));
    float sum = 0.f;
    for (int i = 0; i < 8; ++i) { const int key = lane + 64 * i; const float e = __expf(ps[key] - mx); ps[key] = e; sum += e; }
    for (int o = 32; o; o >>= 1) sum += __shfl_xor(sum, o);
    __builtin_amdgcn_fence(__ATOMIC_RELEASE, "wavefront"); __builtin_amdgcn_wave_barrier();
    const float rs = 1.f / sum;
    float a0 = 0.f, a1 = 0.f, a2 = 0.f, a3 = 0.f; const int d = 4 * lane;
    for (int key = 0; key < 512; ++key) { const float p = ps[key]; const u16* vr = vb + key * 256 + d; a0 += p * bf2f(vr[0]); a1 += p * bf2f(vr[1]); a2 += p * bf2f(vr[2]); a3 += p * bf2f(vr[3]); }
    const int dd = d & 31, g = (dd & 15) >> 2;
    const unsigned long long val = (unsigned long long)cvtpk(a0 * rs, a1 * rs) | ((unsigned long long)cvtpk(a2 * rs, a3 * rs) << 32);
    __hip_atomic_store((gu64*)(ows + ((size_t)(b * 15 + (r >> 4)) * 128 + h * 8 + (d >> 5)) * 1024 + (16 * g + (r & 15)) * 16 + (dd >> 4) * 8), val, __ATOMIC_RELAXED, __HIP_MEMORY_SCOPE_AGENT);
    __builtin_amdgcn_fence(__ATOMIC_RELEASE, "wavefront"); __builtin_amdgcn_wave_barrier();
  }
}

__global__ __launch_bounds__(256, 1) void k1_attn(const u16* __restrict__ x, const u16* __restrict__ kc, const u16* __restrict__ vc, const u16* __restrict__ wq,
                                                   const u16* __restrict__ wo, char* __restrict__ ws, u16* __restrict__ out) {
  extern __shared__ __attribute__((aligned(1024))) char lds[];
  const unsigned w = __builtin_amdgcn_readfirstlane(threadIdx.x >> 6);
  unsigned state, tag;
  {
    const unsigned blk = item_of(blockIdx.x), b = blk >> 4, h = blk & 15;
    asm volatile(
  "L_k1_begin_%=:\n\t"
  "s_and_b32 s96, %11, 15\n\t"
  "s_lshl_b32 s70, s96, 9\n\t"
  "s_sub_u32 s87, 3670016, s70\n\t"
  "s_mov_b64 s[44:45], %3\n\t"
  "s_mov_b32 s46, s87\n\t"
  "s_mov_b32 s47, 0x20000\n\t"
  "s_mov_b32 s90, %9\n\t"
  "s_mov_b32 s89, %10\n\t"
  "s_mov_b32 s99, %11\n\t"
  "v_mbcnt_lo_u32_b32 v0, -1, 0\n\t"
  "v_mbcnt_hi_u32_b32 v0, -1, v0\n\t"
  "s_lshl_b32 s69, s90, 12\n\t"
  "s_add_u32 s69, s69, s89\n\t"
  "s_lshr_b32 s70, s90, 1\n\t"
  "s_lshl_b32 s73, s70, 18\n\t"
  "s_and_b32 s70, s90, 1\n\t"
  "s_lshl_b32 s70, s70, 7\n\t"
  "s_add_u32 s73, s73, s70\n\t"
  "v_lshrrev_b32 v1, 3, v0\n\t"
  "v_bfe_u32 v2, v0, 1, 2\n\t"
  "v_and_b32 v3, 1, v0\n\t"
  "v_lshlrev_b32 v3, 4, v3\n\t"
  "v_bfe_u32 v32, v0, 4, 1\n\t"
  "v_xor_b32 v32, v32, v2\n\t"
  "v_lshl_add_u32 v32, v32, 5, v3\n\t"
  "v_lshl_add_u32 v4, v1, 13, v32\n\t"
  "v_xor_b32 v32, 64, v4\n\t"
  "v_add_u32 v5, 64512, v32\n\t"
  "v_add_u32 v60, 129024, v4\n\t"
  "v_add_u32 v61, 193536, v32\n\t"
  "s_add_u32 m0, s69, 0\n\t"
  "s_add_u32 s70, s73, 0\n\t"
  "buffer_load_dwordx4 v4, s[44:47], s70 offen offset:0  lds\n\t"
  "buffer_load_dwordx4 v5, s[44:47], s70 offen offset:1024  lds\n\t"
  "buffer_load_dwordx4 v60, s[44:47], s70 offen offset:2048  lds\n\t"
  "buffer_load_dwordx4 v61, s[44:47], s70 offen offset:3072  lds\n\t"
  "s_mov_b64 s[40:41], %2\n\t"
  "s_mov_b32 s42, 215040\n\t"
  "s_mov_b32 s43, 0x20000\n\t"
  "s_lshr_b32 s87, s96, 2\n\t"
  "s_add_u32 s87, s87, 0\n\t"
  "s_and_b32 s87, s87, 3\n\t"
  "s_mul_i32 s64, s87, 14336\n\t"
  "s_lshr_b32 s87, s96, 2\n\t"
  "s_add_u32 s87, s87, 1\n\t"
  "s_and_b32 s87, s87, 3\n\t"
  "s_mul_i32 s65, s87, 14336\n\t"
  "s_lshr_b32 s87, s96, 2\n\t"
  "s_add_u32 s87, s87, 2\n\t"
  "s_and_b32 s87, s87, 3\n\t"
  "s_mul_i32 s66, s87, 14336\n\t"
  "s_lshr_b32 s87, s96, 2\n\t"
  "s_add_u32 s87, s87, 3\n\t"
  "s_and_b32 s87, s87, 3\n\t"
  "s_mul_i32 s67, s87, 14336\n\t"
  "s_mul_i32 s87, s90, 57344\n\t"
  "s_lshl_b32 s71, s90, 13\n\t"
  "s_add_u32 s71, s71, s89\n\t"
  "v_lshrrev_b32 v37, 4, v0\n\t"
  "v_and_b32 v34, 7, v0\n\t"
  "v_xor_b32 v34, v34, v37\n\t"
  "v_lshlrev_b32 v34, 4, v34\n\t"
  "v_mul_u32_u24 v35, 896, v1\n\t"
  "v_add_u32 v35, s87, v35\n\t"
  "v_add_u32 v22, v34, v35\n\t"
  "v_xor_b32 v34, 64, v34\n\t"
  "v_add_u32 v34, v34, v35\n\t"
  "v_add_u32 v23, 6144, v34\n\t"
  "s_add_u32 m0, s71, 65536\n\t"
  "s_add_u32 s70, s64, 0\n\t"
  "buffer_load_dwordx4 v22, s[40:43], s70 offen offset:0 lds\n\t"
  "buffer_load_dwordx4 v23, s[40:43], s70 offen offset:1024 lds\n\t"
  "s_add_u32 m0, s71, 67584\n\t"
  "s_add_u32 s70, s65, 0\n\t"
  "buffer_load_dwordx4 v22, s[40:43], s70 offen offset:0 lds\n\t"
  "buffer_load_dwordx4 v23, s[40:43], s70 offen offset:1024 lds\n\t"
  "s_add_u32 m0, s71, 69632\n\t"
  "s_add_u32 s70, s66, 0\n\t"
  "buffer_load_dwordx4 v22, s[40:43], s70 offen offset:0 lds\n\t"
  "buffer_load_dwordx4 v23, s[40:43], s70 offen offset:1024 lds\n\t"
  "s_add_u32 m0, s71, 71680\n\t"
  "s_add_u32 s70, s67, 0\n\t"
  "buffer_load_dwordx4 v22, s[40:43], s70 offen offset:0 lds\n\t"
  "buffer_load_dwordx4 v23, s[40:43], s70 offen offset:1024 lds\n\t"
  "s_getpc_b64 s[84:85]\n\t"
  "L_k1_pc_%=:\n\t"
  "s_cmp_eq_u32 s90, 3\n\t"
  "s_cselect_b32 s87, 128, 0\n\t"
  "s_lshr_b32 s70, s99, 2\n\t"
  "s_and_b32 s70, s70, 12\n\t"
  "s_and_b32 s86, s99, 3\n\t"
  "s_or_b32 s70, s70, s86\n\t"
  "s_lshl_b32 s70, s70, 13\n\t"
  "v_mul_u32_u24 v34, s87, v0\n\t"
  "v_add_u32 v34, s70, v34\n\t"
  "v_min_u32 v34, L_k1_end_all-L_k1_pc_%=, v34\n\t"
  "global_load_dword v58, v34, s[84:85]\n\t"
  "s_add_u32 m0, s69, 16384\n\t"
  "s_add_u32 s70, s73, 524288\n\t"
  "buffer_load_dwordx4 v4, s[44:47], s70 offen offset:0  lds\n\t"
  "buffer_load_dwordx4 v5, s[44:47], s70 offen offset:1024  lds\n\t"
  "buffer_load_dwordx4 v60, s[44:47], s70 offen offset:2048  lds\n\t"
  "buffer_load_dwordx4 v61, s[44:47], s70 offen offset:3072  lds\n\t"
  "s_add_u32 m0, s71, 98304\n\t"
  "s_add_u32 s70, s64, 128\n\t"
  "buffer_load_dwordx4 v22, s[40:43], s70 offen offset:0 lds\n\t"
  "buffer_load_dwordx4 v23, s[40:43], s70 offen offset:1024 lds\n\t"
  "s_add_u32 m0, s71, 100352\n\t"
  "s_add_u32 s70, s65, 128\n\t"
  "buffer_load_dwordx4 v22, s[40:43], s70 offen offset:0 lds\n\t"
  "buffer_load_dwordx4 v23, s[40:43], s70 offen offset:1024 lds\n\t"
  "s_add_u32 m0, s71, 102400\n\t"
  "s_add_u32 s70, s66, 128\n\t"
  "buffer_load_dwordx4 v22, s[40:43], s70 offen offset:0 lds\n\t"
  "buffer_load_dwordx4 v23, s[40:43], s70 offen offset:1024 lds\n\t"
  "s_add_u32 m0, s71, 104448\n\t"
  "s_add_u32 s70, s67, 128\n\t"
  "buffer_load_dwordx4 v22, s[40:43], s70 offen offset:0 lds\n\t"
  "buffer_load_dwordx4 v23, s[40:43], s70 offen offset:1024 lds\n\t"
  "s_mov_b64 s[48:49], %4\n\t"
  "s_mov_b32 s50, 262144\n\t"
  "s_mov_b32 s51, 0x20000\n\t"
  "s_mov_b64 s[52:53], %5\n\t"
  "s_mov_b32 s54, 262144\n\t"
  "s_mov_b32 s55, 0x20000\n\t"
  "s_lshr_b32 s97, s99, 4\n\t"
  "s_mov_b64 s[92:93], %6\n\t"
  "s_add_u32 s92, s92, 0x1e00000\n\ts_addc_u32 s93, s93, 0\n\t"
  "s_mov_b32 s94, 16384\n\t"
  "s_mov_b32 s95, 0x20000\n\t"
  "s_lshl_b32 s70, s97, 7\n\t"
  "v_mov_b32 v21, s70\n\t"
  "s_mul_i32 s70, s97, 1966080\n\t"
  "s_lshl_b32 s87, s96, 13\n\t"
  "s_add_u32 s70, s70, s87\n\t"
  "s_sub_u32 s87, 1966080, s87\n\t"
  "s_mov_b64 s[56:57], %6\n\t"
  "s_add_u32 s56, s56, s70\n\ts_addc_u32 s57, s57, 0\n\t"
  "s_mov_b32 s58, s87\n\t"
  "s_mov_b32 s59, 0x20000\n\t"
  "s_lshl_b32 s74, s90, 18\n\t"
  "s_add_u32 s91, s74, 3145984\n\t"
  "s_add_u32 s70, s74, 0xfff80180\n\t"
  "s_cmp_lt_u32 s90, 2\n\t"
  "s_cselect_b32 s91, s91, s70\n\t"
  "s_add_u32 s98, s96, s97\n\t"
  "s_and_b32 s98, s98, 15\n\t"
  "s_lshl_b32 s98, s98, 14\n\t"
  "s_lshl_b32 s87, s90, 14\n\t"
  "s_add_u32 s70, s96, 0\n\t"
  "s_and_b32 s70, s70, 3\n\t"
  "s_lshl_b32 s75, s70, 7\n\t"
  "s_add_u32 s75, s75, s87\n\t"
  "s_lshl_b32 s79, s70, 11\n\t"
  "s_add_u32 s70, s96, 1\n\t"
  "s_and_b32 s70, s70, 3\n\t"
  "s_lshl_b32 s76, s70, 7\n\t"
  "s_add_u32 s76, s76, s87\n\t"
  "s_lshl_b32 s80, s70, 11\n\t"
  "s_add_u32 s70, s96, 2\n\t"
  "s_and_b32 s70, s70, 3\n\t"
  "s_lshl_b32 s77, s70, 7\n\t"
  "s_add_u32 s77, s77, s87\n\t"
  "s_lshl_b32 s81, s70, 11\n\t"
  "s_add_u32 s70, s96, 3\n\t"
  "s_and_b32 s70, s70, 3\n\t"
  "s_lshl_b32 s78, s70, 7\n\t"
  "s_add_u32 s78, s78, s87\n\t"
  "s_lshl_b32 s82, s70, 11\n\t"
  "s_mov_b32 s83, 0x3db8aa3b\n\t"
  "v_and_b32 v36, 15, v0\n\t"
  "v_lshrrev_b32 v32, 1, v36\n\t"
  "v_xor_b32 v32, v32, v37\n\t"
  "v_lshlrev_b32 v32, 4, v32\n\t"
  "v_lshl_add_u32 v32, v36, 7, v32\n\t"
  "v_add_u32 v32, s71, v32\n\t"
  "v_add_u32 v10, 0x10000, v32\n\t"
  "v_xor_b32 v11, 64, v10\n\t"
  "v_add_u32 v26, 0x10000, v10\n\t"
  "v_add_u32 v27, 0x10000, v11\n\t"
  "v_bfe_u32 v32, v0, 2, 2\n\t"
  "v_lshl_add_u32 v34, v37, 3, v32\n\t"
  "v_lshlrev_b32 v34, 7, v34\n\t"
  "v_and_b32 v35, 3, v0\n\t"
  "v_lshl_add_u32 v34, v35, 3, v34\n\t"
  "v_add_u32 v34, s89, v34\n\t"
  "v_bfe_u32 v35, v0, 3, 2\n\t"
  "v_lshl_add_u32 v12, v35, 5, v34\n\t"
  "v_xor_b32 v13, 32, v12\n\t"
  "v_xor_b32 v14, 64, v12\n\t"
  "v_xor_b32 v15, 96, v12\n\t"
  "v_xor_b32 v32, v37, v2\n\t"
  "v_lshl_add_u32 v32, v32, 5, v3\n\t"
  "v_lshl_add_u32 v30, v1, 9, v32\n\t"
  "s_and_b32 s70, s90, 1\n\t"
  "s_lshl_b32 s70, s70, 3\n\t"
  "s_lshl_b32 s87, s90, 12\n\t"
  "v_lshrrev_b32 v32, 5, v0\n\t"
  "v_and_b32 v34, 31, v0\n\t"
  "v_or_b32 v35, s70, v32\n\t"
  "v_xor_b32 v34, v34, v35\n\t"
  "v_lshlrev_b32 v34, 4, v34\n\t"
  "v_lshl_add_u32 v34, v32, 9, v34\n\t"
  "v_add_u32 v6, s87, v34\n\t"
  "v_xor_b32 v7, 32, v6\n\t"
  "v_xor_b32 v8, 64, v6\n\t"
  "v_xor_b32 v9, 96, v6\n\t"
  "v_bfe_u32 v32, v0, 4, 1\n\t"
  "v_bfe_u32 v34, v0, 5, 1\n\t"
  "v_lshl_add_u32 v32, v32, 1, v34\n\t"
  "v_xor_b32 v32, v32, v36\n\t"
  "v_lshlrev_b32 v32, 4, v32\n\t"
  "v_lshl_add_u32 v32, v36, 9, v32\n\t"
  "v_add_u32 v32, s89, v32\n\t"
  "v_add_u32 v16, 65536, v32\n\t"
  "v_xor_b32 v17, 64, v16\n\t"
  "v_xor_b32 v18, 128, v16\n\t"
  "v_xor_b32 v19, 192, v16\n\t"
  "v_lshlrev_b32 v20, 4, v0\n\t"
  "v_lshlrev_b32 v62, 9, v37\n\t"
  "s_cmp_eq_u32 s90, 3\n\t"
  "s_cselect_b32 s68, 0xff800000, 0\n\t"
  "s_mov_b64 s[84:85], 0\n\t"
  "s_waitcnt vmcnt(13)\n\t"
  "s_barrier\n\t"
  "ds_read_b128 a[0:3], v10 offset:0\n\t"
  "ds_read_b128 a[4:7], v10 offset:2048\n\t"
  "ds_read_b128 a[8:11], v10 offset:4096\n\t"
  "ds_read_b128 a[12:15], v10 offset:6144\n\t"
  "ds_read_b128 a[16:19], v11 offset:0\n\t"
  "ds_read_b128 a[20:23], v11 offset:2048\n\t"
  "ds_read_b128 a[24:27], v11 offset:4096\n\t"
  "ds_read_b128 a[28:31], v11 offset:6144\n\t"
  "ds_read_b64_tr_b16 v[32:33], v12 offset:0\n\t"
  "ds_read_b64_tr_b16 v[34:35], v12 offset:512\n\t"
  "ds_read_b64_tr_b16 v[36:37], v12 offset:8192\n\t"
  "ds_read_b64_tr_b16 v[38:39], v12 offset:8704\n\t"
  "ds_read_b64_tr_b16 v[40:41], v13 offset:0\n\t"
  "ds_read_b64_tr_b16 v[42:43], v13 offset:512\n\t"
  "ds_read_b64_tr_b16 v[44:45], v13 offset:8192\n\t"
  "ds_read_b64_tr_b16 v[46:47], v13 offset:8704\n\t"
  "s_waitcnt lgkmcnt(4)\n\t"
  "v_mfma_f32_16x16x32_bf16 v[64:67], v[32:35], a[0:3], 0\n\t"
  "ds_read_b64_tr_b16 v[48:49], v14 offset:0\n\t"
  "s_add_u32 m0, s69, 32768\n\t"
  "v_mfma_f32_16x16x32_bf16 v[64:67], v[36:39], a[16:19], v[64:67]\n\t"
  "ds_read_b64_tr_b16 v[50:51], v14 offset:512\n\t"
  "s_add_u32 s70, s73, 1048576\n\t"
  "v_mfma_f32_16x16x32_bf16 v[68:71], v[36:39], a[20:23], 0\n\t"
  "buffer_load_dwordx4 v4, s[44:47], s70 offen offset:0  lds\n\t"
  "v_mfma_f32_16x16x32_bf16 v[68:71], v[32:35], a[4:7], v[68:71]\n\t"
  "v_mfma_f32_16x16x32_bf16 v[72:75], v[32:35], a[8:11], 0\n\t"
  "ds_read_b64_tr_b16 v[52:53], v14 offset:8192\n\t"
  "v_mfma_f32_16x16x32_bf16 v[72:75], v[36:39], a[24:27], v[72:75]\n\t"
  "ds_read_b64_tr_b16 v[54:55], v14 offset:8704\n\t"
  "v_mfma_f32_16x16x32_bf16 v[76:79], v[36:39], a[28:31], 0\n\t"
  "buffer_load_dwordx4 v5, s[44:47], s70 offen offset:1024  lds\n\t"
  "v_mfma_f32_16x16x32_bf16 v[76:79], v[32:35], a[12:15], v[76:79]\n\t"
  "s_waitcnt lgkmcnt(4)\n\t"
  "v_mfma_f32_16x16x32_bf16 v[80:83], v[40:43], a[0:3], 0\n\t"
  "ds_read_b64_tr_b16 v[32:33], v15 offset:0\n\t"
  "v_mfma_f32_16x16x32_bf16 v[80:83], v[44:47], a[16:19], v[80:83]\n\t"
  "ds_read_b64_tr_b16 v[34:35], v15 offset:512\n\t"
  "v_mfma_f32_16x16x32_bf16 v[84:87], v[44:47], a[20:23], 0\n\t"
  "buffer_load_dwordx4 v60, s[44:47], s70 offen offset:2048  lds\n\t"
  "v_mfma_f32_16x16x32_bf16 v[84:87], v[40:43], a[4:7], v[84:87]\n\t"
  "v_mfma_f32_16x16x32_bf16 v[88:91], v[40:43], a[8:11], 0\n\t"
  "ds_read_b64_tr_b16 v[36:37], v15 offset:8192\n\t"
  "v_mfma_f32_16x16x32_bf16 v[88:91], v[44:47], a[24:27], v[88:91]\n\t"
  "ds_read_b64_tr_b16 v[38:39], v15 offset:8704\n\t"
  "v_mfma_f32_16x16x32_bf16 v[92:95], v[44:47], a[28:31], 0\n\t"
  "buffer_load_dwordx4 v61, s[44:47], s70 offen offset:3072  lds\n\t"
  "s_add_u32 m0, s71, 131072\n\t"
  "v_mfma_f32_16x16x32_bf16 v[92:95], v[40:43], a[12:15], v[92:95]\n\t"
  "s_add_u32 s70, s64, 256\n\t"
  "s_waitcnt lgkmcnt(4)\n\t"
  "v_mfma_f32_16x16x32_bf16 v[96:99], v[48:51], a[0:3], 0\n\t"
  "ds_read_b64_tr_b16 v[40:41], v12 offset:4096\n\t"
  "v_mfma_f32_16x16x32_bf16 v[96:99], v[52:55], a[16:19], v[96:99]\n\t"
  "ds_read_b64_tr_b16 v[42:43], v12 offset:4608\n\t"
  "v_mfma_f32_16x16x32_bf16 v[100:103], v[52:55], a[20:23], 0\n\t"
  "buffer_load_dwordx4 v22, s[40:43], s70 offen offset:0 lds\n\t"
  "v_mfma_f32_16x16x32_bf16 v[100:103], v[48:51], a[4:7], v[100:103]\n\t"
  "v_mfma_f32_16x16x32_bf16 v[104:107], v[48:51], a[8:11], 0\n\t"
  "ds_read_b64_tr_b16 v[44:45], v12 offset:12288\n\t"
  "v_mfma_f32_16x16x32_bf16 v[104:107], v[52:55], a[24:27], v[104:107]\n\t"
  "ds_read_b64_tr_b16 v[46:47], v12 offset:12800\n\t"
  "v_mfma_f32_16x16x32_bf16 v[108:111], v[52:55], a[28:31], 0\n\t"
  "buffer_load_dwordx4 v23, s[40:43], s70 offen offset:1024 lds\n\t"
  "s_add_u32 m0, s71, 133120\n\t"
  "v_mfma_f32_16x16x32_bf16 v[108:111], v[48:51], a[12:15], v[108:111]\n\t"
  "s_add_u32 s70, s65, 256\n\t"
  "s_waitcnt lgkmcnt(4)\n\t"
  "v_mfma_f32_16x16x32_bf16 v[112:115], v[32:35], a[0:3], 0\n\t"
  "ds_read_b64_tr_b16 v[48:49], v13 offset:4096\n\t"
  "v_mfma_f32_16x16x32_bf16 v[112:115], v[36:39], a[16:19], v[112:115]\n\t"
  "ds_read_b64_tr_b16 v[50:51], v13 offset:4608\n\t"
  "v_mfma_f32_16x16x32_bf16 v[116:119], v[36:39], a[20:23], 0\n\t"
  "buffer_load_dwordx4 v22, s[40:43], s70 offen offset:0 lds\n\t"
  "v_mfma_f32_16x16x32_bf16 v[116:119], v[32:35], a[4:7], v[116:119]\n\t"
  "v_mfma_f32_16x16x32_bf16 v[120:123], v[32:35], a[8:11], 0\n\t"
  "ds_read_b64_tr_b16 v[52:53], v13 offset:12288\n\t"
  "v_mfma_f32_16x16x32_bf16 v[120:123], v[36:39], a[24:27], v[120:123]\n\t"
  "ds_read_b64_tr_b16 v[54:55], v13 offset:12800\n\t"
  "v_mfma_f32_16x16x32_bf16 v[124:127], v[36:39], a[28:31], 0\n\t"
  "buffer_load_dwordx4 v23, s[40:43], s70 offen offset:1024 lds\n\t"
  "s_add_u32 m0, s71, 135168\n\t"
  "v_mfma_f32_16x16x32_bf16 v[124:127], v[32:35], a[12:15], v[124:127]\n\t"
  "s_add_u32 s70, s66, 256\n\t"
  "s_waitcnt lgkmcnt(4)\n\t"
  "v_mfma_f32_16x16x32_bf16 v[128:131], v[40:43], a[0:3], 0\n\t"
  "ds_read_b64_tr_b16 v[32:33], v14 offset:4096\n\t"
  "v_mfma_f32_16x16x32_bf16 v[128:131], v[44:47], a[16:19], v[128:131]\n\t"
  "ds_read_b64_tr_b16 v[34:35], v14 offset:4608\n\t"
  "v_mfma_f32_16x16x32_bf16 v[132:135], v[44:47], a[20:23], 0\n\t"
  "buffer_load_dwordx4 v22, s[40:43], s70 offen offset:0 lds\n\t"
  "v_mfma_f32_16x16x32_bf16 v[132:135], v[40:43], a[4:7], v[132:135]\n\t"
  "v_mfma_f32_16x16x32_bf16 v[136:139], v[40:43], a[8:11], 0\n\t"
  "ds_read_b64_tr_b16 v[36:37], v14 offset:12288\n\t"
  "v_mfma_f32_16x16x32_bf16 v[136:139], v[44:47], a[24:27], v[136:139]\n\t"
  "ds_read_b64_tr_b16 v[38:39], v14 offset:12800\n\t"
  "v_mfma_f32_16x16x32_bf16 v[140:143], v[44:47], a[28:31], 0\n\t"
  "buffer_load_dwordx4 v23, s[40:43], s70 offen offset:1024 lds\n\t"
  "v_mfma_f32_16x16x32_bf16 v[140:143], v[40:43], a[12:15], v[140:143]\n\t"
  "s_waitcnt lgkmcnt(4)\n\t"
  "v_mfma_f32_16x16x32_bf16 v[144:147], v[48:51], a[0:3], 0\n\t"
  "ds_read_b64_tr_b16 v[40:41], v15 offset:4096\n\t"
  "v_mfma_f32_16x16x32_bf16 v[144:147], v[52:55], a[16:19], v[144:147]\n\t"
  "ds_read_b64_tr_b16 v[42:43], v15 offset:4608\n\t"
  "s_waitcnt vmcnt(10)\n\t"
  "v_mfma_f32_16x16x32_bf16 v[148:151], v[52:55], a[20:23], 0\n\t"
  "ds_read_b128 a[32:35], v10 offset:32768\n\t"
  "v_mfma_f32_16x16x32_bf16 v[148:151], v[48:51], a[4:7], v[148:151]\n\t"
  "s_barrier\n\t"
  "v_mfma_f32_16x16x32_bf16 v[152:155], v[48:51], a[8:11], 0\n\t"
  "ds_read_b64_tr_b16 v[44:45], v15 offset:12288\n\t"
  "v_mfma_f32_16x16x32_bf16 v[152:155], v[52:55], a[24:27], v[152:155]\n\t"
  "ds_read_b64_tr_b16 v[46:47], v15 offset:12800\n\t"
  "v_mfma_f32_16x16x32_bf16 v[156:159], v[52:55], a[28:31], 0\n\t"
  "ds_read_b128 a[36:39], v10 offset:34816\n\t"
  "v_mfma_f32_16x16x32_bf16 v[156:159], v[48:51], a[12:15], v[156:159]\n\t"
  "ds_read_b128 a[40:43], v10 offset:36864\n\t"
  "s_waitcnt lgkmcnt(7)\n\t"
  "v_mfma_f32_16x16x32_bf16 v[160:163], v[32:35], a[0:3], 0\n\t"
  "ds_read_b64_tr_b16 v[48:49], v12 offset:16384\n\t"
  "v_mfma_f32_16x16x32_bf16 v[160:163], v[36:39], a[16:19], v[160:163]\n\t"
  "ds_read_b64_tr_b16 v[50:51], v12 offset:16896\n\t"
  "v_mfma_f32_16x16x32_bf16 v[164:167], v[36:39], a[20:23], 0\n\t"
  "ds_read_b128 a[44:47], v10 offset:38912\n\t"
  "v_mfma_f32_16x16x32_bf16 v[164:167], v[32:35], a[4:7], v[164:167]\n\t"
  "ds_read_b128 a[48:51], v11 offset:32768\n\t"
  "v_mfma_f32_16x16x32_bf16 v[168:171], v[32:35], a[8:11], 0\n\t"
  "ds_read_b64_tr_b16 v[52:53], v12 offset:24576\n\t"
  "v_mfma_f32_16x16x32_bf16 v[168:171], v[36:39], a[24:27], v[168:171]\n\t"
  "ds_read_b64_tr_b16 v[54:55], v12 offset:25088\n\t"
  "s_add_u32 m0, s71, 137216\n\t"
  "v_mfma_f32_16x16x32_bf16 v[172:175], v[36:39], a[28:31], 0\n\t"
  "ds_read_b128 a[52:55], v11 offset:34816\n\t"
  "s_add_u32 s70, s67, 256\n\t"
  "v_mfma_f32_16x16x32_bf16 v[172:175], v[32:35], a[12:15], v[172:175]\n\t"
  "ds_read_b128 a[56:59], v11 offset:36864\n\t"
  "s_waitcnt lgkmcnt(10)\n\t"
  "v_mfma_f32_16x16x32_bf16 v[176:179], v[40:43], a[0:3], 0\n\t"
  "ds_read_b64_tr_b16 v[32:33], v13 offset:16384\n\t"
  "v_mfma_f32_16x16x32_bf16 v[176:179], v[44:47], a[16:19], v[176:179]\n\t"
  "ds_read_b64_tr_b16 v[34:35], v13 offset:16896\n\t"
  "v_mfma_f32_16x16x32_bf16 v[180:183], v[44:47], a[20:23], 0\n\t"
  "ds_read_b128 a[60:63], v11 offset:38912\n\t"
  "v_mfma_f32_16x16x32_bf16 v[180:183], v[40:43], a[4:7], v[180:183]\n\t"
  "buffer_load_dwordx4 v22, s[40:43], s70 offen offset:0 lds\n\t"
  "v_mfma_f32_16x16x32_bf16 v[184:187], v[40:43], a[8:11], 0\n\t"
  "ds_read_b64_tr_b16 v[36:37], v13 offset:24576\n\t"
  "v_mfma_f32_16x16x32_bf16 v[184:187], v[44:47], a[24:27], v[184:187]\n\t"
  "ds_read_b64_tr_b16 v[38:39], v13 offset:25088\n\t"
  "v_mfma_f32_16x16x32_bf16 v[188:191], v[44:47], a[28:31], 0\n\t"
  "buffer_load_dwordx4 v23, s[40:43], s70 offen offset:1024 lds\n\t"
  "s_add_u32 m0, s69, 49152\n\t"
  "v_mfma_f32_16x16x32_bf16 v[188:191], v[40:43], a[12:15], v[188:191]\n\t"
  "s_add_u32 s70, s73, 1572864\n\t"
  "s_waitcnt lgkmcnt(7)\n\t"
  "v_mfma_f32_16x16x32_bf16 v[64:67], v[48:51], a[32:35], v[64:67]\n\t"
  "ds_read_b64_tr_b16 v[40:41], v14 offset:16384\n\t"
  "v_mfma_f32_16x16x32_bf16 v[64:67], v[52:55], a[48:51], v[64:67]\n\t"
  "ds_read_b64_tr_b16 v[42:43], v14 offset:16896\n\t"
  "s_waitcnt lgkmcnt(8)\n\t"
  "v_mfma_f32_16x16x32_bf16 v[68:71], v[52:55], a[52:55], v[68:71]\n\t"
  "buffer_load_dwordx4 v4, s[44:47], s70 offen offset:0  lds\n\t"
  "v_mfma_f32_16x16x32_bf16 v[68:71], v[48:51], a[36:39], v[68:71]\n\t"
  "v_mfma_f32_16x16x32_bf16 v[72:75], v[48:51], a[40:43], v[72:75]\n\t"
  "ds_read_b64_tr_b16 v[44:45], v14 offset:24576\n\t"
  "s_waitcnt lgkmcnt(8)\n\t"
  "v_mfma_f32_16x16x32_bf16 v[72:75], v[52:55], a[56:59], v[72:75]\n\t"
  "ds_read_b64_tr_b16 v[46:47], v14 offset:25088\n\t"
  "s_waitcnt lgkmcnt(6)\n\t"
  "v_mfma_f32_16x16x32_bf16 v[76:79], v[52:55], a[60:63], v[76:79]\n\t"
  "buffer_load_dwordx4 v5, s[44:47], s70 offen offset:1024  lds\n\t"
  "v_mfma_f32_16x16x32_bf16 v[76:79], v[48:51], a[44:47], v[76:79]\n\t"
  "s_waitcnt lgkmcnt(4)\n\t"
  "v_mfma_f32_16x16x32_bf16 v[80:83], v[32:35], a[32:35], v[80:83]\n\t"
  "ds_read_b64_tr_b16 v[48:49], v15 offset:16384\n\t"
  "v_mfma_f32_16x16x32_bf16 v[80:83], v[36:39], a[48:51], v[80:83]\n\t"
  "ds_read_b64_tr_b16 v[50:51], v15 offset:16896\n\t"
  "v_mfma_f32_16x16x32_bf16 v[84:87], v[36:39], a[52:55], v[84:87]\n\t"
  "buffer_load_dwordx4 v60, s[44:47], s70 offen offset:2048  lds\n\t"
  "v_mfma_f32_16x16x32_bf16 v[84:87], v[32:35], a[36:39], v[84:87]\n\t"
  "v_mfma_f32_16x16x32_bf16 v[88:91], v[32:35], a[40:43], v[88:91]\n\t"
  "ds_read_b64_tr_b16 v[52:53], v15 offset:24576\n\t"
  "v_mfma_f32_16x16x32_bf16 v[88:91], v[36:39], a[56:59], v[88:91]\n\t"
  "ds_read_b64_tr_b16 v[54:55], v15 offset:25088\n\t"
  "v_mfma_f32_16x16x32_bf16 v[92:95], v[36:39], a[60:63], v[92:95]\n\t"
  "buffer_load_dwordx4 v61, s[44:47], s70 offen offset:3072  lds\n\t"
  "s_add_u32 m0, s71, 65536\n\t"
  "v_mfma_f32_16x16x32_bf16 v[92:95], v[32:35], a[44:47], v[92:95]\n\t"
  "s_add_u32 s70, s64, 384\n\t"
  "s_waitcnt lgkmcnt(4)\n\t"
  "v_mfma_f32_16x16x32_bf16 v[96:99], v[40:43], a[32:35], v[96:99]\n\t"
  "ds_read_b64_tr_b16 v[32:33], v12 offset:20480\n\t"
  "v_mfma_f32_16x16x32_bf16 v[96:99], v[44:47], a[48:51], v[96:99]\n\t"
  "ds_read_b64_tr_b16 v[34:35], v12 offset:20992\n\t"
  "v_mfma_f32_16x16x32_bf16 v[100:103], v[44:47], a[52:55], v[100:103]\n\t"
  "buffer_load_dwordx4 v22, s[40:43], s70 offen offset:0 lds\n\t"
  "v_mfma_f32_16x16x32_bf16 v[100:103], v[40:43], a[36:39], v[100:103]\n\t"
  "v_mfma_f32_16x16x32_bf16 v[104:107], v[40:43], a[40:43], v[104:107]\n\t"
  "ds_read_b64_tr_b16 v[36:37], v12 offset:28672\n\t"
  "v_mfma_f32_16x16x32_bf16 v[104:107], v[44:47], a[56:59], v[104:107]\n\t"
  "ds_read_b64_tr_b16 v[38:39], v12 offset:29184\n\t"
  "v_mfma_f32_16x16x32_bf16 v[108:111], v[44:47], a[60:63], v[108:111]\n\t"
  "buffer_load_dwordx4 v23, s[40:43], s70 offen offset:1024 lds\n\t"
  "s_add_u32 m0, s71, 67584\n\t"
  "v_mfma_f32_16x16x32_bf16 v[108:111], v[40:43], a[44:47], v[108:111]\n\t"
  "s_add_u32 s70, s65, 384\n\t"
  "s_waitcnt lgkmcnt(4)\n\t"
  "v_mfma_f32_16x16x32_bf16 v[112:115], v[48:51], a[32:35], v[112:115]\n\t"
  "ds_read_b64_tr_b16 v[40:41], v13 offset:20480\n\t"
  "v_mfma_f32_16x16x32_bf16 v[112:115], v[52:55], a[48:51], v[112:115]\n\t"
  "ds_read_b64_tr_b16 v[42:43], v13 offset:20992\n\t"
  "v_mfma_f32_16x16x32_bf16 v[116:119], v[52:55], a[52:55], v[116:119]\n\t"
  "buffer_load_dwordx4 v22, s[40:43], s70 offen offset:0 lds\n\t"
  "v_mfma_f32_16x16x32_bf16 v[116:119], v[48:51], a[36:39], v[116:119]\n\t"
  "v_mfma_f32_16x16x32_bf16 v[120:123], v[48:51], a[40:43], v[120:123]\n\t"
  "ds_read_b64_tr_b16 v[44:45], v13 offset:28672\n\t"
  "v_mfma_f32_16x16x32_bf16 v[120:123], v[52:55], a[56:59], v[120:123]\n\t"
  "ds_read_b64_tr_b16 v[46:47], v13 offset:29184\n\t"
  "v_mfma_f32_16x16x32_bf16 v[124:127], v[52:55], a[60:63], v[124:127]\n\t"
  "buffer_load_dwordx4 v23, s[40:43], s70 offen offset:1024 lds\n\t"
  "s_add_u32 m0, s71, 69632\n\t"
  "v_mfma_f32_16x16x32_bf16 v[124:127], v[48:51], a[44:47], v[124:127]\n\t"
  "s_add_u32 s70, s66, 384\n\t"
  "s_waitcnt lgkmcnt(4)\n\t"
  "v_mfma_f32_16x16x32_bf16 v[128:131], v[32:35], a[32:35], v[128:131]\n\t"
  "ds_read_b64_tr_b16 v[48:49], v14 offset:20480\n\t"
  "v_mfma_f32_16x16x32_bf16 v[128:131], v[36:39], a[48:51], v[128:131]\n\t"
  "ds_read_b64_tr_b16 v[50:51], v14 offset:20992\n\t"
  "v_mfma_f32_16x16x32_bf16 v[132:135], v[36:39], a[52:55], v[132:135]\n\t"
  "buffer_load_dwordx4 v22, s[40:43], s70 offen offset:0 lds\n\t"
  "v_mfma_f32_16x16x32_bf16 v[132:135], v[32:35], a[36:39], v[132:135]\n\t"
  "v_mfma_f32_16x16x32_bf16 v[136:139], v[32:35], a[40:43], v[136:139]\n\t"
  "ds_read_b64_tr_b16 v[52:53], v14 offset:28672\n\t"
  "v_mfma_f32_16x16x32_bf16 v[136:139], v[36:39], a[56:59], v[136:139]\n\t"
  "ds_read_b64_tr_b16 v[54:55], v14 offset:29184\n\t"
  "v_mfma_f32_16x16x32_bf16 v[140:143], v[36:39], a[60:63], v[140:143]\n\t"
  "buffer_load_dwordx4 v23, s[40:43], s70 offen offset:1024 lds\n\t"
  "v_mfma_f32_16x16x32_bf16 v[140:143], v[32:35], a[44:47], v[140:143]\n\t"
  "s_waitcnt lgkmcnt(4)\n\t"
  "v_mfma_f32_16x16x32_bf16 v[144:147], v[40:43], a[32:35], v[144:147]\n\t"
  "ds_read_b64_tr_b16 v[32:33], v15 offset:20480\n\t"
  "v_mfma_f32_16x16x32_bf16 v[144:147], v[44:47], a[48:51], v[144:147]\n\t"
  "ds_read_b64_tr_b16 v[34:35], v15 offset:20992\n\t"
  "s_waitcnt vmcnt(10)\n\t"
  "v_mfma_f32_16x16x32_bf16 v[148:151], v[44:47], a[52:55], v[148:151]\n\t"
  "ds_read_b128 a[64:67], v26 offset:0\n\t"
  "v_mfma_f32_16x16x32_bf16 v[148:151], v[40:43], a[36:39], v[148:151]\n\t"
  "s_barrier\n\t"
  "v_mfma_f32_16x16x32_bf16 v[152:155], v[40:43], a[40:43], v[152:155]\n\t"
  "ds_read_b64_tr_b16 v[36:37], v15 offset:28672\n\t"
  "v_mfma_f32_16x16x32_bf16 v[152:155], v[44:47], a[56:59], v[152:155]\n\t"
  "ds_read_b64_tr_b16 v[38:39], v15 offset:29184\n\t"
  "v_mfma_f32_16x16x32_bf16 v[156:159], v[44:47], a[60:63], v[156:159]\n\t"
  "ds_read_b128 a[68:71], v26 offset:2048\n\t"
  "v_mfma_f32_16x16x32_bf16 v[156:159], v[40:43], a[44:47], v[156:159]\n\t"
  "ds_read_b128 a[72:75], v26 offset:4096\n\t"
  "s_waitcnt lgkmcnt(7)\n\t"
  "v_mfma_f32_16x16x32_bf16 v[160:163], v[48:51], a[32:35], v[160:163]\n\t"
  "ds_read_b64_tr_b16 v[40:41], v12 offset:32768\n\t"
  "v_mfma_f32_16x16x32_bf16 v[160:163], v[52:55], a[48:51], v[160:163]\n\t"
  "ds_read_b64_tr_b16 v[42:43], v12 offset:33280\n\t"
  "v_mfma_f32_16x16x32_bf16 v[164:167], v[52:55], a[52:55], v[164:167]\n\t"
  "ds_read_b128 a[76:79], v26 offset:6144\n\t"
  "v_mfma_f32_16x16x32_bf16 v[164:167], v[48:51], a[36:39], v[164:167]\n\t"
  "ds_read_b128 a[80:83], v27 offset:0\n\t"
  "v_mfma_f32_16x16x32_bf16 v[168:171], v[48:51], a[40:43], v[168:171]\n\t"
  "ds_read_b64_tr_b16 v[44:45], v12 offset:40960\n\t"
  "v_mfma_f32_16x16x32_bf16 v[168:171], v[52:55], a[56:59], v[168:171]\n\t"
  "ds_read_b64_tr_b16 v[46:47], v12 offset:41472\n\t"
  "s_add_u32 m0, s71, 71680\n\t"
  "v_mfma_f32_16x16x32_bf16 v[172:175], v[52:55], a[60:63], v[172:175]\n\t"
  "ds_read_b128 a[84:87], v27 offset:2048\n\t"
  "s_add_u32 s70, s67, 384\n\t"
  "v_mfma_f32_16x16x32_bf16 v[172:175], v[48:51], a[44:47], v[172:175]\n\t"
  "ds_read_b128 a[88:91], v27 offset:4096\n\t"
  "s_waitcnt lgkmcnt(10)\n\t"
  "v_mfma_f32_16x16x32_bf16 v[176:179], v[32:35], a[32:35], v[176:179]\n\t"
  "ds_read_b64_tr_b16 v[48:49], v13 offset:32768\n\t"
  "v_mfma_f32_16x16x32_bf16 v[176:179], v[36:39], a[48:51], v[176:179]\n\t"
  "ds_read_b64_tr_b16 v[50:51], v13 offset:33280\n\t"
  "v_mfma_f32_16x16x32_bf16 v[180:183], v[36:39], a[52:55], v[180:183]\n\t"
  "ds_read_b128 a[92:95], v27 offset:6144\n\t"
  "v_mfma_f32_16x16x32_bf16 v[180:183], v[32:35], a[36:39], v[180:183]\n\t"
  "buffer_load_dwordx4 v22, s[40:43], s70 offen offset:0 lds\n\t"
  "v_mfma_f32_16x16x32_bf16 v[184:187], v[32:35], a[40:43], v[184:187]\n\t"
  "ds_read_b64_tr_b16 v[52:53], v13 offset:40960\n\t"
  "v_mfma_f32_16x16x32_bf16 v[184:187], v[36:39], a[56:59], v[184:187]\n\t"
  "ds_read_b64_tr_b16 v[54:55], v13 offset:41472\n\t"
  "v_mfma_f32_16x16x32_bf16 v[188:191], v[36:39], a[60:63], v[188:191]\n\t"
  "buffer_load_dwordx4 v23, s[40:43], s70 offen offset:1024 lds\n\t"
  "s_add_u32 m0, s71, 98304\n\t"
  "v_mfma_f32_16x16x32_bf16 v[188:191], v[32:35], a[44:47], v[188:191]\n\t"
  "s_add_u32 s70, s64, 512\n\t"
  "s_waitcnt lgkmcnt(7)\n\t"
  "v_mfma_f32_16x16x32_bf16 v[64:67], v[40:43], a[64:67], v[64:67]\n\t"
  "ds_read_b64_tr_b16 v[32:33], v14 offset:32768\n\t"
  "v_mfma_f32_16x16x32_bf16 v[64:67], v[44:47], a[80:83], v[64:67]\n\t"
  "ds_read_b64_tr_b16 v[34:35], v14 offset:33280\n\t"
  "s_waitcnt lgkmcnt(8)\n\t"
  "v_mfma_f32_16x16x32_bf16 v[68:71], v[44:47], a[84:87], v[68:71]\n\t"
  "buffer_load_dwordx4 v22, s[40:43], s70 offen offset:0 lds\n\t"
  "v_mfma_f32_16x16x32_bf16 v[68:71], v[40:43], a[68:71], v[68:71]\n\t"
  "v_mfma_f32_16x16x32_bf16 v[72:75], v[40:43], a[72:75], v[72:75]\n\t"
  "ds_read_b64_tr_b16 v[36:37], v14 offset:40960\n\t"
  "s_waitcnt lgkmcnt(8)\n\t"
  "v_mfma_f32_16x16x32_bf16 v[72:75], v[44:47], a[88:91], v[72:75]\n\t"
  "ds_read_b64_tr_b16 v[38:39], v14 offset:41472\n\t"
  "s_waitcnt lgkmcnt(6)\n\t"
  "v_mfma_f32_16x16x32_bf16 v[76:79], v[44:47], a[92:95], v[76:79]\n\t"
  "buffer_load_dwordx4 v23, s[40:43], s70 offen offset:1024 lds\n\t"
  "s_add_u32 m0, s71, 100352\n\t"
  "v_mfma_f32_16x16x32_bf16 v[76:79], v[40:43], a[76:79], v[76:79]\n\t"
  "s_add_u32 s70, s65, 512\n\t"
  "s_waitcnt lgkmcnt(4)\n\t"
  "v_mfma_f32_16x16x32_bf16 v[80:83], v[48:51], a[64:67], v[80:83]\n\t"
  "ds_read_b64_tr_b16 v[40:41], v15 offset:32768\n\t"
  "v_mfma_f32_16x16x32_bf16 v[80:83], v[52:55], a[80:83], v[80:83]\n\t"
  "ds_read_b64_tr_b16 v[42:43], v15 offset:33280\n\t"
  "v_mfma_f32_16x16x32_bf16 v[84:87], v[52:55], a[84:87], v[84:87]\n\t"
  "buffer_load_dwordx4 v22, s[40:43], s70 offen offset:0 lds\n\t"
  "v_mfma_f32_16x16x32_bf16 v[84:87], v[48:51], a[68:71], v[84:87]\n\t"
  "v_mfma_f32_16x16x32_bf16 v[88:91], v[48:51], a[72:75], v[88:91]\n\t"
  "ds_read_b64_tr_b16 v[44:45], v15 offset:40960\n\t"
  "v_mfma_f32_16x16x32_bf16 v[88:91], v[52:55], a[88:91], v[88:91]\n\t"
  "ds_read_b64_tr_b16 v[46:47], v15 offset:41472\n\t"
  "v_mfma_f32_16x16x32_bf16 v[92:95], v[52:55], a[92:95], v[92:95]\n\t"
  "buffer_load_dwordx4 v23, s[40:43], s70 offen offset:1024 lds\n\t"
  "s_add_u32 m0, s71, 102400\n\t"
  "v_mfma_f32_16x16x32_bf16 v[92:95], v[48:51], a[76:79], v[92:95]\n\t"
  "s_add_u32 s70, s66, 512\n\t"
  "s_waitcnt lgkmcnt(4)\n\t"
  "v_mfma_f32_16x16x32_bf16 v[96:99], v[32:35], a[64:67], v[96:99]\n\t"
  "ds_read_b64_tr_b16 v[48:49], v12 offset:36864\n\t"
  "v_mfma_f32_16x16x32_bf16 v[96:99], v[36:39], a[80:83], v[96:99]\n\t"
  "ds_read_b64_tr_b16 v[50:51], v12 offset:37376\n\t"
  "v_mfma_f32_16x16x32_bf16 v[100:103], v[36:39], a[84:87], v[100:103]\n\t"
  "buffer_load_dwordx4 v22, s[40:43], s70 offen offset:0 lds\n\t"
  "v_mfma_f32_16x16x32_bf16 v[100:103], v[32:35], a[68:71], v[100:103]\n\t"
  "v_mfma_f32_16x16x32_bf16 v[104:107], v[32:35], a[72:75], v[104:107]\n\t"
  "ds_read_b64_tr_b16 v[52:53], v12 offset:45056\n\t"
  "v_mfma_f32_16x16x32_bf16 v[104:107], v[36:39], a[88:91], v[104:107]\n\t"
  "ds_read_b64_tr_b16 v[54:55], v12 offset:45568\n\t"
  "v_mfma_f32_16x16x32_bf16 v[108:111], v[36:39], a[92:95], v[108:111]\n\t"
  "buffer_load_dwordx4 v23, s[40:43], s70 offen offset:1024 lds\n\t"
  "s_add_u32 m0, s71, 104448\n\t"
  "v_mfma_f32_16x16x32_bf16 v[108:111], v[32:35], a[76:79], v[108:111]\n\t"
  "s_add_u32 s70, s67, 512\n\t"
  "s_waitcnt lgkmcnt(4)\n\t"
  "v_mfma_f32_16x16x32_bf16 v[112:115], v[40:43], a[64:67], v[112:115]\n\t"
  "ds_read_b64_tr_b16 v[32:33], v13 offset:36864\n\t"
  "v_mfma_f32_16x16x32_bf16 v[112:115], v[44:47], a[80:83], v[112:115]\n\t"
  "ds_read_b64_tr_b16 v[34:35], v13 offset:37376\n\t"
  "v_mfma_f32_16x16x32_bf16 v[116:119], v[44:47], a[84:87], v[116:119]\n\t"
  "buffer_load_dwordx4 v22, s[40:43], s70 offen offset:0 lds\n\t"
  "v_mfma_f32_16x16x32_bf16 v[116:119], v[40:43], a[68:71], v[116:119]\n\t"
  "v_mfma_f32_16x16x32_bf16 v[120:123], v[40:43], a[72:75], v[120:123]\n\t"
  "ds_read_b64_tr_b16 v[36:37], v13 offset:45056\n\t"
  "v_mfma_f32_16x16x32_bf16 v[120:123], v[44:47], a[88:91], v[120:123]\n\t"
  "ds_read_b64_tr_b16 v[38:39], v13 offset:45568\n\t"
  "v_mfma_f32_16x16x32_bf16 v[124:127], v[44:47], a[92:95], v[124:127]\n\t"
  "buffer_load_dwordx4 v23, s[40:43], s70 offen offset:1024 lds\n\t"
  "s_add_u32 m0, s69, 0\n\t"
  "v_mfma_f32_16x16x32_bf16 v[124:127], v[40:43], a[76:79], v[124:127]\n\t"
  "s_add_u32 s70, s73, 2097152\n\t"
  "s_waitcnt lgkmcnt(4)\n\t"
  "v_mfma_f32_16x16x32_bf16 v[128:131], v[48:51], a[64:67], v[128:131]\n\t"
  "ds_read_b64_tr_b16 v[40:41], v14 offset:36864\n\t"
  "v_mfma_f32_16x16x32_bf16 v[128:131], v[52:55], a[80:83], v[128:131]\n\t"
  "ds_read_b64_tr_b16 v[42:43], v14 offset:37376\n\t"
  "v_mfma_f32_16x16x32_bf16 v[132:135], v[52:55], a[84:87], v[132:135]\n\t"
  "buffer_load_dwordx4 v4, s[44:47], s70 offen offset:0  lds\n\t"
  "v_mfma_f32_16x16x32_bf16 v[132:135], v[48:51], a[68:71], v[132:135]\n\t"
  "v_mfma_f32_16x16x32_bf16 v[136:139], v[48:51], a[72:75], v[136:139]\n\t"
  "ds_read_b64_tr_b16 v[44:45], v14 offset:45056\n\t"
  "v_mfma_f32_16x16x32_bf16 v[136:139], v[52:55], a[88:91], v[136:139]\n\t"
  "ds_read_b64_tr_b16 v[46:47], v14 offset:45568\n\t"
  "v_mfma_f32_16x16x32_bf16 v[140:143], v[52:55], a[92:95], v[140:143]\n\t"
  "buffer_load_dwordx4 v5, s[44:47], s70 offen offset:1024  lds\n\t"
  "v_mfma_f32_16x16x32_bf16 v[140:143], v[48:51], a[76:79], v[140:143]\n\t"
  "s_waitcnt lgkmcnt(4)\n\t"
  "v_mfma_f32_16x16x32_bf16 v[144:147], v[32:35], a[64:67], v[144:147]\n\t"
  "ds_read_b64_tr_b16 v[48:49], v15 offset:36864\n\t"
  "v_mfma_f32_16x16x32_bf16 v[144:147], v[36:39], a[80:83], v[144:147]\n\t"
  "ds_read_b64_tr_b16 v[50:51], v15 offset:37376\n\t"
  "s_waitcnt vmcnt(10)\n\t"
  "v_mfma_f32_16x16x32_bf16 v[148:151], v[36:39], a[84:87], v[148:151]\n\t"
  "ds_read_b128 a[96:99], v10 offset:0\n\t"
  "v_mfma_f32_16x16x32_bf16 v[148:151], v[32:35], a[68:71], v[148:151]\n\t"
  "s_barrier\n\t"
  "v_mfma_f32_16x16x32_bf16 v[152:155], v[32:35], a[72:75], v[152:155]\n\t"
  "ds_read_b64_tr_b16 v[52:53], v15 offset:45056\n\t"
  "v_mfma_f32_16x16x32_bf16 v[152:155], v[36:39], a[88:91], v[152:155]\n\t"
  "ds_read_b64_tr_b16 v[54:55], v15 offset:45568\n\t"
  "v_mfma_f32_16x16x32_bf16 v[156:159], v[36:39], a[92:95], v[156:159]\n\t"
  "ds_read_b128 a[100:103], v10 offset:2048\n\t"
  "v_mfma_f32_16x16x32_bf16 v[156:159], v[32:35], a[76:79], v[156:159]\n\t"
  "ds_read_b128 a[104:107], v10 offset:4096\n\t"
  "s_waitcnt lgkmcnt(7)\n\t"
  "v_mfma_f32_16x16x32_bf16 v[160:163], v[40:43], a[64:67], v[160:163]\n\t"
  "ds_read_b64_tr_b16 v[32:33], v12 offset:49152\n\t"
  "v_mfma_f32_16x16x32_bf16 v[160:163], v[44:47], a[80:83], v[160:163]\n\t"
  "ds_read_b64_tr_b16 v[34:35], v12 offset:49664\n\t"
  "v_mfma_f32_16x16x32_bf16 v[164:167], v[44:47], a[84:87], v[164:167]\n\t"
  "ds_read_b128 a[108:111], v10 offset:6144\n\t"
  "v_mfma_f32_16x16x32_bf16 v[164:167], v[40:43], a[68:71], v[164:167]\n\t"
  "ds_read_b128 a[112:115], v11 offset:0\n\t"
  "v_mfma_f32_16x16x32_bf16 v[168:171], v[40:43], a[72:75], v[168:171]\n\t"
  "ds_read_b64_tr_b16 v[36:37], v12 offset:57344\n\t"
  "v_mfma_f32_16x16x32_bf16 v[168:171], v[44:47], a[88:91], v[168:171]\n\t"
  "ds_read_b64_tr_b16 v[38:39], v12 offset:57856\n\t"
  "v_mfma_f32_16x16x32_bf16 v[172:175], v[44:47], a[92:95], v[172:175]\n\t"
  "ds_read_b128 a[116:119], v11 offset:2048\n\t"
  "v_mfma_f32_16x16x32_bf16 v[172:175], v[40:43], a[76:79], v[172:175]\n\t"
  "ds_read_b128 a[120:123], v11 offset:4096\n\t"
  "s_waitcnt lgkmcnt(10)\n\t"
  "v_mfma_f32_16x16x32_bf16 v[176:179], v[48:51], a[64:67], v[176:179]\n\t"
  "ds_read_b64_tr_b16 v[40:41], v13 offset:49152\n\t"
  "v_mfma_f32_16x16x32_bf16 v[176:179], v[52:55], a[80:83], v[176:179]\n\t"
  "ds_read_b64_tr_b16 v[42:43], v13 offset:49664\n\t"
  "v_mfma_f32_16x16x32_bf16 v[180:183], v[52:55], a[84:87], v[180:183]\n\t"
  "ds_read_b128 a[124:127], v11 offset:6144\n\t"
  "v_mfma_f32_16x16x32_bf16 v[180:183], v[48:51], a[68:71], v[180:183]\n\t"
  "buffer_load_dwordx4 v60, s[44:47], s70 offen offset:2048  lds\n\t"
  "v_mfma_f32_16x16x32_bf16 v[184:187], v[48:51], a[72:75], v[184:187]\n\t"
  "ds_read_b64_tr_b16 v[44:45], v13 offset:57344\n\t"
  "v_mfma_f32_16x16x32_bf16 v[184:187], v[52:55], a[88:91], v[184:187]\n\t"
  "ds_read_b64_tr_b16 v[46:47], v13 offset:57856\n\t"
  "v_mfma_f32_16x16x32_bf16 v[188:191], v[52:55], a[92:95], v[188:191]\n\t"
  "buffer_load_dwordx4 v61, s[44:47], s70 offen offset:3072  lds\n\t"
  "s_add_u32 m0, s71, 131072\n\t"
  "v_mfma_f32_16x16x32_bf16 v[188:191], v[48:51], a[76:79], v[188:191]\n\t"
  "s_add_u32 s70, s64, 640\n\t"
  "s_waitcnt lgkmcnt(7)\n\t"
  "v_mfma_f32_16x16x32_bf16 v[64:67], v[32:35], a[96:99], v[64:67]\n\t"
  "ds_read_b64_tr_b16 v[48:49], v14 offset:49152\n\t"
  "v_mfma_f32_16x16x32_bf16 v[64:67], v[36:39], a[112:115], v[64:67]\n\t"
  "ds_read_b64_tr_b16 v[50:51], v14 offset:49664\n\t"
  "s_waitcnt lgkmcnt(8)\n\t"
  "v_mfma_f32_16x16x32_bf16 v[68:71], v[36:39], a[116:119], v[68:71]\n\t"
  "buffer_load_dwordx4 v22, s[40:43], s70 offen offset:0 lds\n\t"
  "v_mfma_f32_16x16x32_bf16 v[68:71], v[32:35], a[100:103], v[68:71]\n\t"
  "v_mfma_f32_16x16x32_bf16 v[72:75], v[32:35], a[104:107], v[72:75]\n\t"
  "ds_read_b64_tr_b16 v[52:53], v14 offset:57344\n\t"
  "s_waitcnt lgkmcnt(8)\n\t"
  "v_mfma_f32_16x16x32_bf16 v[72:75], v[36:39], a[120:123], v[72:75]\n\t"
  "ds_read_b64_tr_b16 v[54:55], v14 offset:57856\n\t"
  "s_waitcnt lgkmcnt(6)\n\t"
  "v_mfma_f32_16x16x32_bf16 v[76:79], v[36:39], a[124:127], v[76:79]\n\t"
  "buffer_load_dwordx4 v23, s[40:43], s70 offen offset:1024 lds\n\t"
  "s_add_u32 m0, s71, 133120\n\t"
  "v_mfma_f32_16x16x32_bf16 v[76:79], v[32:35], a[108:111], v[76:79]\n\t"
  "s_add_u32 s70, s65, 640\n\t"
  "s_waitcnt lgkmcnt(4)\n\t"
  "v_mfma_f32_16x16x32_bf16 v[80:83], v[40:43], a[96:99], v[80:83]\n\t"
  "ds_read_b64_tr_b16 v[32:33], v15 offset:49152\n\t"
  "v_mfma_f32_16x16x32_bf16 v[80:83], v[44:47], a[112:115], v[80:83]\n\t"
  "ds_read_b64_tr_b16 v[34:35], v15 offset:49664\n\t"
  "v_mfma_f32_16x16x32_bf16 v[84:87], v[44:47], a[116:119], v[84:87]\n\t"
  "buffer_load_dwordx4 v22, s[40:43], s70 offen offset:0 lds\n\t"
  "v_mfma_f32_16x16x32_bf16 v[84:87], v[40:43], a[100:103], v[84:87]\n\t"
  "v_mfma_f32_16x16x32_bf16 v[88:91], v[40:43], a[104:107], v[88:91]\n\t"
  "ds_read_b64_tr_b16 v[36:37], v15 offset:57344\n\t"
  "v_mfma_f32_16x16x32_bf16 v[88:91], v[44:47], a[120:123], v[88:91]\n\t"
  "ds_read_b64_tr_b16 v[38:39], v15 offset:57856\n\t"
  "v_mfma_f32_16x16x32_bf16 v[92:95], v[44:47], a[124:127], v[92:95]\n\t"
  "buffer_load_dwordx4 v23, s[40:43], s70 offen offset:1024 lds\n\t"
  "s_add_u32 m0, s71, 135168\n\t"
  "v_mfma_f32_16x16x32_bf16 v[92:95], v[40:43], a[108:111], v[92:95]\n\t"
  "s_add_u32 s70, s66, 640\n\t"
  "s_waitcnt lgkmcnt(4)\n\t"
  "v_mfma_f32_16x16x32_bf16 v[96:99], v[48:51], a[96:99], v[96:99]\n\t"
  "ds_read_b64_tr_b16 v[40:41], v12 offset:53248\n\t"
  "v_mfma_f32_16x16x32_bf16 v[96:99], v[52:55], a[112:115], v[96:99]\n\t"
  "ds_read_b64_tr_b16 v[42:43], v12 offset:53760\n\t"
  "v_mfma_f32_16x16x32_bf16 v[100:103], v[52:55], a[116:119], v[100:103]\n\t"
  "buffer_load_dwordx4 v22, s[40:43], s70 offen offset:0 lds\n\t"
  "v_mfma_f32_16x16x32_bf16 v[100:103], v[48:51], a[100:103], v[100:103]\n\t"
  "v_mfma_f32_16x16x32_bf16 v[104:107], v[48:51], a[104:107], v[104:107]\n\t"
  "ds_read_b64_tr_b16 v[44:45], v12 offset:61440\n\t"
  "v_mfma_f32_16x16x32_bf16 v[104:107], v[52:55], a[120:123], v[104:107]\n\t"
  "ds_read_b64_tr_b16 v[46:47], v12 offset:61952\n\t"
  "v_mfma_f32_16x16x32_bf16 v[108:111], v[52:55], a[124:127], v[108:111]\n\t"
  "buffer_load_dwordx4 v23, s[40:43], s70 offen offset:1024 lds\n\t"
  "s_add_u32 m0, s71, 137216\n\t"
  "v_mfma_f32_16x16x32_bf16 v[108:111], v[48:51], a[108:111], v[108:111]\n\t"
  "s_add_u32 s70, s67, 640\n\t"
  "s_waitcnt lgkmcnt(4)\n\t"
  "v_mfma_f32_16x16x32_bf16 v[112:115], v[32:35], a[96:99], v[112:115]\n\t"
  "ds_read_b64_tr_b16 v[48:49], v13 offset:53248\n\t"
  "v_mfma_f32_16x16x32_bf16 v[112:115], v[36:39], a[112:115], v[112:115]\n\t"
  "ds_read_b64_tr_b16 v[50:51], v13 offset:53760\n\t"
  "v_mfma_f32_16x16x32_bf16 v[116:119], v[36:39], a[116:119], v[116:119]\n\t"
  "buffer_load_dwordx4 v22, s[40:43], s70 offen offset:0 lds\n\t"
  "v_mfma_f32_16x16x32_bf16 v[116:119], v[32:35], a[100:103], v[116:119]\n\t"
  "v_mfma_f32_16x16x32_bf16 v[120:123], v[32:35], a[104:107], v[120:123]\n\t"
  "ds_read_b64_tr_b16 v[52:53], v13 offset:61440\n\t"
  "v_mfma_f32_16x16x32_bf16 v[120:123], v[36:39], a[120:123], v[120:123]\n\t"
  "ds_read_b64_tr_b16 v[54:55], v13 offset:61952\n\t"
  "v_mfma_f32_16x16x32_bf16 v[124:127], v[36:39], a[124:127], v[124:127]\n\t"
  "buffer_load_dwordx4 v23, s[40:43], s70 offen offset:1024 lds\n\t"
  "s_add_u32 m0, s69, 16384\n\t"
  "v_mfma_f32_16x16x32_bf16 v[124:127], v[32:35], a[108:111], v[124:127]\n\t"
  "s_add_u32 s70, s73, 2621440\n\t"
  "s_waitcnt lgkmcnt(4)\n\t"
  "v_mfma_f32_16x16x32_bf16 v[128:131], v[40:43], a[96:99], v[128:131]\n\t"
  "ds_read_b64_tr_b16 v[32:33], v14 offset:53248\n\t"
  "v_mfma_f32_16x16x32_bf16 v[128:131], v[44:47], a[112:115], v[128:131]\n\t"
  "ds_read_b64_tr_b16 v[34:35], v14 offset:53760\n\t"
  "v_mfma_f32_16x16x32_bf16 v[132:135], v[44:47], a[116:119], v[132:135]\n\t"
  "buffer_load_dwordx4 v4, s[44:47], s70 offen offset:0  lds\n\t"
  "v_mfma_f32_16x16x32_bf16 v[132:135], v[40:43], a[100:103], v[132:135]\n\t"
  "v_mfma_f32_16x16x32_bf16 v[136:139], v[40:43], a[104:107], v[136:139]\n\t"
  "ds_read_b64_tr_b16 v[36:37], v14 offset:61440\n\t"
  "v_mfma_f32_16x16x32_bf16 v[136:139], v[44:47], a[120:123], v[136:139]\n\t"
  "ds_read_b64_tr_b16 v[38:39], v14 offset:61952\n\t"
  "v_mfma_f32_16x16x32_bf16 v[140:143], v[44:47], a[124:127], v[140:143]\n\t"
  "buffer_load_dwordx4 v5, s[44:47], s70 offen offset:1024  lds\n\t"
  "v_mfma_f32_16x16x32_bf16 v[140:143], v[40:43], a[108:111], v[140:143]\n\t"
  "s_waitcnt lgkmcnt(4)\n\t"
  "v_mfma_f32_16x16x32_bf16 v[144:147], v[48:51], a[96:99], v[144:147]\n\t"
  "ds_read_b64_tr_b16 v[40:41], v15 offset:53248\n\t"
  "v_mfma_f32_16x16x32_bf16 v[144:147], v[52:55], a[112:115], v[144:147]\n\t"
  "ds_read_b64_tr_b16 v[42:43], v15 offset:53760\n\t"
  "s_waitcnt vmcnt(14)\n\t"
  "v_mfma_f32_16x16x32_bf16 v[148:151], v[52:55], a[116:119], v[148:151]\n\t"
  "ds_read_b128 a[128:131], v10 offset:32768\n\t"
  "v_mfma_f32_16x16x32_bf16 v[148:151], v[48:51], a[100:103], v[148:151]\n\t"
  "s_waitcnt vmcnt(10)\n\t"
  "s_barrier\n\t"
  "v_mfma_f32_16x16x32_bf16 v[152:155], v[48:51], a[104:107], v[152:155]\n\t"
  "ds_read_b64_tr_b16 v[44:45], v15 offset:61440\n\t"
  "v_mfma_f32_16x16x32_bf16 v[152:155], v[52:55], a[120:123], v[152:155]\n\t"
  "ds_read_b64_tr_b16 v[46:47], v15 offset:61952\n\t"
  "v_mfma_f32_16x16x32_bf16 v[156:159], v[52:55], a[124:127], v[156:159]\n\t"
  "ds_read_b128 a[132:135], v10 offset:34816\n\t"
  "v_mfma_f32_16x16x32_bf16 v[156:159], v[48:51], a[108:111], v[156:159]\n\t"
  "ds_read_b128 a[136:139], v10 offset:36864\n\t"
  "s_waitcnt lgkmcnt(7)\n\t"
  "v_mfma_f32_16x16x32_bf16 v[160:163], v[32:35], a[96:99], v[160:163]\n\t"
  "ds_read_b64_tr_b16 v[48:49], v12 offset:0\n\t"
  "v_mfma_f32_16x16x32_bf16 v[160:163], v[36:39], a[112:115], v[160:163]\n\t"
  "ds_read_b64_tr_b16 v[50:51], v12 offset:512\n\t"
  "v_mfma_f32_16x16x32_bf16 v[164:167], v[36:39], a[116:119], v[164:167]\n\t"
  "ds_read_b128 a[140:143], v10 offset:38912\n\t"
  "v_mfma_f32_16x16x32_bf16 v[164:167], v[32:35], a[100:103], v[164:167]\n\t"
  "ds_read_b128 a[144:147], v11 offset:32768\n\t"
  "v_mfma_f32_16x16x32_bf16 v[168:171], v[32:35], a[104:107], v[168:171]\n\t"
  "ds_read_b64_tr_b16 v[52:53], v12 offset:8192\n\t"
  "v_mfma_f32_16x16x32_bf16 v[168:171], v[36:39], a[120:123], v[168:171]\n\t"
  "ds_read_b64_tr_b16 v[54:55], v12 offset:8704\n\t"
  "v_mfma_f32_16x16x32_bf16 v[172:175], v[36:39], a[124:127], v[172:175]\n\t"
  "ds_read_b128 a[148:151], v11 offset:34816\n\t"
  "v_mfma_f32_16x16x32_bf16 v[172:175], v[32:35], a[108:111], v[172:175]\n\t"
  "ds_read_b128 a[152:155], v11 offset:36864\n\t"
  "s_waitcnt lgkmcnt(10)\n\t"
  "v_mfma_f32_16x16x32_bf16 v[176:179], v[40:43], a[96:99], v[176:179]\n\t"
  "ds_read_b64_tr_b16 v[32:33], v13 offset:0\n\t"
  "v_mfma_f32_16x16x32_bf16 v[176:179], v[44:47], a[112:115], v[176:179]\n\t"
  "ds_read_b64_tr_b16 v[34:35], v13 offset:512\n\t"
  "v_mfma_f32_16x16x32_bf16 v[180:183], v[44:47], a[116:119], v[180:183]\n\t"
  "ds_read_b128 a[156:159], v11 offset:38912\n\t"
  "v_mfma_f32_16x16x32_bf16 v[180:183], v[40:43], a[100:103], v[180:183]\n\t"
  "buffer_load_dwordx4 v60, s[44:47], s70 offen offset:2048  lds\n\t"
  "v_mfma_f32_16x16x32_bf16 v[184:187], v[40:43], a[104:107], v[184:187]\n\t"
  "ds_read_b64_tr_b16 v[36:37], v13 offset:8192\n\t"
  "v_mfma_f32_16x16x32_bf16 v[184:187], v[44:47], a[120:123], v[184:187]\n\t"
  "ds_read_b64_tr_b16 v[38:39], v13 offset:8704\n\t"
  "v_mfma_f32_16x16x32_bf16 v[188:191], v[44:47], a[124:127], v[188:191]\n\t"
  "buffer_load_dwordx4 v61, s[44:47], s70 offen offset:3072  lds\n\t"
  "s_add_u32 m0, s71, 65536\n\t"
  "v_mfma_f32_16x16x32_bf16 v[188:191], v[40:43], a[108:111], v[188:191]\n\t"
  "s_add_u32 s70, s64, 768\n\t"
  "s_waitcnt lgkmcnt(7)\n\t"
  "v_mfma_f32_16x16x32_bf16 v[64:67], v[48:51], a[128:131], v[64:67]\n\t"
  "ds_read_b64_tr_b16 v[40:41], v14 offset:0\n\t"
  "v_mfma_f32_16x16x32_bf16 v[64:67], v[52:55], a[144:147], v[64:67]\n\t"
  "ds_read_b64_tr_b16 v[42:43], v14 offset:512\n\t"
  "s_waitcnt lgkmcnt(8)\n\t"
  "v_mfma_f32_16x16x32_bf16 v[68:71], v[52:55], a[148:151], v[68:71]\n\t"
  "buffer_load_dwordx4 v22, s[40:43], s70 offen offset:0 lds\n\t"
  "v_mfma_f32_16x16x32_bf16 v[68:71], v[48:51], a[132:135], v[68:71]\n\t"
  "v_mfma_f32_16x16x32_bf16 v[72:75], v[48:51], a[136:139], v[72:75]\n\t"
  "ds_read_b64_tr_b16 v[44:45], v14 offset:8192\n\t"
  "s_waitcnt lgkmcnt(8)\n\t"
  "v_mfma_f32_16x16x32_bf16 v[72:75], v[52:55], a[152:155], v[72:75]\n\t"
  "ds_read_b64_tr_b16 v[46:47], v14 offset:8704\n\t"
  "s_waitcnt lgkmcnt(6)\n\t"
  "v_mfma_f32_16x16x32_bf16 v[76:79], v[52:55], a[156:159], v[76:79]\n\t"
  "buffer_load_dwordx4 v23, s[40:43], s70 offen offset:1024 lds\n\t"
  "s_add_u32 m0, s71, 67584\n\t"
  "v_mfma_f32_16x16x32_bf16 v[76:79], v[48:51], a[140:143], v[76:79]\n\t"
  "s_add_u32 s70, s65, 768\n\t"
  "s_waitcnt lgkmcnt(4)\n\t"
  "v_mfma_f32_16x16x32_bf16 v[80:83], v[32:35], a[128:131], v[80:83]\n\t"
  "ds_read_b64_tr_b16 v[48:49], v15 offset:0\n\t"
  "v_mfma_f32_16x16x32_bf16 v[80:83], v[36:39], a[144:147], v[80:83]\n\t"
  "ds_read_b64_tr_b16 v[50:51], v15 offset:512\n\t"
  "v_mfma_f32_16x16x32_bf16 v[84:87], v[36:39], a[148:151], v[84:87]\n\t"
  "buffer_load_dwordx4 v22, s[40:43], s70 offen offset:0 lds\n\t"
  "v_mfma_f32_16x16x32_bf16 v[84:87], v[32:35], a[132:135], v[84:87]\n\t"
  "v_mfma_f32_16x16x32_bf16 v[88:91], v[32:35], a[136:139], v[88:91]\n\t"
  "ds_read_b64_tr_b16 v[52:53], v15 offset:8192\n\t"
  "v_mfma_f32_16x16x32_bf16 v[88:91], v[36:39], a[152:155], v[88:91]\n\t"
  "ds_read_b64_tr_b16 v[54:55], v15 offset:8704\n\t"
  "v_mfma_f32_16x16x32_bf16 v[92:95], v[36:39], a[156:159], v[92:95]\n\t"
  "buffer_load_dwordx4 v23, s[40:43], s70 offen offset:1024 lds\n\t"
  "s_add_u32 m0, s71, 69632\n\t"
  "v_mfma_f32_16x16x32_bf16 v[92:95], v[32:35], a[140:143], v[92:95]\n\t"
  "s_add_u32 s70, s66, 768\n\t"
  "s_waitcnt lgkmcnt(4)\n\t"
  "v_mfma_f32_16x16x32_bf16 v[96:99], v[40:43], a[128:131], v[96:99]\n\t"
  "ds_read_b64_tr_b16 v[32:33], v12 offset:4096\n\t"
  "v_mfma_f32_16x16x32_bf16 v[96:99], v[44:47], a[144:147], v[96:99]\n\t"
  "ds_read_b64_tr_b16 v[34:35], v12 offset:4608\n\t"
  "v_mfma_f32_16x16x32_bf16 v[100:103], v[44:47], a[148:151], v[100:103]\n\t"
  "buffer_load_dwordx4 v22, s[40:43], s70 offen offset:0 lds\n\t"
  "v_mfma_f32_16x16x32_bf16 v[100:103], v[40:43], a[132:135], v[100:103]\n\t"
  "v_mfma_f32_16x16x32_bf16 v[104:107], v[40:43], a[136:139], v[104:107]\n\t"
  "ds_read_b64_tr_b16 v[36:37], v12 offset:12288\n\t"
  "v_mfma_f32_16x16x32_bf16 v[104:107], v[44:47], a[152:155], v[104:107]\n\t"
  "ds_read_b64_tr_b16 v[38:39], v12 offset:12800\n\t"
  "v_mfma_f32_16x16x32_bf16 v[108:111], v[44:47], a[156:159], v[108:111]\n\t"
  "buffer_load_dwordx4 v23, s[40:43], s70 offen offset:1024 lds\n\t"
  "s_add_u32 m0, s71, 71680\n\t"
  "v_mfma_f32_16x16x32_bf16 v[108:111], v[40:43], a[140:143], v[108:111]\n\t"
  "s_add_u32 s70, s67, 768\n\t"
  "s_waitcnt lgkmcnt(4)\n\t"
  "v_mfma_f32_16x16x32_bf16 v[112:115], v[48:51], a[128:131], v[112:115]\n\t"
  "ds_read_b64_tr_b16 v[40:41], v13 offset:4096\n\t"
  "v_mfma_f32_16x16x32_bf16 v[112:115], v[52:55], a[144:147], v[112:115]\n\t"
  "ds_read_b64_tr_b16 v[42:43], v13 offset:4608\n\t"
  "v_mfma_f32_16x16x32_bf16 v[116:119], v[52:55], a[148:151], v[116:119]\n\t"
  "buffer_load_dwordx4 v22, s[40:43], s70 offen offset:0 lds\n\t"
  "v_mfma_f32_16x16x32_bf16 v[116:119], v[48:51], a[132:135], v[116:119]\n\t"
  "v_mfma_f32_16x16x32_bf16 v[120:123], v[48:51], a[136:139], v[120:123]\n\t"
  "ds_read_b64_tr_b16 v[44:45], v13 offset:12288\n\t"
  "v_mfma_f32_16x16x32_bf16 v[120:123], v[52:55], a[152:155], v[120:123]\n\t"
  "ds_read_b64_tr_b16 v[46:47], v13 offset:12800\n\t"
  "v_mfma_f32_16x16x32_bf16 v[124:127], v[52:55], a[156:159], v[124:127]\n\t"
  "buffer_load_dwordx4 v23, s[40:43], s70 offen offset:1024 lds\n\t"
  "s_add_u32 m0, s69, 32768\n\t"
  "v_mfma_f32_16x16x32_bf16 v[124:127], v[48:51], a[140:143], v[124:127]\n\t"
  "s_add_u32 s70, s73, 3145728\n\t"
  "s_waitcnt lgkmcnt(4)\n\t"
  "v_mfma_f32_16x16x32_bf16 v[128:131], v[32:35], a[128:131], v[128:131]\n\t"
  "ds_read_b64_tr_b16 v[48:49], v14 offset:4096\n\t"
  "v_mfma_f32_16x16x32_bf16 v[128:131], v[36:39], a[144:147], v[128:131]\n\t"
  "ds_read_b64_tr_b16 v[50:51], v14 offset:4608\n\t"
  "v_mfma_f32_16x16x32_bf16 v[132:135], v[36:39], a[148:151], v[132:135]\n\t"
  "buffer_load_dwordx4 v4, s[44:47], s70 offen offset:0  lds\n\t"
  "v_mfma_f32_16x16x32_bf16 v[132:135], v[32:35], a[132:135], v[132:135]\n\t"
  "v_mfma_f32_16x16x32_bf16 v[136:139], v[32:35], a[136:139], v[136:139]\n\t"
  "ds_read_b64_tr_b16 v[52:53], v14 offset:12288\n\t"
  "v_mfma_f32_16x16x32_bf16 v[136:139], v[36:39], a[152:155], v[136:139]\n\t"
  "ds_read_b64_tr_b16 v[54:55], v14 offset:12800\n\t"
  "v_mfma_f32_16x16x32_bf16 v[140:143], v[36:39], a[156:159], v[140:143]\n\t"
  "buffer_load_dwordx4 v5, s[44:47], s70 offen offset:1024  lds\n\t"
  "v_mfma_f32_16x16x32_bf16 v[140:143], v[32:35], a[140:143], v[140:143]\n\t"
  "s_waitcnt lgkmcnt(4)\n\t"
  "v_mfma_f32_16x16x32_bf16 v[144:147], v[40:43], a[128:131], v[144:147]\n\t"
  "ds_read_b64_tr_b16 v[32:33], v15 offset:4096\n\t"
  "v_mfma_f32_16x16x32_bf16 v[144:147], v[44:47], a[144:147], v[144:147]\n\t"
  "ds_read_b64_tr_b16 v[34:35], v15 offset:4608\n\t"
  "s_waitcnt vmcnt(14)\n\t"
  "v_mfma_f32_16x16x32_bf16 v[148:151], v[44:47], a[148:151], v[148:151]\n\t"
  "ds_read_b128 a[160:163], v26 offset:0\n\t"
  "v_mfma_f32_16x16x32_bf16 v[148:151], v[40:43], a[132:135], v[148:151]\n\t"
  "s_waitcnt vmcnt(10)\n\t"
  "s_barrier\n\t"
  "v_mfma_f32_16x16x32_bf16 v[152:155], v[40:43], a[136:139], v[152:155]\n\t"
  "ds_read_b64_tr_b16 v[36:37], v15 offset:12288\n\t"
  "v_mfma_f32_16x16x32_bf16 v[152:155], v[44:47], a[152:155], v[152:155]\n\t"
  "ds_read_b64_tr_b16 v[38:39], v15 offset:12800\n\t"
  "v_mfma_f32_16x16x32_bf16 v[156:159], v[44:47], a[156:159], v[156:159]\n\t"
  "ds_read_b128 a[164:167], v26 offset:2048\n\t"
  "v_mfma_f32_16x16x32_bf16 v[156:159], v[40:43], a[140:143], v[156:159]\n\t"
  "ds_read_b128 a[168:171], v26 offset:4096\n\t"
  "s_waitcnt lgkmcnt(7)\n\t"
  "v_mfma_f32_16x16x32_bf16 v[160:163], v[48:51], a[128:131], v[160:163]\n\t"
  "ds_read_b64_tr_b16 v[40:41], v12 offset:16384\n\t"
  "v_mfma_f32_16x16x32_bf16 v[160:163], v[52:55], a[144:147], v[160:163]\n\t"
  "ds_read_b64_tr_b16 v[42:43], v12 offset:16896\n\t"
  "v_mfma_f32_16x16x32_bf16 v[164:167], v[52:55], a[148:151], v[164:167]\n\t"
  "ds_read_b128 a[172:175], v26 offset:6144\n\t"
  "v_mfma_f32_16x16x32_bf16 v[164:167], v[48:51], a[132:135], v[164:167]\n\t"
  "ds_read_b128 a[176:179], v27 offset:0\n\t"
  "v_mfma_f32_16x16x32_bf16 v[168:171], v[48:51], a[136:139], v[168:171]\n\t"
  "ds_read_b64_tr_b16 v[44:45], v12 offset:24576\n\t"
  "v_mfma_f32_16x16x32_bf16 v[168:171], v[52:55], a[152:155], v[168:171]\n\t"
  "ds_read_b64_tr_b16 v[46:47], v12 offset:25088\n\t"
  "v_mfma_f32_16x16x32_bf16 v[172:175], v[52:55], a[156:159], v[172:175]\n\t"
  "ds_read_b128 a[180:183], v27 offset:2048\n\t"
  "v_mfma_f32_16x16x32_bf16 v[172:175], v[48:51], a[140:143], v[172:175]\n\t"
  "ds_read_b128 a[184:187], v27 offset:4096\n\t"
  "s_waitcnt lgkmcnt(10)\n\t"
  "v_mfma_f32_16x16x32_bf16 v[176:179], v[32:35], a[128:131], v[176:179]\n\t"
  "ds_read_b64_tr_b16 v[48:49], v13 offset:16384\n\t"
  "v_mfma_f32_16x16x32_bf16 v[176:179], v[36:39], a[144:147], v[176:179]\n\t"
  "ds_read_b64_tr_b16 v[50:51], v13 offset:16896\n\t"
  "v_mfma_f32_16x16x32_bf16 v[180:183], v[36:39], a[148:151], v[180:183]\n\t"
  "ds_read_b128 a[188:191], v27 offset:6144\n\t"
  "v_mfma_f32_16x16x32_bf16 v[180:183], v[32:35], a[132:135], v[180:183]\n\t"
  "buffer_load_dwordx4 v60, s[44:47], s70 offen offset:2048  lds\n\t"
  "v_mfma_f32_16x16x32_bf16 v[184:187], v[32:35], a[136:139], v[184:187]\n\t"
  "ds_read_b64_tr_b16 v[52:53], v13 offset:24576\n\t"
  "v_mfma_f32_16x16x32_bf16 v[184:187], v[36:39], a[152:155], v[184:187]\n\t"
  "ds_read_b64_tr_b16 v[54:55], v13 offset:25088\n\t"
  "v_mfma_f32_16x16x32_bf16 v[188:191], v[36:39], a[156:159], v[188:191]\n\t"
  "buffer_load_dwordx4 v61, s[44:47], s70 offen offset:3072  lds\n\t"
  "s_add_u32 m0, s69, 49152\n\t"
  "v_mfma_f32_16x16x32_bf16 v[188:191], v[32:35], a[140:143], v[188:191]\n\t"
  "s_add_u32 s70, s74, 256\n\t"
  "s_waitcnt lgkmcnt(7)\n\t"
  "v_mfma_f32_16x16x32_bf16 v[64:67], v[40:43], a[160:163], v[64:67]\n\t"
  "ds_read_b64_tr_b16 v[32:33], v14 offset:16384\n\t"
  "v_mfma_f32_16x16x32_bf16 v[64:67], v[44:47], a[176:179], v[64:67]\n\t"
  "ds_read_b64_tr_b16 v[34:35], v14 offset:16896\n\t"
  "s_waitcnt lgkmcnt(8)\n\t"
  "v_mfma_f32_16x16x32_bf16 v[68:71], v[44:47], a[180:183], v[68:71]\n\t"
  "buffer_load_dwordx4 v4, s[44:47], s70 offen offset:0  lds\n\t"
  "v_mfma_f32_16x16x32_bf16 v[68:71], v[40:43], a[164:167], v[68:71]\n\t"
  "v_mfma_f32_16x16x32_bf16 v[72:75], v[40:43], a[168:171], v[72:75]\n\t"
  "ds_read_b64_tr_b16 v[36:37], v14 offset:24576\n\t"
  "s_waitcnt lgkmcnt(8)\n\t"
  "v_mfma_f32_16x16x32_bf16 v[72:75], v[44:47], a[184:187], v[72:75]\n\t"
  "ds_read_b64_tr_b16 v[38:39], v14 offset:25088\n\t"
  "s_waitcnt lgkmcnt(6)\n\t"
  "v_mfma_f32_16x16x32_bf16 v[76:79], v[44:47], a[188:191], v[76:79]\n\t"
  "buffer_load_dwordx4 v5, s[44:47], s70 offen offset:1024  lds\n\t"
  "v_mfma_f32_16x16x32_bf16 v[76:79], v[40:43], a[172:175], v[76:79]\n\t"
  "s_waitcnt lgkmcnt(4)\n\t"
  "v_mfma_f32_16x16x32_bf16 v[80:83], v[48:51], a[160:163], v[80:83]\n\t"
  "ds_read_b64_tr_b16 v[40:41], v15 offset:16384\n\t"
  "v_mfma_f32_16x16x32_bf16 v[80:83], v[52:55], a[176:179], v[80:83]\n\t"
  "ds_read_b64_tr_b16 v[42:43], v15 offset:16896\n\t"
  "v_mfma_f32_16x16x32_bf16 v[84:87], v[52:55], a[180:183], v[84:87]\n\t"
  "buffer_load_dwordx4 v60, s[44:47], s70 offen offset:2048  lds\n\t"
  "v_mfma_f32_16x16x32_bf16 v[84:87], v[48:51], a[164:167], v[84:87]\n\t"
  "v_mfma_f32_16x16x32_bf16 v[88:91], v[48:51], a[168:171], v[88:91]\n\t"
  "ds_read_b64_tr_b16 v[44:45], v15 offset:24576\n\t"
  "v_mfma_f32_16x16x32_bf16 v[88:91], v[52:55], a[184:187], v[88:91]\n\t"
  "ds_read_b64_tr_b16 v[46:47], v15 offset:25088\n\t"
  "v_mfma_f32_16x16x32_bf16 v[92:95], v[52:55], a[188:191], v[92:95]\n\t"
  "buffer_load_dwordx4 v61, s[44:47], s70 offen offset:3072  lds\n\t"
  "s_add_u32 m0, s69, 98304\n\t"
  "v_mfma_f32_16x16x32_bf16 v[92:95], v[48:51], a[172:175], v[92:95]\n\t"
  "s_mov_b32 s70, 0\n\t"
  "s_waitcnt lgkmcnt(4)\n\t"
  "v_mfma_f32_16x16x32_bf16 v[96:99], v[32:35], a[160:163], v[96:99]\n\t"
  "ds_read_b64_tr_b16 v[48:49], v12 offset:20480\n\t"
  "v_mfma_f32_16x16x32_bf16 v[96:99], v[36:39], a[176:179], v[96:99]\n\t"
  "ds_read_b64_tr_b16 v[50:51], v12 offset:20992\n\t"
  "v_mfma_f32_16x16x32_bf16 v[100:103], v[36:39], a[180:183], v[100:103]\n\t"
  "buffer_load_dwordx4 v6, s[48:51], s70 offen offset:0 nt lds\n\t"
  "v_mfma_f32_16x16x32_bf16 v[100:103], v[32:35], a[164:167], v[100:103]\n\t"
  "v_mfma_f32_16x16x32_bf16 v[104:107], v[32:35], a[168:171], v[104:107]\n\t"
  "ds_read_b64_tr_b16 v[52:53], v12 offset:28672\n\t"
  "v_mfma_f32_16x16x32_bf16 v[104:107], v[36:39], a[184:187], v[104:107]\n\t"
  "ds_read_b64_tr_b16 v[54:55], v12 offset:29184\n\t"
  "v_mfma_f32_16x16x32_bf16 v[108:111], v[36:39], a[188:191], v[108:111]\n\t"
  "buffer_load_dwordx4 v7, s[48:51], s70 offen offset:1024 nt lds\n\t"
  "v_mfma_f32_16x16x32_bf16 v[108:111], v[32:35], a[172:175], v[108:111]\n\t"
  "s_waitcnt lgkmcnt(4)\n\t"
  "v_mfma_f32_16x16x32_bf16 v[112:115], v[40:43], a[160:163], v[112:115]\n\t"
  "ds_read_b64_tr_b16 v[32:33], v13 offset:20480\n\t"
  "v_mfma_f32_16x16x32_bf16 v[112:115], v[44:47], a[176:179], v[112:115]\n\t"
  "ds_read_b64_tr_b16 v[34:35], v13 offset:20992\n\t"
  "v_mfma_f32_16x16x32_bf16 v[116:119], v[44:47], a[180:183], v[116:119]\n\t"
  "buffer_load_dwordx4 v8, s[48:51], s70 offen offset:2048 nt lds\n\t"
  "v_mfma_f32_16x16x32_bf16 v[116:119], v[40:43], a[164:167], v[116:119]\n\t"
  "v_mfma_f32_16x16x32_bf16 v[120:123], v[40:43], a[168:171], v[120:123]\n\t"
  "ds_read_b64_tr_b16 v[36:37], v13 offset:28672\n\t"
  "v_mfma_f32_16x16x32_bf16 v[120:123], v[44:47], a[184:187], v[120:123]\n\t"
  "ds_read_b64_tr_b16 v[38:39], v13 offset:29184\n\t"
  "v_mfma_f32_16x16x32_bf16 v[124:127], v[44:47], a[188:191], v[124:127]\n\t"
  "v_mfma_f32_16x16x32_bf16 v[124:127], v[40:43], a[172:175], v[124:127]\n\t"
  "s_waitcnt lgkmcnt(4)\n\t"
  "v_mfma_f32_16x16x32_bf16 v[128:131], v[48:51], a[160:163], v[128:131]\n\t"
  "ds_read_b64_tr_b16 v[40:41], v14 offset:20480\n\t"
  "v_mfma_f32_16x16x32_bf16 v[128:131], v[52:55], a[176:179], v[128:131]\n\t"
  "ds_read_b64_tr_b16 v[42:43], v14 offset:20992\n\t"
  "v_mfma_f32_16x16x32_bf16 v[132:135], v[52:55], a[180:183], v[132:135]\n\t"
  "v_mfma_f32_16x16x32_bf16 v[132:135], v[48:51], a[164:167], v[132:135]\n\t"
  "v_mfma_f32_16x16x32_bf16 v[136:139], v[48:51], a[168:171], v[136:139]\n\t"
  "ds_read_b64_tr_b16 v[44:45], v14 offset:28672\n\t"
  "v_mfma_f32_16x16x32_bf16 v[136:139], v[52:55], a[184:187], v[136:139]\n\t"
  "ds_read_b64_tr_b16 v[46:47], v14 offset:29184\n\t"
  "v_mfma_f32_16x16x32_bf16 v[140:143], v[52:55], a[188:191], v[140:143]\n\t"
  "v_mfma_f32_16x16x32_bf16 v[140:143], v[48:51], a[172:175], v[140:143]\n\t"
  "s_waitcnt lgkmcnt(4)\n\t"
  "v_mfma_f32_16x16x32_bf16 v[144:147], v[32:35], a[160:163], v[144:147]\n\t"
  "ds_read_b64_tr_b16 v[48:49], v15 offset:20480\n\t"
  "v_mfma_f32_16x16x32_bf16 v[144:147], v[36:39], a[176:179], v[144:147]\n\t"
  "ds_read_b64_tr_b16 v[50:51], v15 offset:20992\n\t"
  "s_waitcnt vmcnt(11)\n\t"
  "v_mfma_f32_16x16x32_bf16 v[148:151], v[36:39], a[180:183], v[148:151]\n\t"
  "ds_read_b128 a[192:195], v10 offset:0\n\t"
  "v_mfma_f32_16x16x32_bf16 v[148:151], v[32:35], a[164:167], v[148:151]\n\t"
  "s_waitcnt vmcnt(7)\n\t"
  "s_barrier\n\t"
  "v_mfma_f32_16x16x32_bf16 v[152:155], v[32:35], a[168:171], v[152:155]\n\t"
  "ds_read_b64_tr_b16 v[52:53], v15 offset:28672\n\t"
  "v_mfma_f32_16x16x32_bf16 v[152:155], v[36:39], a[184:187], v[152:155]\n\t"
  "ds_read_b64_tr_b16 v[54:55], v15 offset:29184\n\t"
  "v_mfma_f32_16x16x32_bf16 v[156:159], v[36:39], a[188:191], v[156:159]\n\t"
  "ds_read_b128 a[196:199], v10 offset:2048\n\t"
  "v_mfma_f32_16x16x32_bf16 v[156:159], v[32:35], a[172:175], v[156:159]\n\t"
  "ds_read_b128 a[200:203], v10 offset:4096\n\t"
  "s_waitcnt lgkmcnt(7)\n\t"
  "v_mfma_f32_16x16x32_bf16 v[160:163], v[40:43], a[160:163], v[160:163]\n\t"
  "ds_read_b64_tr_b16 v[32:33], v12 offset:32768\n\t"
  "v_mfma_f32_16x16x32_bf16 v[160:163], v[44:47], a[176:179], v[160:163]\n\t"
  "ds_read_b64_tr_b16 v[34:35], v12 offset:33280\n\t"
  "v_mfma_f32_16x16x32_bf16 v[164:167], v[44:47], a[180:183], v[164:167]\n\t"
  "ds_read_b128 a[204:207], v10 offset:6144\n\t"
  "v_mfma_f32_16x16x32_bf16 v[164:167], v[40:43], a[164:167], v[164:167]\n\t"
  "ds_read_b128 a[208:211], v11 offset:0\n\t"
  "v_mfma_f32_16x16x32_bf16 v[168:171], v[40:43], a[168:171], v[168:171]\n\t"
  "ds_read_b64_tr_b16 v[36:37], v12 offset:40960\n\t"
  "v_mfma_f32_16x16x32_bf16 v[168:171], v[44:47], a[184:187], v[168:171]\n\t"
  "ds_read_b64_tr_b16 v[38:39], v12 offset:41472\n\t"
  "s_add_u32 m0, s69, 0\n\t"
  "v_mfma_f32_16x16x32_bf16 v[172:175], v[44:47], a[188:191], v[172:175]\n\t"
  "ds_read_b128 a[212:215], v11 offset:2048\n\t"
  "s_add_u32 s70, s74, 1048832\n\t"
  "v_mfma_f32_16x16x32_bf16 v[172:175], v[40:43], a[172:175], v[172:175]\n\t"
  "ds_read_b128 a[216:219], v11 offset:4096\n\t"
  "s_waitcnt lgkmcnt(10)\n\t"
  "v_mfma_f32_16x16x32_bf16 v[176:179], v[48:51], a[160:163], v[176:179]\n\t"
  "ds_read_b64_tr_b16 v[40:41], v13 offset:32768\n\t"
  "v_mfma_f32_16x16x32_bf16 v[176:179], v[52:55], a[176:179], v[176:179]\n\t"
  "ds_read_b64_tr_b16 v[42:43], v13 offset:33280\n\t"
  "v_mfma_f32_16x16x32_bf16 v[180:183], v[52:55], a[180:183], v[180:183]\n\t"
  "ds_read_b128 a[220:223], v11 offset:6144\n\t"
  "v_mfma_f32_16x16x32_bf16 v[180:183], v[48:51], a[164:167], v[180:183]\n\t"
  "buffer_load_dwordx4 v4, s[44:47], s70 offen offset:0  lds\n\t"
  "v_mfma_f32_16x16x32_bf16 v[184:187], v[48:51], a[168:171], v[184:187]\n\t"
  "ds_read_b64_tr_b16 v[44:45], v13 offset:40960\n\t"
  "v_mfma_f32_16x16x32_bf16 v[184:187], v[52:55], a[184:187], v[184:187]\n\t"
  "ds_read_b64_tr_b16 v[46:47], v13 offset:41472\n\t"
  "v_mfma_f32_16x16x32_bf16 v[188:191], v[52:55], a[188:191], v[188:191]\n\t"
  "buffer_load_dwordx4 v5, s[44:47], s70 offen offset:1024  lds\n\t"
  "v_mfma_f32_16x16x32_bf16 v[188:191], v[48:51], a[172:175], v[188:191]\n\t"
  "s_waitcnt lgkmcnt(7)\n\t"
  "v_mfma_f32_16x16x32_bf16 v[64:67], v[32:35], a[192:195], v[64:67]\n\t"
  "ds_read_b64_tr_b16 v[48:49], v14 offset:32768\n\t"
  "v_mfma_f32_16x16x32_bf16 v[64:67], v[36:39], a[208:211], v[64:67]\n\t"
  "ds_read_b64_tr_b16 v[50:51], v14 offset:33280\n\t"
  "s_waitcnt lgkmcnt(8)\n\t"
  "v_mfma_f32_16x16x32_bf16 v[68:71], v[36:39], a[212:215], v[68:71]\n\t"
  "buffer_load_dwordx4 v60, s[44:47], s70 offen offset:2048  lds\n\t"
  "v_mfma_f32_16x16x32_bf16 v[68:71], v[32:35], a[196:199], v[68:71]\n\t"
  "v_mfma_f32_16x16x32_bf16 v[72:75], v[32:35], a[200:203], v[72:75]\n\t"
  "ds_read_b64_tr_b16 v[52:53], v14 offset:40960\n\t"
  "s_waitcnt lgkmcnt(8)\n\t"
  "v_mfma_f32_16x16x32_bf16 v[72:75], v[36:39], a[216:219], v[72:75]\n\t"
  "ds_read_b64_tr_b16 v[54:55], v14 offset:41472\n\t"
  "s_waitcnt lgkmcnt(6)\n\t"
  "v_mfma_f32_16x16x32_bf16 v[76:79], v[36:39], a[220:223], v[76:79]\n\t"
  "buffer_load_dwordx4 v61, s[44:47], s70 offen offset:3072  lds\n\t"
  "s_add_u32 m0, s69, 98304\n\t"
  "v_mfma_f32_16x16x32_bf16 v[76:79], v[32:35], a[204:207], v[76:79]\n\t"
  "s_mov_b32 s70, 0\n\t"
  "s_waitcnt lgkmcnt(4)\n\t"
  "v_mfma_f32_16x16x32_bf16 v[80:83], v[40:43], a[192:195], v[80:83]\n\t"
  "ds_read_b64_tr_b16 v[32:33], v15 offset:32768\n\t"
  "v_mfma_f32_16x16x32_bf16 v[80:83], v[44:47], a[208:211], v[80:83]\n\t"
  "ds_read_b64_tr_b16 v[34:35], v15 offset:33280\n\t"
  "v_mfma_f32_16x16x32_bf16 v[84:87], v[44:47], a[212:215], v[84:87]\n\t"
  "buffer_load_dwordx4 v9, s[48:51], s70 offen offset:3072 nt lds\n\t"
  "s_add_u32 m0, s69, 114688\n\t"
  "v_mfma_f32_16x16x32_bf16 v[84:87], v[40:43], a[196:199], v[84:87]\n\t"
  "s_mov_b32 s70, 16384\n\t"
  "v_mfma_f32_16x16x32_bf16 v[88:91], v[40:43], a[200:203], v[88:91]\n\t"
  "ds_read_b64_tr_b16 v[36:37], v15 offset:40960\n\t"
  "v_mfma_f32_16x16x32_bf16 v[88:91], v[44:47], a[216:219], v[88:91]\n\t"
  "ds_read_b64_tr_b16 v[38:39], v15 offset:41472\n\t"
  "v_mfma_f32_16x16x32_bf16 v[92:95], v[44:47], a[220:223], v[92:95]\n\t"
  "buffer_load_dwordx4 v6, s[48:51], s70 offen offset:0 nt lds\n\t"
  "v_mfma_f32_16x16x32_bf16 v[92:95], v[40:43], a[204:207], v[92:95]\n\t"
  "s_waitcnt lgkmcnt(4)\n\t"
  "v_mfma_f32_16x16x32_bf16 v[96:99], v[48:51], a[192:195], v[96:99]\n\t"
  "ds_read_b64_tr_b16 v[40:41], v12 offset:36864\n\t"
  "v_mfma_f32_16x16x32_bf16 v[96:99], v[52:55], a[208:211], v[96:99]\n\t"
  "ds_read_b64_tr_b16 v[42:43], v12 offset:37376\n\t"
  "v_mfma_f32_16x16x32_bf16 v[100:103], v[52:55], a[212:215], v[100:103]\n\t"
  "buffer_load_dwordx4 v7, s[48:51], s70 offen offset:1024 nt lds\n\t"
  "v_mfma_f32_16x16x32_bf16 v[100:103], v[48:51], a[196:199], v[100:103]\n\t"
  "v_mfma_f32_16x16x32_bf16 v[104:107], v[48:51], a[200:203], v[104:107]\n\t"
  "ds_read_b64_tr_b16 v[44:45], v12 offset:45056\n\t"
  "v_mfma_f32_16x16x32_bf16 v[104:107], v[52:55], a[216:219], v[104:107]\n\t"
  "ds_read_b64_tr_b16 v[46:47], v12 offset:45568\n\t"
  "v_mfma_f32_16x16x32_bf16 v[108:111], v[52:55], a[220:223], v[108:111]\n\t"
  "v_mfma_f32_16x16x32_bf16 v[108:111], v[48:51], a[204:207], v[108:111]\n\t"
  "s_waitcnt lgkmcnt(4)\n\t"
  "v_mfma_f32_16x16x32_bf16 v[112:115], v[32:35], a[192:195], v[112:115]\n\t"
  "ds_read_b64_tr_b16 v[48:49], v13 offset:36864\n\t"
  "v_mfma_f32_16x16x32_bf16 v[112:115], v[36:39], a[208:211], v[112:115]\n\t"
  "ds_read_b64_tr_b16 v[50:51], v13 offset:37376\n\t"
  "v_mfma_f32_16x16x32_bf16 v[116:119], v[36:39], a[212:215], v[116:119]\n\t"
  "v_mfma_f32_16x16x32_bf16 v[116:119], v[32:35], a[196:199], v[116:119]\n\t"
  "v_mfma_f32_16x16x32_bf16 v[120:123], v[32:35], a[200:203], v[120:123]\n\t"
  "ds_read_b64_tr_b16 v[52:53], v13 offset:45056\n\t"
  "v_mfma_f32_16x16x32_bf16 v[120:123], v[36:39], a[216:219], v[120:123]\n\t"
  "ds_read_b64_tr_b16 v[54:55], v13 offset:45568\n\t"
  "v_mfma_f32_16x16x32_bf16 v[124:127], v[36:39], a[220:223], v[124:127]\n\t"
  "v_mfma_f32_16x16x32_bf16 v[124:127], v[32:35], a[204:207], v[124:127]\n\t"
  "s_waitcnt lgkmcnt(4)\n\t"
  "v_mfma_f32_16x16x32_bf16 v[128:131], v[40:43], a[192:195], v[128:131]\n\t"
  "ds_read_b64_tr_b16 v[32:33], v14 offset:36864\n\t"
  "v_mfma_f32_16x16x32_bf16 v[128:131], v[44:47], a[208:211], v[128:131]\n\t"
  "ds_read_b64_tr_b16 v[34:35], v14 offset:37376\n\t"
  "v_mfma_f32_16x16x32_bf16 v[132:135], v[44:47], a[212:215], v[132:135]\n\t"
  "v_mfma_f32_16x16x32_bf16 v[132:135], v[40:43], a[196:199], v[132:135]\n\t"
  "v_mfma_f32_16x16x32_bf16 v[136:139], v[40:43], a[200:203], v[136:139]\n\t"
  "ds_read_b64_tr_b16 v[36:37], v14 offset:45056\n\t"
  "v_mfma_f32_16x16x32_bf16 v[136:139], v[44:47], a[216:219], v[136:139]\n\t"
  "ds_read_b64_tr_b16 v[38:39], v14 offset:45568\n\t"
  "v_mfma_f32_16x16x32_bf16 v[140:143], v[44:47], a[220:223], v[140:143]\n\t"
  "v_mfma_f32_16x16x32_bf16 v[140:143], v[40:43], a[204:207], v[140:143]\n\t"
  "s_waitcnt lgkmcnt(4)\n\t"
  "v_mfma_f32_16x16x32_bf16 v[144:147], v[48:51], a[192:195], v[144:147]\n\t"
  "ds_read_b64_tr_b16 v[40:41], v15 offset:36864\n\t"
  "s_add_u32 m0, s69, 16384\n\t"
  "v_mfma_f32_16x16x32_bf16 v[144:147], v[52:55], a[208:211], v[144:147]\n\t"
  "ds_read_b64_tr_b16 v[42:43], v15 offset:37376\n\t"
  "s_add_u32 s70, s74, 2097408\n\t"
  "v_mfma_f32_16x16x32_bf16 v[148:151], v[52:55], a[212:215], v[148:151]\n\t"
  "v_mfma_f32_16x16x32_bf16 v[148:151], v[48:51], a[196:199], v[148:151]\n\t"
  "s_waitcnt vmcnt(10)\n\t"
  "s_barrier\n\t"
  "v_mfma_f32_16x16x32_bf16 v[152:155], v[48:51], a[200:203], v[152:155]\n\t"
  "ds_read_b64_tr_b16 v[44:45], v15 offset:45056\n\t"
  "v_mfma_f32_16x16x32_bf16 v[152:155], v[52:55], a[216:219], v[152:155]\n\t"
  "ds_read_b64_tr_b16 v[46:47], v15 offset:45568\n\t"
  "v_mfma_f32_16x16x32_bf16 v[156:159], v[52:55], a[220:223], v[156:159]\n\t"
  "buffer_load_dwordx4 v4, s[44:47], s70 offen offset:0  lds\n\t"
  "v_mfma_f32_16x16x32_bf16 v[156:159], v[48:51], a[204:207], v[156:159]\n\t"
  "s_waitcnt lgkmcnt(4)\n\t"
  "v_mfma_f32_16x16x32_bf16 v[160:163], v[32:35], a[192:195], v[160:163]\n\t"
  "ds_read_b64_tr_b16 v[48:49], v12 offset:49152\n\t"
  "v_mfma_f32_16x16x32_bf16 v[160:163], v[36:39], a[208:211], v[160:163]\n\t"
  "ds_read_b64_tr_b16 v[50:51], v12 offset:49664\n\t"
  "v_mfma_f32_16x16x32_bf16 v[164:167], v[36:39], a[212:215], v[164:167]\n\t"
  "buffer_load_dwordx4 v5, s[44:47], s70 offen offset:1024  lds\n\t"
  "v_mfma_f32_16x16x32_bf16 v[164:167], v[32:35], a[196:199], v[164:167]\n\t"
  "v_mfma_f32_16x16x32_bf16 v[168:171], v[32:35], a[200:203], v[168:171]\n\t"
  "ds_read_b64_tr_b16 v[52:53], v12 offset:53248\n\t"
  "v_mfma_f32_16x16x32_bf16 v[168:171], v[36:39], a[216:219], v[168:171]\n\t"
  "ds_read_b64_tr_b16 v[54:55], v12 offset:53760\n\t"
  "v_mfma_f32_16x16x32_bf16 v[172:175], v[36:39], a[220:223], v[172:175]\n\t"
  "buffer_load_dwordx4 v60, s[44:47], s70 offen offset:2048  lds\n\t"
  "v_mfma_f32_16x16x32_bf16 v[172:175], v[32:35], a[204:207], v[172:175]\n\t"
  "s_waitcnt lgkmcnt(4)\n\t"
  "v_mfma_f32_16x16x32_bf16 v[176:179], v[40:43], a[192:195], v[176:179]\n\t"
  "ds_read_b64_tr_b16 v[32:33], v13 offset:49152\n\t"
  "v_mfma_f32_16x16x32_bf16 v[176:179], v[44:47], a[208:211], v[176:179]\n\t"
  "ds_read_b64_tr_b16 v[34:35], v13 offset:49664\n\t"
  "v_mfma_f32_16x16x32_bf16 v[180:183], v[44:47], a[212:215], v[180:183]\n\t"
  "buffer_load_dwordx4 v61, s[44:47], s70 offen offset:3072  lds\n\t"
  "s_add_u32 m0, s69, 114688\n\t"
  "v_mfma_f32_16x16x32_bf16 v[180:183], v[40:43], a[196:199], v[180:183]\n\t"
  "s_mov_b32 s70, 16384\n\t"
  "v_mfma_f32_16x16x32_bf16 v[184:187], v[40:43], a[200:203], v[184:187]\n\t"
  "ds_read_b64_tr_b16 v[36:37], v13 offset:53248\n\t"
  "v_mfma_f32_16x16x32_bf16 v[184:187], v[44:47], a[216:219], v[184:187]\n\t"
  "ds_read_b64_tr_b16 v[38:39], v13 offset:53760\n\t"
  "v_mfma_f32_16x16x32_bf16 v[188:191], v[44:47], a[220:223], v[188:191]\n\t"
  "buffer_load_dwordx4 v8, s[48:51], s70 offen offset:2048 nt lds\n\t"
  "v_mfma_f32_16x16x32_bf16 v[188:191], v[40:43], a[204:207], v[188:191]\n\t"
  "s_waitcnt lgkmcnt(4)\n\t"
  "v_mfma_f32_16x16x32_bf16 v[192:195], v[48:51], a[0:3], 0\n\t"
  "ds_read_b64_tr_b16 v[40:41], v14 offset:49152\n\t"
  "v_mfma_f32_16x16x32_bf16 v[192:195], v[52:55], a[16:19], v[192:195]\n\t"
  "ds_read_b64_tr_b16 v[42:43], v14 offset:49664\n\t"
  "v_mfma_f32_16x16x32_bf16 v[196:199], v[52:55], a[20:23], 0\n\t"
  "buffer_load_dwordx4 v9, s[48:51], s70 offen offset:3072 nt lds\n\t"
  "s_add_u32 m0, s69, 131072\n\t"
  "v_mfma_f32_16x16x32_bf16 v[196:199], v[48:51], a[4:7], v[196:199]\n\t"
  "s_mov_b32 s70, 32768\n\t"
  "v_mul_f32 v64, s83, v64\n\t"
  "v_mfma_f32_16x16x32_bf16 v[200:203], v[48:51], a[8:11], 0\n\t"
  "ds_read_b64_tr_b16 v[44:45], v14 offset:53248\n\t"
  "v_mul_f32 v65, s83, v65\n\t"
  "v_mfma_f32_16x16x32_bf16 v[200:203], v[52:55], a[24:27], v[200:203]\n\t"
  "ds_read_b64_tr_b16 v[46:47], v14 offset:53760\n\t"
  "v_mul_f32 v66, s83, v66\n\t"
  "v_mfma_f32_16x16x32_bf16 v[204:207], v[52:55], a[28:31], 0\n\t"
  "v_mul_f32 v67, s83, v67\n\t"
  "v_mul_f32 v80, s83, v80\n\t"
  "v_mfma_f32_16x16x32_bf16 v[204:207], v[48:51], a[12:15], v[204:207]\n\t"
  "buffer_load_dwordx4 v6, s[48:51], s70 offen offset:0 nt lds\n\t"
  "s_waitcnt lgkmcnt(4)\n\t"
  "v_mfma_f32_16x16x32_bf16 v[208:211], v[32:35], a[0:3], 0\n\t"
  "ds_read_b64_tr_b16 v[48:49], v15 offset:49152\n\t"
  "v_mul_f32 v81, s83, v81\n\t"
  "v_mfma_f32_16x16x32_bf16 v[208:211], v[36:39], a[16:19], v[208:211]\n\t"
  "ds_read_b64_tr_b16 v[50:51], v15 offset:49664\n\t"
  "v_mul_f32 v82, s83, v82\n\t"
  "v_mfma_f32_16x16x32_bf16 v[212:215], v[36:39], a[20:23], 0\n\t"
  "v_mul_f32 v83, s83, v83\n\t"
  "v_cvt_pk_bf16_f32 v64, v64, v65\n\t"
  "v_mfma_f32_16x16x32_bf16 v[212:215], v[32:35], a[4:7], v[212:215]\n\t"
  "v_cvt_pk_bf16_f32 v65, v66, v67\n\t"
  "v_cvt_pk_bf16_f32 v66, v80, v81\n\t"
  "v_mfma_f32_16x16x32_bf16 v[216:219], v[32:35], a[8:11], 0\n\t"
  "ds_read_b64_tr_b16 v[52:53], v15 offset:53248\n\t"
  "v_cvt_pk_bf16_f32 v67, v82, v83\n\t"
  "v_mfma_f32_16x16x32_bf16 v[216:219], v[36:39], a[24:27], v[216:219]\n\t"
  "ds_read_b64_tr_b16 v[54:55], v15 offset:53760\n\t"
  "v_mul_f32 v68, s83, v68\n\t"
  "v_mfma_f32_16x16x32_bf16 v[220:223], v[36:39], a[28:31], 0\n\t"
  "v_mul_f32 v69, s83, v69\n\t"
  "v_mul_f32 v70, s83, v70\n\t"
  "v_mfma_f32_16x16x32_bf16 v[220:223], v[32:35], a[12:15], v[220:223]\n\t"
  "v_mul_f32 v71, s83, v71\n\t"
  "s_waitcnt lgkmcnt(4)\n\t"
  "v_mfma_f32_16x16x32_bf16 v[224:227], v[40:43], a[0:3], 0\n\t"
  "ds_read_b64_tr_b16 v[32:33], v12 offset:57344\n\t"
  "v_permlane16_swap_b32 v64, v66\n\t"
  "v_mfma_f32_16x16x32_bf16 v[224:227], v[44:47], a[16:19], v[224:227]\n\t"
  "ds_read_b64_tr_b16 v[34:35], v12 offset:57856\n\t"
  "v_permlane16_swap_b32 v65, v67\n\t"
  "v_mfma_f32_16x16x32_bf16 v[228:231], v[44:47], a[20:23], 0\n\t"
  "v_mul_f32 v84, s83, v84\n\t"
  "v_mul_f32 v85, s83, v85\n\t"
  "v_mfma_f32_16x16x32_bf16 v[228:231], v[40:43], a[4:7], v[228:231]\n\t"
  "v_mul_f32 v86, s83, v86\n\t"
  "v_mul_f32 v87, s83, v87\n\t"
  "v_mfma_f32_16x16x32_bf16 v[232:235], v[40:43], a[8:11], 0\n\t"
  "ds_read_b64_tr_b16 v[36:37], v12 offset:61440\n\t"
  "v_cvt_pk_bf16_f32 v68, v68, v69\n\t"
  "v_mfma_f32_16x16x32_bf16 v[232:235], v[44:47], a[24:27], v[232:235]\n\t"
  "ds_read_b64_tr_b16 v[38:39], v12 offset:61952\n\t"
  "v_cvt_pk_bf16_f32 v69, v70, v71\n\t"
  "v_mfma_f32_16x16x32_bf16 v[236:239], v[44:47], a[28:31], 0\n\t"
  "v_cvt_pk_bf16_f32 v70, v84, v85\n\t"
  "v_cvt_pk_bf16_f32 v71, v86, v87\n\t"
  "v_mfma_f32_16x16x32_bf16 v[236:239], v[40:43], a[12:15], v[236:239]\n\t"
  "v_mul_f32 v72, s83, v72\n\t"
  "s_waitcnt lgkmcnt(4)\n\t"
  "v_mfma_f32_16x16x32_bf16 v[240:243], v[48:51], a[0:3], 0\n\t"
  "ds_read_b64_tr_b16 v[40:41], v13 offset:57344\n\t"
  "v_mul_f32 v73, s83, v73\n\t"
  "v_mfma_f32_16x16x32_bf16 v[240:243], v[52:55], a[16:19], v[240:243]\n\t"
  "ds_read_b64_tr_b16 v[42:43], v13 offset:57856\n\t"
  "v_mul_f32 v74, s83, v74\n\t"
  "v_mfma_f32_16x16x32_bf16 v[244:247], v[52:55], a[20:23], 0\n\t"
  "v_mul_f32 v75, s83, v75\n\t"
  "v_permlane16_swap_b32 v68, v70\n\t"
  "v_mfma_f32_16x16x32_bf16 v[244:247], v[48:51], a[4:7], v[244:247]\n\t"
  "v_permlane16_swap_b32 v69, v71\n\t"
  "v_mul_f32 v88, s83, v88\n\t"
  "v_mfma_f32_16x16x32_bf16 v[248:251], v[48:51], a[8:11], 0\n\t"
  "ds_read_b64_tr_b16 v[44:45], v13 offset:61440\n\t"
  "v_mul_f32 v89, s83, v89\n\t"
  "v_mfma_f32_16x16x32_bf16 v[248:251], v[52:55], a[24:27], v[248:251]\n\t"
  "ds_read_b64_tr_b16 v[46:47], v13 offset:61952\n\t"
  "v_mul_f32 v90, s83, v90\n\t"
  "v_mfma_f32_16x16x32_bf16 v[252:255], v[52:55], a[28:31], 0\n\t"
  "v_mul_f32 v91, s83, v91\n\t"
  "v_cvt_pk_bf16_f32 v72, v72, v73\n\t"
  "v_mfma_f32_16x16x32_bf16 v[252:255], v[48:51], a[12:15], v[252:255]\n\t"
  "v_cvt_pk_bf16_f32 v73, v74, v75\n\t"
  "s_waitcnt lgkmcnt(4)\n\t"
  "v_mfma_f32_16x16x32_bf16 v[192:195], v[32:35], a[32:35], v[192:195]\n\t"
  "ds_read_b64_tr_b16 v[48:49], v14 offset:57344\n\t"
  "v_cvt_pk_bf16_f32 v74, v88, v89\n\t"
  "v_mfma_f32_16x16x32_bf16 v[192:195], v[36:39], a[48:51], v[192:195]\n\t"
  "ds_read_b64_tr_b16 v[50:51], v14 offset:57856\n\t"
  "v_cvt_pk_bf16_f32 v75, v90, v91\n\t"
  "v_mfma_f32_16x16x32_bf16 v[196:199], v[36:39], a[52:55], v[196:199]\n\t"
  "v_mul_f32 v76, s83, v76\n\t"
  "v_mul_f32 v77, s83, v77\n\t"
  "v_mfma_f32_16x16x32_bf16 v[196:199], v[32:35], a[36:39], v[196:199]\n\t"
  "v_mul_f32 v78, s83, v78\n\t"
  "v_mul_f32 v79, s83, v79\n\t"
  "v_mfma_f32_16x16x32_bf16 v[200:203], v[32:35], a[40:43], v[200:203]\n\t"
  "ds_read_b64_tr_b16 v[52:53], v14 offset:61440\n\t"
  "v_permlane16_swap_b32 v72, v74\n\t"
  "v_mfma_f32_16x16x32_bf16 v[200:203], v[36:39], a[56:59], v[200:203]\n\t"
  "ds_read_b64_tr_b16 v[54:55], v14 offset:61952\n\t"
  "v_permlane16_swap_b32 v73, v75\n\t"
  "v_mfma_f32_16x16x32_bf16 v[204:207], v[36:39], a[60:63], v[204:207]\n\t"
  "v_mul_f32 v92, s83, v92\n\t"
  "v_mul_f32 v93, s83, v93\n\t"
  "v_mfma_f32_16x16x32_bf16 v[204:207], v[32:35], a[44:47], v[204:207]\n\t"
  "v_mul_f32 v94, s83, v94\n\t"
  "s_waitcnt lgkmcnt(4)\n\t"
  "v_mfma_f32_16x16x32_bf16 v[208:211], v[40:43], a[32:35], v[208:211]\n\t"
  "ds_read_b64_tr_b16 v[32:33], v15 offset:57344\n\t"
  "s_add_u32 m0, s69, 32768\n\t"
  "v_mfma_f32_16x16x32_bf16 v[208:211], v[44:47], a[48:51], v[208:211]\n\t"
  "ds_read_b64_tr_b16 v[34:35], v15 offset:57856\n\t"
  "s_mov_b32 s70, s91\n\t"
  "v_mfma_f32_16x16x32_bf16 v[212:215], v[44:47], a[52:55], v[212:215]\n\t"
  "v_mul_f32 v95, s83, v95\n\t"
  "v_cvt_pk_bf16_f32 v76, v76, v77\n\t"
  "v_mfma_f32_16x16x32_bf16 v[212:215], v[40:43], a[36:39], v[212:215]\n\t"
  "s_waitcnt vmcnt(10)\n\t"
  "s_barrier\n\t"
  "v_mfma_f32_16x16x32_bf16 v[216:219], v[40:43], a[40:43], v[216:219]\n\t"
  "ds_read_b64_tr_b16 v[36:37], v15 offset:61440\n\t"
  "v_cvt_pk_bf16_f32 v77, v78, v79\n\t"
  "v_mfma_f32_16x16x32_bf16 v[216:219], v[44:47], a[56:59], v[216:219]\n\t"
  "ds_read_b64_tr_b16 v[38:39], v15 offset:61952\n\t"
  "v_cvt_pk_bf16_f32 v78, v92, v93\n\t"
  "v_mfma_f32_16x16x32_bf16 v[220:223], v[44:47], a[60:63], v[220:223]\n\t"
  "buffer_load_dwordx4 v4, s[44:47], s70 offen offset:0  lds\n\t"
  "v_cvt_pk_bf16_f32 v79, v94, v95\n\t"
  "v_mfma_f32_16x16x32_bf16 v[220:223], v[40:43], a[44:47], v[220:223]\n\t"
  "v_mul_f32 v96, s83, v96\n\t"
  "s_waitcnt lgkmcnt(4)\n\t"
  "v_mfma_f32_16x16x32_bf16 v[224:227], v[48:51], a[32:35], v[224:227]\n\t"
  "ds_read_b64_tr_b16 v[40:41], v12 offset:0\n\t"
  "v_mul_f32 v97, s83, v97\n\t"
  "v_mfma_f32_16x16x32_bf16 v[224:227], v[52:55], a[48:51], v[224:227]\n\t"
  "ds_read_b64_tr_b16 v[42:43], v12 offset:512\n\t"
  "v_mul_f32 v98, s83, v98\n\t"
  "v_mfma_f32_16x16x32_bf16 v[228:231], v[52:55], a[52:55], v[228:231]\n\t"
  "v_mul_f32 v99, s83, v99\n\t"
  "v_permlane16_swap_b32 v76, v78\n\t"
  "v_mfma_f32_16x16x32_bf16 v[228:231], v[48:51], a[36:39], v[228:231]\n\t"
  "buffer_load_dwordx4 v5, s[44:47], s70 offen offset:1024  lds\n\t"
  "v_permlane16_swap_b32 v77, v79\n\t"
  "v_mfma_f32_16x16x32_bf16 v[232:235], v[48:51], a[40:43], v[232:235]\n\t"
  "ds_read_b64_tr_b16 v[44:45], v12 offset:4096\n\t"
  "v_mul_f32 v112, s83, v112\n\t"
  "v_mfma_f32_16x16x32_bf16 v[232:235], v[52:55], a[56:59], v[232:235]\n\t"
  "ds_read_b64_tr_b16 v[46:47], v12 offset:4608\n\t"
  "v_mul_f32 v113, s83, v113\n\t"
  "v_mfma_f32_16x16x32_bf16 v[236:239], v[52:55], a[60:63], v[236:239]\n\t"
  "v_mul_f32 v114, s83, v114\n\t"
  "v_mul_f32 v115, s83, v115\n\t"
  "v_mfma_f32_16x16x32_bf16 v[236:239], v[48:51], a[44:47], v[236:239]\n\t"
  "v_cvt_pk_bf16_f32 v96, v96, v97\n\t"
  "s_waitcnt lgkmcnt(4)\n\t"
  "v_mfma_f32_16x16x32_bf16 v[240:243], v[32:35], a[32:35], v[240:243]\n\t"
  "ds_read_b64_tr_b16 v[48:49], v13 offset:0\n\t"
  "v_cvt_pk_bf16_f32 v97, v98, v99\n\t"
  "v_mfma_f32_16x16x32_bf16 v[240:243], v[36:39], a[48:51], v[240:243]\n\t"
  "ds_read_b64_tr_b16 v[50:51], v13 offset:512\n\t"
  "v_cvt_pk_bf16_f32 v98, v112, v113\n\t"
  "v_mfma_f32_16x16x32_bf16 v[244:247], v[36:39], a[52:55], v[244:247]\n\t"
  "buffer_load_dwordx4 v60, s[44:47], s70 offen offset:2048  lds\n\t"
  "v_cvt_pk_bf16_f32 v99, v114, v115\n\t"
  "v_mfma_f32_16x16x32_bf16 v[244:247], v[32:35], a[36:39], v[244:247]\n\t"
  "v_mul_f32 v100, s83, v100\n\t"
  "v_mul_f32 v101, s83, v101\n\t"
  "v_mfma_f32_16x16x32_bf16 v[248:251], v[32:35], a[40:43], v[248:251]\n\t"
  "ds_read_b64_tr_b16 v[52:53], v13 offset:4096\n\t"
  "v_mul_f32 v102, s83, v102\n\t"
  "v_mfma_f32_16x16x32_bf16 v[248:251], v[36:39], a[56:59], v[248:251]\n\t"
  "ds_read_b64_tr_b16 v[54:55], v13 offset:4608\n\t"
  "v_mul_f32 v103, s83, v103\n\t"
  "v_mfma_f32_16x16x32_bf16 v[252:255], v[36:39], a[60:63], v[252:255]\n\t"
  "v_permlane16_swap_b32 v96, v98\n\t"
  "v_permlane16_swap_b32 v97, v99\n\t"
  "v_mfma_f32_16x16x32_bf16 v[252:255], v[32:35], a[44:47], v[252:255]\n\t"
  "buffer_load_dwordx4 v61, s[44:47], s70 offen offset:3072  lds\n\t"
  "s_waitcnt lgkmcnt(4)\n\t"
  "v_mfma_f32_16x16x32_bf16 v[192:195], v[40:43], a[64:67], v[192:195]\n\t"
  "ds_read_b64_tr_b16 v[32:33], v14 offset:0\n\t"
  "s_add_u32 m0, s69, 131072\n\t"
  "v_mfma_f32_16x16x32_bf16 v[192:195], v[44:47], a[80:83], v[192:195]\n\t"
  "ds_read_b64_tr_b16 v[34:35], v14 offset:512\n\t"
  "s_mov_b32 s70, 32768\n\t"
  "v_mfma_f32_16x16x32_bf16 v[196:199], v[44:47], a[84:87], v[196:199]\n\t"
  "v_mul_f32 v116, s83, v116\n\t"
  "v_mul_f32 v117, s83, v117\n\t"
  "v_mfma_f32_16x16x32_bf16 v[196:199], v[40:43], a[68:71], v[196:199]\n\t"
  "v_mul_f32 v118, s83, v118\n\t"
  "v_mul_f32 v119, s83, v119\n\t"
  "v_mfma_f32_16x16x32_bf16 v[200:203], v[40:43], a[72:75], v[200:203]\n\t"
  "ds_read_b64_tr_b16 v[36:37], v14 offset:4096\n\t"
  "v_cvt_pk_bf16_f32 v100, v100, v101\n\t"
  "v_mfma_f32_16x16x32_bf16 v[200:203], v[44:47], a[88:91], v[200:203]\n\t"
  "ds_read_b64_tr_b16 v[38:39], v14 offset:4608\n\t"
  "v_cvt_pk_bf16_f32 v101, v102, v103\n\t"
  "v_mfma_f32_16x16x32_bf16 v[204:207], v[44:47], a[92:95], v[204:207]\n\t"
  "buffer_load_dwordx4 v7, s[48:51], s70 offen offset:1024 nt lds\n\t"
  "v_cvt_pk_bf16_f32 v102, v116, v117\n\t"
  "v_mfma_f32_16x16x32_bf16 v[204:207], v[40:43], a[76:79], v[204:207]\n\t"
  "v_cvt_pk_bf16_f32 v103, v118, v119\n\t"
  "s_waitcnt lgkmcnt(4)\n\t"
  "v_mfma_f32_16x16x32_bf16 v[208:211], v[48:51], a[64:67], v[208:211]\n\t"
  "ds_read_b64_tr_b16 v[40:41], v15 offset:0\n\t"
  "v_mul_f32 v104, s83, v104\n\t"
  "v_mfma_f32_16x16x32_bf16 v[208:211], v[52:55], a[80:83], v[208:211]\n\t"
  "ds_read_b64_tr_b16 v[42:43], v15 offset:512\n\t"
  "v_mul_f32 v105, s83, v105\n\t"
  "v_mfma_f32_16x16x32_bf16 v[212:215], v[52:55], a[84:87], v[212:215]\n\t"
  "v_mul_f32 v106, s83, v106\n\t"
  "v_mul_f32 v107, s83, v107\n\t"
  "v_mfma_f32_16x16x32_bf16 v[212:215], v[48:51], a[68:71], v[212:215]\n\t"
  "buffer_load_dwordx4 v8, s[48:51], s70 offen offset:2048 nt lds\n\t"
  "v_permlane16_swap_b32 v100, v102\n\t"
  "v_mfma_f32_16x16x32_bf16 v[216:219], v[48:51], a[72:75], v[216:219]\n\t"
  "ds_read_b64_tr_b16 v[44:45], v15 offset:4096\n\t"
  "v_permlane16_swap_b32 v101, v103\n\t"
  "v_mfma_f32_16x16x32_bf16 v[216:219], v[52:55], a[88:91], v[216:219]\n\t"
  "ds_read_b64_tr_b16 v[46:47], v15 offset:4608\n\t"
  "v_mul_f32 v120, s83, v120\n\t"
  "v_mfma_f32_16x16x32_bf16 v[220:223], v[52:55], a[92:95], v[220:223]\n\t"
  "v_mul_f32 v121, s83, v121\n\t"
  "v_mul_f32 v122, s83, v122\n\t"
  "v_mfma_f32_16x16x32_bf16 v[220:223], v[48:51], a[76:79], v[220:223]\n\t"
  "v_mul_f32 v123, s83, v123\n\t"
  "s_waitcnt lgkmcnt(4)\n\t"
  "v_mfma_f32_16x16x32_bf16 v[224:227], v[32:35], a[64:67], v[224:227]\n\t"
  "ds_read_b64_tr_b16 v[48:49], v12 offset:8192\n\t"
  "v_cvt_pk_bf16_f32 v104, v104, v105\n\t"
  "v_mfma_f32_16x16x32_bf16 v[224:227], v[36:39], a[80:83], v[224:227]\n\t"
  "ds_read_b64_tr_b16 v[50:51], v12 offset:8704\n\t"
  "v_cvt_pk_bf16_f32 v105, v106, v107\n\t"
  "v_mfma_f32_16x16x32_bf16 v[228:231], v[36:39], a[84:87], v[228:231]\n\t"
  "buffer_load_dwordx4 v9, s[48:51], s70 offen offset:3072 nt lds\n\t"
  "v_cvt_pk_bf16_f32 v106, v120, v121\n\t"
  "v_mfma_f32_16x16x32_bf16 v[228:231], v[32:35], a[68:71], v[228:231]\n\t"
  "v_cvt_pk_bf16_f32 v107, v122, v123\n\t"
  "v_mul_f32 v108, s83, v108\n\t"
  "v_mfma_f32_16x16x32_bf16 v[232:235], v[32:35], a[72:75], v[232:235]\n\t"
  "ds_read_b64_tr_b16 v[52:53], v12 offset:12288\n\t"
  "v_mul_f32 v109, s83, v109\n\t"
  "v_mfma_f32_16x16x32_bf16 v[232:235], v[36:39], a[88:91], v[232:235]\n\t"
  "ds_read_b64_tr_b16 v[54:55], v12 offset:12800\n\t"
  "v_mul_f32 v110, s83, v110\n\t"
  "v_mfma_f32_16x16x32_bf16 v[236:239], v[36:39], a[92:95], v[236:239]\n\t"
  "v_mul_f32 v111, s83, v111\n\t"
  "v_permlane16_swap_b32 v104, v106\n\t"
  "v_mfma_f32_16x16x32_bf16 v[236:239], v[32:35], a[76:79], v[236:239]\n\t"
  "v_permlane16_swap_b32 v105, v107\n\t"
  "s_waitcnt lgkmcnt(4)\n\t"
  "v_mfma_f32_16x16x32_bf16 v[240:243], v[40:43], a[64:67], v[240:243]\n\t"
  "ds_read_b64_tr_b16 v[32:33], v13 offset:8192\n\t"
  "v_mul_f32 v124, s83, v124\n\t"
  "v_mfma_f32_16x16x32_bf16 v[240:243], v[44:47], a[80:83], v[240:243]\n\t"
  "ds_read_b64_tr_b16 v[34:35], v13 offset:8704\n\t"
  "v_mul_f32 v125, s83, v125\n\t"
  "v_mfma_f32_16x16x32_bf16 v[244:247], v[44:47], a[84:87], v[244:247]\n\t"
  "v_mul_f32 v126, s83, v126\n\t"
  "v_mul_f32 v127, s83, v127\n\t"
  "v_mfma_f32_16x16x32_bf16 v[244:247], v[40:43], a[68:71], v[244:247]\n\t"
  "v_cvt_pk_bf16_f32 v108, v108, v109\n\t"
  "v_cvt_pk_bf16_f32 v109, v110, v111\n\t"
  "v_mfma_f32_16x16x32_bf16 v[248:251], v[40:43], a[72:75], v[248:251]\n\t"
  "ds_read_b64_tr_b16 v[36:37], v13 offset:12288\n\t"
  "v_cvt_pk_bf16_f32 v110, v124, v125\n\t"
  "v_mfma_f32_16x16x32_bf16 v[248:251], v[44:47], a[88:91], v[248:251]\n\t"
  "ds_read_b64_tr_b16 v[38:39], v13 offset:12800\n\t"
  "v_cvt_pk_bf16_f32 v111, v126, v127\n\t"
  "v_mfma_f32_16x16x32_bf16 v[252:255], v[44:47], a[92:95], v[252:255]\n\t"
  "v_mul_f32 v128, s83, v128\n\t"
  "v_mul_f32 v129, s83, v129\n\t"
  "v_mfma_f32_16x16x32_bf16 v[252:255], v[40:43], a[76:79], v[252:255]\n\t"
  "v_mul_f32 v130, s83, v130\n\t"
  "s_waitcnt lgkmcnt(4)\n\t"
  "v_mfma_f32_16x16x32_bf16 v[192:195], v[48:51], a[96:99], v[192:195]\n\t"
  "ds_read_b64_tr_b16 v[40:41], v14 offset:8192\n\t"
  "v_mul_f32 v131, s83, v131\n\t"
  "v_mfma_f32_16x16x32_bf16 v[192:195], v[52:55], a[112:115], v[192:195]\n\t"
  "ds_read_b64_tr_b16 v[42:43], v14 offset:8704\n\t"
  "v_permlane16_swap_b32 v108, v110\n\t"
  "v_mfma_f32_16x16x32_bf16 v[196:199], v[52:55], a[116:119], v[196:199]\n\t"
  "v_permlane16_swap_b32 v109, v111\n\t"
  "v_mul_f32 v144, s83, v144\n\t"
  "v_mfma_f32_16x16x32_bf16 v[196:199], v[48:51], a[100:103], v[196:199]\n\t"
  "v_mul_f32 v145, s83, v145\n\t"
  "v_mul_f32 v146, s83, v146\n\t"
  "v_mfma_f32_16x16x32_bf16 v[200:203], v[48:51], a[104:107], v[200:203]\n\t"
  "ds_read_b64_tr_b16 v[44:45], v14 offset:12288\n\t"
  "v_mul_f32 v147, s83, v147\n\t"
  "v_mfma_f32_16x16x32_bf16 v[200:203], v[52:55], a[120:123], v[200:203]\n\t"
  "ds_read_b64_tr_b16 v[46:47], v14 offset:12800\n\t"
  "v_cvt_pk_bf16_f32 v128, v128, v129\n\t"
  "v_mfma_f32_16x16x32_bf16 v[204:207], v[52:55], a[124:127], v[204:207]\n\t"
  "v_cvt_pk_bf16_f32 v129, v130, v131\n\t"
  "v_cvt_pk_bf16_f32 v130, v144, v145\n\t"
  "v_mfma_f32_16x16x32_bf16 v[204:207], v[48:51], a[108:111], v[204:207]\n\t"
  "v_cvt_pk_bf16_f32 v131, v146, v147\n\t"
  "s_waitcnt lgkmcnt(4)\n\t"
  "v_mfma_f32_16x16x32_bf16 v[208:211], v[32:35], a[96:99], v[208:211]\n\t"
  "ds_read_b64_tr_b16 v[48:49], v15 offset:8192\n\t"
  "s_add_u32 m0, s69, 49152\n\t"
  "v_mfma_f32_16x16x32_bf16 v[208:211], v[36:39], a[112:115], v[208:211]\n\t"
  "ds_read_b64_tr_b16 v[50:51], v15 offset:8704\n\t"
  "s_add_u32 s70, s74, 524672\n\t"
  "v_mfma_f32_16x16x32_bf16 v[212:215], v[36:39], a[116:119], v[212:215]\n\t"
  "v_mul_f32 v132, s83, v132\n\t"
  "v_mul_f32 v133, s83, v133\n\t"
  "v_mfma_f32_16x16x32_bf16 v[212:215], v[32:35], a[100:103], v[212:215]\n\t"
  "s_waitcnt vmcnt(10)\n\t"
  "s_barrier\n\t"
  "v_mfma_f32_16x16x32_bf16 v[216:219], v[32:35], a[104:107], v[216:219]\n\t"
  "ds_read_b64_tr_b16 v[52:53], v15 offset:12288\n\t"
  "v_mul_f32 v134, s83, v134\n\t"
  "v_mfma_f32_16x16x32_bf16 v[216:219], v[36:39], a[120:123], v[216:219]\n\t"
  "ds_read_b64_tr_b16 v[54:55], v15 offset:12800\n\t"
  "v_mul_f32 v135, s83, v135\n\t"
  "v_mfma_f32_16x16x32_bf16 v[220:223], v[36:39], a[124:127], v[220:223]\n\t"
  "buffer_load_dwordx4 v4, s[44:47], s70 offen offset:0  lds\n\t"
  "v_permlane16_swap_b32 v128, v130\n\t"
  "v_mfma_f32_16x16x32_bf16 v[220:223], v[32:35], a[108:111], v[220:223]\n\t"
  "v_permlane16_swap_b32 v129, v131\n\t"
  "s_waitcnt lgkmcnt(4)\n\t"
  "v_mfma_f32_16x16x32_bf16 v[224:227], v[40:43], a[96:99], v[224:227]\n\t"
  "ds_read_b64_tr_b16 v[32:33], v12 offset:16384\n\t"
  "v_mul_f32 v148, s83, v148\n\t"
  "v_mfma_f32_16x16x32_bf16 v[224:227], v[44:47], a[112:115], v[224:227]\n\t"
  "ds_read_b64_tr_b16 v[34:35], v12 offset:16896\n\t"
  "v_mul_f32 v149, s83, v149\n\t"
  "v_mfma_f32_16x16x32_bf16 v[228:231], v[44:47], a[116:119], v[228:231]\n\t"
  "v_mul_f32 v150, s83, v150\n\t"
  "v_mul_f32 v151, s83, v151\n\t"
  "v_mfma_f32_16x16x32_bf16 v[228:231], v[40:43], a[100:103], v[228:231]\n\t"
  "buffer_load_dwordx4 v5, s[44:47], s70 offen offset:1024  lds\n\t"
  "v_cvt_pk_bf16_f32 v132, v132, v133\n\t"
  "v_mfma_f32_16x16x32_bf16 v[232:235], v[40:43], a[104:107], v[232:235]\n\t"
  "ds_read_b64_tr_b16 v[36:37], v12 offset:20480\n\t"
  "v_cvt_pk_bf16_f32 v133, v134, v135\n\t"
  "v_mfma_f32_16x16x32_bf16 v[232:235], v[44:47], a[120:123], v[232:235]\n\t"
  "ds_read_b64_tr_b16 v[38:39], v12 offset:20992\n\t"
  "v_cvt_pk_bf16_f32 v134, v148, v149\n\t"
  "v_mfma_f32_16x16x32_bf16 v[236:239], v[44:47], a[124:127], v[236:239]\n\t"
  "v_cvt_pk_bf16_f32 v135, v150, v151\n\t"
  "v_mul_f32 v136, s83, v136\n\t"
  "v_mfma_f32_16x16x32_bf16 v[236:239], v[40:43], a[108:111], v[236:239]\n\t"
  "v_mul_f32 v137, s83, v137\n\t"
  "s_waitcnt lgkmcnt(4)\n\t"
  "v_mfma_f32_16x16x32_bf16 v[240:243], v[48:51], a[96:99], v[240:243]\n\t"
  "ds_read_b64_tr_b16 v[40:41], v13 offset:16384\n\t"
  "v_mul_f32 v138, s83, v138\n\t"
  "v_mfma_f32_16x16x32_bf16 v[240:243], v[52:55], a[112:115], v[240:243]\n\t"
  "ds_read_b64_tr_b16 v[42:43], v13 offset:16896\n\t"
  "v_mul_f32 v139, s83, v139\n\t"
  "v_mfma_f32_16x16x32_bf16 v[244:247], v[52:55], a[116:119], v[244:247]\n\t"
  "buffer_load_dwordx4 v60, s[44:47], s70 offen offset:2048  lds\n\t"
  "v_permlane16_swap_b32 v132, v134\n\t"
  "v_mfma_f32_16x16x32_bf16 v[244:247], v[48:51], a[100:103], v[244:247]\n\t"
  "v_permlane16_swap_b32 v133, v135\n\t"
  "v_mul_f32 v152, s83, v152\n\t"
  "v_mfma_f32_16x16x32_bf16 v[248:251], v[48:51], a[104:107], v[248:251]\n\t"
  "ds_read_b64_tr_b16 v[44:45], v13 offset:20480\n\t"
  "v_mul_f32 v153, s83, v153\n\t"
  "v_mfma_f32_16x16x32_bf16 v[248:251], v[52:55], a[120:123], v[248:251]\n\t"
  "ds_read_b64_tr_b16 v[46:47], v13 offset:20992\n\t"
  "v_mul_f32 v154, s83, v154\n\t"
  "v_mfma_f32_16x16x32_bf16 v[252:255], v[52:55], a[124:127], v[252:255]\n\t"
  "v_mul_f32 v155, s83, v155\n\t"
  "v_cvt_pk_bf16_f32 v136, v136, v137\n\t"
  "v_mfma_f32_16x16x32_bf16 v[252:255], v[48:51], a[108:111], v[252:255]\n\t"
  "buffer_load_dwordx4 v61, s[44:47], s70 offen offset:3072  lds\n\t"
  "s_waitcnt lgkmcnt(4)\n\t"
  "v_mfma_f32_16x16x32_bf16 v[192:195], v[32:35], a[128:131], v[192:195]\n\t"
  "ds_read_b64_tr_b16 v[48:49], v14 offset:16384\n\t"
  "s_add_u32 m0, s69, 147456\n\t"
  "v_mfma_f32_16x16x32_bf16 v[192:195], v[36:39], a[144:147], v[192:195]\n\t"
  "ds_read_b64_tr_b16 v[50:51], v14 offset:16896\n\t"
  "s_mov_b32 s70, 49152\n\t"
  "v_mfma_f32_16x16x32_bf16 v[196:199], v[36:39], a[148:151], v[196:199]\n\t"
  "v_cvt_pk_bf16_f32 v137, v138, v139\n\t"
  "v_cvt_pk_bf16_f32 v138, v152, v153\n\t"
  "v_mfma_f32_16x16x32_bf16 v[196:199], v[32:35], a[132:135], v[196:199]\n\t"
  "v_cvt_pk_bf16_f32 v139, v154, v155\n\t"
  "v_mul_f32 v140, s83, v140\n\t"
  "v_mfma_f32_16x16x32_bf16 v[200:203], v[32:35], a[136:139], v[200:203]\n\t"
  "ds_read_b64_tr_b16 v[52:53], v14 offset:20480\n\t"
  "v_mul_f32 v141, s83, v141\n\t"
  "v_mfma_f32_16x16x32_bf16 v[200:203], v[36:39], a[152:155], v[200:203]\n\t"
  "ds_read_b64_tr_b16 v[54:55], v14 offset:20992\n\t"
  "v_mul_f32 v142, s83, v142\n\t"
  "v_mfma_f32_16x16x32_bf16 v[204:207], v[36:39], a[156:159], v[204:207]\n\t"
  "buffer_load_dwordx4 v6, s[48:51], s70 offen offset:0 nt lds\n\t"
  "v_mul_f32 v143, s83, v143\n\t"
  "v_mfma_f32_16x16x32_bf16 v[204:207], v[32:35], a[140:143], v[204:207]\n\t"
  "v_permlane16_swap_b32 v136, v138\n\t"
  "s_waitcnt lgkmcnt(4)\n\t"
  "v_mfma_f32_16x16x32_bf16 v[208:211], v[40:43], a[128:131], v[208:211]\n\t"
  "ds_read_b64_tr_b16 v[32:33], v15 offset:16384\n\t"
  "v_permlane16_swap_b32 v137, v139\n\t"
  "v_mfma_f32_16x16x32_bf16 v[208:211], v[44:47], a[144:147], v[208:211]\n\t"
  "ds_read_b64_tr_b16 v[34:35], v15 offset:16896\n\t"
  "v_mul_f32 v156, s83, v156\n\t"
  "v_mfma_f32_16x16x32_bf16 v[212:215], v[44:47], a[148:151], v[212:215]\n\t"
  "v_mul_f32 v157, s83, v157\n\t"
  "v_mul_f32 v158, s83, v158\n\t"
  "v_mfma_f32_16x16x32_bf16 v[212:215], v[40:43], a[132:135], v[212:215]\n\t"
  "buffer_load_dwordx4 v7, s[48:51], s70 offen offset:1024 nt lds\n\t"
  "v_mul_f32 v159, s83, v159\n\t"
  "v_mfma_f32_16x16x32_bf16 v[216:219], v[40:43], a[136:139], v[216:219]\n\t"
  "ds_read_b64_tr_b16 v[36:37], v15 offset:20480\n\t"
  "v_cvt_pk_bf16_f32 v140, v140, v141\n\t"
  "v_mfma_f32_16x16x32_bf16 v[216:219], v[44:47], a[152:155], v[216:219]\n\t"
  "ds_read_b64_tr_b16 v[38:39], v15 offset:20992\n\t"
  "v_cvt_pk_bf16_f32 v141, v142, v143\n\t"
  "v_mfma_f32_16x16x32_bf16 v[220:223], v[44:47], a[156:159], v[220:223]\n\t"
  "v_cvt_pk_bf16_f32 v142, v156, v157\n\t"
  "v_cvt_pk_bf16_f32 v143, v158, v159\n\t"
  "v_mfma_f32_16x16x32_bf16 v[220:223], v[40:43], a[140:143], v[220:223]\n\t"
  "v_mul_f32 v160, s83, v160\n\t"
  "s_waitcnt lgkmcnt(4)\n\t"
  "v_mfma_f32_16x16x32_bf16 v[224:227], v[48:51], a[128:131], v[224:227]\n\t"
  "ds_read_b64_tr_b16 v[40:41], v12 offset:24576\n\t"
  "v_mul_f32 v161, s83, v161\n\t"
  "v_mfma_f32_16x16x32_bf16 v[224:227], v[52:55], a[144:147], v[224:227]\n\t"
  "ds_read_b64_tr_b16 v[42:43], v12 offset:25088\n\t"
  "v_mul_f32 v162, s83, v162\n\t"
  "v_mfma_f32_16x16x32_bf16 v[228:231], v[52:55], a[148:151], v[228:231]\n\t"
  "buffer_load_dwordx4 v8, s[48:51], s70 offen offset:2048 nt lds\n\t"
  "v_mul_f32 v163, s83, v163\n\t"
  "v_mfma_f32_16x16x32_bf16 v[228:231], v[48:51], a[132:135], v[228:231]\n\t"
  "v_permlane16_swap_b32 v140, v142\n\t"
  "v_permlane16_swap_b32 v141, v143\n\t"
  "v_mfma_f32_16x16x32_bf16 v[232:235], v[48:51], a[136:139], v[232:235]\n\t"
  "ds_read_b64_tr_b16 v[44:45], v12 offset:28672\n\t"
  "v_mul_f32 v176, s83, v176\n\t"
  "v_mfma_f32_16x16x32_bf16 v[232:235], v[52:55], a[152:155], v[232:235]\n\t"
  "ds_read_b64_tr_b16 v[46:47], v12 offset:29184\n\t"
  "v_mul_f32 v177, s83, v177\n\t"
  "v_mfma_f32_16x16x32_bf16 v[236:239], v[52:55], a[156:159], v[236:239]\n\t"
  "v_mul_f32 v178, s83, v178\n\t"
  "v_mul_f32 v179, s83, v179\n\t"
  "v_mfma_f32_16x16x32_bf16 v[236:239], v[48:51], a[140:143], v[236:239]\n\t"
  "v_cvt_pk_bf16_f32 v160, v160, v161\n\t"
  "s_waitcnt lgkmcnt(4)\n\t"
  "v_mfma_f32_16x16x32_bf16 v[240:243], v[32:35], a[128:131], v[240:243]\n\t"
  "ds_read_b64_tr_b16 v[48:49], v13 offset:24576\n\t"
  "v_cvt_pk_bf16_f32 v161, v162, v163\n\t"
  "v_mfma_f32_16x16x32_bf16 v[240:243], v[36:39], a[144:147], v[240:243]\n\t"
  "ds_read_b64_tr_b16 v[50:51], v13 offset:25088\n\t"
  "v_cvt_pk_bf16_f32 v162, v176, v177\n\t"
  "v_mfma_f32_16x16x32_bf16 v[244:247], v[36:39], a[148:151], v[244:247]\n\t"
  "v_cvt_pk_bf16_f32 v163, v178, v179\n\t"
  "v_mul_f32 v164, s83, v164\n\t"
  "v_mfma_f32_16x16x32_bf16 v[244:247], v[32:35], a[132:135], v[244:247]\n\t"
  "v_mul_f32 v165, s83, v165\n\t"
  "v_mul_f32 v166, s83, v166\n\t"
  "v_mfma_f32_16x16x32_bf16 v[248:251], v[32:35], a[136:139], v[248:251]\n\t"
  "ds_read_b64_tr_b16 v[52:53], v13 offset:28672\n\t"
  "v_mul_f32 v167, s83, v167\n\t"
  "v_mfma_f32_16x16x32_bf16 v[248:251], v[36:39], a[152:155], v[248:251]\n\t"
  "ds_read_b64_tr_b16 v[54:55], v13 offset:29184\n\t"
  "v_permlane16_swap_b32 v160, v162\n\t"
  "v_mfma_f32_16x16x32_bf16 v[252:255], v[36:39], a[156:159], v[252:255]\n\t"
  "v_permlane16_swap_b32 v161, v163\n\t"
  "v_mul_f32 v180, s83, v180\n\t"
  "v_mfma_f32_16x16x32_bf16 v[252:255], v[32:35], a[140:143], v[252:255]\n\t"
  "v_mul_f32 v181, s83, v181\n\t"
  "s_waitcnt lgkmcnt(4)\n\t"
  "v_mfma_f32_16x16x32_bf16 v[192:195], v[40:43], a[160:163], v[192:195]\n\t"
  "ds_read_b64_tr_b16 v[32:33], v14 offset:24576\n\t"
  "v_mul_f32 v182, s83, v182\n\t"
  "v_mfma_f32_16x16x32_bf16 v[192:195], v[44:47], a[176:179], v[192:195]\n\t"
  "ds_read_b64_tr_b16 v[34:35], v14 offset:25088\n\t"
  "v_mul_f32 v183, s83, v183\n\t"
  "v_mfma_f32_16x16x32_bf16 v[196:199], v[44:47], a[180:183], v[196:199]\n\t"
  "v_cvt_pk_bf16_f32 v164, v164, v165\n\t"
  "v_cvt_pk_bf16_f32 v165, v166, v167\n\t"
  "v_mfma_f32_16x16x32_bf16 v[196:199], v[40:43], a[164:167], v[196:199]\n\t"
  "v_cvt_pk_bf16_f32 v166, v180, v181\n\t"
  "v_cvt_pk_bf16_f32 v167, v182, v183\n\t"
  "v_mfma_f32_16x16x32_bf16 v[200:203], v[40:43], a[168:171], v[200:203]\n\t"
  "ds_read_b64_tr_b16 v[36:37], v14 offset:28672\n\t"
  "v_mul_f32 v168, s83, v168\n\t"
  "v_mfma_f32_16x16x32_bf16 v[200:203], v[44:47], a[184:187], v[200:203]\n\t"
  "ds_read_b64_tr_b16 v[38:39], v14 offset:29184\n\t"
  "v_mul_f32 v169, s83, v169\n\t"
  "v_mfma_f32_16x16x32_bf16 v[204:207], v[44:47], a[188:191], v[204:207]\n\t"
  "v_mul_f32 v170, s83, v170\n\t"
  "v_mul_f32 v171, s83, v171\n\t"
  "v_mfma_f32_16x16x32_bf16 v[204:207], v[40:43], a[172:175], v[204:207]\n\t"
  "v_permlane16_swap_b32 v164, v166\n\t"
  "s_waitcnt lgkmcnt(4)\n\t"
  "v_mfma_f32_16x16x32_bf16 v[208:211], v[48:51], a[160:163], v[208:211]\n\t"
  "ds_read_b64_tr_b16 v[40:41], v15 offset:24576\n\t"
  "s_add_u32 m0, s69, 0\n\t"
  "v_mfma_f32_16x16x32_bf16 v[208:211], v[52:55], a[176:179], v[208:211]\n\t"
  "ds_read_b64_tr_b16 v[42:43], v15 offset:25088\n\t"
  "s_add_u32 s70, s74, 1573248\n\t"
  "v_mfma_f32_16x16x32_bf16 v[212:215], v[52:55], a[180:183], v[212:215]\n\t"
  "v_permlane16_swap_b32 v165, v167\n\t"
  "v_mul_f32 v184, s83, v184\n\t"
  "v_mfma_f32_16x16x32_bf16 v[212:215], v[48:51], a[164:167], v[212:215]\n\t"
  "s_waitcnt vmcnt(10)\n\t"
  "s_barrier\n\t"
  "v_mfma_f32_16x16x32_bf16 v[216:219], v[48:51], a[168:171], v[216:219]\n\t"
  "ds_read_b64_tr_b16 v[44:45], v15 offset:28672\n\t"
  "v_mul_f32 v185, s83, v185\n\t"
  "v_mfma_f32_16x16x32_bf16 v[216:219], v[52:55], a[184:187], v[216:219]\n\t"
  "ds_read_b64_tr_b16 v[46:47], v15 offset:29184\n\t"
  "v_mul_f32 v186, s83, v186\n\t"
  "v_mfma_f32_16x16x32_bf16 v[220:223], v[52:55], a[188:191], v[220:223]\n\t"
  "buffer_load_dwordx4 v4, s[44:47], s70 offen offset:0  lds\n\t"
  "v_mul_f32 v187, s83, v187\n\t"
  "v_mfma_f32_16x16x32_bf16 v[220:223], v[48:51], a[172:175], v[220:223]\n\t"
  "v_cvt_pk_bf16_f32 v168, v168, v169\n\t"
  "s_waitcnt lgkmcnt(4)\n\t"
  "v_mfma_f32_16x16x32_bf16 v[224:227], v[32:35], a[160:163], v[224:227]\n\t"
  "ds_read_b64_tr_b16 v[48:49], v12 offset:32768\n\t"
  "v_cvt_pk_bf16_f32 v169, v170, v171\n\t"
  "v_mfma_f32_16x16x32_bf16 v[224:227], v[36:39], a[176:179], v[224:227]\n\t"
  "ds_read_b64_tr_b16 v[50:51], v12 offset:33280\n\t"
  "v_cvt_pk_bf16_f32 v170, v184, v185\n\t"
  "v_mfma_f32_16x16x32_bf16 v[228:231], v[36:39], a[180:183], v[228:231]\n\t"
  "v_cvt_pk_bf16_f32 v171, v186, v187\n\t"
  "v_mul_f32 v172, s83, v172\n\t"
  "v_mfma_f32_16x16x32_bf16 v[228:231], v[32:35], a[164:167], v[228:231]\n\t"
  "buffer_load_dwordx4 v5, s[44:47], s70 offen offset:1024  lds\n\t"
  "v_mul_f32 v173, s83, v173\n\t"
  "v_mfma_f32_16x16x32_bf16 v[232:235], v[32:35], a[168:171], v[232:235]\n\t"
  "ds_read_b64_tr_b16 v[52:53], v12 offset:36864\n\t"
  "v_mul_f32 v174, s83, v174\n\t"
  "v_mfma_f32_16x16x32_bf16 v[232:235], v[36:39], a[184:187], v[232:235]\n\t"
  "ds_read_b64_tr_b16 v[54:55], v12 offset:37376\n\t"
  "v_mul_f32 v175, s83, v175\n\t"
  "v_mfma_f32_16x16x32_bf16 v[236:239], v[36:39], a[188:191], v[236:239]\n\t"
  "v_permlane16_swap_b32 v168, v170\n\t"
  "v_permlane16_swap_b32 v169, v171\n\t"
  "v_mfma_f32_16x16x32_bf16 v[236:239], v[32:35], a[172:175], v[236:239]\n\t"
  "v_mul_f32 v188, s83, v188\n\t"
  "s_waitcnt lgkmcnt(4)\n\t"
  "v_mfma_f32_16x16x32_bf16 v[240:243], v[40:43], a[160:163], v[240:243]\n\t"
  "ds_read_b64_tr_b16 v[32:33], v13 offset:32768\n\t"
  "v_mul_f32 v189, s83, v189\n\t"
  "v_mfma_f32_16x16x32_bf16 v[240:243], v[44:47], a[176:179], v[240:243]\n\t"
  "ds_read_b64_tr_b16 v[34:35], v13 offset:33280\n\t"
  "v_mul_f32 v190, s83, v190\n\t"
  "v_mfma_f32_16x16x32_bf16 v[244:247], v[44:47], a[180:183], v[244:247]\n\t"
  "buffer_load_dwordx4 v60, s[44:47], s70 offen offset:2048  lds\n\t"
  "v_mul_f32 v191, s83, v191\n\t"
  "v_mfma_f32_16x16x32_bf16 v[244:247], v[40:43], a[164:167], v[244:247]\n\t"
  "v_cvt_pk_bf16_f32 v172, v172, v173\n\t"
  "v_cvt_pk_bf16_f32 v173, v174, v175\n\t"
  "v_mfma_f32_16x16x32_bf16 v[248:251], v[40:43], a[168:171], v[248:251]\n\t"
  "ds_read_b64_tr_b16 v[36:37], v13 offset:36864\n\t"
  "v_cvt_pk_bf16_f32 v174, v188, v189\n\t"
  "v_mfma_f32_16x16x32_bf16 v[248:251], v[44:47], a[184:187], v[248:251]\n\t"
  "ds_read_b64_tr_b16 v[38:39], v13 offset:37376\n\t"
  "v_cvt_pk_bf16_f32 v175, v190, v191\n\t"
  "v_mfma_f32_16x16x32_bf16 v[252:255], v[44:47], a[188:191], v[252:255]\n\t"
  "v_permlane16_swap_b32 v172, v174\n\t"
  "v_permlane16_swap_b32 v173, v175\n\t"
  "v_mfma_f32_16x16x32_bf16 v[252:255], v[40:43], a[172:175], v[252:255]\n\t"
  "buffer_load_dwordx4 v61, s[44:47], s70 offen offset:3072  lds\n\t"
  "s_waitcnt lgkmcnt(4)\n\t"
  "v_mfma_f32_16x16x32_bf16 v[192:195], v[48:51], a[192:195], v[192:195]\n\t"
  "ds_read_b64_tr_b16 v[40:41], v14 offset:32768\n\t"
  "s_add_u32 m0, s69, 147456\n\t"
  "v_mfma_f32_16x16x32_bf16 v[192:195], v[52:55], a[208:211], v[192:195]\n\t"
  "ds_read_b64_tr_b16 v[42:43], v14 offset:33280\n\t"
  "s_mov_b32 s70, 49152\n\t"
  "v_mfma_f32_16x16x32_bf16 v[196:199], v[52:55], a[212:215], v[196:199]\n\t"
  "v_mfma_f32_16x16x32_bf16 v[196:199], v[48:51], a[196:199], v[196:199]\n\t"
  "v_mfma_f32_16x16x32_bf16 v[200:203], v[48:51], a[200:203], v[200:203]\n\t"
  "ds_read_b64_tr_b16 v[44:45], v14 offset:36864\n\t"
  "v_mfma_f32_16x16x32_bf16 v[200:203], v[52:55], a[216:219], v[200:203]\n\t"
  "ds_read_b64_tr_b16 v[46:47], v14 offset:37376\n\t"
  "v_mfma_f32_16x16x32_bf16 v[204:207], v[52:55], a[220:223], v[204:207]\n\t"
  "buffer_load_dwordx4 v9, s[48:51], s70 offen offset:3072 nt lds\n\t"
  "s_add_u32 m0, s69, 65536\n\t"
  "v_mfma_f32_16x16x32_bf16 v[204:207], v[48:51], a[204:207], v[204:207]\n\t"
  "s_mov_b32 s70, 65536\n\t"
  "s_waitcnt lgkmcnt(4)\n\t"
  "v_mfma_f32_16x16x32_bf16 v[208:211], v[32:35], a[192:195], v[208:211]\n\t"
  "ds_read_b64_tr_b16 v[48:49], v15 offset:32768\n\t"
  "v_mfma_f32_16x16x32_bf16 v[208:211], v[36:39], a[208:211], v[208:211]\n\t"
  "ds_read_b64_tr_b16 v[50:51], v15 offset:33280\n\t"
  "v_mfma_f32_16x16x32_bf16 v[212:215], v[36:39], a[212:215], v[212:215]\n\t"
  "v_mfma_f32_16x16x32_bf16 v[212:215], v[32:35], a[196:199], v[212:215]\n\t"
  "buffer_load_dwordx4 v6, s[48:51], s70 offen offset:0 nt lds\n\t"
  "v_mfma_f32_16x16x32_bf16 v[216:219], v[32:35], a[200:203], v[216:219]\n\t"
  "ds_read_b64_tr_b16 v[52:53], v15 offset:36864\n\t"
  "v_mfma_f32_16x16x32_bf16 v[216:219], v[36:39], a[216:219], v[216:219]\n\t"
  "ds_read_b64_tr_b16 v[54:55], v15 offset:37376\n\t"
  "v_mfma_f32_16x16x32_bf16 v[220:223], v[36:39], a[220:223], v[220:223]\n\t"
  "v_mfma_f32_16x16x32_bf16 v[220:223], v[32:35], a[204:207], v[220:223]\n\t"
  "s_waitcnt lgkmcnt(4)\n\t"
  "v_mfma_f32_16x16x32_bf16 v[224:227], v[40:43], a[192:195], v[224:227]\n\t"
  "ds_read_b64_tr_b16 v[32:33], v12 offset:40960\n\t"
  "v_mfma_f32_16x16x32_bf16 v[224:227], v[44:47], a[208:211], v[224:227]\n\t"
  "ds_read_b64_tr_b16 v[34:35], v12 offset:41472\n\t"
  "v_mfma_f32_16x16x32_bf16 v[228:231], v[44:47], a[212:215], v[228:231]\n\t"
  "buffer_load_dwordx4 v7, s[48:51], s70 offen offset:1024 nt lds\n\t"
  "v_mfma_f32_16x16x32_bf16 v[228:231], v[40:43], a[196:199], v[228:231]\n\t"
  "v_mfma_f32_16x16x32_bf16 v[232:235], v[40:43], a[200:203], v[232:235]\n\t"
  "ds_read_b64_tr_b16 v[36:37], v12 offset:45056\n\t"
  "v_mfma_f32_16x16x32_bf16 v[232:235], v[44:47], a[216:219], v[232:235]\n\t"
  "ds_read_b64_tr_b16 v[38:39], v12 offset:45568\n\t"
  "v_mfma_f32_16x16x32_bf16 v[236:239], v[44:47], a[220:223], v[236:239]\n\t"
  "v_mfma_f32_16x16x32_bf16 v[236:239], v[40:43], a[204:207], v[236:239]\n\t"
  "s_waitcnt lgkmcnt(4)\n\t"
  "v_mfma_f32_16x16x32_bf16 v[240:243], v[48:51], a[192:195], v[240:243]\n\t"
  "ds_read_b64_tr_b16 v[40:41], v13 offset:40960\n\t"
  "v_mfma_f32_16x16x32_bf16 v[240:243], v[52:55], a[208:211], v[240:243]\n\t"
  "ds_read_b64_tr_b16 v[42:43], v13 offset:41472\n\t"
  "v_mfma_f32_16x16x32_bf16 v[244:247], v[52:55], a[212:215], v[244:247]\n\t"
  "v_mfma_f32_16x16x32_bf16 v[244:247], v[48:51], a[196:199], v[244:247]\n\t"
  "v_mfma_f32_16x16x32_bf16 v[248:251], v[48:51], a[200:203], v[248:251]\n\t"
  "ds_read_b64_tr_b16 v[44:45], v13 offset:45056\n\t"
  "v_mfma_f32_16x16x32_bf16 v[248:251], v[52:55], a[216:219], v[248:251]\n\t"
  "ds_read_b64_tr_b16 v[46:47], v13 offset:45568\n\t"
  "v_mfma_f32_16x16x32_bf16 v[252:255], v[52:55], a[220:223], v[252:255]\n\t"
  "v_mfma_f32_16x16x32_bf16 v[252:255], v[48:51], a[204:207], v[252:255]\n\t"
  "s_waitcnt lgkmcnt(4)\n\t"
  "v_mfma_f32_16x16x32_bf16 v[80:83], v[32:35], a[0:3], 0\n\t"
  "ds_read_b64_tr_b16 v[48:49], v14 offset:40960\n\t"
  "v_mfma_f32_16x16x32_bf16 v[80:83], v[36:39], a[16:19], v[80:83]\n\t"
  "ds_read_b64_tr_b16 v[50:51], v14 offset:41472\n\t"
  "v_mfma_f32_16x16x32_bf16 v[84:87], v[36:39], a[20:23], 0\n\t"
  "v_mfma_f32_16x16x32_bf16 v[84:87], v[32:35], a[4:7], v[84:87]\n\t"
  "v_mul_f32 v192, s83, v192\n\t"
  "v_mul_f32 v193, s83, v193\n\t"
  "v_mfma_f32_16x16x32_bf16 v[88:91], v[32:35], a[8:11], 0\n\t"
  "ds_read_b64_tr_b16 v[52:53], v14 offset:45056\n\t"
  "v_mul_f32 v194, s83, v194\n\t"
  "v_mfma_f32_16x16x32_bf16 v[88:91], v[36:39], a[24:27], v[88:91]\n\t"
  "ds_read_b64_tr_b16 v[54:55], v14 offset:45568\n\t"
  "v_mul_f32 v195, s83, v195\n\t"
  "v_mfma_f32_16x16x32_bf16 v[92:95], v[36:39], a[28:31], 0\n\t"
  "v_mul_f32 v208, s83, v208\n\t"
  "v_mul_f32 v209, s83, v209\n\t"
  "v_mfma_f32_16x16x32_bf16 v[92:95], v[32:35], a[12:15], v[92:95]\n\t"
  "v_mul_f32 v210, s83, v210\n\t"
  "s_waitcnt lgkmcnt(4)\n\t"
  "v_mfma_f32_16x16x32_bf16 v[112:115], v[40:43], a[0:3], 0\n\t"
  "ds_read_b64_tr_b16 v[32:33], v15 offset:40960\n\t"
  "s_add_u32 m0, s69, 16384\n\t"
  "v_mfma_f32_16x16x32_bf16 v[112:115], v[44:47], a[16:19], v[112:115]\n\t"
  "ds_read_b64_tr_b16 v[34:35], v15 offset:41472\n\t"
  "s_add_u32 s70, s74, 2621824\n\t"
  "v_mfma_f32_16x16x32_bf16 v[116:119], v[44:47], a[20:23], 0\n\t"
  "v_mul_f32 v211, s83, v211\n\t"
  "v_cvt_pk_bf16_f32 v192, v192, v193\n\t"
  "v_mfma_f32_16x16x32_bf16 v[116:119], v[40:43], a[4:7], v[116:119]\n\t"
  "s_waitcnt vmcnt(10)\n\t"
  "s_barrier\n\t"
  "v_mfma_f32_16x16x32_bf16 v[120:123], v[40:43], a[8:11], 0\n\t"
  "ds_read_b64_tr_b16 v[36:37], v15 offset:45056\n\t"
  "v_cvt_pk_bf16_f32 v193, v194, v195\n\t"
  "v_mfma_f32_16x16x32_bf16 v[120:123], v[44:47], a[24:27], v[120:123]\n\t"
  "ds_read_b64_tr_b16 v[38:39], v15 offset:45568\n\t"
  "v_cvt_pk_bf16_f32 v194, v208, v209\n\t"
  "v_mfma_f32_16x16x32_bf16 v[124:127], v[44:47], a[28:31], 0\n\t"
  "buffer_load_dwordx4 v4, s[44:47], s70 offen offset:0  lds\n\t"
  "v_cvt_pk_bf16_f32 v195, v210, v211\n\t"
  "v_mfma_f32_16x16x32_bf16 v[124:127], v[40:43], a[12:15], v[124:127]\n\t"
  "v_mul_f32 v196, s83, v196\n\t"
  "s_waitcnt lgkmcnt(4)\n\t"
  "v_mfma_f32_16x16x32_bf16 v[144:147], v[48:51], a[0:3], 0\n\t"
  "ds_read_b64_tr_b16 v[40:41], v12 offset:49152\n\t"
  "v_mul_f32 v197, s83, v197\n\t"
  "v_mfma_f32_16x16x32_bf16 v[144:147], v[52:55], a[16:19], v[144:147]\n\t"
  "ds_read_b64_tr_b16 v[42:43], v12 offset:49664\n\t"
  "v_mul_f32 v198, s83, v198\n\t"
  "v_mfma_f32_16x16x32_bf16 v[148:151], v[52:55], a[20:23], 0\n\t"
  "v_mul_f32 v199, s83, v199\n\t"
  "v_permlane16_swap_b32 v192, v194\n\t"
  "v_mfma_f32_16x16x32_bf16 v[148:151], v[48:51], a[4:7], v[148:151]\n\t"
  "buffer_load_dwordx4 v5, s[44:47], s70 offen offset:1024  lds\n\t"
  "v_permlane16_swap_b32 v193, v195\n\t"
  "v_mfma_f32_16x16x32_bf16 v[152:155], v[48:51], a[8:11], 0\n\t"
  "ds_read_b64_tr_b16 v[44:45], v12 offset:53248\n\t"
  "v_mul_f32 v212, s83, v212\n\t"
  "v_mfma_f32_16x16x32_bf16 v[152:155], v[52:55], a[24:27], v[152:155]\n\t"
  "ds_read_b64_tr_b16 v[46:47], v12 offset:53760\n\t"
  "v_mul_f32 v213, s83, v213\n\t"
  "v_mfma_f32_16x16x32_bf16 v[156:159], v[52:55], a[28:31], 0\n\t"
  "v_mul_f32 v214, s83, v214\n\t"
  "v_mul_f32 v215, s83, v215\n\t"
  "v_mfma_f32_16x16x32_bf16 v[156:159], v[48:51], a[12:15], v[156:159]\n\t"
  "v_cvt_pk_bf16_f32 v196, v196, v197\n\t"
  "s_waitcnt lgkmcnt(4)\n\t"
  "v_mfma_f32_16x16x32_bf16 v[176:179], v[32:35], a[0:3], 0\n\t"
  "ds_read_b64_tr_b16 v[48:49], v13 offset:49152\n\t"
  "v_cvt_pk_bf16_f32 v197, v198, v199\n\t"
  "v_mfma_f32_16x16x32_bf16 v[176:179], v[36:39], a[16:19], v[176:179]\n\t"
  "ds_read_b64_tr_b16 v[50:51], v13 offset:49664\n\t"
  "v_cvt_pk_bf16_f32 v198, v212, v213\n\t"
  "v_mfma_f32_16x16x32_bf16 v[180:183], v[36:39], a[20:23], 0\n\t"
  "buffer_load_dwordx4 v60, s[44:47], s70 offen offset:2048  lds\n\t"
  "v_cvt_pk_bf16_f32 v199, v214, v215\n\t"
  "v_mfma_f32_16x16x32_bf16 v[180:183], v[32:35], a[4:7], v[180:183]\n\t"
  "v_mul_f32 v200, s83, v200\n\t"
  "v_mul_f32 v201, s83, v201\n\t"
  "v_mfma_f32_16x16x32_bf16 v[184:187], v[32:35], a[8:11], 0\n\t"
  "ds_read_b64_tr_b16 v[52:53], v13 offset:53248\n\t"
  "v_mul_f32 v202, s83, v202\n\t"
  "v_mfma_f32_16x16x32_bf16 v[184:187], v[36:39], a[24:27], v[184:187]\n\t"
  "ds_read_b64_tr_b16 v[54:55], v13 offset:53760\n\t"
  "v_mul_f32 v203, s83, v203\n\t"
  "v_mfma_f32_16x16x32_bf16 v[188:191], v[36:39], a[28:31], 0\n\t"
  "v_permlane16_swap_b32 v196, v198\n\t"
  "v_permlane16_swap_b32 v197, v199\n\t"
  "v_mfma_f32_16x16x32_bf16 v[188:191], v[32:35], a[12:15], v[188:191]\n\t"
  "buffer_load_dwordx4 v61, s[44:47], s70 offen offset:3072  lds\n\t"
  "s_waitcnt lgkmcnt(4)\n\t"
  "v_mfma_f32_16x16x32_bf16 v[80:83], v[40:43], a[32:35], v[80:83]\n\t"
  "ds_read_b64_tr_b16 v[32:33], v14 offset:49152\n\t"
  "s_add_u32 m0, s69, 65536\n\t"
  "v_mfma_f32_16x16x32_bf16 v[80:83], v[44:47], a[48:51], v[80:83]\n\t"
  "ds_read_b64_tr_b16 v[34:35], v14 offset:49664\n\t"
  "s_mov_b32 s70, 65536\n\t"
  "v_mfma_f32_16x16x32_bf16 v[84:87], v[44:47], a[52:55], v[84:87]\n\t"
  "v_mul_f32 v216, s83, v216\n\t"
  "v_mul_f32 v217, s83, v217\n\t"
  "v_mfma_f32_16x16x32_bf16 v[84:87], v[40:43], a[36:39], v[84:87]\n\t"
  "v_mul_f32 v218, s83, v218\n\t"
  "v_mul_f32 v219, s83, v219\n\t"
  "v_mfma_f32_16x16x32_bf16 v[88:91], v[40:43], a[40:43], v[88:91]\n\t"
  "ds_read_b64_tr_b16 v[36:37], v14 offset:53248\n\t"
  "v_cvt_pk_bf16_f32 v200, v200, v201\n\t"
  "v_mfma_f32_16x16x32_bf16 v[88:91], v[44:47], a[56:59], v[88:91]\n\t"
  "ds_read_b64_tr_b16 v[38:39], v14 offset:53760\n\t"
  "v_cvt_pk_bf16_f32 v201, v202, v203\n\t"
  "v_mfma_f32_16x16x32_bf16 v[92:95], v[44:47], a[60:63], v[92:95]\n\t"
  "buffer_load_dwordx4 v8, s[48:51], s70 offen offset:2048 nt lds\n\t"
  "v_cvt_pk_bf16_f32 v202, v216, v217\n\t"
  "v_mfma_f32_16x16x32_bf16 v[92:95], v[40:43], a[44:47], v[92:95]\n\t"
  "v_cvt_pk_bf16_f32 v203, v218, v219\n\t"
  "s_waitcnt lgkmcnt(4)\n\t"
  "v_mfma_f32_16x16x32_bf16 v[112:115], v[48:51], a[32:35], v[112:115]\n\t"
  "ds_read_b64_tr_b16 v[40:41], v15 offset:49152\n\t"
  "v_mul_f32 v204, s83, v204\n\t"
  "v_mfma_f32_16x16x32_bf16 v[112:115], v[52:55], a[48:51], v[112:115]\n\t"
  "ds_read_b64_tr_b16 v[42:43], v15 offset:49664\n\t"
  "v_mul_f32 v205, s83, v205\n\t"
  "v_mfma_f32_16x16x32_bf16 v[116:119], v[52:55], a[52:55], v[116:119]\n\t"
  "v_mul_f32 v206, s83, v206\n\t"
  "v_mul_f32 v207, s83, v207\n\t"
  "v_mfma_f32_16x16x32_bf16 v[116:119], v[48:51], a[36:39], v[116:119]\n\t"
  "buffer_load_dwordx4 v9, s[48:51], s70 offen offset:3072 nt lds\n\t"
  "v_permlane16_swap_b32 v200, v202\n\t"
  "v_mfma_f32_16x16x32_bf16 v[120:123], v[48:51], a[40:43], v[120:123]\n\t"
  "ds_read_b64_tr_b16 v[44:45], v15 offset:53248\n\t"
  "s_add_u32 m0, s69, 81920\n\t"
  "v_mfma_f32_16x16x32_bf16 v[120:123], v[52:55], a[56:59], v[120:123]\n\t"
  "ds_read_b64_tr_b16 v[46:47], v15 offset:53760\n\t"
  "s_mov_b32 s70, 81920\n\t"
  "v_mfma_f32_16x16x32_bf16 v[124:127], v[52:55], a[60:63], v[124:127]\n\t"
  "v_permlane16_swap_b32 v201, v203\n\t"
  "v_mul_f32 v220, s83, v220\n\t"
  "v_mfma_f32_16x16x32_bf16 v[124:127], v[48:51], a[44:47], v[124:127]\n\t"
  "v_mul_f32 v221, s83, v221\n\t"
  "s_waitcnt lgkmcnt(4)\n\t"
  "v_mfma_f32_16x16x32_bf16 v[144:147], v[32:35], a[32:35], v[144:147]\n\t"
  "ds_read_b64_tr_b16 v[48:49], v12 offset:57344\n\t"
  "v_mul_f32 v222, s83, v222\n\t"
  "v_mfma_f32_16x16x32_bf16 v[144:147], v[36:39], a[48:51], v[144:147]\n\t"
  "ds_read_b64_tr_b16 v[50:51], v12 offset:57856\n\t"
  "v_mul_f32 v223, s83, v223\n\t"
  "v_mfma_f32_16x16x32_bf16 v[148:151], v[36:39], a[52:55], v[148:151]\n\t"
  "buffer_load_dwordx4 v6, s[48:51], s70 offen offset:0 nt lds\n\t"
  "v_cvt_pk_bf16_f32 v204, v204, v205\n\t"
  "v_mfma_f32_16x16x32_bf16 v[148:151], v[32:35], a[36:39], v[148:151]\n\t"
  "v_cvt_pk_bf16_f32 v205, v206, v207\n\t"
  "v_cvt_pk_bf16_f32 v206, v220, v221\n\t"
  "v_mfma_f32_16x16x32_bf16 v[152:155], v[32:35], a[40:43], v[152:155]\n\t"
  "ds_read_b64_tr_b16 v[52:53], v12 offset:61440\n\t"
  "v_cvt_pk_bf16_f32 v207, v222, v223\n\t"
  "v_mfma_f32_16x16x32_bf16 v[152:155], v[36:39], a[56:59], v[152:155]\n\t"
  "ds_read_b64_tr_b16 v[54:55], v12 offset:61952\n\t"
  "v_mul_f32 v224, s83, v224\n\t"
  "v_mfma_f32_16x16x32_bf16 v[156:159], v[36:39], a[60:63], v[156:159]\n\t"
  "v_mul_f32 v225, s83, v225\n\t"
  "v_mul_f32 v226, s83, v226\n\t"
  "v_mfma_f32_16x16x32_bf16 v[156:159], v[32:35], a[44:47], v[156:159]\n\t"
  "v_mul_f32 v227, s83, v227\n\t"
  "s_waitcnt lgkmcnt(4)\n\t"
  "v_mfma_f32_16x16x32_bf16 v[176:179], v[40:43], a[32:35], v[176:179]\n\t"
  "ds_read_b64_tr_b16 v[32:33], v13 offset:57344\n\t"
  "v_permlane16_swap_b32 v204, v206\n\t"
  "v_mfma_f32_16x16x32_bf16 v[176:179], v[44:47], a[48:51], v[176:179]\n\t"
  "ds_read_b64_tr_b16 v[34:35], v13 offset:57856\n\t"
  "v_permlane16_swap_b32 v205, v207\n\t"
  "v_mfma_f32_16x16x32_bf16 v[180:183], v[44:47], a[52:55], v[180:183]\n\t"
  "v_mul_f32 v240, s83, v240\n\t"
  "v_mul_f32 v241, s83, v241\n\t"
  "v_mfma_f32_16x16x32_bf16 v[180:183], v[40:43], a[36:39], v[180:183]\n\t"
  "v_mul_f32 v242, s83, v242\n\t"
  "v_mul_f32 v243, s83, v243\n\t"
  "v_mfma_f32_16x16x32_bf16 v[184:187], v[40:43], a[40:43], v[184:187]\n\t"
  "ds_read_b64_tr_b16 v[36:37], v13 offset:61440\n\t"
  "v_cvt_pk_bf16_f32 v224, v224, v225\n\t"
  "v_mfma_f32_16x16x32_bf16 v[184:187], v[44:47], a[56:59], v[184:187]\n\t"
  "ds_read_b64_tr_b16 v[38:39], v13 offset:61952\n\t"
  "v_cvt_pk_bf16_f32 v225, v226, v227\n\t"
  "v_mfma_f32_16x16x32_bf16 v[188:191], v[44:47], a[60:63], v[188:191]\n\t"
  "v_cvt_pk_bf16_f32 v226, v240, v241\n\t"
  "v_cvt_pk_bf16_f32 v227, v242, v243\n\t"
  "v_mfma_f32_16x16x32_bf16 v[188:191], v[40:43], a[44:47], v[188:191]\n\t"
  "v_mul_f32 v228, s83, v228\n\t"
  "s_waitcnt lgkmcnt(4)\n\t"
  "v_mfma_f32_16x16x32_bf16 v[80:83], v[48:51], a[64:67], v[80:83]\n\t"
  "ds_read_b64_tr_b16 v[40:41], v14 offset:57344\n\t"
  "v_mul_f32 v229, s83, v229\n\t"
  "v_mfma_f32_16x16x32_bf16 v[80:83], v[52:55], a[80:83], v[80:83]\n\t"
  "ds_read_b64_tr_b16 v[42:43], v14 offset:57856\n\t"
  "v_mul_f32 v230, s83, v230\n\t"
  "v_mfma_f32_16x16x32_bf16 v[84:87], v[52:55], a[84:87], v[84:87]\n\t"
  "v_mul_f32 v231, s83, v231\n\t"
  "v_permlane16_swap_b32 v224, v226\n\t"
  "v_mfma_f32_16x16x32_bf16 v[84:87], v[48:51], a[68:71], v[84:87]\n\t"
  "v_permlane16_swap_b32 v225, v227\n\t"
  "v_mul_f32 v244, s83, v244\n\t"
  "v_mfma_f32_16x16x32_bf16 v[88:91], v[48:51], a[72:75], v[88:91]\n\t"
  "ds_read_b64_tr_b16 v[44:45], v14 offset:61440\n\t"
  "v_mul_f32 v245, s83, v245\n\t"
  "v_mfma_f32_16x16x32_bf16 v[88:91], v[52:55], a[88:91], v[88:91]\n\t"
  "ds_read_b64_tr_b16 v[46:47], v14 offset:61952\n\t"
  "v_mul_f32 v246, s83, v246\n\t"
  "v_mfma_f32_16x16x32_bf16 v[92:95], v[52:55], a[92:95], v[92:95]\n\t"
  "v_mul_f32 v247, s83, v247\n\t"
  "v_cvt_pk_bf16_f32 v228, v228, v229\n\t"
  "v_mfma_f32_16x16x32_bf16 v[92:95], v[48:51], a[76:79], v[92:95]\n\t"
  "v_cvt_pk_bf16_f32 v229, v230, v231\n\t"
  "s_waitcnt lgkmcnt(4)\n\t"
  "v_mfma_f32_16x16x32_bf16 v[112:115], v[32:35], a[64:67], v[112:115]\n\t"
  "ds_read_b64_tr_b16 v[48:49], v15 offset:57344\n\t"
  "s_add_u32 m0, s69, 32768\n\t"
  "v_mfma_f32_16x16x32_bf16 v[112:115], v[36:39], a[80:83], v[112:115]\n\t"
  "ds_read_b64_tr_b16 v[50:51], v15 offset:57856\n\t"
  "s_mov_b32 s70, 98304\n\t"
  "v_mfma_f32_16x16x32_bf16 v[116:119], v[36:39], a[84:87], v[116:119]\n\t"
  "v_cvt_pk_bf16_f32 v230, v244, v245\n\t"
  "v_cvt_pk_bf16_f32 v231, v246, v247\n\t"
  "v_mfma_f32_16x16x32_bf16 v[116:119], v[32:35], a[68:71], v[116:119]\n\t"
  "s_waitcnt vmcnt(10)\n\t"
  "s_barrier\n\t"
  "v_mfma_f32_16x16x32_bf16 v[120:123], v[32:35], a[72:75], v[120:123]\n\t"
  "ds_read_b64_tr_b16 v[52:53], v15 offset:61440\n\t"
  "v_mul_f32 v232, s83, v232\n\t"
  "v_mfma_f32_16x16x32_bf16 v[120:123], v[36:39], a[88:91], v[120:123]\n\t"
  "ds_read_b64_tr_b16 v[54:55], v15 offset:61952\n\t"
  "v_mul_f32 v233, s83, v233\n\t"
  "v_mfma_f32_16x16x32_bf16 v[124:127], v[36:39], a[92:95], v[124:127]\n\t"
  "buffer_load_dwordx4 v6, s[48:51], s70 offen offset:0 nt lds\n\t"
  "v_mul_f32 v234, s83, v234\n\t"
  "v_mfma_f32_16x16x32_bf16 v[124:127], v[32:35], a[76:79], v[124:127]\n\t"
  "v_mul_f32 v235, s83, v235\n\t"
  "s_waitcnt lgkmcnt(4)\n\t"
  "v_mfma_f32_16x16x32_bf16 v[144:147], v[40:43], a[64:67], v[144:147]\n\t"
  "ds_read_b64_tr_b16 v[32:33], v12 offset:0\n\t"
  "v_permlane16_swap_b32 v228, v230\n\t"
  "v_mfma_f32_16x16x32_bf16 v[144:147], v[44:47], a[80:83], v[144:147]\n\t"
  "ds_read_b64_tr_b16 v[34:35], v12 offset:512\n\t"
  "v_permlane16_swap_b32 v229, v231\n\t"
  "v_mfma_f32_16x16x32_bf16 v[148:151], v[44:47], a[84:87], v[148:151]\n\t"
  "v_mul_f32 v248, s83, v248\n\t"
  "v_mul_f32 v249, s83, v249\n\t"
  "v_mfma_f32_16x16x32_bf16 v[148:151], v[40:43], a[68:71], v[148:151]\n\t"
  "buffer_load_dwordx4 v7, s[48:51], s70 offen offset:1024 nt lds\n\t"
  "v_mul_f32 v250, s83, v250\n\t"
  "v_mfma_f32_16x16x32_bf16 v[152:155], v[40:43], a[72:75], v[152:155]\n\t"
  "ds_read_b64_tr_b16 v[36:37], v12 offset:4096\n\t"
  "v_mul_f32 v251, s83, v251\n\t"
  "v_mfma_f32_16x16x32_bf16 v[152:155], v[44:47], a[88:91], v[152:155]\n\t"
  "ds_read_b64_tr_b16 v[38:39], v12 offset:4608\n\t"
  "v_cvt_pk_bf16_f32 v232, v232, v233\n\t"
  "v_mfma_f32_16x16x32_bf16 v[156:159], v[44:47], a[92:95], v[156:159]\n\t"
  "v_cvt_pk_bf16_f32 v233, v234, v235\n\t"
  "v_cvt_pk_bf16_f32 v234, v248, v249\n\t"
  "v_mfma_f32_16x16x32_bf16 v[156:159], v[40:43], a[76:79], v[156:159]\n\t"
  "v_cvt_pk_bf16_f32 v235, v250, v251\n\t"
  "s_waitcnt lgkmcnt(4)\n\t"
  "v_mfma_f32_16x16x32_bf16 v[176:179], v[48:51], a[64:67], v[176:179]\n\t"
  "ds_read_b64_tr_b16 v[40:41], v13 offset:0\n\t"
  "v_mul_f32 v236, s83, v236\n\t"
  "v_mfma_f32_16x16x32_bf16 v[176:179], v[52:55], a[80:83], v[176:179]\n\t"
  "ds_read_b64_tr_b16 v[42:43], v13 offset:512\n\t"
  "v_mul_f32 v237, s83, v237\n\t"
  "v_mfma_f32_16x16x32_bf16 v[180:183], v[52:55], a[84:87], v[180:183]\n\t"
  "buffer_load_dwordx4 v8, s[48:51], s70 offen offset:2048 nt lds\n\t"
  "v_mul_f32 v238, s83, v238\n\t"
  "v_mfma_f32_16x16x32_bf16 v[180:183], v[48:51], a[68:71], v[180:183]\n\t"
  "v_mul_f32 v239, s83, v239\n\t"
  "v_permlane16_swap_b32 v232, v234\n\t"
  "v_mfma_f32_16x16x32_bf16 v[184:187], v[48:51], a[72:75], v[184:187]\n\t"
  "ds_read_b64_tr_b16 v[44:45], v13 offset:4096\n\t"
  "v_permlane16_swap_b32 v233, v235\n\t"
  "v_mfma_f32_16x16x32_bf16 v[184:187], v[52:55], a[88:91], v[184:187]\n\t"
  "ds_read_b64_tr_b16 v[46:47], v13 offset:4608\n\t"
  "v_mul_f32 v252, s83, v252\n\t"
  "v_mfma_f32_16x16x32_bf16 v[188:191], v[52:55], a[92:95], v[188:191]\n\t"
  "v_mul_f32 v253, s83, v253\n\t"
  "v_mul_f32 v254, s83, v254\n\t"
  "v_mfma_f32_16x16x32_bf16 v[188:191], v[48:51], a[76:79], v[188:191]\n\t"
  "buffer_load_dwordx4 v9, s[48:51], s70 offen offset:3072 nt lds\n\t"
  "s_waitcnt lgkmcnt(4)\n\t"
  "v_mfma_f32_16x16x32_bf16 v[80:83], v[32:35], a[96:99], v[80:83]\n\t"
  "ds_read_b64_tr_b16 v[48:49], v14 offset:0\n\t"
  "s_add_u32 m0, s69, 81920\n\t"
  "v_mfma_f32_16x16x32_bf16 v[80:83], v[36:39], a[112:115], v[80:83]\n\t"
  "ds_read_b64_tr_b16 v[50:51], v14 offset:512\n\t"
  "s_mov_b32 s70, 81920\n\t"
  "v_mfma_f32_16x16x32_bf16 v[84:87], v[36:39], a[116:119], v[84:87]\n\t"
  "v_mul_f32 v255, s83, v255\n\t"
  "v_cvt_pk_bf16_f32 v236, v236, v237\n\t"
  "v_mfma_f32_16x16x32_bf16 v[84:87], v[32:35], a[100:103], v[84:87]\n\t"
  "v_cvt_pk_bf16_f32 v237, v238, v239\n\t"
  "v_cvt_pk_bf16_f32 v238, v252, v253\n\t"
  "v_mfma_f32_16x16x32_bf16 v[88:91], v[32:35], a[104:107], v[88:91]\n\t"
  "ds_read_b64_tr_b16 v[52:53], v14 offset:4096\n\t"
  "v_cvt_pk_bf16_f32 v239, v254, v255\n\t"
  "v_mfma_f32_16x16x32_bf16 v[88:91], v[36:39], a[120:123], v[88:91]\n\t"
  "ds_read_b64_tr_b16 v[54:55], v14 offset:4608\n\t"
  "v_permlane16_swap_b32 v236, v238\n\t"
  "v_mfma_f32_16x16x32_bf16 v[92:95], v[36:39], a[124:127], v[92:95]\n\t"
  "buffer_load_dwordx4 v7, s[48:51], s70 offen offset:1024 nt lds\n\t"
  "v_permlane16_swap_b32 v237, v239\n\t"
  "v_mfma_f32_16x16x32_bf16 v[92:95], v[32:35], a[108:111], v[92:95]\n\t"
  "v_add_u32 v4, 0, v30\n\t"
  "s_waitcnt lgkmcnt(4)\n\t"
  "v_mfma_f32_16x16x32_bf16 v[112:115], v[40:43], a[96:99], v[112:115]\n\t"
  "ds_read_b64_tr_b16 v[32:33], v15 offset:0\n\t"
  "v_add_u32 v5, 3072, v30\n\t"
  "v_mfma_f32_16x16x32_bf16 v[112:115], v[44:47], a[112:115], v[112:115]\n\t"
  "ds_read_b64_tr_b16 v[34:35], v15 offset:512\n\t"
  "v_add_u32 v60, 6144, v30\n\t"
  "v_mfma_f32_16x16x32_bf16 v[116:119], v[44:47], a[116:119], v[116:119]\n\t"
  "v_add_u32 v61, 9216, v30\n\t"
  "v_mfma_f32_16x16x32_bf16 v[116:119], v[40:43], a[100:103], v[116:119]\n\t"
  "buffer_load_dwordx4 v8, s[48:51], s70 offen offset:2048 nt lds\n\t"
  "v_mfma_f32_16x16x32_bf16 v[120:123], v[40:43], a[104:107], v[120:123]\n\t"
  "ds_read_b64_tr_b16 v[36:37], v15 offset:4096\n\t"
  "v_mfma_f32_16x16x32_bf16 v[120:123], v[44:47], a[120:123], v[120:123]\n\t"
  "ds_read_b64_tr_b16 v[38:39], v15 offset:4608\n\t"
  "v_mfma_f32_16x16x32_bf16 v[124:127], v[44:47], a[124:127], v[124:127]\n\t"
  "s_cmp_eq_u32 s64, 43008\n\ts_cselect_b32 s64, s68, 0\n\t"
  "v_mfma_f32_16x16x32_bf16 v[124:127], v[40:43], a[108:111], v[124:127]\n\t"
  "s_waitcnt lgkmcnt(4)\n\t"
  "v_mfma_f32_16x16x32_bf16 v[144:147], v[48:51], a[96:99], v[144:147]\n\t"
  "ds_read_b64_tr_b16 v[40:41], v12 offset:8192\n\t"
  "v_mfma_f32_16x16x32_bf16 v[144:147], v[52:55], a[112:115], v[144:147]\n\t"
  "ds_read_b64_tr_b16 v[42:43], v12 offset:8704\n\t"
  "v_mfma_f32_16x16x32_bf16 v[148:151], v[52:55], a[116:119], v[148:151]\n\t"
  "buffer_load_dwordx4 v9, s[48:51], s70 offen offset:3072 nt lds\n\t"
  "v_mfma_f32_16x16x32_bf16 v[148:151], v[48:51], a[100:103], v[148:151]\n\t"
  "s_cmp_eq_u32 s65, 43008\n\ts_cselect_b32 s65, s68, 0\n\t"
  "v_mfma_f32_16x16x32_bf16 v[152:155], v[48:51], a[104:107], v[152:155]\n\t"
  "ds_read_b64_tr_b16 v[44:45], v12 offset:12288\n\t"
  "v_mfma_f32_16x16x32_bf16 v[152:155], v[52:55], a[120:123], v[152:155]\n\t"
  "ds_read_b64_tr_b16 v[46:47], v12 offset:12800\n\t"
  "v_mfma_f32_16x16x32_bf16 v[156:159], v[52:55], a[124:127], v[156:159]\n\t"
  "s_cmp_eq_u32 s66, 43008\n\ts_cselect_b32 s66, s68, 0\n\t"
  "v_mfma_f32_16x16x32_bf16 v[156:159], v[48:51], a[108:111], v[156:159]\n\t"
  "s_waitcnt lgkmcnt(4)\n\t"
  "v_mfma_f32_16x16x32_bf16 v[176:179], v[32:35], a[96:99], v[176:179]\n\t"
  "ds_read_b64_tr_b16 v[48:49], v13 offset:8192\n\t"
  "v_mfma_f32_16x16x32_bf16 v[176:179], v[36:39], a[112:115], v[176:179]\n\t"
  "ds_read_b64_tr_b16 v[50:51], v13 offset:8704\n\t"
  "v_mfma_f32_16x16x32_bf16 v[180:183], v[36:39], a[116:119], v[180:183]\n\t"
  "s_cmp_eq_u32 s67, 43008\n\ts_cselect_b32 s67, s68, 0\n\t"
  "v_mfma_f32_16x16x32_bf16 v[180:183], v[32:35], a[100:103], v[180:183]\n\t"
  "v_mov_b32 v56, 0x3f803f80\n\t"
  "v_mov_b32 v57, 0x3f803f80\n\t"
  "v_mfma_f32_16x16x32_bf16 v[184:187], v[32:35], a[104:107], v[184:187]\n\t"
  "ds_read_b64_tr_b16 v[52:53], v13 offset:12288\n\t"
  "v_mov_b32 v58, 0x3f803f80\n\t"
  "v_mfma_f32_16x16x32_bf16 v[184:187], v[36:39], a[120:123], v[184:187]\n\t"
  "ds_read_b64_tr_b16 v[54:55], v13 offset:12800\n\t"
  "v_mov_b32 v59, 0x3f803f80\n\t"
  "v_mfma_f32_16x16x32_bf16 v[188:191], v[36:39], a[124:127], v[188:191]\n\t"
  "v_mfma_f32_16x16x32_bf16 v[188:191], v[32:35], a[108:111], v[188:191]\n\t"
  "s_waitcnt lgkmcnt(4)\n\t"
  "v_mfma_f32_16x16x32_bf16 v[80:83], v[40:43], a[128:131], v[80:83]\n\t"
  "ds_read_b64_tr_b16 v[32:33], v14 offset:8192\n\t"
  "v_mfma_f32_16x16x32_bf16 v[80:83], v[44:47], a[144:147], v[80:83]\n\t"
  "ds_read_b64_tr_b16 v[34:35], v14 offset:8704\n\t"
  "v_mfma_f32_16x16x32_bf16 v[84:87], v[44:47], a[148:151], v[84:87]\n\t"
  "v_mfma_f32_16x16x32_bf16 v[84:87], v[40:43], a[132:135], v[84:87]\n\t"
  "v_mfma_f32_16x16x32_bf16 v[88:91], v[40:43], a[136:139], v[88:91]\n\t"
  "ds_read_b64_tr_b16 v[36:37], v14 offset:12288\n\t"
  "v_mfma_f32_16x16x32_bf16 v[88:91], v[44:47], a[152:155], v[88:91]\n\t"
  "ds_read_b64_tr_b16 v[38:39], v14 offset:12800\n\t"
  "v_mfma_f32_16x16x32_bf16 v[92:95], v[44:47], a[156:159], v[92:95]\n\t"
  "v_mfma_f32_16x16x32_bf16 v[92:95], v[40:43], a[140:143], v[92:95]\n\t"
  "s_waitcnt lgkmcnt(4)\n\t"
  "v_mfma_f32_16x16x32_bf16 v[112:115], v[48:51], a[128:131], v[112:115]\n\t"
  "ds_read_b64_tr_b16 v[40:41], v15 offset:8192\n\t"
  "s_add_u32 m0, s69, 49152\n\t"
  "v_mfma_f32_16x16x32_bf16 v[112:115], v[52:55], a[144:147], v[112:115]\n\t"
  "ds_read_b64_tr_b16 v[42:43], v15 offset:8704\n\t"
  "s_mov_b32 s70, 114688\n\t"
  "v_mfma_f32_16x16x32_bf16 v[116:119], v[52:55], a[148:151], v[116:119]\n\t"
  "v_mfma_f32_16x16x32_bf16 v[116:119], v[48:51], a[132:135], v[116:119]\n\t"
  "s_waitcnt vmcnt(10)\n\t"
  "s_barrier\n\t"
  "v_mfma_f32_16x16x32_bf16 v[120:123], v[48:51], a[136:139], v[120:123]\n\t"
  "ds_read_b64_tr_b16 v[44:45], v15 offset:12288\n\t"
  "v_mfma_f32_16x16x32_bf16 v[120:123], v[52:55], a[152:155], v[120:123]\n\t"
  "ds_read_b64_tr_b16 v[46:47], v15 offset:12800\n\t"
  "v_mfma_f32_16x16x32_bf16 v[124:127], v[52:55], a[156:159], v[124:127]\n\t"
  "buffer_load_dwordx4 v6, s[48:51], s70 offen offset:0 nt lds\n\t"
  "v_mfma_f32_16x16x32_bf16 v[124:127], v[48:51], a[140:143], v[124:127]\n\t"
  "s_waitcnt lgkmcnt(4)\n\t"
  "v_mfma_f32_16x16x32_bf16 v[144:147], v[32:35], a[128:131], v[144:147]\n\t"
  "ds_read_b64_tr_b16 v[48:49], v12 offset:16384\n\t"
  "v_mfma_f32_16x16x32_bf16 v[144:147], v[36:39], a[144:147], v[144:147]\n\t"
  "ds_read_b64_tr_b16 v[50:51], v12 offset:16896\n\t"
  "v_mfma_f32_16x16x32_bf16 v[148:151], v[36:39], a[148:151], v[148:151]\n\t"
  "v_mfma_f32_16x16x32_bf16 v[148:151], v[32:35], a[132:135], v[148:151]\n\t"
  "buffer_load_dwordx4 v7, s[48:51], s70 offen offset:1024 nt lds\n\t"
  "v_mfma_f32_16x16x32_bf16 v[152:155], v[32:35], a[136:139], v[152:155]\n\t"
  "ds_read_b64_tr_b16 v[52:53], v12 offset:20480\n\t"
  "v_mfma_f32_16x16x32_bf16 v[152:155], v[36:39], a[152:155], v[152:155]\n\t"
  "ds_read_b64_tr_b16 v[54:55], v12 offset:20992\n\t"
  "v_mfma_f32_16x16x32_bf16 v[156:159], v[36:39], a[156:159], v[156:159]\n\t"
  "v_mfma_f32_16x16x32_bf16 v[156:159], v[32:35], a[140:143], v[156:159]\n\t"
  "s_waitcnt lgkmcnt(4)\n\t"
  "v_mfma_f32_16x16x32_bf16 v[176:179], v[40:43], a[128:131], v[176:179]\n\t"
  "ds_read_b64_tr_b16 v[32:33], v13 offset:16384\n\t"
  "v_mfma_f32_16x16x32_bf16 v[176:179], v[44:47], a[144:147], v[176:179]\n\t"
  "ds_read_b64_tr_b16 v[34:35], v13 offset:16896\n\t"
  "v_mfma_f32_16x16x32_bf16 v[180:183], v[44:47], a[148:151], v[180:183]\n\t"
  "buffer_load_dwordx4 v8, s[48:51], s70 offen offset:2048 nt lds\n\t"
  "v_mfma_f32_16x16x32_bf16 v[180:183], v[40:43], a[132:135], v[180:183]\n\t"
  "v_mfma_f32_16x16x32_bf16 v[184:187], v[40:43], a[136:139], v[184:187]\n\t"
  "ds_read_b64_tr_b16 v[36:37], v13 offset:20480\n\t"
  "v_mfma_f32_16x16x32_bf16 v[184:187], v[44:47], a[152:155], v[184:187]\n\t"
  "ds_read_b64_tr_b16 v[38:39], v13 offset:20992\n\t"
  "v_mfma_f32_16x16x32_bf16 v[188:191], v[44:47], a[156:159], v[188:191]\n\t"
  "v_mfma_f32_16x16x32_bf16 v[188:191], v[40:43], a[140:143], v[188:191]\n\t"
  "buffer_load_dwordx4 v9, s[48:51], s70 offen offset:3072 nt lds\n\t"
  "s_waitcnt lgkmcnt(4)\n\t"
  "v_mfma_f32_16x16x32_bf16 v[80:83], v[48:51], a[160:163], v[80:83]\n\t"
  "ds_read_b64_tr_b16 v[40:41], v14 offset:16384\n\t"
  "v_mfma_f32_16x16x32_bf16 v[80:83], v[52:55], a[176:179], v[80:83]\n\t"
  "ds_read_b64_tr_b16 v[42:43], v14 offset:16896\n\t"
  "v_mfma_f32_16x16x32_bf16 v[84:87], v[52:55], a[180:183], v[84:87]\n\t"
  "v_mfma_f32_16x16x32_bf16 v[84:87], v[48:51], a[164:167], v[84:87]\n\t"
  "v_mfma_f32_16x16x32_bf16 v[88:91], v[48:51], a[168:171], v[88:91]\n\t"
  "ds_read_b64_tr_b16 v[44:45], v14 offset:20480\n\t"
  "v_mfma_f32_16x16x32_bf16 v[88:91], v[52:55], a[184:187], v[88:91]\n\t"
  "ds_read_b64_tr_b16 v[46:47], v14 offset:20992\n\t"
  "v_mfma_f32_16x16x32_bf16 v[92:95], v[52:55], a[188:191], v[92:95]\n\t"
  "buffer_load_dword v31, v21, s[92:95], 0 offen sc1\n\t"
  "v_mfma_f32_16x16x32_bf16 v[92:95], v[48:51], a[172:175], v[92:95]\n\t"
  "s_waitcnt lgkmcnt(4)\n\t"
  "v_mfma_f32_16x16x32_bf16 v[112:115], v[32:35], a[160:163], v[112:115]\n\t"
  "ds_read_b64_tr_b16 v[48:49], v15 offset:16384\n\t"
  "v_mfma_f32_16x16x32_bf16 v[112:115], v[36:39], a[176:179], v[112:115]\n\t"
  "ds_read_b64_tr_b16 v[50:51], v15 offset:16896\n\t"
  "s_lshr_b32 s86, s96, 2\n\t"
  "v_mfma_f32_16x16x32_bf16 v[116:119], v[36:39], a[180:183], v[116:119]\n\t"
  "s_mul_i32 s87, s86, 917504\n\t"
  "s_mov_b64 s[60:61], %7\n\t"
  "v_mfma_f32_16x16x32_bf16 v[116:119], v[32:35], a[164:167], v[116:119]\n\t"
  "s_add_u32 s60, s60, s87\n\ts_addc_u32 s61, s61, 0\n\t"
  "v_mfma_f32_16x16x32_bf16 v[120:123], v[32:35], a[168:171], v[120:123]\n\t"
  "ds_read_b64_tr_b16 v[52:53], v15 offset:20480\n\t"
  "s_mov_b32 s62, 917504\n\t"
  "v_mfma_f32_16x16x32_bf16 v[120:123], v[36:39], a[184:187], v[120:123]\n\t"
  "ds_read_b64_tr_b16 v[54:55], v15 offset:20992\n\t"
  "s_mov_b32 s63, 0x20000\n\t"
  "v_mfma_f32_16x16x32_bf16 v[124:127], v[36:39], a[188:191], v[124:127]\n\t"
  "s_and_b32 s87, s97, 7\n\t"
  "s_lshl_b32 s87, s87, 2\n\t"
  "v_mfma_f32_16x16x32_bf16 v[124:127], v[32:35], a[172:175], v[124:127]\n\t"
  "s_and_b32 s86, s96, 3\n\t"
  "s_waitcnt lgkmcnt(4)\n\t"
  "v_mfma_f32_16x16x32_bf16 v[144:147], v[40:43], a[160:163], v[144:147]\n\t"
  "ds_read_b64_tr_b16 v[32:33], v12 offset:24576\n\t"
  "s_add_u32 s87, s87, s86\n\t"
  "v_mfma_f32_16x16x32_bf16 v[144:147], v[44:47], a[176:179], v[144:147]\n\t"
  "ds_read_b64_tr_b16 v[34:35], v12 offset:25088\n\t"
  "s_lshl_b32 s87, s87, 2\n\t"
  "v_mfma_f32_16x16x32_bf16 v[148:151], v[44:47], a[180:183], v[148:151]\n\t"
  "s_add_u32 s87, s87, s90\n\t"
  "s_mul_i32 s87, s87, 7168\n\t"
  "v_mfma_f32_16x16x32_bf16 v[148:151], v[40:43], a[164:167], v[148:151]\n\t"
  "v_lshlrev_b32 v10, 3, v20\n\t"
  "v_and_b32 v10, 0x380, v10\n\t"
  "v_mfma_f32_16x16x32_bf16 v[152:155], v[40:43], a[168:171], v[152:155]\n\t"
  "ds_read_b64_tr_b16 v[36:37], v12 offset:28672\n\t"
  "v_add_u32 v10, s87, v10\n\t"
  "v_mfma_f32_16x16x32_bf16 v[152:155], v[44:47], a[184:187], v[152:155]\n\t"
  "ds_read_b64_tr_b16 v[38:39], v12 offset:29184\n\t"
  "v_mfma_f32_16x16x32_bf16 v[156:159], v[44:47], a[188:191], v[156:159]\n\t"
  "v_mfma_f32_16x16x32_bf16 v[156:159], v[40:43], a[172:175], v[156:159]\n\t"
  "s_waitcnt lgkmcnt(4)\n\t"
  "v_mfma_f32_16x16x32_bf16 v[176:179], v[48:51], a[160:163], v[176:179]\n\t"
  "ds_read_b64_tr_b16 v[40:41], v13 offset:24576\n\t"
  "v_mfma_f32_16x16x32_bf16 v[176:179], v[52:55], a[176:179], v[176:179]\n\t"
  "ds_read_b64_tr_b16 v[42:43], v13 offset:25088\n\t"
  "v_mfma_f32_16x16x32_bf16 v[180:183], v[52:55], a[180:183], v[180:183]\n\t"
  "v_mfma_f32_16x16x32_bf16 v[180:183], v[48:51], a[164:167], v[180:183]\n\t"
  "v_mfma_f32_16x16x32_bf16 v[184:187], v[48:51], a[168:171], v[184:187]\n\t"
  "ds_read_b64_tr_b16 v[44:45], v13 offset:28672\n\t"
  "v_mfma_f32_16x16x32_bf16 v[184:187], v[52:55], a[184:187], v[184:187]\n\t"
  "ds_read_b64_tr_b16 v[46:47], v13 offset:29184\n\t"
  "v_mfma_f32_16x16x32_bf16 v[188:191], v[52:55], a[188:191], v[188:191]\n\t"
  "v_mfma_f32_16x16x32_bf16 v[188:191], v[48:51], a[172:175], v[188:191]\n\t"
  "s_waitcnt lgkmcnt(4)\n\t"
  "v_mfma_f32_16x16x32_bf16 v[80:83], v[32:35], a[192:195], v[80:83]\n\t"
  "ds_read_b64_tr_b16 v[48:49], v14 offset:24576\n\t"
  "v_mfma_f32_16x16x32_bf16 v[80:83], v[36:39], a[208:211], v[80:83]\n\t"
  "ds_read_b64_tr_b16 v[50:51], v14 offset:25088\n\t"
  "v_mfma_f32_16x16x32_bf16 v[84:87], v[36:39], a[212:215], v[84:87]\n\t"
  "v_mfma_f32_16x16x32_bf16 v[84:87], v[32:35], a[196:199], v[84:87]\n\t"
  "v_mfma_f32_16x16x32_bf16 v[88:91], v[32:35], a[200:203], v[88:91]\n\t"
  "ds_read_b64_tr_b16 v[52:53], v14 offset:28672\n\t"
  "v_mfma_f32_16x16x32_bf16 v[88:91], v[36:39], a[216:219], v[88:91]\n\t"
  "ds_read_b64_tr_b16 v[54:55], v14 offset:29184\n\t"
  "v_mfma_f32_16x16x32_bf16 v[92:95], v[36:39], a[220:223], v[92:95]\n\t"
  "v_mfma_f32_16x16x32_bf16 v[92:95], v[32:35], a[204:207], v[92:95]\n\t"
  "s_waitcnt lgkmcnt(4)\n\t"
  "v_mfma_f32_16x16x32_bf16 v[112:115], v[40:43], a[192:195], v[112:115]\n\t"
  "ds_read_b64_tr_b16 v[32:33], v15 offset:24576\n\t"
  "s_add_u32 m0, s69, 0\n\t"
  "v_mfma_f32_16x16x32_bf16 v[112:115], v[44:47], a[208:211], v[112:115]\n\t"
  "ds_read_b64_tr_b16 v[34:35], v15 offset:25088\n\t"
  "s_mov_b32 s70, 131072\n\t"
  "v_mfma_f32_16x16x32_bf16 v[116:119], v[44:47], a[212:215], v[116:119]\n\t"
  "v_mfma_f32_16x16x32_bf16 v[116:119], v[40:43], a[196:199], v[116:119]\n\t"
  "s_barrier\n\t"
  "v_mfma_f32_16x16x32_bf16 v[120:123], v[40:43], a[200:203], v[120:123]\n\t"
  "ds_read_b64_tr_b16 v[36:37], v15 offset:28672\n\t"
  "v_mfma_f32_16x16x32_bf16 v[120:123], v[44:47], a[216:219], v[120:123]\n\t"
  "ds_read_b64_tr_b16 v[38:39], v15 offset:29184\n\t"
  "v_sub_u32 v12, v12, v62\n\t"
  "v_mfma_f32_16x16x32_bf16 v[124:127], v[44:47], a[220:223], v[124:127]\n\t"
  "buffer_load_dwordx4 v6, s[48:51], s70 offen offset:0 nt lds\n\t"
  "v_sub_u32 v13, v13, v62\n\t"
  "v_mfma_f32_16x16x32_bf16 v[124:127], v[40:43], a[204:207], v[124:127]\n\t"
  "v_sub_u32 v14, v14, v62\n\t"
  "s_waitcnt lgkmcnt(4)\n\t"
  "v_mfma_f32_16x16x32_bf16 v[144:147], v[48:51], a[192:195], v[144:147]\n\t"
  "ds_read_b128 v[40:43], v16 offset:32768\n\t"
  "v_sub_u32 v15, v15, v62\n\t"
  "v_mfma_f32_16x16x32_bf16 v[144:147], v[52:55], a[208:211], v[144:147]\n\t"
  "v_mfma_f32_16x16x32_bf16 v[148:151], v[52:55], a[212:215], v[148:151]\n\t"
  "v_mfma_f32_16x16x32_bf16 v[148:151], v[48:51], a[196:199], v[148:151]\n\t"
  "buffer_load_dwordx4 v7, s[48:51], s70 offen offset:1024 nt lds\n\t"
  "v_mfma_f32_16x16x32_bf16 v[152:155], v[48:51], a[200:203], v[152:155]\n\t"
  "ds_read_b128 v[44:47], v17 offset:32768\n\t"
  "v_mul_f32 v80, s83, v80\n\t"
  "v_mfma_f32_16x16x32_bf16 v[152:155], v[52:55], a[216:219], v[152:155]\n\t"
  "v_mul_f32 v81, s83, v81\n\t"
  "v_mul_f32 v82, s83, v82\n\t"
  "v_mfma_f32_16x16x32_bf16 v[156:159], v[52:55], a[220:223], v[156:159]\n\t"
  "v_mul_f32 v83, s83, v83\n\t"
  "v_mul_f32 v112, s83, v112\n\t"
  "v_mfma_f32_16x16x32_bf16 v[156:159], v[48:51], a[204:207], v[156:159]\n\t"
  "v_mul_f32 v113, s83, v113\n\t"
  "s_waitcnt lgkmcnt(2)\n\t"
  "v_mfma_f32_16x16x32_bf16 v[176:179], v[32:35], a[192:195], v[176:179]\n\t"
  "ds_read_b128 v[48:51], v18 offset:32768\n\t"
  "v_mul_f32 v114, s83, v114\n\t"
  "v_mul_f32 v115, s83, v115\n\t"
  "v_mfma_f32_16x16x32_bf16 v[176:179], v[36:39], a[208:211], v[176:179]\n\t"
  "buffer_load_dwordx4 v8, s[48:51], s70 offen offset:2048 nt lds\n\t"
  "v_cvt_pk_bf16_f32 v80, v80, v81\n\t"
  "v_mfma_f32_16x16x32_bf16 v[180:183], v[36:39], a[212:215], v[180:183]\n\t"
  "v_cvt_pk_bf16_f32 v81, v82, v83\n\t"
  "v_cvt_pk_bf16_f32 v82, v112, v113\n\t"
  "v_mfma_f32_16x16x32_bf16 v[180:183], v[32:35], a[196:199], v[180:183]\n\t"
  "v_cvt_pk_bf16_f32 v83, v114, v115\n\t"
  "v_mul_f32 v84, s83, v84\n\t"
  "v_mfma_f32_16x16x32_bf16 v[184:187], v[32:35], a[200:203], v[184:187]\n\t"
  "ds_read_b128 v[52:55], v19 offset:32768\n\t"
  "v_mul_f32 v85, s83, v85\n\t"
  "v_mfma_f32_16x16x32_bf16 v[184:187], v[36:39], a[216:219], v[184:187]\n\t"
  "v_mul_f32 v86, s83, v86\n\t"
  "v_mul_f32 v87, s83, v87\n\t"
  "v_mfma_f32_16x16x32_bf16 v[188:191], v[36:39], a[220:223], v[188:191]\n\t"
  "buffer_load_dwordx4 v9, s[48:51], s70 offen offset:3072 nt lds\n\t"
  "v_permlane16_swap_b32 v80, v82\n\t"
  "v_mfma_f32_16x16x32_bf16 v[188:191], v[32:35], a[204:207], v[188:191]\n\t"
  "v_permlane16_swap_b32 v81, v83\n\t"
  "s_waitcnt lgkmcnt(2)\n\t"
  "v_mfma_f32_16x16x32_bf16 v[208:211], v[40:43], v[64:67], 0\n\t"
  "ds_read_b128 v[32:35], v16 offset:33024\n\t"
  "v_mul_f32 v116, s83, v116\n\t"
  "v_mul_f32 v117, s83, v117\n\t"
  "v_mfma_f32_16x16x32_bf16 v[208:211], v[44:47], v[96:99], v[208:211]\n\t"
  "v_mul_f32 v118, s83, v118\n\t"
  "v_mul_f32 v119, s83, v119\n\t"
  "v_mfma_f32_16x16x32_bf16 v[212:215], v[44:47], v[100:103], 0\n\t"
  "v_cvt_pk_bf16_f32 v84, v84, v85\n\t"
  "v_cvt_pk_bf16_f32 v85, v86, v87\n\t"
  "v_mfma_f32_16x16x32_bf16 v[212:215], v[40:43], v[68:71], v[212:215]\n\t"
  "v_cvt_pk_bf16_f32 v86, v116, v117\n\t"
  "v_cvt_pk_bf16_f32 v87, v118, v119\n\t"
  "v_mul_f32 v144, s83, v144\n\t"
  "v_mul_f32 v145, s83, v145\n\t"
  "v_mul_f32 v146, s83, v146\n\t"
  "v_mfma_f32_16x16x32_bf16 v[216:219], v[40:43], v[72:75], 0\n\t"
  "ds_read_b128 v[36:39], v17 offset:33024\n\t"
  "v_mul_f32 v88, s83, v88\n\t"
  "v_mul_f32 v147, s83, v147\n\t"
  "v_mul_f32 v176, s83, v176\n\t"
  "v_mul_f32 v177, s83, v177\n\t"
  "v_mfma_f32_16x16x32_bf16 v[216:219], v[44:47], v[104:107], v[216:219]\n\t"
  "v_mul_f32 v89, s83, v89\n\t"
  "v_mul_f32 v90, s83, v90\n\t"
  "v_mul_f32 v178, s83, v178\n\t"
  "v_mul_f32 v179, s83, v179\n\t"
  "v_cvt_pk_bf16_f32 v144, v144, v145\n\t"
  "v_mfma_f32_16x16x32_bf16 v[220:223], v[44:47], v[108:111], 0\n\t"
  "v_mul_f32 v91, s83, v91\n\t"
  "v_permlane16_swap_b32 v84, v86\n\t"
  "v_cvt_pk_bf16_f32 v145, v146, v147\n\t"
  "v_cvt_pk_bf16_f32 v146, v176, v177\n\t"
  "v_cvt_pk_bf16_f32 v147, v178, v179\n\t"
  "v_mfma_f32_16x16x32_bf16 v[220:223], v[40:43], v[76:79], v[220:223]\n\t"
  "v_permlane16_swap_b32 v85, v87\n\t"
  "v_mul_f32 v148, s83, v148\n\t"
  "v_mul_f32 v149, s83, v149\n\t"
  "v_mul_f32 v150, s83, v150\n\t"
  "s_waitcnt lgkmcnt(2)\n\t"
  "v_mfma_f32_16x16x32_bf16 v[208:211], v[48:51], v[128:131], v[208:211]\n\t"
  "ds_read_b128 v[40:43], v18 offset:33024\n\t"
  "v_mul_f32 v120, s83, v120\n\t"
  "v_mul_f32 v121, s83, v121\n\t"
  "v_mul_f32 v151, s83, v151\n\t"
  "v_permlane16_swap_b32 v144, v146\n\t"
  "v_mfma_f32_16x16x32_bf16 v[208:211], v[52:55], v[160:163], v[208:211]\n\t"
  "v_mul_f32 v122, s83, v122\n\t"
  "v_mul_f32 v123, s83, v123\n\t"
  "v_permlane16_swap_b32 v145, v147\n\t"
  "v_mul_f32 v180, s83, v180\n\t"
  "v_mul_f32 v181, s83, v181\n\t"
  "v_mfma_f32_16x16x32_bf16 v[212:215], v[52:55], v[164:167], v[212:215]\n\t"
  "v_cvt_pk_bf16_f32 v88, v88, v89\n\t"
  "v_cvt_pk_bf16_f32 v89, v90, v91\n\t"
  "v_mul_f32 v182, s83, v182\n\t"
  "v_mul_f32 v183, s83, v183\n\t"
  "v_cvt_pk_bf16_f32 v148, v148, v149\n\t"
  "v_mfma_f32_16x16x32_bf16 v[212:215], v[48:51], v[132:135], v[212:215]\n\t"
  "v_cvt_pk_bf16_f32 v90, v120, v121\n\t"
  "v_cvt_pk_bf16_f32 v91, v122, v123\n\t"
  "v_cvt_pk_bf16_f32 v149, v150, v151\n\t"
  "v_cvt_pk_bf16_f32 v150, v180, v181\n\t"
  "v_cvt_pk_bf16_f32 v151, v182, v183\n\t"
  "v_mfma_f32_16x16x32_bf16 v[216:219], v[48:51], v[136:139], v[216:219]\n\t"
  "ds_read_b128 v[44:47], v19 offset:33024\n\t"
  "v_mul_f32 v92, s83, v92\n\t"
  "v_mul_f32 v152, s83, v152\n\t"
  "v_mul_f32 v153, s83, v153\n\t"
  "v_mul_f32 v154, s83, v154\n\t"
  "v_mfma_f32_16x16x32_bf16 v[216:219], v[52:55], v[168:171], v[216:219]\n\t"
  "v_mul_f32 v93, s83, v93\n\t"
  "v_mul_f32 v94, s83, v94\n\t"
  "v_mul_f32 v155, s83, v155\n\t"
  "v_permlane16_swap_b32 v148, v150\n\t"
  "v_permlane16_swap_b32 v149, v151\n\t"
  "v_mfma_f32_16x16x32_bf16 v[220:223], v[52:55], v[172:175], v[220:223]\n\t"
  "v_mul_f32 v95, s83, v95\n\t"
  "v_permlane16_swap_b32 v88, v90\n\t"
  "v_mul_f32 v184, s83, v184\n\t"
  "v_mul_f32 v185, s83, v185\n\t"
  "v_mul_f32 v186, s83, v186\n\t"
  "v_mfma_f32_16x16x32_bf16 v[220:223], v[48:51], v[140:143], v[220:223]\n\t"
  "v_permlane16_swap_b32 v89, v91\n\t"
  "v_mul_f32 v187, s83, v187\n\t"
  "v_cvt_pk_bf16_f32 v152, v152, v153\n\t"
  "v_cvt_pk_bf16_f32 v153, v154, v155\n\t"
  "s_waitcnt lgkmcnt(2)\n\t"
  "v_mfma_f32_16x16x32_bf16 v[208:211], v[32:35], v[192:195], v[208:211]\n\t"
  "ds_read_b128 v[48:51], v16 offset:40960\n\t"
  "v_mul_f32 v124, s83, v124\n\t"
  "v_mul_f32 v125, s83, v125\n\t"
  "v_cvt_pk_bf16_f32 v154, v184, v185\n\t"
  "v_cvt_pk_bf16_f32 v155, v186, v187\n\t"
  "v_mfma_f32_16x16x32_bf16 v[208:211], v[36:39], v[224:227], v[208:211]\n\t"
  "v_mul_f32 v126, s83, v126\n\t"
  "v_mul_f32 v127, s83, v127\n\t"
  "v_mul_f32 v156, s83, v156\n\t"
  "v_mul_f32 v157, s83, v157\n\t"
  "v_mul_f32 v158, s83, v158\n\t"
  "v_mfma_f32_16x16x32_bf16 v[212:215], v[36:39], v[228:231], v[212:215]\n\t"
  "v_cvt_pk_bf16_f32 v92, v92, v93\n\t"
  "v_cvt_pk_bf16_f32 v93, v94, v95\n\t"
  "v_mul_f32 v159, s83, v159\n\t"
  "v_permlane16_swap_b32 v152, v154\n\t"
  "v_permlane16_swap_b32 v153, v155\n\t"
  "v_mfma_f32_16x16x32_bf16 v[212:215], v[32:35], v[196:199], v[212:215]\n\t"
  "v_cvt_pk_bf16_f32 v94, v124, v125\n\t"
  "v_cvt_pk_bf16_f32 v95, v126, v127\n\t"
  "v_mul_f32 v188, s83, v188\n\t"
  "v_mul_f32 v189, s83, v189\n\t"
  "v_mul_f32 v190, s83, v190\n\t"
  "v_mfma_f32_16x16x32_bf16 v[216:219], v[32:35], v[200:203], v[216:219]\n\t"
  "ds_read_b128 v[52:55], v17 offset:40960\n\t"
  "v_permlane16_swap_b32 v92, v94\n\t"
  "v_mul_f32 v191, s83, v191\n\t"
  "v_cvt_pk_bf16_f32 v156, v156, v157\n\t"
  "v_cvt_pk_bf16_f32 v157, v158, v159\n\t"
  "v_mfma_f32_16x16x32_bf16 v[216:219], v[36:39], v[232:235], v[216:219]\n\t"
  "v_permlane16_swap_b32 v93, v95\n\t"
  "v_cvt_pk_bf16_f32 v158, v188, v189\n\t"
  "v_cvt_pk_bf16_f32 v159, v190, v191\n\t"
  "s_nop 0\n\t"
  "v_permlane16_swap_b32 v156, v158\n\t"
  "v_permlane16_swap_b32 v157, v159\n\t"
  "v_mfma_f32_16x16x32_bf16 v[220:223], v[36:39], v[236:239], v[220:223]\n\t"
  "v_mfma_f32_16x16x32_bf16 v[220:223], v[32:35], v[204:207], v[220:223]\n\t"
  "s_waitcnt lgkmcnt(2)\n\t"
  "v_mfma_f32_16x16x32_bf16 v[208:211], v[40:43], v[80:83], v[208:211]\n\t"
  "ds_read_b128 v[32:35], v18 offset:40960\n\t"
  "v_mfma_f32_16x16x32_bf16 v[208:211], v[44:47], v[144:147], v[208:211]\n\t"
  "v_mfma_f32_16x16x32_bf16 v[212:215], v[44:47], v[148:151], v[212:215]\n\t"
  "v_mfma_f32_16x16x32_bf16 v[212:215], v[40:43], v[84:87], v[212:215]\n\t"
  "v_mfma_f32_16x16x32_bf16 v[216:219], v[40:43], v[88:91], v[216:219]\n\t"
  "ds_read_b128 v[36:39], v19 offset:40960\n\t"
  "v_mfma_f32_16x16x32_bf16 v[216:219], v[44:47], v[152:155], v[216:219]\n\t"
  "v_mfma_f32_16x16x32_bf16 v[220:223], v[44:47], v[156:159], v[220:223]\n\t"
  "v_mfma_f32_16x16x32_bf16 v[220:223], v[40:43], v[92:95], v[220:223]\n\t"
  "s_nop 0\n\t"
  "v_max3_f32 v22, v208, v209, v210\n\t"
  "v_max_f32 v22, v22, v211\n\t"
  "v_max3_f32 v23, v212, v213, v214\n\t"
  "v_max_f32 v23, v23, v215\n\t"
  "s_nop 0\n\t"
  "v_max3_f32 v24, v216, v217, v218\n\t"
  "v_max_f32 v24, v24, v219\n\t"
  "v_max3_f32 v25, v220, v221, v222\n\t"
  "v_max_f32 v25, v25, v223\n\t"
  "v_mov_b32 v26, v22\n\t"
  "v_mov_b32 v27, v23\n\t"
  "v_mov_b32 v28, v24\n\t"
  "v_mov_b32 v29, v25\n\t"
  "v_permlane16_swap_b32 v22, v26\n\t"
  "v_permlane16_swap_b32 v23, v27\n\t"
  "v_permlane16_swap_b32 v24, v28\n\t"
  "v_permlane16_swap_b32 v25, v29\n\t"
  "v_max_f32 v22, v22, v26\n\t"
  "v_max_f32 v23, v23, v27\n\t"
  "v_max_f32 v24, v24, v28\n\t"
  "v_max_f32 v25, v25, v29\n\t"
  "v_mov_b32 v26, v22\n\t"
  "v_mov_b32 v27, v23\n\t"
  "v_mov_b32 v28, v24\n\t"
  "v_mov_b32 v29, v25\n\t"
  "v_permlane32_swap_b32 v22, v26\n\t"
  "v_permlane32_swap_b32 v23, v27\n\t"
  "v_permlane32_swap_b32 v24, v28\n\t"
  "v_permlane32_swap_b32 v25, v29\n\t"
  "v_max_f32 v22, v22, v26\n\t"
  "v_max_f32 v23, v23, v27\n\t"
  "v_max_f32 v24, v24, v28\n\t"
  "v_max_f32 v25, v25, v29\n\t"
  "v_sub_f32 v112, s64, v22\n\t"
  "v_sub_f32 v113, s64, v22\n\t"
  "v_sub_f32 v114, s64, v22\n\t"
  "v_sub_f32 v115, s64, v22\n\t"
  "v_sub_f32 v116, s65, v23\n\t"
  "v_sub_f32 v117, s65, v23\n\t"
  "v_sub_f32 v118, s65, v23\n\t"
  "v_sub_f32 v119, s65, v23\n\t"
  "v_sub_f32 v120, s66, v24\n\t"
  "v_sub_f32 v121, s66, v24\n\t"
  "v_sub_f32 v122, s66, v24\n\t"
  "v_sub_f32 v123, s66, v24\n\t"
  "v_sub_f32 v124, s67, v25\n\t"
  "v_sub_f32 v125, s67, v25\n\t"
  "v_sub_f32 v126, s67, v25\n\t"
  "v_sub_f32 v127, s67, v25\n\t"
  "s_waitcnt lgkmcnt(2)\n\t"
  "v_mfma_f32_16x16x32_bf16 v[240:243], v[48:51], v[64:67], v[112:115]\n\t"
  "ds_read_b128 v[40:43], v16 offset:41216\n\t"
  "v_mfma_f32_16x16x32_bf16 v[240:243], v[52:55], v[96:99], v[240:243]\n\t"
  "v_add_f32 v208, v112, v208\n\t"
  "v_add_f32 v209, v112, v209\n\t"
  "v_mfma_f32_16x16x32_bf16 v[244:247], v[52:55], v[100:103], v[116:119]\n\t"
  "v_exp_f32 v208, v208\n\t"
  "v_mfma_f32_16x16x32_bf16 v[244:247], v[48:51], v[68:71], v[244:247]\n\t"
  "v_exp_f32 v209, v209\n\t"
  "s_nop 0\n\t"
  "v_cvt_pk_bf16_f32 v0, v208, v209\n\t"
  "v_mfma_f32_16x16x32_bf16 v[248:251], v[48:51], v[72:75], v[120:123]\n\t"
  "ds_read_b128 v[44:47], v17 offset:41216\n\t"
  "v_accvgpr_write_b32 a0, v0\n\t"
  "v_add_f32 v210, v112, v210\n\t"
  "v_mfma_f32_16x16x32_bf16 v[248:251], v[52:55], v[104:107], v[248:251]\n\t"
  "v_add_f32 v211, v112, v211\n\t"
  "v_exp_f32 v210, v210\n\t"
  "v_mfma_f32_16x16x32_bf16 v[252:255], v[52:55], v[108:111], v[124:127]\n\t"
  "v_exp_f32 v211, v211\n\t"
  "s_nop 0\n\t"
  "v_cvt_pk_bf16_f32 v1, v210, v211\n\t"
  "v_mfma_f32_16x16x32_bf16 v[252:255], v[48:51], v[76:79], v[252:255]\n\t"
  "s_add_u32 m0, s69, 16384\n\t"
  "v_accvgpr_write_b32 a1, v1\n\t"
  "s_waitcnt lgkmcnt(2)\n\t"
  "v_mfma_f32_16x16x32_bf16 v[240:243], v[32:35], v[128:131], v[240:243]\n\t"
  "ds_read_b128 v[48:51], v18 offset:41216\n\t"
  "s_mov_b32 s70, 147456\n\t"
  "v_add_f32 v212, v116, v212\n\t"
  "v_mfma_f32_16x16x32_bf16 v[240:243], v[36:39], v[160:163], v[240:243]\n\t"
  "v_add_f32 v213, v116, v213\n\t"
  "v_exp_f32 v212, v212\n\t"
  "v_mfma_f32_16x16x32_bf16 v[244:247], v[36:39], v[164:167], v[244:247]\n\t"
  "v_exp_f32 v213, v213\n\t"
  "s_nop 0\n\t"
  "v_cvt_pk_bf16_f32 v2, v212, v213\n\t"
  "v_mfma_f32_16x16x32_bf16 v[244:247], v[32:35], v[132:135], v[244:247]\n\t"
  "v_accvgpr_write_b32 a4, v2\n\t"
  "s_barrier\n\t"
  "v_mfma_f32_16x16x32_bf16 v[248:251], v[32:35], v[136:139], v[248:251]\n\t"
  "ds_read_b128 v[52:55], v19 offset:41216\n\t"
  "v_add_f32 v214, v116, v214\n\t"
  "v_add_f32 v215, v116, v215\n\t"
  "v_mfma_f32_16x16x32_bf16 v[248:251], v[36:39], v[168:171], v[248:251]\n\t"
  "buffer_load_dwordx4 v6, s[48:51], s70 offen offset:0 nt lds\n\t"
  "v_exp_f32 v214, v214\n\t"
  "v_mfma_f32_16x16x32_bf16 v[252:255], v[36:39], v[172:175], v[252:255]\n\t"
  "v_exp_f32 v215, v215\n\t"
  "s_nop 0\n\t"
  "v_cvt_pk_bf16_f32 v3, v214, v215\n\t"
  "v_mfma_f32_16x16x32_bf16 v[252:255], v[32:35], v[140:143], v[252:255]\n\t"
  "v_accvgpr_write_b32 a5, v3\n\t"
  "v_add_f32 v216, v120, v216\n\t"
  "s_waitcnt lgkmcnt(2)\n\t"
  "v_mfma_f32_16x16x32_bf16 v[240:243], v[40:43], v[192:195], v[240:243]\n\t"
  "ds_read_b128 v[32:35], v16 offset:49152\n\t"
  "v_add_f32 v217, v120, v217\n\t"
  "v_mfma_f32_16x16x32_bf16 v[240:243], v[44:47], v[224:227], v[240:243]\n\t"
  "v_exp_f32 v216, v216\n\t"
  "v_mfma_f32_16x16x32_bf16 v[244:247], v[44:47], v[228:231], v[244:247]\n\t"
  "buffer_load_dwordx4 v7, s[48:51], s70 offen offset:1024 nt lds\n\t"
  "v_exp_f32 v217, v217\n\t"
  "v_mfma_f32_16x16x32_bf16 v[244:247], v[40:43], v[196:199], v[244:247]\n\t"
  "v_cvt_pk_bf16_f32 v0, v216, v217\n\t"
  "v_accvgpr_write_b32 a8, v0\n\t"
  "v_add_f32 v218, v120, v218\n\t"
  "v_mfma_f32_16x16x32_bf16 v[248:251], v[40:43], v[200:203], v[248:251]\n\t"
  "ds_read_b128 v[36:39], v17 offset:49152\n\t"
  "v_add_f32 v219, v120, v219\n\t"
  "v_mfma_f32_16x16x32_bf16 v[248:251], v[44:47], v[232:235], v[248:251]\n\t"
  "v_exp_f32 v218, v218\n\t"
  "v_mfma_f32_16x16x32_bf16 v[252:255], v[44:47], v[236:239], v[252:255]\n\t"
  "v_exp_f32 v219, v219\n\t"
  "s_nop 0\n\t"
  "v_cvt_pk_bf16_f32 v1, v218, v219\n\t"
  "v_mfma_f32_16x16x32_bf16 v[252:255], v[40:43], v[204:207], v[252:255]\n\t"
  "buffer_load_dwordx4 v8, s[48:51], s70 offen offset:2048 nt lds\n\t"
  "v_accvgpr_write_b32 a9, v1\n\t"
  "s_waitcnt lgkmcnt(2)\n\t"
  "v_mfma_f32_16x16x32_bf16 v[240:243], v[48:51], v[80:83], v[240:243]\n\t"
  "ds_read_b128 v[40:43], v18 offset:49152\n\t"
  "v_add_f32 v220, v124, v220\n\t"
  "v_add_f32 v221, v124, v221\n\t"
  "v_mfma_f32_16x16x32_bf16 v[240:243], v[52:55], v[144:147], v[240:243]\n\t"
  "v_exp_f32 v220, v220\n\t"
  "v_mfma_f32_16x16x32_bf16 v[244:247], v[52:55], v[148:151], v[244:247]\n\t"
  "v_exp_f32 v221, v221\n\t"
  "s_nop 0\n\t"
  "v_cvt_pk_bf16_f32 v2, v220, v221\n\t"
  "v_mfma_f32_16x16x32_bf16 v[244:247], v[48:51], v[84:87], v[244:247]\n\t"
  "v_accvgpr_write_b32 a12, v2\n\t"
  "v_add_f32 v222, v124, v222\n\t"
  "v_add_f32 v223, v124, v223\n\t"
  "v_mfma_f32_16x16x32_bf16 v[248:251], v[48:51], v[88:91], v[248:251]\n\t"
  "ds_read_b128 v[44:47], v19 offset:49152\n\t"
  "v_exp_f32 v222, v222\n\t"
  "v_mfma_f32_16x16x32_bf16 v[248:251], v[52:55], v[152:155], v[248:251]\n\t"
  "buffer_load_dwordx4 v9, s[48:51], s70 offen offset:3072 nt lds\n\t"
  "v_exp_f32 v223, v223\n\t"
  "v_mfma_f32_16x16x32_bf16 v[252:255], v[52:55], v[156:159], v[252:255]\n\t"
  "v_cvt_pk_bf16_f32 v3, v222, v223\n\t"
  "v_accvgpr_write_b32 a13, v3\n\t"
  "v_mfma_f32_16x16x32_bf16 v[252:255], v[48:51], v[92:95], v[252:255]\n\t"
  "s_waitcnt lgkmcnt(2)\n\t"
  "v_mfma_f32_16x16x32_bf16 v[208:211], v[32:35], v[64:67], v[112:115]\n\t"
  "ds_read_b128 v[48:51], v16 offset:49408\n\t"
  "v_mfma_f32_16x16x32_bf16 v[208:211], v[36:39], v[96:99], v[208:211]\n\t"
  "v_exp_f32 v240, v240\n\t"
  "v_mfma_f32_16x16x32_bf16 v[212:215], v[36:39], v[100:103], v[116:119]\n\t"
  "v_exp_f32 v241, v241\n\t"
  "v_mfma_f32_16x16x32_bf16 v[212:215], v[32:35], v[68:71], v[212:215]\n\t"
  "v_mfma_f32_16x16x32_bf16 v[216:219], v[32:35], v[72:75], v[120:123]\n\t"
  "ds_read_b128 v[52:55], v17 offset:49408\n\t"
  "v_cvt_pk_bf16_f32 v0, v240, v241\n\t"
  "v_mfma_f32_16x16x32_bf16 v[216:219], v[36:39], v[104:107], v[216:219]\n\t"
  "v_exp_f32 v242, v242\n\t"
  "v_mfma_f32_16x16x32_bf16 v[220:223], v[36:39], v[108:111], v[124:127]\n\t"
  "v_exp_f32 v243, v243\n\t"
  "v_mfma_f32_16x16x32_bf16 v[220:223], v[32:35], v[76:79], v[220:223]\n\t"
  "v_accvgpr_write_b32 a2, v0\n\t"
  "s_waitcnt lgkmcnt(2)\n\t"
  "v_mfma_f32_16x16x32_bf16 v[208:211], v[40:43], v[128:131], v[208:211]\n\t"
  "ds_read_b128 v[32:35], v18 offset:49408\n\t"
  "v_cvt_pk_bf16_f32 v1, v242, v243\n\t"
  "v_mfma_f32_16x16x32_bf16 v[208:211], v[44:47], v[160:163], v[208:211]\n\t"
  "v_exp_f32 v244, v244\n\t"
  "v_mfma_f32_16x16x32_bf16 v[212:215], v[44:47], v[164:167], v[212:215]\n\t"
  "v_exp_f32 v245, v245\n\t"
  "v_mfma_f32_16x16x32_bf16 v[212:215], v[40:43], v[132:135], v[212:215]\n\t"
  "v_accvgpr_write_b32 a3, v1\n\t"
  "v_mfma_f32_16x16x32_bf16 v[216:219], v[40:43], v[136:139], v[216:219]\n\t"
  "ds_read_b128 v[36:39], v19 offset:49408\n\t"
  "v_cvt_pk_bf16_f32 v2, v244, v245\n\t"
  "v_mfma_f32_16x16x32_bf16 v[216:219], v[44:47], v[168:171], v[216:219]\n\t"
  "v_exp_f32 v246, v246\n\t"
  "v_mfma_f32_16x16x32_bf16 v[220:223], v[44:47], v[172:175], v[220:223]\n\t"
  "v_exp_f32 v247, v247\n\t"
  "v_mfma_f32_16x16x32_bf16 v[220:223], v[40:43], v[140:143], v[220:223]\n\t"
  "v_accvgpr_write_b32 a6, v2\n\t"
  "s_waitcnt lgkmcnt(2)\n\t"
  "v_mfma_f32_16x16x32_bf16 v[208:211], v[48:51], v[192:195], v[208:211]\n\t"
  "ds_read_b128 v[40:43], v16 offset:57344\n\t"
  "v_cvt_pk_bf16_f32 v3, v246, v247\n\t"
  "v_mfma_f32_16x16x32_bf16 v[208:211], v[52:55], v[224:227], v[208:211]\n\t"
  "v_exp_f32 v248, v248\n\t"
  "v_mfma_f32_16x16x32_bf16 v[212:215], v[52:55], v[228:231], v[212:215]\n\t"
  "v_exp_f32 v249, v249\n\t"
  "v_mfma_f32_16x16x32_bf16 v[212:215], v[48:51], v[196:199], v[212:215]\n\t"
  "v_accvgpr_write_b32 a7, v3\n\t"
  "v_mfma_f32_16x16x32_bf16 v[216:219], v[48:51], v[200:203], v[216:219]\n\t"
  "ds_read_b128 v[44:47], v17 offset:57344\n\t"
  "v_cvt_pk_bf16_f32 v0, v248, v249\n\t"
  "v_mfma_f32_16x16x32_bf16 v[216:219], v[52:55], v[232:235], v[216:219]\n\t"
  "v_exp_f32 v250, v250\n\t"
  "v_mfma_f32_16x16x32_bf16 v[220:223], v[52:55], v[236:239], v[220:223]\n\t"
  "v_exp_f32 v251, v251\n\t"
  "v_mfma_f32_16x16x32_bf16 v[220:223], v[48:51], v[204:207], v[220:223]\n\t"
  "v_accvgpr_write_b32 a10, v0\n\t"
  "s_waitcnt lgkmcnt(2)\n\t"
  "v_mfma_f32_16x16x32_bf16 v[208:211], v[32:35], v[80:83], v[208:211]\n\t"
  "ds_read_b128 v[48:51], v18 offset:57344\n\t"
  "v_cvt_pk_bf16_f32 v1, v250, v251\n\t"
  "v_mfma_f32_16x16x32_bf16 v[208:211], v[36:39], v[144:147], v[208:211]\n\t"
  "v_exp_f32 v252, v252\n\t"
  "v_mfma_f32_16x16x32_bf16 v[212:215], v[36:39], v[148:151], v[212:215]\n\t"
  "v_exp_f32 v253, v253\n\t"
  "v_mfma_f32_16x16x32_bf16 v[212:215], v[32:35], v[84:87], v[212:215]\n\t"
  "buffer_load_dword v11, v10, s[60:63], 0 offen offset:0\n\t"
  "v_accvgpr_write_b32 a11, v1\n\t"
  "v_mfma_f32_16x16x32_bf16 v[216:219], v[32:35], v[88:91], v[216:219]\n\t"
  "ds_read_b128 v[52:55], v19 offset:57344\n\t"
  "v_cvt_pk_bf16_f32 v2, v252, v253\n\t"
  "v_mfma_f32_16x16x32_bf16 v[216:219], v[36:39], v[152:155], v[216:219]\n\t"
  "v_exp_f32 v254, v254\n\t"
  "v_mfma_f32_16x16x32_bf16 v[220:223], v[36:39], v[156:159], v[220:223]\n\t"
  "v_exp_f32 v255, v255\n\t"
  "v_mfma_f32_16x16x32_bf16 v[220:223], v[32:35], v[92:95], v[220:223]\n\t"
  "v_accvgpr_write_b32 a14, v2\n\t"
  "s_waitcnt lgkmcnt(2)\n\t"
  "v_mfma_f32_16x16x32_bf16 v[240:243], v[40:43], v[64:67], v[112:115]\n\t"
  "ds_read_b128 v[32:35], v16 offset:57600\n\t"
  "v_cvt_pk_bf16_f32 v3, v254, v255\n\t"
  "v_mfma_f32_16x16x32_bf16 v[240:243], v[44:47], v[96:99], v[240:243]\n\t"
  "v_exp_f32 v208, v208\n\t"
  "v_mfma_f32_16x16x32_bf16 v[244:247], v[44:47], v[100:103], v[116:119]\n\t"
  "v_exp_f32 v209, v209\n\t"
  "v_mfma_f32_16x16x32_bf16 v[244:247], v[40:43], v[68:71], v[244:247]\n\t"
  "v_accvgpr_write_b32 a15, v3\n\t"
  "v_mfma_f32_16x16x32_bf16 v[248:251], v[40:43], v[72:75], v[120:123]\n\t"
  "ds_read_b128 v[36:39], v17 offset:57600\n\t"
  "v_cvt_pk_bf16_f32 v0, v208, v209\n\t"
  "v_mfma_f32_16x16x32_bf16 v[248:251], v[44:47], v[104:107], v[248:251]\n\t"
  "v_exp_f32 v210, v210\n\t"
  "v_mfma_f32_16x16x32_bf16 v[252:255], v[44:47], v[108:111], v[124:127]\n\t"
  "v_exp_f32 v211, v211\n\t"
  "v_mfma_f32_16x16x32_bf16 v[252:255], v[40:43], v[76:79], v[252:255]\n\t"
  "v_accvgpr_write_b32 a16, v0\n\t"
  "s_waitcnt lgkmcnt(2)\n\t"
  "v_mfma_f32_16x16x32_bf16 v[240:243], v[48:51], v[128:131], v[240:243]\n\t"
  "ds_read_b128 v[40:43], v18 offset:57600\n\t"
  "v_cvt_pk_bf16_f32 v1, v210, v211\n\t"
  "v_mfma_f32_16x16x32_bf16 v[240:243], v[52:55], v[160:163], v[240:243]\n\t"
  "v_exp_f32 v212, v212\n\t"
  "v_mfma_f32_16x16x32_bf16 v[244:247], v[52:55], v[164:167], v[244:247]\n\t"
  "v_exp_f32 v213, v213\n\t"
  "v_mfma_f32_16x16x32_bf16 v[244:247], v[48:51], v[132:135], v[244:247]\n\t"
  "v_accvgpr_write_b32 a17, v1\n\t"
  "v_mfma_f32_16x16x32_bf16 v[248:251], v[48:51], v[136:139], v[248:251]\n\t"
  "ds_read_b128 v[44:47], v19 offset:57600\n\t"
  "v_cvt_pk_bf16_f32 v2, v212, v213\n\t"
  "v_add_u32 v16, 0x10000, v16\n\t"
  "v_mfma_f32_16x16x32_bf16 v[248:251], v[52:55], v[168:171], v[248:251]\n\t"
  "v_exp_f32 v214, v214\n\t"
  "v_add_u32 v17, 0x10000, v17\n\t"
  "v_mfma_f32_16x16x32_bf16 v[252:255], v[52:55], v[172:175], v[252:255]\n\t"
  "v_exp_f32 v215, v215\n\t"
  "v_add_u32 v18, 0x10000, v18\n\t"
  "v_mfma_f32_16x16x32_bf16 v[252:255], v[48:51], v[140:143], v[252:255]\n\t"
  "v_accvgpr_write_b32 a20, v2\n\t"
  "v_add_u32 v19, 0x10000, v19\n\t"
  "s_waitcnt lgkmcnt(2)\n\t"
  "v_mfma_f32_16x16x32_bf16 v[240:243], v[32:35], v[192:195], v[240:243]\n\t"
  "ds_read_b128 v[48:51], v16 offset:0\n\t"
  "v_cvt_pk_bf16_f32 v3, v214, v215\n\t"
  "v_mfma_f32_16x16x32_bf16 v[240:243], v[36:39], v[224:227], v[240:243]\n\t"
  "v_exp_f32 v216, v216\n\t"
  "v_mfma_f32_16x16x32_bf16 v[244:247], v[36:39], v[228:231], v[244:247]\n\t"
  "v_exp_f32 v217, v217\n\t"
  "v_mfma_f32_16x16x32_bf16 v[244:247], v[32:35], v[196:199], v[244:247]\n\t"
  "v_accvgpr_write_b32 a21, v3\n\t"
  "v_mfma_f32_16x16x32_bf16 v[248:251], v[32:35], v[200:203], v[248:251]\n\t"
  "ds_read_b128 v[52:55], v17 offset:0\n\t"
  "v_cvt_pk_bf16_f32 v0, v216, v217\n\t"
  "v_mfma_f32_16x16x32_bf16 v[248:251], v[36:39], v[232:235], v[248:251]\n\t"
  "v_exp_f32 v218, v218\n\t"
  "v_mfma_f32_16x16x32_bf16 v[252:255], v[36:39], v[236:239], v[252:255]\n\t"
  "v_exp_f32 v219, v219\n\t"
  "v_mfma_f32_16x16x32_bf16 v[252:255], v[32:35], v[204:207], v[252:255]\n\t"
  "v_accvgpr_write_b32 a24, v0\n\t"
  "s_waitcnt lgkmcnt(2)\n\t"
  "v_mfma_f32_16x16x32_bf16 v[240:243], v[40:43], v[80:83], v[240:243]\n\t"
  "ds_read_b128 v[32:35], v18 offset:0\n\t"
  "v_cvt_pk_bf16_f32 v1, v218, v219\n\t"
  "v_mfma_f32_16x16x32_bf16 v[240:243], v[44:47], v[144:147], v[240:243]\n\t"
  "v_exp_f32 v220, v220\n\t"
  "v_mfma_f32_16x16x32_bf16 v[244:247], v[44:47], v[148:151], v[244:247]\n\t"
  "v_exp_f32 v221, v221\n\t"
  "v_mfma_f32_16x16x32_bf16 v[244:247], v[40:43], v[84:87], v[244:247]\n\t"
  "v_accvgpr_write_b32 a25, v1\n\t"
  "v_mfma_f32_16x16x32_bf16 v[248:251], v[40:43], v[88:91], v[248:251]\n\t"
  "ds_read_b128 v[36:39], v19 offset:0\n\t"
  "v_cvt_pk_bf16_f32 v2, v220, v221\n\t"
  "v_mfma_f32_16x16x32_bf16 v[248:251], v[44:47], v[152:155], v[248:251]\n\t"
  "v_exp_f32 v222, v222\n\t"
  "v_mfma_f32_16x16x32_bf16 v[252:255], v[44:47], v[156:159], v[252:255]\n\t"
  "v_exp_f32 v223, v223\n\t"
  "v_mfma_f32_16x16x32_bf16 v[252:255], v[40:43], v[92:95], v[252:255]\n\t"
  "v_accvgpr_write_b32 a28, v2\n\t"
  "s_waitcnt lgkmcnt(2)\n\t"
  "v_mfma_f32_16x16x32_bf16 v[208:211], v[48:51], v[64:67], v[112:115]\n\t"
  "ds_read_b128 v[40:43], v16 offset:256\n\t"
  "v_cvt_pk_bf16_f32 v3, v222, v223\n\t"
  "v_mfma_f32_16x16x32_bf16 v[208:211], v[52:55], v[96:99], v[208:211]\n\t"
  "v_exp_f32 v240, v240\n\t"
  "v_mfma_f32_16x16x32_bf16 v[212:215], v[52:55], v[100:103], v[116:119]\n\t"
  "v_exp_f32 v241, v241\n\t"
  "v_mfma_f32_16x16x32_bf16 v[212:215], v[48:51], v[68:71], v[212:215]\n\t"
  "v_accvgpr_write_b32 a29, v3\n\t"
  "v_mfma_f32_16x16x32_bf16 v[216:219], v[48:51], v[72:75], v[120:123]\n\t"
  "ds_read_b128 v[44:47], v17 offset:256\n\t"
  "v_cvt_pk_bf16_f32 v0, v240, v241\n\t"
  "v_mfma_f32_16x16x32_bf16 v[216:219], v[52:55], v[104:107], v[216:219]\n\t"
  "v_exp_f32 v242, v242\n\t"
  "v_mfma_f32_16x16x32_bf16 v[220:223], v[52:55], v[108:111], v[124:127]\n\t"
  "v_exp_f32 v243, v243\n\t"
  "v_mfma_f32_16x16x32_bf16 v[220:223], v[48:51], v[76:79], v[220:223]\n\t"
  "v_accvgpr_write_b32 a18, v0\n\t"
  "s_waitcnt lgkmcnt(2)\n\t"
  "v_mfma_f32_16x16x32_bf16 v[208:211], v[32:35], v[128:131], v[208:211]\n\t"
  "ds_read_b128 v[48:51], v18 offset:256\n\t"
  "v_cvt_pk_bf16_f32 v1, v242, v243\n\t"
  "v_mfma_f32_16x16x32_bf16 v[208:211], v[36:39], v[160:163], v[208:211]\n\t"
  "v_exp_f32 v244, v244\n\t"
  "v_mfma_f32_16x16x32_bf16 v[212:215], v[36:39], v[164:167], v[212:215]\n\t"
  "v_exp_f32 v245, v245\n\t"
  "v_mfma_f32_16x16x32_bf16 v[212:215], v[32:35], v[132:135], v[212:215]\n\t"
  "v_accvgpr_write_b32 a19, v1\n\t"
  "v_mfma_f32_16x16x32_bf16 v[216:219], v[32:35], v[136:139], v[216:219]\n\t"
  "ds_read_b128 v[52:55], v19 offset:256\n\t"
  "v_cvt_pk_bf16_f32 v2, v244, v245\n\t"
  "v_mfma_f32_16x16x32_bf16 v[216:219], v[36:39], v[168:171], v[216:219]\n\t"
  "v_exp_f32 v246, v246\n\t"
  "v_mfma_f32_16x16x32_bf16 v[220:223], v[36:39], v[172:175], v[220:223]\n\t"
  "v_exp_f32 v247, v247\n\t"
  "v_mfma_f32_16x16x32_bf16 v[220:223], v[32:35], v[140:143], v[220:223]\n\t"
  "v_accvgpr_write_b32 a22, v2\n\t"
  "s_waitcnt lgkmcnt(2)\n\t"
  "v_mfma_f32_16x16x32_bf16 v[208:211], v[40:43], v[192:195], v[208:211]\n\t"
  "ds_read_b128 v[32:35], v16 offset:8192\n\t"
  "v_cvt_pk_bf16_f32 v3, v246, v247\n\t"
  "v_mfma_f32_16x16x32_bf16 v[208:211], v[44:47], v[224:227], v[208:211]\n\t"
  "v_exp_f32 v248, v248\n\t"
  "v_mfma_f32_16x16x32_bf16 v[212:215], v[44:47], v[228:231], v[212:215]\n\t"
  "v_exp_f32 v249, v249\n\t"
  "v_mfma_f32_16x16x32_bf16 v[212:215], v[40:43], v[196:199], v[212:215]\n\t"
  "v_accvgpr_write_b32 a23, v3\n\t"
  "v_mfma_f32_16x16x32_bf16 v[216:219], v[40:43], v[200:203], v[216:219]\n\t"
  "ds_read_b128 v[36:39], v17 offset:8192\n\t"
  "v_cvt_pk_bf16_f32 v0, v248, v249\n\t"
  "v_mfma_f32_16x16x32_bf16 v[216:219], v[44:47], v[232:235], v[216:219]\n\t"
  "v_exp_f32 v250, v250\n\t"
  "v_mfma_f32_16x16x32_bf16 v[220:223], v[44:47], v[236:239], v[220:223]\n\t"
  "v_exp_f32 v251, v251\n\t"
  "v_mfma_f32_16x16x32_bf16 v[220:223], v[40:43], v[204:207], v[220:223]\n\t"
  "v_accvgpr_write_b32 a26, v0\n\t"
  "s_waitcnt lgkmcnt(2)\n\t"
  "v_mfma_f32_16x16x32_bf16 v[208:211], v[48:51], v[80:83], v[208:211]\n\t"
  "ds_read_b128 v[40:43], v18 offset:8192\n\t"
  "v_cvt_pk_bf16_f32 v1, v250, v251\n\t"
  "v_mfma_f32_16x16x32_bf16 v[208:211], v[52:55], v[144:147], v[208:211]\n\t"
  "v_exp_f32 v252, v252\n\t"
  "v_mfma_f32_16x16x32_bf16 v[212:215], v[52:55], v[148:151], v[212:215]\n\t"
  "v_exp_f32 v253, v253\n\t"
  "v_mfma_f32_16x16x32_bf16 v[212:215], v[48:51], v[84:87], v[212:215]\n\t"
  "v_accvgpr_write_b32 a27, v1\n\t"
  "v_mfma_f32_16x16x32_bf16 v[216:219], v[48:51], v[88:91], v[216:219]\n\t"
  "ds_read_b128 v[44:47], v19 offset:8192\n\t"
  "v_cvt_pk_bf16_f32 v2, v252, v253\n\t"
  "v_mfma_f32_16x16x32_bf16 v[216:219], v[52:55], v[152:155], v[216:219]\n\t"
  "v_exp_f32 v254, v254\n\t"
  "v_mfma_f32_16x16x32_bf16 v[220:223], v[52:55], v[156:159], v[220:223]\n\t"
  "v_exp_f32 v255, v255\n\t"
  "v_mfma_f32_16x16x32_bf16 v[220:223], v[48:51], v[92:95], v[220:223]\n\t"
  "v_accvgpr_write_b32 a30, v2\n\t"
  "s_waitcnt lgkmcnt(2)\n\t"
  "v_mfma_f32_16x16x32_bf16 v[240:243], v[32:35], v[64:67], v[112:115]\n\t"
  "ds_read_b128 v[48:51], v16 offset:8448\n\t"
  "v_cvt_pk_bf16_f32 v3, v254, v255\n\t"
  "v_mfma_f32_16x16x32_bf16 v[240:243], v[36:39], v[96:99], v[240:243]\n\t"
  "v_exp_f32 v208, v208\n\t"
  "v_mfma_f32_16x16x32_bf16 v[244:247], v[36:39], v[100:103], v[116:119]\n\t"
  "v_exp_f32 v209, v209\n\t"
  "v_mfma_f32_16x16x32_bf16 v[244:247], v[32:35], v[68:71], v[244:247]\n\t"
  "v_accvgpr_write_b32 a31, v3\n\t"
  "v_mfma_f32_16x16x32_bf16 v[248:251], v[32:35], v[72:75], v[120:123]\n\t"
  "ds_read_b128 v[52:55], v17 offset:8448\n\t"
  "v_cvt_pk_bf16_f32 v0, v208, v209\n\t"
  "v_mfma_f32_16x16x32_bf16 v[248:251], v[36:39], v[104:107], v[248:251]\n\t"
  "v_exp_f32 v210, v210\n\t"
  "v_mfma_f32_16x16x32_bf16 v[252:255], v[36:39], v[108:111], v[124:127]\n\t"
  "v_exp_f32 v211, v211\n\t"
  "v_mfma_f32_16x16x32_bf16 v[252:255], v[32:35], v[76:79], v[252:255]\n\t"
  "v_accvgpr_write_b32 a32, v0\n\t"
  "s_waitcnt lgkmcnt(2)\n\t"
  "v_mfma_f32_16x16x32_bf16 v[240:243], v[40:43], v[128:131], v[240:243]\n\t"
  "ds_read_b128 v[32:35], v18 offset:8448\n\t"
  "v_cvt_pk_bf16_f32 v1, v210, v211\n\t"
  "v_mfma_f32_16x16x32_bf16 v[240:243], v[44:47], v[160:163], v[240:243]\n\t"
  "v_exp_f32 v212, v212\n\t"
  "v_mfma_f32_16x16x32_bf16 v[244:247], v[44:47], v[164:167], v[244:247]\n\t"
  "v_exp_f32 v213, v213\n\t"
  "v_mfma_f32_16x16x32_bf16 v[244:247], v[40:43], v[132:135], v[244:247]\n\t"
  "v_accvgpr_write_b32 a33, v1\n\t"
  "s_waitcnt vmcnt(22)\n\t"
  "s_barrier\n\t"
  "v_mfma_f32_16x16x32_bf16 v[248:251], v[40:43], v[136:139], v[248:251]\n\t"
  "ds_read_b128 v[36:39], v19 offset:8448\n\t"
  "v_cvt_pk_bf16_f32 v2, v212, v213\n\t"
  "v_mfma_f32_16x16x32_bf16 v[248:251], v[44:47], v[168:171], v[248:251]\n\t"
  "v_exp_f32 v214, v214\n\t"
  "v_mfma_f32_16x16x32_bf16 v[252:255], v[44:47], v[172:175], v[252:255]\n\t"
  "v_exp_f32 v215, v215\n\t"
  "v_mfma_f32_16x16x32_bf16 v[252:255], v[40:43], v[140:143], v[252:255]\n\t"
  "v_accvgpr_write_b32 a36, v2\n\t"
  "s_waitcnt lgkmcnt(2)\n\t"
  "v_mfma_f32_16x16x32_bf16 v[240:243], v[48:51], v[192:195], v[240:243]\n\t"
  "ds_read_b128 v[40:43], v16 offset:16384\n\t"
  "v_cvt_pk_bf16_f32 v3, v214, v215\n\t"
  "v_mfma_f32_16x16x32_bf16 v[240:243], v[52:55], v[224:227], v[240:243]\n\t"
  "v_exp_f32 v216, v216\n\t"
  "v_mfma_f32_16x16x32_bf16 v[244:247], v[52:55], v[228:231], v[244:247]\n\t"
  "v_exp_f32 v217, v217\n\t"
  "v_mfma_f32_16x16x32_bf16 v[244:247], v[48:51], v[196:199], v[244:247]\n\t"
  "v_accvgpr_write_b32 a37, v3\n\t"
  "s_add_u32 m0, s69, 98304\n\t"
  "v_mfma_f32_16x16x32_bf16 v[248:251], v[48:51], v[200:203], v[248:251]\n\t"
  "ds_read_b128 v[44:47], v17 offset:16384\n\t"
  "v_cvt_pk_bf16_f32 v0, v216, v217\n\t"
  "v_mfma_f32_16x16x32_bf16 v[248:251], v[52:55], v[232:235], v[248:251]\n\t"
  "v_exp_f32 v218, v218\n\t"
  "v_mfma_f32_16x16x32_bf16 v[252:255], v[52:55], v[236:239], v[252:255]\n\t"
  "v_exp_f32 v219, v219\n\t"
  "v_mfma_f32_16x16x32_bf16 v[252:255], v[48:51], v[204:207], v[252:255]\n\t"
  "v_accvgpr_write_b32 a40, v0\n\t"
  "s_waitcnt lgkmcnt(2)\n\t"
  "v_mfma_f32_16x16x32_bf16 v[240:243], v[32:35], v[80:83], v[240:243]\n\t"
  "ds_read_b128 v[48:51], v18 offset:16384\n\t"
  "v_cvt_pk_bf16_f32 v1, v218, v219\n\t"
  "v_mfma_f32_16x16x32_bf16 v[240:243], v[36:39], v[144:147], v[240:243]\n\t"
  "v_exp_f32 v220, v220\n\t"
  "v_mfma_f32_16x16x32_bf16 v[244:247], v[36:39], v[148:151], v[244:247]\n\t"
  "v_exp_f32 v221, v221\n\t"
  "v_mfma_f32_16x16x32_bf16 v[244:247], v[32:35], v[84:87], v[244:247]\n\t"
  "v_accvgpr_write_b32 a41, v1\n\t"
  "s_mov_b32 s70, 163840\n\t"
  "v_mfma_f32_16x16x32_bf16 v[248:251], v[32:35], v[88:91], v[248:251]\n\t"
  "ds_read_b128 v[52:55], v19 offset:16384\n\t"
  "v_cvt_pk_bf16_f32 v2, v220, v221\n\t"
  "v_mfma_f32_16x16x32_bf16 v[248:251], v[36:39], v[152:155], v[248:251]\n\t"
  "v_exp_f32 v222, v222\n\t"
  "v_mfma_f32_16x16x32_bf16 v[252:255], v[36:39], v[156:159], v[252:255]\n\t"
  "v_exp_f32 v223, v223\n\t"
  "v_mfma_f32_16x16x32_bf16 v[252:255], v[32:35], v[92:95], v[252:255]\n\t"
  "v_accvgpr_write_b32 a44, v2\n\t"
  "s_waitcnt lgkmcnt(2)\n\t"
  "v_mfma_f32_16x16x32_bf16 v[208:211], v[40:43], v[64:67], v[112:115]\n\t"
  "ds_read_b128 v[32:35], v16 offset:16640\n\t"
  "v_cvt_pk_bf16_f32 v3, v222, v223\n\t"
  "v_mfma_f32_16x16x32_bf16 v[208:211], v[44:47], v[96:99], v[208:211]\n\t"
  "v_exp_f32 v240, v240\n\t"
  "v_mfma_f32_16x16x32_bf16 v[212:215], v[44:47], v[100:103], v[116:119]\n\t"
  "v_exp_f32 v241, v241\n\t"
  "v_mfma_f32_16x16x32_bf16 v[212:215], v[40:43], v[68:71], v[212:215]\n\t"
  "buffer_load_dwordx4 v6, s[48:51], s70 offen offset:0 nt lds\n\t"
  "v_accvgpr_write_b32 a45, v3\n\t"
  "v_mfma_f32_16x16x32_bf16 v[216:219], v[40:43], v[72:75], v[120:123]\n\t"
  "ds_read_b128 v[36:39], v17 offset:16640\n\t"
  "v_cvt_pk_bf16_f32 v0, v240, v241\n\t"
  "v_mfma_f32_16x16x32_bf16 v[216:219], v[44:47], v[104:107], v[216:219]\n\t"
  "v_exp_f32 v242, v242\n\t"
  "v_mfma_f32_16x16x32_bf16 v[220:223], v[44:47], v[108:111], v[124:127]\n\t"
  "v_exp_f32 v243, v243\n\t"
  "v_mfma_f32_16x16x32_bf16 v[220:223], v[40:43], v[76:79], v[220:223]\n\t"
  "v_accvgpr_write_b32 a34, v0\n\t"
  "s_waitcnt lgkmcnt(2)\n\t"
  "v_mfma_f32_16x16x32_bf16 v[208:211], v[48:51], v[128:131], v[208:211]\n\t"
  "ds_read_b128 v[40:43], v18 offset:16640\n\t"
  "v_cvt_pk_bf16_f32 v1, v242, v243\n\t"
  "v_mfma_f32_16x16x32_bf16 v[208:211], v[52:55], v[160:163], v[208:211]\n\t"
  "v_exp_f32 v244, v244\n\t"
  "v_mfma_f32_16x16x32_bf16 v[212:215], v[52:55], v[164:167], v[212:215]\n\t"
  "v_exp_f32 v245, v245\n\t"
  "v_mfma_f32_16x16x32_bf16 v[212:215], v[48:51], v[132:135], v[212:215]\n\t"
  "buffer_load_dwordx4 v7, s[48:51], s70 offen offset:1024 nt lds\n\t"
  "v_accvgpr_write_b32 a35, v1\n\t"
  "v_mfma_f32_16x16x32_bf16 v[216:219], v[48:51], v[136:139], v[216:219]\n\t"
  "ds_read_b128 v[44:47], v19 offset:16640\n\t"
  "v_cvt_pk_bf16_f32 v2, v244, v245\n\t"
  "v_mfma_f32_16x16x32_bf16 v[216:219], v[52:55], v[168:171], v[216:219]\n\t"
  "v_exp_f32 v246, v246\n\t"
  "v_mfma_f32_16x16x32_bf16 v[220:223], v[52:55], v[172:175], v[220:223]\n\t"
  "v_exp_f32 v247, v247\n\t"
  "v_mfma_f32_16x16x32_bf16 v[220:223], v[48:51], v[140:143], v[220:223]\n\t"
  "v_accvgpr_write_b32 a38, v2\n\t"
  "s_waitcnt lgkmcnt(2)\n\t"
  "v_mfma_f32_16x16x32_bf16 v[208:211], v[32:35], v[192:195], v[208:211]\n\t"
  "ds_read_b128 v[48:51], v16 offset:24576\n\t"
  "v_cvt_pk_bf16_f32 v3, v246, v247\n\t"
  "v_mfma_f32_16x16x32_bf16 v[208:211], v[36:39], v[224:227], v[208:211]\n\t"
  "v_exp_f32 v248, v248\n\t"
  "v_mfma_f32_16x16x32_bf16 v[212:215], v[36:39], v[228:231], v[212:215]\n\t"
  "v_exp_f32 v249, v249\n\t"
  "v_mfma_f32_16x16x32_bf16 v[212:215], v[32:35], v[196:199], v[212:215]\n\t"
  "buffer_load_dwordx4 v8, s[48:51], s70 offen offset:2048 nt lds\n\t"
  "v_accvgpr_write_b32 a39, v3\n\t"
  "v_mfma_f32_16x16x32_bf16 v[216:219], v[32:35], v[200:203], v[216:219]\n\t"
  "ds_read_b128 v[52:55], v17 offset:24576\n\t"
  "v_cvt_pk_bf16_f32 v0, v248, v249\n\t"
  "v_mfma_f32_16x16x32_bf16 v[216:219], v[36:39], v[232:235], v[216:219]\n\t"
  "v_exp_f32 v250, v250\n\t"
  "v_mfma_f32_16x16x32_bf16 v[220:223], v[36:39], v[236:239], v[220:223]\n\t"
  "v_exp_f32 v251, v251\n\t"
  "v_mfma_f32_16x16x32_bf16 v[220:223], v[32:35], v[204:207], v[220:223]\n\t"
  "v_accvgpr_write_b32 a42, v0\n\t"
  "s_waitcnt lgkmcnt(2)\n\t"
  "v_mfma_f32_16x16x32_bf16 v[208:211], v[40:43], v[80:83], v[208:211]\n\t"
  "ds_read_b128 v[32:35], v18 offset:24576\n\t"
  "v_cvt_pk_bf16_f32 v1, v250, v251\n\t"
  "v_mfma_f32_16x16x32_bf16 v[208:211], v[44:47], v[144:147], v[208:211]\n\t"
  "v_exp_f32 v252, v252\n\t"
  "v_mfma_f32_16x16x32_bf16 v[212:215], v[44:47], v[148:151], v[212:215]\n\t"
  "v_exp_f32 v253, v253\n\t"
  "v_mfma_f32_16x16x32_bf16 v[212:215], v[40:43], v[84:87], v[212:215]\n\t"
  "buffer_load_dwordx4 v9, s[48:51], s70 offen offset:3072 nt lds\n\t"
  "v_accvgpr_write_b32 a43, v1\n\t"
  "v_mfma_f32_16x16x32_bf16 v[216:219], v[40:43], v[88:91], v[216:219]\n\t"
  "ds_read_b128 v[36:39], v19 offset:24576\n\t"
  "v_cvt_pk_bf16_f32 v2, v252, v253\n\t"
  "v_mfma_f32_16x16x32_bf16 v[216:219], v[44:47], v[152:155], v[216:219]\n\t"
  "v_exp_f32 v254, v254\n\t"
  "v_mfma_f32_16x16x32_bf16 v[220:223], v[44:47], v[156:159], v[220:223]\n\t"
  "v_exp_f32 v255, v255\n\t"
  "v_mfma_f32_16x16x32_bf16 v[220:223], v[40:43], v[92:95], v[220:223]\n\t"
  "v_accvgpr_write_b32 a46, v2\n\t"
  "s_waitcnt lgkmcnt(2)\n\t"
  "v_mfma_f32_16x16x32_bf16 v[240:243], v[48:51], v[64:67], v[112:115]\n\t"
  "ds_read_b128 v[40:43], v16 offset:24832\n\t"
  "v_cvt_pk_bf16_f32 v3, v254, v255\n\t"
  "v_mfma_f32_16x16x32_bf16 v[240:243], v[52:55], v[96:99], v[240:243]\n\t"
  "v_exp_f32 v208, v208\n\t"
  "v_mfma_f32_16x16x32_bf16 v[244:247], v[52:55], v[100:103], v[116:119]\n\t"
  "v_exp_f32 v209, v209\n\t"
  "v_mfma_f32_16x16x32_bf16 v[244:247], v[48:51], v[68:71], v[244:247]\n\t"
  "v_accvgpr_write_b32 a47, v3\n\t"
  "s_add_u32 m0, s69, 114688\n\t"
  "v_mfma_f32_16x16x32_bf16 v[248:251], v[48:51], v[72:75], v[120:123]\n\t"
  "ds_read_b128 v[44:47], v17 offset:24832\n\t"
  "v_cvt_pk_bf16_f32 v0, v208, v209\n\t"
  "v_mfma_f32_16x16x32_bf16 v[248:251], v[52:55], v[104:107], v[248:251]\n\t"
  "v_exp_f32 v210, v210\n\t"
  "v_mfma_f32_16x16x32_bf16 v[252:255], v[52:55], v[108:111], v[124:127]\n\t"
  "v_exp_f32 v211, v211\n\t"
  "v_mfma_f32_16x16x32_bf16 v[252:255], v[48:51], v[76:79], v[252:255]\n\t"
  "v_accvgpr_write_b32 a48, v0\n\t"
  "s_waitcnt lgkmcnt(2)\n\t"
  "v_mfma_f32_16x16x32_bf16 v[240:243], v[32:35], v[128:131], v[240:243]\n\t"
  "ds_read_b128 v[48:51], v18 offset:24832\n\t"
  "v_cvt_pk_bf16_f32 v1, v210, v211\n\t"
  "v_mfma_f32_16x16x32_bf16 v[240:243], v[36:39], v[160:163], v[240:243]\n\t"
  "v_exp_f32 v212, v212\n\t"
  "v_mfma_f32_16x16x32_bf16 v[244:247], v[36:39], v[164:167], v[244:247]\n\t"
  "v_exp_f32 v213, v213\n\t"
  "v_mfma_f32_16x16x32_bf16 v[244:247], v[32:35], v[132:135], v[244:247]\n\t"
  "v_accvgpr_write_b32 a49, v1\n\t"
  "s_mov_b32 s70, 180224\n\t"
  "v_mfma_f32_16x16x32_bf16 v[248:251], v[32:35], v[136:139], v[248:251]\n\t"
  "ds_read_b128 v[52:55], v19 offset:24832\n\t"
  "v_cvt_pk_bf16_f32 v2, v212, v213\n\t"
  "v_add_u32 v16, 0xffff0000, v16\n\t"
  "v_mfma_f32_16x16x32_bf16 v[248:251], v[36:39], v[168:171], v[248:251]\n\t"
  "v_exp_f32 v214, v214\n\t"
  "v_add_u32 v17, 0xffff0000, v17\n\t"
  "v_mfma_f32_16x16x32_bf16 v[252:255], v[36:39], v[172:175], v[252:255]\n\t"
  "v_exp_f32 v215, v215\n\t"
  "v_add_u32 v18, 0xffff0000, v18\n\t"
  "v_mfma_f32_16x16x32_bf16 v[252:255], v[32:35], v[140:143], v[252:255]\n\t"
  "v_accvgpr_write_b32 a52, v2\n\t"
  "v_add_u32 v19, 0xffff0000, v19\n\t"
  "s_waitcnt lgkmcnt(2)\n\t"
  "v_mfma_f32_16x16x32_bf16 v[240:243], v[40:43], v[192:195], v[240:243]\n\t"
  "ds_read_b128 v[32:35], v16 offset:0\n\t"
  "v_cvt_pk_bf16_f32 v3, v214, v215\n\t"
  "v_mfma_f32_16x16x32_bf16 v[240:243], v[44:47], v[224:227], v[240:243]\n\t"
  "v_exp_f32 v216, v216\n\t"
  "v_mfma_f32_16x16x32_bf16 v[244:247], v[44:47], v[228:231], v[244:247]\n\t"
  "v_exp_f32 v217, v217\n\t"
  "v_mfma_f32_16x16x32_bf16 v[244:247], v[40:43], v[196:199], v[244:247]\n\t"
  "buffer_load_dwordx4 v6, s[48:51], s70 offen offset:0 nt lds\n\t"
  "v_accvgpr_write_b32 a53, v3\n\t"
  "v_mfma_f32_16x16x32_bf16 v[248:251], v[40:43], v[200:203], v[248:251]\n\t"
  "ds_read_b128 v[36:39], v17 offset:0\n\t"
  "v_cvt_pk_bf16_f32 v0, v216, v217\n\t"
  "v_mfma_f32_16x16x32_bf16 v[248:251], v[44:47], v[232:235], v[248:251]\n\t"
  "v_exp_f32 v218, v218\n\t"
  "v_mfma_f32_16x16x32_bf16 v[252:255], v[44:47], v[236:239], v[252:255]\n\t"
  "v_exp_f32 v219, v219\n\t"
  "v_mfma_f32_16x16x32_bf16 v[252:255], v[40:43], v[204:207], v[252:255]\n\t"
  "v_accvgpr_write_b32 a56, v0\n\t"
  "s_waitcnt lgkmcnt(2)\n\t"
  "v_mfma_f32_16x16x32_bf16 v[240:243], v[48:51], v[80:83], v[240:243]\n\t"
  "ds_read_b128 v[40:43], v18 offset:0\n\t"
  "v_cvt_pk_bf16_f32 v1, v218, v219\n\t"
  "v_mfma_f32_16x16x32_bf16 v[240:243], v[52:55], v[144:147], v[240:243]\n\t"
  "v_exp_f32 v220, v220\n\t"
  "v_mfma_f32_16x16x32_bf16 v[244:247], v[52:55], v[148:151], v[244:247]\n\t"
  "v_exp_f32 v221, v221\n\t"
  "v_mfma_f32_16x16x32_bf16 v[244:247], v[48:51], v[84:87], v[244:247]\n\t"
  "buffer_load_dwordx4 v7, s[48:51], s70 offen offset:1024 nt lds\n\t"
  "v_accvgpr_write_b32 a57, v1\n\t"
  "v_mfma_f32_16x16x32_bf16 v[248:251], v[48:51], v[88:91], v[248:251]\n\t"
  "ds_read_b128 v[44:47], v19 offset:0\n\t"
  "v_cvt_pk_bf16_f32 v2, v220, v221\n\t"
  "v_mfma_f32_16x16x32_bf16 v[248:251], v[52:55], v[152:155], v[248:251]\n\t"
  "v_exp_f32 v222, v222\n\t"
  "v_mfma_f32_16x16x32_bf16 v[252:255], v[52:55], v[156:159], v[252:255]\n\t"
  "v_exp_f32 v223, v223\n\t"
  "v_mfma_f32_16x16x32_bf16 v[252:255], v[48:51], v[92:95], v[252:255]\n\t"
  "v_accvgpr_write_b32 a60, v2\n\t"
  "s_waitcnt lgkmcnt(2)\n\t"
  "v_mfma_f32_16x16x32_bf16 v[208:211], v[32:35], v[64:67], v[112:115]\n\t"
  "ds_read_b128 v[48:51], v16 offset:256\n\t"
  "v_cvt_pk_bf16_f32 v3, v222, v223\n\t"
  "v_mfma_f32_16x16x32_bf16 v[208:211], v[36:39], v[96:99], v[208:211]\n\t"
  "v_exp_f32 v240, v240\n\t"
  "v_mfma_f32_16x16x32_bf16 v[212:215], v[36:39], v[100:103], v[116:119]\n\t"
  "v_exp_f32 v241, v241\n\t"
  "v_mfma_f32_16x16x32_bf16 v[212:215], v[32:35], v[68:71], v[212:215]\n\t"
  "buffer_load_dwordx4 v8, s[48:51], s70 offen offset:2048 nt lds\n\t"
  "v_accvgpr_write_b32 a61, v3\n\t"
  "v_mfma_f32_16x16x32_bf16 v[216:219], v[32:35], v[72:75], v[120:123]\n\t"
  "ds_read_b128 v[52:55], v17 offset:256\n\t"
  "v_cvt_pk_bf16_f32 v0, v240, v241\n\t"
  "v_mfma_f32_16x16x32_bf16 v[216:219], v[36:39], v[104:107], v[216:219]\n\t"
  "v_exp_f32 v242, v242\n\t"
  "v_mfma_f32_16x16x32_bf16 v[220:223], v[36:39], v[108:111], v[124:127]\n\t"
  "v_exp_f32 v243, v243\n\t"
  "v_mfma_f32_16x16x32_bf16 v[220:223], v[32:35], v[76:79], v[220:223]\n\t"
  "v_accvgpr_write_b32 a50, v0\n\t"
  "s_waitcnt lgkmcnt(2)\n\t"
  "v_mfma_f32_16x16x32_bf16 v[208:211], v[40:43], v[128:131], v[208:211]\n\t"
  "ds_read_b128 v[32:35], v18 offset:256\n\t"
  "v_cvt_pk_bf16_f32 v1, v242, v243\n\t"
  "v_mfma_f32_16x16x32_bf16 v[208:211], v[44:47], v[160:163], v[208:211]\n\t"
  "v_exp_f32 v244, v244\n\t"
  "v_mfma_f32_16x16x32_bf16 v[212:215], v[44:47], v[164:167], v[212:215]\n\t"
  "v_exp_f32 v245, v245\n\t"
  "v_mfma_f32_16x16x32_bf16 v[212:215], v[40:43], v[132:135], v[212:215]\n\t"
  "buffer_load_dwordx4 v9, s[48:51], s70 offen offset:3072 nt lds\n\t"
  "v_accvgpr_write_b32 a51, v1\n\t"
  "v_mfma_f32_16x16x32_bf16 v[216:219], v[40:43], v[136:139], v[216:219]\n\t"
  "ds_read_b128 v[36:39], v19 offset:256\n\t"
  "v_cvt_pk_bf16_f32 v2, v244, v245\n\t"
  "v_mfma_f32_16x16x32_bf16 v[216:219], v[44:47], v[168:171], v[216:219]\n\t"
  "v_exp_f32 v246, v246\n\t"
  "v_mfma_f32_16x16x32_bf16 v[220:223], v[44:47], v[172:175], v[220:223]\n\t"
  "v_exp_f32 v247, v247\n\t"
  "v_mfma_f32_16x16x32_bf16 v[220:223], v[40:43], v[140:143], v[220:223]\n\t"
  "v_accvgpr_write_b32 a54, v2\n\t"
  "s_waitcnt lgkmcnt(2)\n\t"
  "v_mfma_f32_16x16x32_bf16 v[208:211], v[48:51], v[192:195], v[208:211]\n\t"
  "ds_read_b128 v[40:43], v16 offset:8192\n\t"
  "v_cvt_pk_bf16_f32 v3, v246, v247\n\t"
  "v_mfma_f32_16x16x32_bf16 v[208:211], v[52:55], v[224:227], v[208:211]\n\t"
  "v_exp_f32 v248, v248\n\t"
  "v_mfma_f32_16x16x32_bf16 v[212:215], v[52:55], v[228:231], v[212:215]\n\t"
  "v_exp_f32 v249, v249\n\t"
  "v_mfma_f32_16x16x32_bf16 v[212:215], v[48:51], v[196:199], v[212:215]\n\t"
  "buffer_load_dword v11, v10, s[60:63], 0 offen offset:1024\n\t"
  "v_accvgpr_write_b32 a55, v3\n\t"
  "v_mfma_f32_16x16x32_bf16 v[216:219], v[48:51], v[200:203], v[216:219]\n\t"
  "ds_read_b128 v[44:47], v17 offset:8192\n\t"
  "v_cvt_pk_bf16_f32 v0, v248, v249\n\t"
  "v_mfma_f32_16x16x32_bf16 v[216:219], v[52:55], v[232:235], v[216:219]\n\t"
  "v_exp_f32 v250, v250\n\t"
  "v_mfma_f32_16x16x32_bf16 v[220:223], v[52:55], v[236:239], v[220:223]\n\t"
  "v_exp_f32 v251, v251\n\t"
  "v_mfma_f32_16x16x32_bf16 v[220:223], v[48:51], v[204:207], v[220:223]\n\t"
  "v_accvgpr_write_b32 a58, v0\n\t"
  "s_waitcnt lgkmcnt(2)\n\t"
  "v_mfma_f32_16x16x32_bf16 v[208:211], v[32:35], v[80:83], v[208:211]\n\t"
  "ds_read_b128 v[48:51], v18 offset:8192\n\t"
  "v_cvt_pk_bf16_f32 v1, v250, v251\n\t"
  "v_mfma_f32_16x16x32_bf16 v[208:211], v[36:39], v[144:147], v[208:211]\n\t"
  "v_exp_f32 v252, v252\n\t"
  "v_mfma_f32_16x16x32_bf16 v[212:215], v[36:39], v[148:151], v[212:215]\n\t"
  "v_exp_f32 v253, v253\n\t"
  "v_mfma_f32_16x16x32_bf16 v[212:215], v[32:35], v[84:87], v[212:215]\n\t"
  "v_accvgpr_write_b32 a59, v1\n\t"
  "v_mfma_f32_16x16x32_bf16 v[216:219], v[32:35], v[88:91], v[216:219]\n\t"
  "ds_read_b128 v[52:55], v19 offset:8192\n\t"
  "v_cvt_pk_bf16_f32 v2, v252, v253\n\t"
  "v_mfma_f32_16x16x32_bf16 v[216:219], v[36:39], v[152:155], v[216:219]\n\t"
  "v_exp_f32 v254, v254\n\t"
  "v_mfma_f32_16x16x32_bf16 v[220:223], v[36:39], v[156:159], v[220:223]\n\t"
  "v_exp_f32 v255, v255\n\t"
  "v_mfma_f32_16x16x32_bf16 v[220:223], v[32:35], v[92:95], v[220:223]\n\t"
  "v_accvgpr_write_b32 a62, v2\n\t"
  "s_waitcnt lgkmcnt(2)\n\t"
  "v_mfma_f32_16x16x32_bf16 v[240:243], v[40:43], v[64:67], v[112:115]\n\t"
  "ds_read_b128 v[32:35], v16 offset:8448\n\t"
  "v_cvt_pk_bf16_f32 v3, v254, v255\n\t"
  "v_mfma_f32_16x16x32_bf16 v[240:243], v[44:47], v[96:99], v[240:243]\n\t"
  "v_exp_f32 v208, v208\n\t"
  "v_mfma_f32_16x16x32_bf16 v[244:247], v[44:47], v[100:103], v[116:119]\n\t"
  "v_exp_f32 v209, v209\n\t"
  "v_mfma_f32_16x16x32_bf16 v[244:247], v[40:43], v[68:71], v[244:247]\n\t"
  "v_accvgpr_write_b32 a63, v3\n\t"
  "v_mfma_f32_16x16x32_bf16 v[248:251], v[40:43], v[72:75], v[120:123]\n\t"
  "ds_read_b128 v[36:39], v17 offset:8448\n\t"
  "v_cvt_pk_bf16_f32 v0, v208, v209\n\t"
  "v_mfma_f32_16x16x32_bf16 v[248:251], v[44:47], v[104:107], v[248:251]\n\t"
  "v_exp_f32 v210, v210\n\t"
  "v_mfma_f32_16x16x32_bf16 v[252:255], v[44:47], v[108:111], v[124:127]\n\t"
  "v_exp_f32 v211, v211\n\t"
  "v_mfma_f32_16x16x32_bf16 v[252:255], v[40:43], v[76:79], v[252:255]\n\t"
  "v_accvgpr_write_b32 a64, v0\n\t"
  "s_waitcnt lgkmcnt(2)\n\t"
  "v_mfma_f32_16x16x32_bf16 v[240:243], v[48:51], v[128:131], v[240:243]\n\t"
  "ds_read_b128 v[40:43], v18 offset:8448\n\t"
  "v_cvt_pk_bf16_f32 v1, v210, v211\n\t"
  "v_mfma_f32_16x16x32_bf16 v[240:243], v[52:55], v[160:163], v[240:243]\n\t"
  "v_exp_f32 v212, v212\n\t"
  "v_mfma_f32_16x16x32_bf16 v[244:247], v[52:55], v[164:167], v[244:247]\n\t"
  "v_exp_f32 v213, v213\n\t"
  "v_mfma_f32_16x16x32_bf16 v[244:247], v[48:51], v[132:135], v[244:247]\n\t"
  "v_accvgpr_write_b32 a65, v1\n\t"
  "s_waitcnt vmcnt(23)\n\t"
  "s_barrier\n\t"
  "v_mfma_f32_16x16x32_bf16 v[248:251], v[48:51], v[136:139], v[248:251]\n\t"
  "ds_read_b128 v[44:47], v19 offset:8448\n\t"
  "v_cvt_pk_bf16_f32 v2, v212, v213\n\t"
  "v_mfma_f32_16x16x32_bf16 v[248:251], v[52:55], v[168:171], v[248:251]\n\t"
  "v_exp_f32 v214, v214\n\t"
  "v_mfma_f32_16x16x32_bf16 v[252:255], v[52:55], v[172:175], v[252:255]\n\t"
  "v_exp_f32 v215, v215\n\t"
  "v_mfma_f32_16x16x32_bf16 v[252:255], v[48:51], v[140:143], v[252:255]\n\t"
  "v_accvgpr_write_b32 a68, v2\n\t"
  "s_waitcnt lgkmcnt(2)\n\t"
  "v_mfma_f32_16x16x32_bf16 v[240:243], v[32:35], v[192:195], v[240:243]\n\t"
  "ds_read_b128 v[48:51], v16 offset:16384\n\t"
  "v_cvt_pk_bf16_f32 v3, v214, v215\n\t"
  "v_mfma_f32_16x16x32_bf16 v[240:243], v[36:39], v[224:227], v[240:243]\n\t"
  "v_exp_f32 v216, v216\n\t"
  "v_mfma_f32_16x16x32_bf16 v[244:247], v[36:39], v[228:231], v[244:247]\n\t"
  "v_exp_f32 v217, v217\n\t"
  "v_mfma_f32_16x16x32_bf16 v[244:247], v[32:35], v[196:199], v[244:247]\n\t"
  "v_accvgpr_write_b32 a69, v3\n\t"
  "s_add_u32 m0, s69, 131072\n\t"
  "v_mfma_f32_16x16x32_bf16 v[248:251], v[32:35], v[200:203], v[248:251]\n\t"
  "ds_read_b128 v[52:55], v17 offset:16384\n\t"
  "v_cvt_pk_bf16_f32 v0, v216, v217\n\t"
  "v_mfma_f32_16x16x32_bf16 v[248:251], v[36:39], v[232:235], v[248:251]\n\t"
  "v_exp_f32 v218, v218\n\t"
  "v_mfma_f32_16x16x32_bf16 v[252:255], v[36:39], v[236:239], v[252:255]\n\t"
  "v_exp_f32 v219, v219\n\t"
  "v_mfma_f32_16x16x32_bf16 v[252:255], v[32:35], v[204:207], v[252:255]\n\t"
  "v_accvgpr_write_b32 a72, v0\n\t"
  "s_waitcnt lgkmcnt(2)\n\t"
  "v_mfma_f32_16x16x32_bf16 v[240:243], v[40:43], v[80:83], v[240:243]\n\t"
  "ds_read_b128 v[32:35], v18 offset:16384\n\t"
  "v_cvt_pk_bf16_f32 v1, v218, v219\n\t"
  "v_mfma_f32_16x16x32_bf16 v[240:243], v[44:47], v[144:147], v[240:243]\n\t"
  "v_exp_f32 v220, v220\n\t"
  "v_mfma_f32_16x16x32_bf16 v[244:247], v[44:47], v[148:151], v[244:247]\n\t"
  "v_exp_f32 v221, v221\n\t"
  "v_mfma_f32_16x16x32_bf16 v[244:247], v[40:43], v[84:87], v[244:247]\n\t"
  "v_accvgpr_write_b32 a73, v1\n\t"
  "s_mov_b32 s70, 196608\n\t"
  "v_mfma_f32_16x16x32_bf16 v[248:251], v[40:43], v[88:91], v[248:251]\n\t"
  "ds_read_b128 v[36:39], v19 offset:16384\n\t"
  "v_cvt_pk_bf16_f32 v2, v220, v221\n\t"
  "v_mfma_f32_16x16x32_bf16 v[248:251], v[44:47], v[152:155], v[248:251]\n\t"
  "v_exp_f32 v222, v222\n\t"
  "v_mfma_f32_16x16x32_bf16 v[252:255], v[44:47], v[156:159], v[252:255]\n\t"
  "v_exp_f32 v223, v223\n\t"
  "v_mfma_f32_16x16x32_bf16 v[252:255], v[40:43], v[92:95], v[252:255]\n\t"
  "v_accvgpr_write_b32 a76, v2\n\t"
  "s_waitcnt lgkmcnt(2)\n\t"
  "v_mfma_f32_16x16x32_bf16 v[208:211], v[48:51], v[64:67], v[112:115]\n\t"
  "ds_read_b128 v[40:43], v16 offset:16640\n\t"
  "v_cvt_pk_bf16_f32 v3, v222, v223\n\t"
  "v_mfma_f32_16x16x32_bf16 v[208:211], v[52:55], v[96:99], v[208:211]\n\t"
  "v_exp_f32 v240, v240\n\t"
  "v_mfma_f32_16x16x32_bf16 v[212:215], v[52:55], v[100:103], v[116:119]\n\t"
  "v_exp_f32 v241, v241\n\t"
  "v_mfma_f32_16x16x32_bf16 v[212:215], v[48:51], v[68:71], v[212:215]\n\t"
  "buffer_load_dwordx4 v6, s[48:51], s70 offen offset:0 nt lds\n\t"
  "v_accvgpr_write_b32 a77, v3\n\t"
  "v_mfma_f32_16x16x32_bf16 v[216:219], v[48:51], v[72:75], v[120:123]\n\t"
  "ds_read_b128 v[44:47], v17 offset:16640\n\t"
  "v_cvt_pk_bf16_f32 v0, v240, v241\n\t"
  "v_mfma_f32_16x16x32_bf16 v[216:219], v[52:55], v[104:107], v[216:219]\n\t"
  "v_exp_f32 v242, v242\n\t"
  "v_mfma_f32_16x16x32_bf16 v[220:223], v[52:55], v[108:111], v[124:127]\n\t"
  "v_exp_f32 v243, v243\n\t"
  "v_mfma_f32_16x16x32_bf16 v[220:223], v[48:51], v[76:79], v[220:223]\n\t"
  "v_accvgpr_write_b32 a66, v0\n\t"
  "s_waitcnt lgkmcnt(2)\n\t"
  "v_mfma_f32_16x16x32_bf16 v[208:211], v[32:35], v[128:131], v[208:211]\n\t"
  "ds_read_b128 v[48:51], v18 offset:16640\n\t"
  "v_cvt_pk_bf16_f32 v1, v242, v243\n\t"
  "v_mfma_f32_16x16x32_bf16 v[208:211], v[36:39], v[160:163], v[208:211]\n\t"
  "v_exp_f32 v244, v244\n\t"
  "v_mfma_f32_16x16x32_bf16 v[212:215], v[36:39], v[164:167], v[212:215]\n\t"
  "v_exp_f32 v245, v245\n\t"
  "v_mfma_f32_16x16x32_bf16 v[212:215], v[32:35], v[132:135], v[212:215]\n\t"
  "buffer_load_dwordx4 v7, s[48:51], s70 offen offset:1024 nt lds\n\t"
  "v_accvgpr_write_b32 a67, v1\n\t"
  "v_mfma_f32_16x16x32_bf16 v[216:219], v[32:35], v[136:139], v[216:219]\n\t"
  "ds_read_b128 v[52:55], v19 offset:16640\n\t"
  "v_cvt_pk_bf16_f32 v2, v244, v245\n\t"
  "v_mfma_f32_16x16x32_bf16 v[216:219], v[36:39], v[168:171], v[216:219]\n\t"
  "v_exp_f32 v246, v246\n\t"
  "v_mfma_f32_16x16x32_bf16 v[220:223], v[36:39], v[172:175], v[220:223]\n\t"
  "v_exp_f32 v247, v247\n\t"
  "v_mfma_f32_16x16x32_bf16 v[220:223], v[32:35], v[140:143], v[220:223]\n\t"
  "v_accvgpr_write_b32 a70, v2\n\t"
  "s_waitcnt lgkmcnt(2)\n\t"
  "v_mfma_f32_16x16x32_bf16 v[208:211], v[40:43], v[192:195], v[208:211]\n\t"
  "ds_read_b128 v[32:35], v16 offset:24576\n\t"
  "v_cvt_pk_bf16_f32 v3, v246, v247\n\t"
  "v_mfma_f32_16x16x32_bf16 v[208:211], v[44:47], v[224:227], v[208:211]\n\t"
  "v_exp_f32 v248, v248\n\t"
  "v_mfma_f32_16x16x32_bf16 v[212:215], v[44:47], v[228:231], v[212:215]\n\t"
  "v_exp_f32 v249, v249\n\t"
  "v_mfma_f32_16x16x32_bf16 v[212:215], v[40:43], v[196:199], v[212:215]\n\t"
  "buffer_load_dwordx4 v8, s[48:51], s70 offen offset:2048 nt lds\n\t"
  "v_accvgpr_write_b32 a71, v3\n\t"
  "v_mfma_f32_16x16x32_bf16 v[216:219], v[40:43], v[200:203], v[216:219]\n\t"
  "ds_read_b128 v[36:39], v17 offset:24576\n\t"
  "v_cvt_pk_bf16_f32 v0, v248, v249\n\t"
  "v_mfma_f32_16x16x32_bf16 v[216:219], v[44:47], v[232:235], v[216:219]\n\t"
  "v_exp_f32 v250, v250\n\t"
  "v_mfma_f32_16x16x32_bf16 v[220:223], v[44:47], v[236:239], v[220:223]\n\t"
  "v_exp_f32 v251, v251\n\t"
  "v_mfma_f32_16x16x32_bf16 v[220:223], v[40:43], v[204:207], v[220:223]\n\t"
  "v_accvgpr_write_b32 a74, v0\n\t"
  "s_waitcnt lgkmcnt(2)\n\t"
  "v_mfma_f32_16x16x32_bf16 v[208:211], v[48:51], v[80:83], v[208:211]\n\t"
  "ds_read_b128 v[40:43], v18 offset:24576\n\t"
  "v_cvt_pk_bf16_f32 v1, v250, v251\n\t"
  "v_mfma_f32_16x16x32_bf16 v[208:211], v[52:55], v[144:147], v[208:211]\n\t"
  "v_exp_f32 v252, v252\n\t"
  "v_mfma_f32_16x16x32_bf16 v[212:215], v[52:55], v[148:151], v[212:215]\n\t"
  "v_exp_f32 v253, v253\n\t"
  "v_mfma_f32_16x16x32_bf16 v[212:215], v[48:51], v[84:87], v[212:215]\n\t"
  "buffer_load_dwordx4 v9, s[48:51], s70 offen offset:3072 nt lds\n\t"
  "v_accvgpr_write_b32 a75, v1\n\t"
  "v_mfma_f32_16x16x32_bf16 v[216:219], v[48:51], v[88:91], v[216:219]\n\t"
  "ds_read_b128 v[44:47], v19 offset:24576\n\t"
  "v_cvt_pk_bf16_f32 v2, v252, v253\n\t"
  "v_mfma_f32_16x16x32_bf16 v[216:219], v[52:55], v[152:155], v[216:219]\n\t"
  "v_exp_f32 v254, v254\n\t"
  "v_mfma_f32_16x16x32_bf16 v[220:223], v[52:55], v[156:159], v[220:223]\n\t"
  "v_exp_f32 v255, v255\n\t"
  "v_mfma_f32_16x16x32_bf16 v[220:223], v[48:51], v[92:95], v[220:223]\n\t"
  "v_accvgpr_write_b32 a78, v2\n\t"
  "s_waitcnt lgkmcnt(2)\n\t"
  "v_mfma_f32_16x16x32_bf16 v[240:243], v[32:35], v[64:67], v[112:115]\n\t"
  "ds_read_b128 v[48:51], v16 offset:24832\n\t"
  "v_cvt_pk_bf16_f32 v3, v254, v255\n\t"
  "v_mfma_f32_16x16x32_bf16 v[240:243], v[36:39], v[96:99], v[240:243]\n\t"
  "v_exp_f32 v208, v208\n\t"
  "v_mfma_f32_16x16x32_bf16 v[244:247], v[36:39], v[100:103], v[116:119]\n\t"
  "v_exp_f32 v209, v209\n\t"
  "v_mfma_f32_16x16x32_bf16 v[244:247], v[32:35], v[68:71], v[244:247]\n\t"
  "v_accvgpr_write_b32 a79, v3\n\t"
  "s_add_u32 m0, s69, 147456\n\t"
  "v_mfma_f32_16x16x32_bf16 v[248:251], v[32:35], v[72:75], v[120:123]\n\t"
  "ds_read_b128 v[52:55], v17 offset:24832\n\t"
  "v_cvt_pk_bf16_f32 v0, v208, v209\n\t"
  "v_mfma_f32_16x16x32_bf16 v[248:251], v[36:39], v[104:107], v[248:251]\n\t"
  "v_exp_f32 v210, v210\n\t"
  "v_mfma_f32_16x16x32_bf16 v[252:255], v[36:39], v[108:111], v[124:127]\n\t"
  "v_exp_f32 v211, v211\n\t"
  "v_mfma_f32_16x16x32_bf16 v[252:255], v[32:35], v[76:79], v[252:255]\n\t"
  "v_accvgpr_write_b32 a80, v0\n\t"
  "s_waitcnt lgkmcnt(2)\n\t"
  "v_mfma_f32_16x16x32_bf16 v[240:243], v[40:43], v[128:131], v[240:243]\n\t"
  "ds_read_b128 v[32:35], v18 offset:24832\n\t"
  "v_cvt_pk_bf16_f32 v1, v210, v211\n\t"
  "v_mfma_f32_16x16x32_bf16 v[240:243], v[44:47], v[160:163], v[240:243]\n\t"
  "v_exp_f32 v212, v212\n\t"
  "v_mfma_f32_16x16x32_bf16 v[244:247], v[44:47], v[164:167], v[244:247]\n\t"
  "v_exp_f32 v213, v213\n\t"
  "v_mfma_f32_16x16x32_bf16 v[244:247], v[40:43], v[132:135], v[244:247]\n\t"
  "v_accvgpr_write_b32 a81, v1\n\t"
  "s_mov_b32 s70, 212992\n\t"
  "v_mfma_f32_16x16x32_bf16 v[248:251], v[40:43], v[136:139], v[248:251]\n\t"
  "ds_read_b128 v[36:39], v19 offset:24832\n\t"
  "v_cvt_pk_bf16_f32 v2, v212, v213\n\t"
  "v_add_u32 v16, 0xffff0000, v16\n\t"
  "v_mfma_f32_16x16x32_bf16 v[248:251], v[44:47], v[168:171], v[248:251]\n\t"
  "v_exp_f32 v214, v214\n\t"
  "v_add_u32 v17, 0xffff0000, v17\n\t"
  "v_mfma_f32_16x16x32_bf16 v[252:255], v[44:47], v[172:175], v[252:255]\n\t"
  "v_exp_f32 v215, v215\n\t"
  "v_add_u32 v18, 0xffff0000, v18\n\t"
  "v_mfma_f32_16x16x32_bf16 v[252:255], v[40:43], v[140:143], v[252:255]\n\t"
  "v_accvgpr_write_b32 a84, v2\n\t"
  "v_add_u32 v19, 0xffff0000, v19\n\t"
  "s_waitcnt lgkmcnt(2)\n\t"
  "v_mfma_f32_16x16x32_bf16 v[240:243], v[48:51], v[192:195], v[240:243]\n\t"
  "ds_read_b128 v[40:43], v16 offset:32768\n\t"
  "v_cvt_pk_bf16_f32 v3, v214, v215\n\t"
  "v_mfma_f32_16x16x32_bf16 v[240:243], v[52:55], v[224:227], v[240:243]\n\t"
  "v_exp_f32 v216, v216\n\t"
  "v_mfma_f32_16x16x32_bf16 v[244:247], v[52:55], v[228:231], v[244:247]\n\t"
  "v_exp_f32 v217, v217\n\t"
  "v_mfma_f32_16x16x32_bf16 v[244:247], v[48:51], v[196:199], v[244:247]\n\t"
  "buffer_load_dwordx4 v6, s[48:51], s70 offen offset:0 nt lds\n\t"
  "v_accvgpr_write_b32 a85, v3\n\t"
  "v_mfma_f32_16x16x32_bf16 v[248:251], v[48:51], v[200:203], v[248:251]\n\t"
  "ds_read_b128 v[44:47], v17 offset:32768\n\t"
  "v_cvt_pk_bf16_f32 v0, v216, v217\n\t"
  "v_mfma_f32_16x16x32_bf16 v[248:251], v[52:55], v[232:235], v[248:251]\n\t"
  "v_exp_f32 v218, v218\n\t"
  "v_mfma_f32_16x16x32_bf16 v[252:255], v[52:55], v[236:239], v[252:255]\n\t"
  "v_exp_f32 v219, v219\n\t"
  "v_mfma_f32_16x16x32_bf16 v[252:255], v[48:51], v[204:207], v[252:255]\n\t"
  "v_accvgpr_write_b32 a88, v0\n\t"
  "s_waitcnt lgkmcnt(2)\n\t"
  "v_mfma_f32_16x16x32_bf16 v[240:243], v[32:35], v[80:83], v[240:243]\n\t"
  "ds_read_b128 v[48:51], v18 offset:32768\n\t"
  "v_cvt_pk_bf16_f32 v1, v218, v219\n\t"
  "v_mfma_f32_16x16x32_bf16 v[240:243], v[36:39], v[144:147], v[240:243]\n\t"
  "v_exp_f32 v220, v220\n\t"
  "v_mfma_f32_16x16x32_bf16 v[244:247], v[36:39], v[148:151], v[244:247]\n\t"
  "v_exp_f32 v221, v221\n\t"
  "v_mfma_f32_16x16x32_bf16 v[244:247], v[32:35], v[84:87], v[244:247]\n\t"
  "buffer_load_dwordx4 v7, s[48:51], s70 offen offset:1024 nt lds\n\t"
  "v_accvgpr_write_b32 a89, v1\n\t"
  "v_mfma_f32_16x16x32_bf16 v[248:251], v[32:35], v[88:91], v[248:251]\n\t"
  "ds_read_b128 v[52:55], v19 offset:32768\n\t"
  "v_cvt_pk_bf16_f32 v2, v220, v221\n\t"
  "v_mfma_f32_16x16x32_bf16 v[248:251], v[36:39], v[152:155], v[248:251]\n\t"
  "v_exp_f32 v222, v222\n\t"
  "v_mfma_f32_16x16x32_bf16 v[252:255], v[36:39], v[156:159], v[252:255]\n\t"
  "v_exp_f32 v223, v223\n\t"
  "v_mfma_f32_16x16x32_bf16 v[252:255], v[32:35], v[92:95], v[252:255]\n\t"
  "v_accvgpr_write_b32 a92, v2\n\t"
  "s_waitcnt lgkmcnt(2)\n\t"
  "v_mfma_f32_16x16x32_bf16 v[208:211], v[40:43], v[64:67], v[112:115]\n\t"
  "ds_read_b128 v[32:35], v16 offset:33024\n\t"
  "v_cvt_pk_bf16_f32 v3, v222, v223\n\t"
  "v_mfma_f32_16x16x32_bf16 v[208:211], v[44:47], v[96:99], v[208:211]\n\t"
  "v_exp_f32 v240, v240\n\t"
  "v_mfma_f32_16x16x32_bf16 v[212:215], v[44:47], v[100:103], v[116:119]\n\t"
  "v_exp_f32 v241, v241\n\t"
  "v_mfma_f32_16x16x32_bf16 v[212:215], v[40:43], v[68:71], v[212:215]\n\t"
  "buffer_load_dwordx4 v8, s[48:51], s70 offen offset:2048 nt lds\n\t"
  "v_accvgpr_write_b32 a93, v3\n\t"
  "v_mfma_f32_16x16x32_bf16 v[216:219], v[40:43], v[72:75], v[120:123]\n\t"
  "ds_read_b128 v[36:39], v17 offset:33024\n\t"
  "v_cvt_pk_bf16_f32 v0, v240, v241\n\t"
  "v_mfma_f32_16x16x32_bf16 v[216:219], v[44:47], v[104:107], v[216:219]\n\t"
  "v_exp_f32 v242, v242\n\t"
  "v_mfma_f32_16x16x32_bf16 v[220:223], v[44:47], v[108:111], v[124:127]\n\t"
  "v_exp_f32 v243, v243\n\t"
  "v_mfma_f32_16x16x32_bf16 v[220:223], v[40:43], v[76:79], v[220:223]\n\t"
  "v_accvgpr_write_b32 a82, v0\n\t"
  "s_waitcnt lgkmcnt(2)\n\t"
  "v_mfma_f32_16x16x32_bf16 v[208:211], v[48:51], v[128:131], v[208:211]\n\t"
  "ds_read_b128 v[40:43], v18 offset:33024\n\t"
  "v_cvt_pk_bf16_f32 v1, v242, v243\n\t"
  "v_mfma_f32_16x16x32_bf16 v[208:211], v[52:55], v[160:163], v[208:211]\n\t"
  "v_exp_f32 v244, v244\n\t"
  "v_mfma_f32_16x16x32_bf16 v[212:215], v[52:55], v[164:167], v[212:215]\n\t"
  "v_exp_f32 v245, v245\n\t"
  "v_mfma_f32_16x16x32_bf16 v[212:215], v[48:51], v[132:135], v[212:215]\n\t"
  "buffer_load_dwordx4 v9, s[48:51], s70 offen offset:3072 nt lds\n\t"
  "v_accvgpr_write_b32 a83, v1\n\t"
  "v_mfma_f32_16x16x32_bf16 v[216:219], v[48:51], v[136:139], v[216:219]\n\t"
  "ds_read_b128 v[44:47], v19 offset:33024\n\t"
  "v_cvt_pk_bf16_f32 v2, v244, v245\n\t"
  "v_mfma_f32_16x16x32_bf16 v[216:219], v[52:55], v[168:171], v[216:219]\n\t"
  "v_exp_f32 v246, v246\n\t"
  "v_mfma_f32_16x16x32_bf16 v[220:223], v[52:55], v[172:175], v[220:223]\n\t"
  "v_exp_f32 v247, v247\n\t"
  "v_mfma_f32_16x16x32_bf16 v[220:223], v[48:51], v[140:143], v[220:223]\n\t"
  "v_accvgpr_write_b32 a86, v2\n\t"
  "s_waitcnt lgkmcnt(2)\n\t"
  "v_mfma_f32_16x16x32_bf16 v[208:211], v[32:35], v[192:195], v[208:211]\n\t"
  "ds_read_b128 v[48:51], v16 offset:40960\n\t"
  "v_cvt_pk_bf16_f32 v3, v246, v247\n\t"
  "v_mfma_f32_16x16x32_bf16 v[208:211], v[36:39], v[224:227], v[208:211]\n\t"
  "v_exp_f32 v248, v248\n\t"
  "v_mfma_f32_16x16x32_bf16 v[212:215], v[36:39], v[228:231], v[212:215]\n\t"
  "v_exp_f32 v249, v249\n\t"
  "v_mfma_f32_16x16x32_bf16 v[212:215], v[32:35], v[196:199], v[212:215]\n\t"
  "buffer_load_dword v11, v10, s[60:63], 0 offen offset:2048\n\t"
  "v_accvgpr_write_b32 a87, v3\n\t"
  "v_mfma_f32_16x16x32_bf16 v[216:219], v[32:35], v[200:203], v[216:219]\n\t"
  "ds_read_b128 v[52:55], v17 offset:40960\n\t"
  "v_cvt_pk_bf16_f32 v0, v248, v249\n\t"
  "v_mfma_f32_16x16x32_bf16 v[216:219], v[36:39], v[232:235], v[216:219]\n\t"
  "v_exp_f32 v250, v250\n\t"
  "v_mfma_f32_16x16x32_bf16 v[220:223], v[36:39], v[236:239], v[220:223]\n\t"
  "v_exp_f32 v251, v251\n\t"
  "v_mfma_f32_16x16x32_bf16 v[220:223], v[32:35], v[204:207], v[220:223]\n\t"
  "v_accvgpr_write_b32 a90, v0\n\t"
  "s_waitcnt lgkmcnt(2)\n\t"
  "v_mfma_f32_16x16x32_bf16 v[208:211], v[40:43], v[80:83], v[208:211]\n\t"
  "ds_read_b128 v[32:35], v18 offset:40960\n\t"
  "v_cvt_pk_bf16_f32 v1, v250, v251\n\t"
  "v_mfma_f32_16x16x32_bf16 v[208:211], v[44:47], v[144:147], v[208:211]\n\t"
  "v_exp_f32 v252, v252\n\t"
  "v_mfma_f32_16x16x32_bf16 v[212:215], v[44:47], v[148:151], v[212:215]\n\t"
  "v_exp_f32 v253, v253\n\t"
  "v_mfma_f32_16x16x32_bf16 v[212:215], v[40:43], v[84:87], v[212:215]\n\t"
  "v_accvgpr_write_b32 a91, v1\n\t"
  "v_mfma_f32_16x16x32_bf16 v[216:219], v[40:43], v[88:91], v[216:219]\n\t"
  "ds_read_b128 v[36:39], v19 offset:40960\n\t"
  "v_cvt_pk_bf16_f32 v2, v252, v253\n\t"
  "v_mfma_f32_16x16x32_bf16 v[216:219], v[44:47], v[152:155], v[216:219]\n\t"
  "v_exp_f32 v254, v254\n\t"
  "v_mfma_f32_16x16x32_bf16 v[220:223], v[44:47], v[156:159], v[220:223]\n\t"
  "v_exp_f32 v255, v255\n\t"
  "v_mfma_f32_16x16x32_bf16 v[220:223], v[40:43], v[92:95], v[220:223]\n\t"
  "v_accvgpr_write_b32 a94, v2\n\t"
  "s_waitcnt lgkmcnt(2)\n\t"
  "v_mfma_f32_16x16x32_bf16 v[240:243], v[48:51], v[64:67], v[112:115]\n\t"
  "ds_read_b128 v[40:43], v16 offset:41216\n\t"
  "v_cvt_pk_bf16_f32 v3, v254, v255\n\t"
  "v_mfma_f32_16x16x32_bf16 v[240:243], v[52:55], v[96:99], v[240:243]\n\t"
  "v_exp_f32 v208, v208\n\t"
  "v_mfma_f32_16x16x32_bf16 v[244:247], v[52:55], v[100:103], v[116:119]\n\t"
  "v_exp_f32 v209, v209\n\t"
  "v_mfma_f32_16x16x32_bf16 v[244:247], v[48:51], v[68:71], v[244:247]\n\t"
  "v_accvgpr_write_b32 a95, v3\n\t"
  "v_mfma_f32_16x16x32_bf16 v[248:251], v[48:51], v[72:75], v[120:123]\n\t"
  "ds_read_b128 v[44:47], v17 offset:41216\n\t"
  "v_cvt_pk_bf16_f32 v0, v208, v209\n\t"
  "v_mfma_f32_16x16x32_bf16 v[248:251], v[52:55], v[104:107], v[248:251]\n\t"
  "v_exp_f32 v210, v210\n\t"
  "v_mfma_f32_16x16x32_bf16 v[252:255], v[52:55], v[108:111], v[124:127]\n\t"
  "v_exp_f32 v211, v211\n\t"
  "v_mfma_f32_16x16x32_bf16 v[252:255], v[48:51], v[76:79], v[252:255]\n\t"
  "v_accvgpr_write_b32 a96, v0\n\t"
  "s_waitcnt lgkmcnt(2)\n\t"
  "v_mfma_f32_16x16x32_bf16 v[240:243], v[32:35], v[128:131], v[240:243]\n\t"
  "ds_read_b128 v[48:51], v18 offset:41216\n\t"
  "v_cvt_pk_bf16_f32 v1, v210, v211\n\t"
  "v_mfma_f32_16x16x32_bf16 v[240:243], v[36:39], v[160:163], v[240:243]\n\t"
  "v_exp_f32 v212, v212\n\t"
  "v_mfma_f32_16x16x32_bf16 v[244:247], v[36:39], v[164:167], v[244:247]\n\t"
  "v_exp_f32 v213, v213\n\t"
  "v_mfma_f32_16x16x32_bf16 v[244:247], v[32:35], v[132:135], v[244:247]\n\t"
  "v_accvgpr_write_b32 a97, v1\n\t"
  "s_waitcnt vmcnt(23)\n\t"
  "s_barrier\n\t"
  "v_mfma_f32_16x16x32_bf16 v[248:251], v[32:35], v[136:139], v[248:251]\n\t"
  "ds_read_b128 v[52:55], v19 offset:41216\n\t"
  "v_cvt_pk_bf16_f32 v2, v212, v213\n\t"
  "v_mfma_f32_16x16x32_bf16 v[248:251], v[36:39], v[168:171], v[248:251]\n\t"
  "v_exp_f32 v214, v214\n\t"
  "v_mfma_f32_16x16x32_bf16 v[252:255], v[36:39], v[172:175], v[252:255]\n\t"
  "v_exp_f32 v215, v215\n\t"
  "v_mfma_f32_16x16x32_bf16 v[252:255], v[32:35], v[140:143], v[252:255]\n\t"
  "v_accvgpr_write_b32 a100, v2\n\t"
  "s_waitcnt lgkmcnt(2)\n\t"
  "v_mfma_f32_16x16x32_bf16 v[240:243], v[40:43], v[192:195], v[240:243]\n\t"
  "ds_read_b128 v[32:35], v16 offset:49152\n\t"
  "v_cvt_pk_bf16_f32 v3, v214, v215\n\t"
  "v_mfma_f32_16x16x32_bf16 v[240:243], v[44:47], v[224:227], v[240:243]\n\t"
  "v_exp_f32 v216, v216\n\t"
  "v_mfma_f32_16x16x32_bf16 v[244:247], v[44:47], v[228:231], v[244:247]\n\t"
  "v_exp_f32 v217, v217\n\t"
  "v_mfma_f32_16x16x32_bf16 v[244:247], v[40:43], v[196:199], v[244:247]\n\t"
  "v_accvgpr_write_b32 a101, v3\n\t"
  "s_add_u32 m0, s69, 65536\n\t"
  "v_mfma_f32_16x16x32_bf16 v[248:251], v[40:43], v[200:203], v[248:251]\n\t"
  "ds_read_b128 v[36:39], v17 offset:49152\n\t"
  "v_cvt_pk_bf16_f32 v0, v216, v217\n\t"
  "v_mfma_f32_16x16x32_bf16 v[248:251], v[44:47], v[232:235], v[248:251]\n\t"
  "v_exp_f32 v218, v218\n\t"
  "v_mfma_f32_16x16x32_bf16 v[252:255], v[44:47], v[236:239], v[252:255]\n\t"
  "v_exp_f32 v219, v219\n\t"
  "v_mfma_f32_16x16x32_bf16 v[252:255], v[40:43], v[204:207], v[252:255]\n\t"
  "v_accvgpr_write_b32 a104, v0\n\t"
  "s_waitcnt lgkmcnt(2)\n\t"
  "v_mfma_f32_16x16x32_bf16 v[240:243], v[48:51], v[80:83], v[240:243]\n\t"
  "ds_read_b128 v[40:43], v18 offset:49152\n\t"
  "v_cvt_pk_bf16_f32 v1, v218, v219\n\t"
  "v_mfma_f32_16x16x32_bf16 v[240:243], v[52:55], v[144:147], v[240:243]\n\t"
  "v_exp_f32 v220, v220\n\t"
  "v_mfma_f32_16x16x32_bf16 v[244:247], v[52:55], v[148:151], v[244:247]\n\t"
  "v_exp_f32 v221, v221\n\t"
  "v_mfma_f32_16x16x32_bf16 v[244:247], v[48:51], v[84:87], v[244:247]\n\t"
  "v_accvgpr_write_b32 a105, v1\n\t"
  "s_mov_b32 s70, 229376\n\t"
  "v_mfma_f32_16x16x32_bf16 v[248:251], v[48:51], v[88:91], v[248:251]\n\t"
  "ds_read_b128 v[44:47], v19 offset:49152\n\t"
  "v_cvt_pk_bf16_f32 v2, v220, v221\n\t"
  "v_mfma_f32_16x16x32_bf16 v[248:251], v[52:55], v[152:155], v[248:251]\n\t"
  "v_exp_f32 v222, v222\n\t"
  "v_mfma_f32_16x16x32_bf16 v[252:255], v[52:55], v[156:159], v[252:255]\n\t"
  "v_exp_f32 v223, v223\n\t"
  "v_mfma_f32_16x16x32_bf16 v[252:255], v[48:51], v[92:95], v[252:255]\n\t"
  "v_accvgpr_write_b32 a108, v2\n\t"
  "s_waitcnt lgkmcnt(2)\n\t"
  "v_mfma_f32_16x16x32_bf16 v[208:211], v[32:35], v[64:67], v[112:115]\n\t"
  "ds_read_b128 v[48:51], v16 offset:49408\n\t"
  "v_cvt_pk_bf16_f32 v3, v222, v223\n\t"
  "v_mfma_f32_16x16x32_bf16 v[208:211], v[36:39], v[96:99], v[208:211]\n\t"
  "v_exp_f32 v240, v240\n\t"
  "v_mfma_f32_16x16x32_bf16 v[212:215], v[36:39], v[100:103], v[116:119]\n\t"
  "v_exp_f32 v241, v241\n\t"
  "v_mfma_f32_16x16x32_bf16 v[212:215], v[32:35], v[68:71], v[212:215]\n\t"
  "buffer_load_dwordx4 v6, s[48:51], s70 offen offset:0 nt lds\n\t"
  "v_accvgpr_write_b32 a109, v3\n\t"
  "v_mfma_f32_16x16x32_bf16 v[216:219], v[32:35], v[72:75], v[120:123]\n\t"
  "ds_read_b128 v[52:55], v17 offset:49408\n\t"
  "v_cvt_pk_bf16_f32 v0, v240, v241\n\t"
  "v_mfma_f32_16x16x32_bf16 v[216:219], v[36:39], v[104:107], v[216:219]\n\t"
  "v_exp_f32 v242, v242\n\t"
  "v_mfma_f32_16x16x32_bf16 v[220:223], v[36:39], v[108:111], v[124:127]\n\t"
  "v_exp_f32 v243, v243\n\t"
  "v_mfma_f32_16x16x32_bf16 v[220:223], v[32:35], v[76:79], v[220:223]\n\t"
  "v_accvgpr_write_b32 a98, v0\n\t"
  "s_waitcnt lgkmcnt(2)\n\t"
  "v_mfma_f32_16x16x32_bf16 v[208:211], v[40:43], v[128:131], v[208:211]\n\t"
  "ds_read_b128 v[32:35], v18 offset:49408\n\t"
  "v_cvt_pk_bf16_f32 v1, v242, v243\n\t"
  "v_mfma_f32_16x16x32_bf16 v[208:211], v[44:47], v[160:163], v[208:211]\n\t"
  "v_exp_f32 v244, v244\n\t"
  "v_mfma_f32_16x16x32_bf16 v[212:215], v[44:47], v[164:167], v[212:215]\n\t"
  "v_exp_f32 v245, v245\n\t"
  "v_mfma_f32_16x16x32_bf16 v[212:215], v[40:43], v[132:135], v[212:215]\n\t"
  "buffer_load_dwordx4 v7, s[48:51], s70 offen offset:1024 nt lds\n\t"
  "v_accvgpr_write_b32 a99, v1\n\t"
  "v_mfma_f32_16x16x32_bf16 v[216:219], v[40:43], v[136:139], v[216:219]\n\t"
  "ds_read_b128 v[36:39], v19 offset:49408\n\t"
  "v_cvt_pk_bf16_f32 v2, v244, v245\n\t"
  "v_mfma_f32_16x16x32_bf16 v[216:219], v[44:47], v[168:171], v[216:219]\n\t"
  "v_exp_f32 v246, v246\n\t"
  "v_mfma_f32_16x16x32_bf16 v[220:223], v[44:47], v[172:175], v[220:223]\n\t"
  "v_exp_f32 v247, v247\n\t"
  "v_mfma_f32_16x16x32_bf16 v[220:223], v[40:43], v[140:143], v[220:223]\n\t"
  "v_accvgpr_write_b32 a102, v2\n\t"
  "s_waitcnt lgkmcnt(2)\n\t"
  "v_mfma_f32_16x16x32_bf16 v[208:211], v[48:51], v[192:195], v[208:211]\n\t"
  "ds_read_b128 v[40:43], v16 offset:57344\n\t"
  "v_cvt_pk_bf16_f32 v3, v246, v247\n\t"
  "v_mfma_f32_16x16x32_bf16 v[208:211], v[52:55], v[224:227], v[208:211]\n\t"
  "v_exp_f32 v248, v248\n\t"
  "v_mfma_f32_16x16x32_bf16 v[212:215], v[52:55], v[228:231], v[212:215]\n\t"
  "v_exp_f32 v249, v249\n\t"
  "v_mfma_f32_16x16x32_bf16 v[212:215], v[48:51], v[196:199], v[212:215]\n\t"
  "buffer_load_dwordx4 v8, s[48:51], s70 offen offset:2048 nt lds\n\t"
  "v_accvgpr_write_b32 a103, v3\n\t"
  "v_mfma_f32_16x16x32_bf16 v[216:219], v[48:51], v[200:203], v[216:219]\n\t"
  "ds_read_b128 v[44:47], v17 offset:57344\n\t"
  "v_cvt_pk_bf16_f32 v0, v248, v249\n\t"
  "v_mfma_f32_16x16x32_bf16 v[216:219], v[52:55], v[232:235], v[216:219]\n\t"
  "v_exp_f32 v250, v250\n\t"
  "v_mfma_f32_16x16x32_bf16 v[220:223], v[52:55], v[236:239], v[220:223]\n\t"
  "v_exp_f32 v251, v251\n\t"
  "v_mfma_f32_16x16x32_bf16 v[220:223], v[48:51], v[204:207], v[220:223]\n\t"
  "v_accvgpr_write_b32 a106, v0\n\t"
  "s_waitcnt lgkmcnt(2)\n\t"
  "v_mfma_f32_16x16x32_bf16 v[208:211], v[32:35], v[80:83], v[208:211]\n\t"
  "ds_read_b128 v[48:51], v18 offset:57344\n\t"
  "v_cvt_pk_bf16_f32 v1, v250, v251\n\t"
  "v_mfma_f32_16x16x32_bf16 v[208:211], v[36:39], v[144:147], v[208:211]\n\t"
  "v_exp_f32 v252, v252\n\t"
  "v_mfma_f32_16x16x32_bf16 v[212:215], v[36:39], v[148:151], v[212:215]\n\t"
  "v_exp_f32 v253, v253\n\t"
  "v_mfma_f32_16x16x32_bf16 v[212:215], v[32:35], v[84:87], v[212:215]\n\t"
  "buffer_load_dwordx4 v9, s[48:51], s70 offen offset:3072 nt lds\n\t"
  "v_accvgpr_write_b32 a107, v1\n\t"
  "v_mfma_f32_16x16x32_bf16 v[216:219], v[32:35], v[88:91], v[216:219]\n\t"
  "ds_read_b128 v[52:55], v19 offset:57344\n\t"
  "v_cvt_pk_bf16_f32 v2, v252, v253\n\t"
  "v_mfma_f32_16x16x32_bf16 v[216:219], v[36:39], v[152:155], v[216:219]\n\t"
  "v_exp_f32 v254, v254\n\t"
  "v_mfma_f32_16x16x32_bf16 v[220:223], v[36:39], v[156:159], v[220:223]\n\t"
  "v_exp_f32 v255, v255\n\t"
  "v_mfma_f32_16x16x32_bf16 v[220:223], v[32:35], v[92:95], v[220:223]\n\t"
  "v_accvgpr_write_b32 a110, v2\n\t"
  "s_waitcnt lgkmcnt(2)\n\t"
  "v_mfma_f32_16x16x32_bf16 v[240:243], v[40:43], v[64:67], v[112:115]\n\t"
  "ds_read_b128 v[32:35], v16 offset:57600\n\t"
  "v_cvt_pk_bf16_f32 v3, v254, v255\n\t"
  "v_mfma_f32_16x16x32_bf16 v[240:243], v[44:47], v[96:99], v[240:243]\n\t"
  "v_exp_f32 v208, v208\n\t"
  "v_mfma_f32_16x16x32_bf16 v[244:247], v[44:47], v[100:103], v[116:119]\n\t"
  "v_exp_f32 v209, v209\n\t"
  "v_mfma_f32_16x16x32_bf16 v[244:247], v[40:43], v[68:71], v[244:247]\n\t"
  "v_accvgpr_write_b32 a111, v3\n\t"
  "s_add_u32 m0, s69, 81920\n\t"
  "v_mfma_f32_16x16x32_bf16 v[248:251], v[40:43], v[72:75], v[120:123]\n\t"
  "ds_read_b128 v[36:39], v17 offset:57600\n\t"
  "v_cvt_pk_bf16_f32 v0, v208, v209\n\t"
  "v_mfma_f32_16x16x32_bf16 v[248:251], v[44:47], v[104:107], v[248:251]\n\t"
  "v_exp_f32 v210, v210\n\t"
  "v_mfma_f32_16x16x32_bf16 v[252:255], v[44:47], v[108:111], v[124:127]\n\t"
  "v_exp_f32 v211, v211\n\t"
  "v_mfma_f32_16x16x32_bf16 v[252:255], v[40:43], v[76:79], v[252:255]\n\t"
  "v_accvgpr_write_b32 a112, v0\n\t"
  "s_waitcnt lgkmcnt(2)\n\t"
  "v_mfma_f32_16x16x32_bf16 v[240:243], v[48:51], v[128:131], v[240:243]\n\t"
  "ds_read_b128 v[40:43], v18 offset:57600\n\t"
  "v_cvt_pk_bf16_f32 v1, v210, v211\n\t"
  "v_mfma_f32_16x16x32_bf16 v[240:243], v[52:55], v[160:163], v[240:243]\n\t"
  "v_exp_f32 v212, v212\n\t"
  "v_mfma_f32_16x16x32_bf16 v[244:247], v[52:55], v[164:167], v[244:247]\n\t"
  "v_exp_f32 v213, v213\n\t"
  "v_mfma_f32_16x16x32_bf16 v[244:247], v[48:51], v[132:135], v[244:247]\n\t"
  "v_accvgpr_write_b32 a113, v1\n\t"
  "s_mov_b32 s70, 245760\n\t"
  "v_mfma_f32_16x16x32_bf16 v[248:251], v[48:51], v[136:139], v[248:251]\n\t"
  "ds_read_b128 v[44:47], v19 offset:57600\n\t"
  "v_cvt_pk_bf16_f32 v2, v212, v213\n\t"
  "v_mfma_f32_16x16x32_bf16 v[248:251], v[52:55], v[168:171], v[248:251]\n\t"
  "v_exp_f32 v214, v214\n\t"
  "v_mfma_f32_16x16x32_bf16 v[252:255], v[52:55], v[172:175], v[252:255]\n\t"
  "v_exp_f32 v215, v215\n\t"
  "v_mfma_f32_16x16x32_bf16 v[252:255], v[48:51], v[140:143], v[252:255]\n\t"
  "v_accvgpr_write_b32 a116, v2\n\t"
  "s_waitcnt lgkmcnt(2)\n\t"
  "v_mfma_f32_16x16x32_bf16 v[240:243], v[32:35], v[192:195], v[240:243]\n\t"
  "ds_read_b128 v[48:51], v16 offset:0\n\t"
  "v_cvt_pk_bf16_f32 v3, v214, v215\n\t"
  "v_mfma_f32_16x16x32_bf16 v[240:243], v[36:39], v[224:227], v[240:243]\n\t"
  "v_exp_f32 v216, v216\n\t"
  "v_mfma_f32_16x16x32_bf16 v[244:247], v[36:39], v[228:231], v[244:247]\n\t"
  "v_exp_f32 v217, v217\n\t"
  "v_mfma_f32_16x16x32_bf16 v[244:247], v[32:35], v[196:199], v[244:247]\n\t"
  "buffer_load_dwordx4 v6, s[48:51], s70 offen offset:0 nt lds\n\t"
  "v_accvgpr_write_b32 a117, v3\n\t"
  "v_mfma_f32_16x16x32_bf16 v[248:251], v[32:35], v[200:203], v[248:251]\n\t"
  "ds_read_b128 v[52:55], v17 offset:0\n\t"
  "v_cvt_pk_bf16_f32 v0, v216, v217\n\t"
  "v_mfma_f32_16x16x32_bf16 v[248:251], v[36:39], v[232:235], v[248:251]\n\t"
  "v_exp_f32 v218, v218\n\t"
  "v_mfma_f32_16x16x32_bf16 v[252:255], v[36:39], v[236:239], v[252:255]\n\t"
  "v_exp_f32 v219, v219\n\t"
  "v_mfma_f32_16x16x32_bf16 v[252:255], v[32:35], v[204:207], v[252:255]\n\t"
  "v_accvgpr_write_b32 a120, v0\n\t"
  "s_waitcnt lgkmcnt(2)\n\t"
  "v_mfma_f32_16x16x32_bf16 v[240:243], v[40:43], v[80:83], v[240:243]\n\t"
  "ds_read_b128 v[32:35], v18 offset:0\n\t"
  "v_cvt_pk_bf16_f32 v1, v218, v219\n\t"
  "v_mfma_f32_16x16x32_bf16 v[240:243], v[44:47], v[144:147], v[240:243]\n\t"
  "v_exp_f32 v220, v220\n\t"
  "v_mfma_f32_16x16x32_bf16 v[244:247], v[44:47], v[148:151], v[244:247]\n\t"
  "v_exp_f32 v221, v221\n\t"
  "v_mfma_f32_16x16x32_bf16 v[244:247], v[40:43], v[84:87], v[244:247]\n\t"
  "buffer_load_dwordx4 v7, s[48:51], s70 offen offset:1024 nt lds\n\t"
  "v_accvgpr_write_b32 a121, v1\n\t"
  "v_mfma_f32_16x16x32_bf16 v[248:251], v[40:43], v[88:91], v[248:251]\n\t"
  "ds_read_b128 v[36:39], v19 offset:0\n\t"
  "v_cvt_pk_bf16_f32 v2, v220, v221\n\t"
  "v_mfma_f32_16x16x32_bf16 v[248:251], v[44:47], v[152:155], v[248:251]\n\t"
  "v_exp_f32 v222, v222\n\t"
  "v_mfma_f32_16x16x32_bf16 v[252:255], v[44:47], v[156:159], v[252:255]\n\t"
  "v_exp_f32 v223, v223\n\t"
  "v_mfma_f32_16x16x32_bf16 v[252:255], v[40:43], v[92:95], v[252:255]\n\t"
  "v_accvgpr_write_b32 a124, v2\n\t"
  "s_waitcnt lgkmcnt(2)\n\t"
  "v_mfma_f32_16x16x32_bf16 v[208:211], v[48:51], v[64:67], v[112:115]\n\t"
  "ds_read_b128 v[40:43], v16 offset:256\n\t"
  "v_cvt_pk_bf16_f32 v3, v222, v223\n\t"
  "v_mfma_f32_16x16x32_bf16 v[208:211], v[52:55], v[96:99], v[208:211]\n\t"
  "v_exp_f32 v240, v240\n\t"
  "v_mfma_f32_16x16x32_bf16 v[212:215], v[52:55], v[100:103], v[116:119]\n\t"
  "v_exp_f32 v241, v241\n\t"
  "v_mfma_f32_16x16x32_bf16 v[212:215], v[48:51], v[68:71], v[212:215]\n\t"
  "buffer_load_dwordx4 v8, s[48:51], s70 offen offset:2048 nt lds\n\t"
  "v_accvgpr_write_b32 a125, v3\n\t"
  "v_mfma_f32_16x16x32_bf16 v[216:219], v[48:51], v[72:75], v[120:123]\n\t"
  "ds_read_b128 v[44:47], v17 offset:256\n\t"
  "v_cvt_pk_bf16_f32 v0, v240, v241\n\t"
  "v_mfma_f32_16x16x32_bf16 v[216:219], v[52:55], v[104:107], v[216:219]\n\t"
  "v_exp_f32 v242, v242\n\t"
  "v_mfma_f32_16x16x32_bf16 v[220:223], v[52:55], v[108:111], v[124:127]\n\t"
  "v_exp_f32 v243, v243\n\t"
  "v_mfma_f32_16x16x32_bf16 v[220:223], v[48:51], v[76:79], v[220:223]\n\t"
  "v_accvgpr_write_b32 a114, v0\n\t"
  "s_waitcnt lgkmcnt(2)\n\t"
  "v_mfma_f32_16x16x32_bf16 v[208:211], v[32:35], v[128:131], v[208:211]\n\t"
  "ds_read_b128 v[48:51], v18 offset:256\n\t"
  "v_cvt_pk_bf16_f32 v1, v242, v243\n\t"
  "v_mfma_f32_16x16x32_bf16 v[208:211], v[36:39], v[160:163], v[208:211]\n\t"
  "v_exp_f32 v244, v244\n\t"
  "v_mfma_f32_16x16x32_bf16 v[212:215], v[36:39], v[164:167], v[212:215]\n\t"
  "v_exp_f32 v245, v245\n\t"
  "v_mfma_f32_16x16x32_bf16 v[212:215], v[32:35], v[132:135], v[212:215]\n\t"
  "buffer_load_dwordx4 v9, s[48:51], s70 offen offset:3072 nt lds\n\t"
  "v_accvgpr_write_b32 a115, v1\n\t"
  "v_mfma_f32_16x16x32_bf16 v[216:219], v[32:35], v[136:139], v[216:219]\n\t"
  "ds_read_b128 v[52:55], v19 offset:256\n\t"
  "v_cvt_pk_bf16_f32 v2, v244, v245\n\t"
  "v_mfma_f32_16x16x32_bf16 v[216:219], v[36:39], v[168:171], v[216:219]\n\t"
  "v_exp_f32 v246, v246\n\t"
  "v_mfma_f32_16x16x32_bf16 v[220:223], v[36:39], v[172:175], v[220:223]\n\t"
  "v_exp_f32 v247, v247\n\t"
  "v_mfma_f32_16x16x32_bf16 v[220:223], v[32:35], v[140:143], v[220:223]\n\t"
  "v_accvgpr_write_b32 a118, v2\n\t"
  "s_waitcnt lgkmcnt(2)\n\t"
  "v_mfma_f32_16x16x32_bf16 v[208:211], v[40:43], v[192:195], v[208:211]\n\t"
  "ds_read_b128 v[32:35], v16 offset:8192\n\t"
  "v_cvt_pk_bf16_f32 v3, v246, v247\n\t"
  "v_mfma_f32_16x16x32_bf16 v[208:211], v[44:47], v[224:227], v[208:211]\n\t"
  "v_exp_f32 v248, v248\n\t"
  "v_mfma_f32_16x16x32_bf16 v[212:215], v[44:47], v[228:231], v[212:215]\n\t"
  "v_exp_f32 v249, v249\n\t"
  "v_mfma_f32_16x16x32_bf16 v[212:215], v[40:43], v[196:199], v[212:215]\n\t"
  "buffer_load_dword v11, v10, s[60:63], 0 offen offset:3072\n\t"
  "v_accvgpr_write_b32 a119, v3\n\t"
  "v_mfma_f32_16x16x32_bf16 v[216:219], v[40:43], v[200:203], v[216:219]\n\t"
  "ds_read_b128 v[36:39], v17 offset:8192\n\t"
  "v_cvt_pk_bf16_f32 v0, v248, v249\n\t"
  "v_mfma_f32_16x16x32_bf16 v[216:219], v[44:47], v[232:235], v[216:219]\n\t"
  "v_exp_f32 v250, v250\n\t"
  "v_mfma_f32_16x16x32_bf16 v[220:223], v[44:47], v[236:239], v[220:223]\n\t"
  "v_exp_f32 v251, v251\n\t"
  "v_mfma_f32_16x16x32_bf16 v[220:223], v[40:43], v[204:207], v[220:223]\n\t"
  "v_accvgpr_write_b32 a122, v0\n\t"
  "s_waitcnt lgkmcnt(2)\n\t"
  "v_mfma_f32_16x16x32_bf16 v[208:211], v[48:51], v[80:83], v[208:211]\n\t"
  "ds_read_b128 v[40:43], v18 offset:8192\n\t"
  "v_cvt_pk_bf16_f32 v1, v250, v251\n\t"
  "v_mfma_f32_16x16x32_bf16 v[208:211], v[52:55], v[144:147], v[208:211]\n\t"
  "v_exp_f32 v252, v252\n\t"
  "v_mfma_f32_16x16x32_bf16 v[212:215], v[52:55], v[148:151], v[212:215]\n\t"
  "v_exp_f32 v253, v253\n\t"
  "v_mfma_f32_16x16x32_bf16 v[212:215], v[48:51], v[84:87], v[212:215]\n\t"
  "v_accvgpr_write_b32 a123, v1\n\t"
  "v_add_u32 v10, 0x1000, v10\n\t"
  "v_mfma_f32_16x16x32_bf16 v[216:219], v[48:51], v[88:91], v[216:219]\n\t"
  "ds_read_b128 v[44:47], v19 offset:8192\n\t"
  "v_cvt_pk_bf16_f32 v2, v252, v253\n\t"
  "v_mfma_f32_16x16x32_bf16 v[216:219], v[52:55], v[152:155], v[216:219]\n\t"
  "v_exp_f32 v254, v254\n\t"
  "v_mfma_f32_16x16x32_bf16 v[220:223], v[52:55], v[156:159], v[220:223]\n\t"
  "v_exp_f32 v255, v255\n\t"
  "v_mfma_f32_16x16x32_bf16 v[220:223], v[48:51], v[92:95], v[220:223]\n\t"
  "v_accvgpr_write_b32 a126, v2\n\t"
  "s_waitcnt lgkmcnt(2)\n\t"
  "v_mfma_f32_16x16x32_bf16 v[240:243], v[32:35], v[64:67], v[112:115]\n\t"
  "ds_read_b128 v[48:51], v16 offset:8448\n\t"
  "v_cvt_pk_bf16_f32 v3, v254, v255\n\t"
  "v_mfma_f32_16x16x32_bf16 v[240:243], v[36:39], v[96:99], v[240:243]\n\t"
  "v_exp_f32 v208, v208\n\t"
  "v_mfma_f32_16x16x32_bf16 v[244:247], v[36:39], v[100:103], v[116:119]\n\t"
  "v_exp_f32 v209, v209\n\t"
  "v_mfma_f32_16x16x32_bf16 v[244:247], v[32:35], v[68:71], v[244:247]\n\t"
  "v_accvgpr_write_b32 a127, v3\n\t"
  "v_mfma_f32_16x16x32_bf16 v[248:251], v[32:35], v[72:75], v[120:123]\n\t"
  "ds_read_b128 v[52:55], v17 offset:8448\n\t"
  "v_cvt_pk_bf16_f32 v0, v208, v209\n\t"
  "v_mfma_f32_16x16x32_bf16 v[248:251], v[36:39], v[104:107], v[248:251]\n\t"
  "v_exp_f32 v210, v210\n\t"
  "v_mfma_f32_16x16x32_bf16 v[252:255], v[36:39], v[108:111], v[124:127]\n\t"
  "v_exp_f32 v211, v211\n\t"
  "v_mfma_f32_16x16x32_bf16 v[252:255], v[32:35], v[76:79], v[252:255]\n\t"
  "v_accvgpr_write_b32 a128, v0\n\t"
  "s_waitcnt lgkmcnt(2)\n\t"
  "v_mfma_f32_16x16x32_bf16 v[240:243], v[40:43], v[128:131], v[240:243]\n\t"
  "ds_read_b128 v[32:35], v18 offset:8448\n\t"
  "v_cvt_pk_bf16_f32 v1, v210, v211\n\t"
  "v_mfma_f32_16x16x32_bf16 v[240:243], v[44:47], v[160:163], v[240:243]\n\t"
  "v_exp_f32 v212, v212\n\t"
  "v_mfma_f32_16x16x32_bf16 v[244:247], v[44:47], v[164:167], v[244:247]\n\t"
  "v_exp_f32 v213, v213\n\t"
  "v_mfma_f32_16x16x32_bf16 v[244:247], v[40:43], v[132:135], v[244:247]\n\t"
  "v_accvgpr_write_b32 a129, v1\n\t"
  "s_waitcnt vmcnt(23)\n\t"
  "s_barrier\n\t"
  "v_mfma_f32_16x16x32_bf16 v[248:251], v[40:43], v[136:139], v[248:251]\n\t"
  "ds_read_b128 v[36:39], v19 offset:8448\n\t"
  "v_cvt_pk_bf16_f32 v2, v212, v213\n\t"
  "v_mfma_f32_16x16x32_bf16 v[248:251], v[44:47], v[168:171], v[248:251]\n\t"
  "v_exp_f32 v214, v214\n\t"
  "v_mfma_f32_16x16x32_bf16 v[252:255], v[44:47], v[172:175], v[252:255]\n\t"
  "v_exp_f32 v215, v215\n\t"
  "v_mfma_f32_16x16x32_bf16 v[252:255], v[40:43], v[140:143], v[252:255]\n\t"
  "v_accvgpr_write_b32 a132, v2\n\t"
  "s_waitcnt lgkmcnt(2)\n\t"
  "v_mfma_f32_16x16x32_bf16 v[240:243], v[48:51], v[192:195], v[240:243]\n\t"
  "ds_read_b128 v[40:43], v16 offset:16384\n\t"
  "v_cvt_pk_bf16_f32 v3, v214, v215\n\t"
  "v_mfma_f32_16x16x32_bf16 v[240:243], v[52:55], v[224:227], v[240:243]\n\t"
  "v_exp_f32 v216, v216\n\t"
  "v_mfma_f32_16x16x32_bf16 v[244:247], v[52:55], v[228:231], v[244:247]\n\t"
  "v_exp_f32 v217, v217\n\t"
  "v_mfma_f32_16x16x32_bf16 v[244:247], v[48:51], v[196:199], v[244:247]\n\t"
  "v_accvgpr_write_b32 a133, v3\n\t"
  "s_add_u32 m0, s69, 32768\n\t"
  "v_mfma_f32_16x16x32_bf16 v[248:251], v[48:51], v[200:203], v[248:251]\n\t"
  "ds_read_b128 v[44:47], v17 offset:16384\n\t"
  "v_cvt_pk_bf16_f32 v0, v216, v217\n\t"
  "v_mfma_f32_16x16x32_bf16 v[248:251], v[52:55], v[232:235], v[248:251]\n\t"
  "v_exp_f32 v218, v218\n\t"
  "v_mfma_f32_16x16x32_bf16 v[252:255], v[52:55], v[236:239], v[252:255]\n\t"
  "v_exp_f32 v219, v219\n\t"
  "v_mfma_f32_16x16x32_bf16 v[252:255], v[48:51], v[204:207], v[252:255]\n\t"
  "v_accvgpr_write_b32 a136, v0\n\t"
  "s_waitcnt lgkmcnt(2)\n\t"
  "v_mfma_f32_16x16x32_bf16 v[240:243], v[32:35], v[80:83], v[240:243]\n\t"
  "ds_read_b128 v[48:51], v18 offset:16384\n\t"
  "v_cvt_pk_bf16_f32 v1, v218, v219\n\t"
  "v_mfma_f32_16x16x32_bf16 v[240:243], v[36:39], v[144:147], v[240:243]\n\t"
  "v_exp_f32 v220, v220\n\t"
  "v_mfma_f32_16x16x32_bf16 v[244:247], v[36:39], v[148:151], v[244:247]\n\t"
  "v_exp_f32 v221, v221\n\t"
  "v_mfma_f32_16x16x32_bf16 v[244:247], v[32:35], v[84:87], v[244:247]\n\t"
  "v_accvgpr_write_b32 a137, v1\n\t"
  "s_add_u32 s70, s75, 0\n\t"
  "v_mfma_f32_16x16x32_bf16 v[248:251], v[32:35], v[88:91], v[248:251]\n\t"
  "ds_read_b128 v[52:55], v19 offset:16384\n\t"
  "v_cvt_pk_bf16_f32 v2, v220, v221\n\t"
  "v_mfma_f32_16x16x32_bf16 v[248:251], v[36:39], v[152:155], v[248:251]\n\t"
  "v_exp_f32 v222, v222\n\t"
  "v_mfma_f32_16x16x32_bf16 v[252:255], v[36:39], v[156:159], v[252:255]\n\t"
  "v_exp_f32 v223, v223\n\t"
  "v_mfma_f32_16x16x32_bf16 v[252:255], v[32:35], v[92:95], v[252:255]\n\t"
  "v_accvgpr_write_b32 a140, v2\n\t"
  "s_waitcnt lgkmcnt(2)\n\t"
  "v_mfma_f32_16x16x32_bf16 v[208:211], v[40:43], v[64:67], v[112:115]\n\t"
  "ds_read_b128 v[32:35], v16 offset:16640\n\t"
  "v_cvt_pk_bf16_f32 v3, v222, v223\n\t"
  "v_mfma_f32_16x16x32_bf16 v[208:211], v[44:47], v[96:99], v[208:211]\n\t"
  "v_exp_f32 v240, v240\n\t"
  "v_mfma_f32_16x16x32_bf16 v[212:215], v[44:47], v[100:103], v[116:119]\n\t"
  "v_exp_f32 v241, v241\n\t"
  "v_mfma_f32_16x16x32_bf16 v[212:215], v[40:43], v[68:71], v[212:215]\n\t"
  "buffer_load_dwordx4 v4, s[52:55], s70 offen offset:0 nt lds\n\t"
  "v_accvgpr_write_b32 a141, v3\n\t"
  "v_mfma_f32_16x16x32_bf16 v[216:219], v[40:43], v[72:75], v[120:123]\n\t"
  "ds_read_b128 v[36:39], v17 offset:16640\n\t"
  "v_cvt_pk_bf16_f32 v0, v240, v241\n\t"
  "v_mfma_f32_16x16x32_bf16 v[216:219], v[44:47], v[104:107], v[216:219]\n\t"
  "v_exp_f32 v242, v242\n\t"
  "v_mfma_f32_16x16x32_bf16 v[220:223], v[44:47], v[108:111], v[124:127]\n\t"
  "v_exp_f32 v243, v243\n\t"
  "v_mfma_f32_16x16x32_bf16 v[220:223], v[40:43], v[76:79], v[220:223]\n\t"
  "v_accvgpr_write_b32 a130, v0\n\t"
  "s_waitcnt lgkmcnt(2)\n\t"
  "v_mfma_f32_16x16x32_bf16 v[208:211], v[48:51], v[128:131], v[208:211]\n\t"
  "ds_read_b128 v[40:43], v18 offset:16640\n\t"
  "v_cvt_pk_bf16_f32 v1, v242, v243\n\t"
  "v_mfma_f32_16x16x32_bf16 v[208:211], v[52:55], v[160:163], v[208:211]\n\t"
  "v_exp_f32 v244, v244\n\t"
  "v_mfma_f32_16x16x32_bf16 v[212:215], v[52:55], v[164:167], v[212:215]\n\t"
  "v_exp_f32 v245, v245\n\t"
  "v_mfma_f32_16x16x32_bf16 v[212:215], v[48:51], v[132:135], v[212:215]\n\t"
  "buffer_load_dwordx4 v5, s[52:55], s70 offen offset:1024 nt lds\n\t"
  "v_accvgpr_write_b32 a131, v1\n\t"
  "v_mfma_f32_16x16x32_bf16 v[216:219], v[48:51], v[136:139], v[216:219]\n\t"
  "ds_read_b128 v[44:47], v19 offset:16640\n\t"
  "v_cvt_pk_bf16_f32 v2, v244, v245\n\t"
  "v_mfma_f32_16x16x32_bf16 v[216:219], v[52:55], v[168:171], v[216:219]\n\t"
  "v_exp_f32 v246, v246\n\t"
  "v_mfma_f32_16x16x32_bf16 v[220:223], v[52:55], v[172:175], v[220:223]\n\t"
  "v_exp_f32 v247, v247\n\t"
  "v_mfma_f32_16x16x32_bf16 v[220:223], v[48:51], v[140:143], v[220:223]\n\t"
  "v_accvgpr_write_b32 a134, v2\n\t"
  "s_waitcnt lgkmcnt(2)\n\t"
  "v_mfma_f32_16x16x32_bf16 v[208:211], v[32:35], v[192:195], v[208:211]\n\t"
  "ds_read_b128 v[48:51], v16 offset:24576\n\t"
  "v_cvt_pk_bf16_f32 v3, v246, v247\n\t"
  "v_mfma_f32_16x16x32_bf16 v[208:211], v[36:39], v[224:227], v[208:211]\n\t"
  "v_exp_f32 v248, v248\n\t"
  "v_mfma_f32_16x16x32_bf16 v[212:215], v[36:39], v[228:231], v[212:215]\n\t"
  "v_exp_f32 v249, v249\n\t"
  "v_mfma_f32_16x16x32_bf16 v[212:215], v[32:35], v[196:199], v[212:215]\n\t"
  "buffer_load_dwordx4 v60, s[52:55], s70 offen offset:2048 nt lds\n\t"
  "v_accvgpr_write_b32 a135, v3\n\t"
  "v_mfma_f32_16x16x32_bf16 v[216:219], v[32:35], v[200:203], v[216:219]\n\t"
  "ds_read_b128 v[52:55], v17 offset:24576\n\t"
  "v_cvt_pk_bf16_f32 v0, v248, v249\n\t"
  "v_mfma_f32_16x16x32_bf16 v[216:219], v[36:39], v[232:235], v[216:219]\n\t"
  "v_exp_f32 v250, v250\n\t"
  "v_mfma_f32_16x16x32_bf16 v[220:223], v[36:39], v[236:239], v[220:223]\n\t"
  "v_exp_f32 v251, v251\n\t"
  "v_mfma_f32_16x16x32_bf16 v[220:223], v[32:35], v[204:207], v[220:223]\n\t"
  "v_accvgpr_write_b32 a138, v0\n\t"
  "s_waitcnt lgkmcnt(2)\n\t"
  "v_mfma_f32_16x16x32_bf16 v[208:211], v[40:43], v[80:83], v[208:211]\n\t"
  "ds_read_b128 v[32:35], v18 offset:24576\n\t"
  "v_cvt_pk_bf16_f32 v1, v250, v251\n\t"
  "v_mfma_f32_16x16x32_bf16 v[208:211], v[44:47], v[144:147], v[208:211]\n\t"
  "v_exp_f32 v252, v252\n\t"
  "v_mfma_f32_16x16x32_bf16 v[212:215], v[44:47], v[148:151], v[212:215]\n\t"
  "v_exp_f32 v253, v253\n\t"
  "v_mfma_f32_16x16x32_bf16 v[212:215], v[40:43], v[84:87], v[212:215]\n\t"
  "buffer_load_dwordx4 v61, s[52:55], s70 offen offset:3072 nt lds\n\t"
  "v_accvgpr_write_b32 a139, v1\n\t"
  "v_mfma_f32_16x16x32_bf16 v[216:219], v[40:43], v[88:91], v[216:219]\n\t"
  "ds_read_b128 v[36:39], v19 offset:24576\n\t"
  "v_cvt_pk_bf16_f32 v2, v252, v253\n\t"
  "v_mfma_f32_16x16x32_bf16 v[216:219], v[44:47], v[152:155], v[216:219]\n\t"
  "v_exp_f32 v254, v254\n\t"
  "v_mfma_f32_16x16x32_bf16 v[220:223], v[44:47], v[156:159], v[220:223]\n\t"
  "v_exp_f32 v255, v255\n\t"
  "v_mfma_f32_16x16x32_bf16 v[220:223], v[40:43], v[92:95], v[220:223]\n\t"
  "v_accvgpr_write_b32 a142, v2\n\t"
  "s_waitcnt lgkmcnt(2)\n\t"
  "v_mfma_f32_16x16x32_bf16 v[240:243], v[48:51], v[64:67], v[112:115]\n\t"
  "ds_read_b128 v[40:43], v16 offset:24832\n\t"
  "v_cvt_pk_bf16_f32 v3, v254, v255\n\t"
  "v_mfma_f32_16x16x32_bf16 v[240:243], v[52:55], v[96:99], v[240:243]\n\t"
  "v_exp_f32 v208, v208\n\t"
  "v_mfma_f32_16x16x32_bf16 v[244:247], v[52:55], v[100:103], v[116:119]\n\t"
  "v_exp_f32 v209, v209\n\t"
  "v_mfma_f32_16x16x32_bf16 v[244:247], v[48:51], v[68:71], v[244:247]\n\t"
  "v_accvgpr_write_b32 a143, v3\n\t"
  "s_add_u32 m0, s69, 49152\n\t"
  "v_mfma_f32_16x16x32_bf16 v[248:251], v[48:51], v[72:75], v[120:123]\n\t"
  "ds_read_b128 v[44:47], v17 offset:24832\n\t"
  "v_cvt_pk_bf16_f32 v0, v208, v209\n\t"
  "v_mfma_f32_16x16x32_bf16 v[248:251], v[52:55], v[104:107], v[248:251]\n\t"
  "v_exp_f32 v210, v210\n\t"
  "v_mfma_f32_16x16x32_bf16 v[252:255], v[52:55], v[108:111], v[124:127]\n\t"
  "v_exp_f32 v211, v211\n\t"
  "v_mfma_f32_16x16x32_bf16 v[252:255], v[48:51], v[76:79], v[252:255]\n\t"
  "v_accvgpr_write_b32 a144, v0\n\t"
  "s_waitcnt lgkmcnt(2)\n\t"
  "v_mfma_f32_16x16x32_bf16 v[240:243], v[32:35], v[128:131], v[240:243]\n\t"
  "ds_read_b128 v[48:51], v18 offset:24832\n\t"
  "v_cvt_pk_bf16_f32 v1, v210, v211\n\t"
  "v_mfma_f32_16x16x32_bf16 v[240:243], v[36:39], v[160:163], v[240:243]\n\t"
  "v_exp_f32 v212, v212\n\t"
  "v_mfma_f32_16x16x32_bf16 v[244:247], v[36:39], v[164:167], v[244:247]\n\t"
  "v_exp_f32 v213, v213\n\t"
  "v_mfma_f32_16x16x32_bf16 v[244:247], v[32:35], v[132:135], v[244:247]\n\t"
  "v_accvgpr_write_b32 a145, v1\n\t"
  "s_add_u32 s70, s75, 65536\n\t"
  "v_mfma_f32_16x16x32_bf16 v[248:251], v[32:35], v[136:139], v[248:251]\n\t"
  "ds_read_b128 v[52:55], v19 offset:24832\n\t"
  "v_cvt_pk_bf16_f32 v2, v212, v213\n\t"
  "v_add_u32 v16, 0x10000, v16\n\t"
  "v_mfma_f32_16x16x32_bf16 v[248:251], v[36:39], v[168:171], v[248:251]\n\t"
  "v_exp_f32 v214, v214\n\t"
  "v_add_u32 v17, 0x10000, v17\n\t"
  "v_mfma_f32_16x16x32_bf16 v[252:255], v[36:39], v[172:175], v[252:255]\n\t"
  "v_exp_f32 v215, v215\n\t"
  "v_add_u32 v18, 0x10000, v18\n\t"
  "v_mfma_f32_16x16x32_bf16 v[252:255], v[32:35], v[140:143], v[252:255]\n\t"
  "v_accvgpr_write_b32 a148, v2\n\t"
  "v_add_u32 v19, 0x10000, v19\n\t"
  "s_waitcnt lgkmcnt(2)\n\t"
  "v_mfma_f32_16x16x32_bf16 v[240:243], v[40:43], v[192:195], v[240:243]\n\t"
  "ds_read_b128 v[32:35], v16 offset:32768\n\t"
  "v_cvt_pk_bf16_f32 v3, v214, v215\n\t"
  "v_mfma_f32_16x16x32_bf16 v[240:243], v[44:47], v[224:227], v[240:243]\n\t"
  "v_exp_f32 v216, v216\n\t"
  "v_mfma_f32_16x16x32_bf16 v[244:247], v[44:47], v[228:231], v[244:247]\n\t"
  "v_exp_f32 v217, v217\n\t"
  "v_mfma_f32_16x16x32_bf16 v[244:247], v[40:43], v[196:199], v[244:247]\n\t"
  "buffer_load_dwordx4 v4, s[52:55], s70 offen offset:0 nt lds\n\t"
  "v_accvgpr_write_b32 a149, v3\n\t"
  "v_mfma_f32_16x16x32_bf16 v[248:251], v[40:43], v[200:203], v[248:251]\n\t"
  "ds_read_b128 v[36:39], v17 offset:32768\n\t"
  "v_cvt_pk_bf16_f32 v0, v216, v217\n\t"
  "v_mfma_f32_16x16x32_bf16 v[248:251], v[44:47], v[232:235], v[248:251]\n\t"
  "v_exp_f32 v218, v218\n\t"
  "v_mfma_f32_16x16x32_bf16 v[252:255], v[44:47], v[236:239], v[252:255]\n\t"
  "v_exp_f32 v219, v219\n\t"
  "v_mfma_f32_16x16x32_bf16 v[252:255], v[40:43], v[204:207], v[252:255]\n\t"
  "v_accvgpr_write_b32 a152, v0\n\t"
  "s_waitcnt lgkmcnt(2)\n\t"
  "v_mfma_f32_16x16x32_bf16 v[240:243], v[48:51], v[80:83], v[240:243]\n\t"
  "ds_read_b128 v[40:43], v18 offset:32768\n\t"
  "v_cvt_pk_bf16_f32 v1, v218, v219\n\t"
  "v_mfma_f32_16x16x32_bf16 v[240:243], v[52:55], v[144:147], v[240:243]\n\t"
  "v_exp_f32 v220, v220\n\t"
  "v_mfma_f32_16x16x32_bf16 v[244:247], v[52:55], v[148:151], v[244:247]\n\t"
  "v_exp_f32 v221, v221\n\t"
  "v_mfma_f32_16x16x32_bf16 v[244:247], v[48:51], v[84:87], v[244:247]\n\t"
  "buffer_load_dwordx4 v5, s[52:55], s70 offen offset:1024 nt lds\n\t"
  "v_accvgpr_write_b32 a153, v1\n\t"
  "v_mfma_f32_16x16x32_bf16 v[248:251], v[48:51], v[88:91], v[248:251]\n\t"
  "ds_read_b128 v[44:47], v19 offset:32768\n\t"
  "v_cvt_pk_bf16_f32 v2, v220, v221\n\t"
  "v_mfma_f32_16x16x32_bf16 v[248:251], v[52:55], v[152:155], v[248:251]\n\t"
  "v_exp_f32 v222, v222\n\t"
  "v_mfma_f32_16x16x32_bf16 v[252:255], v[52:55], v[156:159], v[252:255]\n\t"
  "v_exp_f32 v223, v223\n\t"
  "v_mfma_f32_16x16x32_bf16 v[252:255], v[48:51], v[92:95], v[252:255]\n\t"
  "v_accvgpr_write_b32 a156, v2\n\t"
  "s_waitcnt lgkmcnt(2)\n\t"
  "v_mfma_f32_16x16x32_bf16 v[208:211], v[32:35], v[64:67], v[112:115]\n\t"
  "ds_read_b128 v[48:51], v16 offset:33024\n\t"
  "v_cvt_pk_bf16_f32 v3, v222, v223\n\t"
  "v_mfma_f32_16x16x32_bf16 v[208:211], v[36:39], v[96:99], v[208:211]\n\t"
  "v_exp_f32 v240, v240\n\t"
  "v_mfma_f32_16x16x32_bf16 v[212:215], v[36:39], v[100:103], v[116:119]\n\t"
  "v_exp_f32 v241, v241\n\t"
  "v_mfma_f32_16x16x32_bf16 v[212:215], v[32:35], v[68:71], v[212:215]\n\t"
  "buffer_load_dwordx4 v60, s[52:55], s70 offen offset:2048 nt lds\n\t"
  "v_accvgpr_write_b32 a157, v3\n\t"
  "v_mfma_f32_16x16x32_bf16 v[216:219], v[32:35], v[72:75], v[120:123]\n\t"
  "ds_read_b128 v[52:55], v17 offset:33024\n\t"
  "v_cvt_pk_bf16_f32 v0, v240, v241\n\t"
  "v_mfma_f32_16x16x32_bf16 v[216:219], v[36:39], v[104:107], v[216:219]\n\t"
  "v_exp_f32 v242, v242\n\t"
  "v_mfma_f32_16x16x32_bf16 v[220:223], v[36:39], v[108:111], v[124:127]\n\t"
  "v_exp_f32 v243, v243\n\t"
  "v_mfma_f32_16x16x32_bf16 v[220:223], v[32:35], v[76:79], v[220:223]\n\t"
  "v_accvgpr_write_b32 a146, v0\n\t"
  "s_waitcnt lgkmcnt(2)\n\t"
  "v_mfma_f32_16x16x32_bf16 v[208:211], v[40:43], v[128:131], v[208:211]\n\t"
  "ds_read_b128 v[32:35], v18 offset:33024\n\t"
  "v_cvt_pk_bf16_f32 v1, v242, v243\n\t"
  "v_mfma_f32_16x16x32_bf16 v[208:211], v[44:47], v[160:163], v[208:211]\n\t"
  "v_exp_f32 v244, v244\n\t"
  "v_mfma_f32_16x16x32_bf16 v[212:215], v[44:47], v[164:167], v[212:215]\n\t"
  "v_exp_f32 v245, v245\n\t"
  "v_mfma_f32_16x16x32_bf16 v[212:215], v[40:43], v[132:135], v[212:215]\n\t"
  "buffer_load_dwordx4 v61, s[52:55], s70 offen offset:3072 nt lds\n\t"
  "v_accvgpr_write_b32 a147, v1\n\t"
  "v_mfma_f32_16x16x32_bf16 v[216:219], v[40:43], v[136:139], v[216:219]\n\t"
  "ds_read_b128 v[36:39], v19 offset:33024\n\t"
  "v_cvt_pk_bf16_f32 v2, v244, v245\n\t"
  "v_mfma_f32_16x16x32_bf16 v[216:219], v[44:47], v[168:171], v[216:219]\n\t"
  "v_exp_f32 v246, v246\n\t"
  "v_mfma_f32_16x16x32_bf16 v[220:223], v[44:47], v[172:175], v[220:223]\n\t"
  "v_exp_f32 v247, v247\n\t"
  "v_mfma_f32_16x16x32_bf16 v[220:223], v[40:43], v[140:143], v[220:223]\n\t"
  "v_accvgpr_write_b32 a150, v2\n\t"
  "s_waitcnt lgkmcnt(2)\n\t"
  "v_mfma_f32_16x16x32_bf16 v[208:211], v[48:51], v[192:195], v[208:211]\n\t"
  "ds_read_b128 v[40:43], v16 offset:40960\n\t"
  "v_cvt_pk_bf16_f32 v3, v246, v247\n\t"
  "v_mfma_f32_16x16x32_bf16 v[208:211], v[52:55], v[224:227], v[208:211]\n\t"
  "v_exp_f32 v248, v248\n\t"
  "v_mfma_f32_16x16x32_bf16 v[212:215], v[52:55], v[228:231], v[212:215]\n\t"
  "v_exp_f32 v249, v249\n\t"
  "v_mfma_f32_16x16x32_bf16 v[212:215], v[48:51], v[196:199], v[212:215]\n\t"
  "buffer_load_dword v11, v10, s[60:63], 0 offen offset:0\n\t"
  "v_accvgpr_write_b32 a151, v3\n\t"
  "v_mfma_f32_16x16x32_bf16 v[216:219], v[48:51], v[200:203], v[216:219]\n\t"
  "ds_read_b128 v[44:47], v17 offset:40960\n\t"
  "v_cvt_pk_bf16_f32 v0, v248, v249\n\t"
  "v_mfma_f32_16x16x32_bf16 v[216:219], v[52:55], v[232:235], v[216:219]\n\t"
  "v_exp_f32 v250, v250\n\t"
  "v_mfma_f32_16x16x32_bf16 v[220:223], v[52:55], v[236:239], v[220:223]\n\t"
  "v_exp_f32 v251, v251\n\t"
  "v_mfma_f32_16x16x32_bf16 v[220:223], v[48:51], v[204:207], v[220:223]\n\t"
  "v_accvgpr_write_b32 a154, v0\n\t"
  "s_waitcnt lgkmcnt(2)\n\t"
  "v_mfma_f32_16x16x32_bf16 v[208:211], v[32:35], v[80:83], v[208:211]\n\t"
  "ds_read_b128 v[48:51], v18 offset:40960\n\t"
  "v_cvt_pk_bf16_f32 v1, v250, v251\n\t"
  "v_mfma_f32_16x16x32_bf16 v[208:211], v[36:39], v[144:147], v[208:211]\n\t"
  "v_exp_f32 v252, v252\n\t"
  "v_mfma_f32_16x16x32_bf16 v[212:215], v[36:39], v[148:151], v[212:215]\n\t"
  "v_exp_f32 v253, v253\n\t"
  "v_mfma_f32_16x16x32_bf16 v[212:215], v[32:35], v[84:87], v[212:215]\n\t"
  "v_accvgpr_write_b32 a155, v1\n\t"
  "v_mfma_f32_16x16x32_bf16 v[216:219], v[32:35], v[88:91], v[216:219]\n\t"
  "ds_read_b128 v[52:55], v19 offset:40960\n\t"
  "v_cvt_pk_bf16_f32 v2, v252, v253\n\t"
  "v_mfma_f32_16x16x32_bf16 v[216:219], v[36:39], v[152:155], v[216:219]\n\t"
  "v_exp_f32 v254, v254\n\t"
  "v_mfma_f32_16x16x32_bf16 v[220:223], v[36:39], v[156:159], v[220:223]\n\t"
  "v_exp_f32 v255, v255\n\t"
  "v_mfma_f32_16x16x32_bf16 v[220:223], v[32:35], v[92:95], v[220:223]\n\t"
  "v_accvgpr_write_b32 a158, v2\n\t"
  "s_waitcnt lgkmcnt(2)\n\t"
  "v_mfma_f32_16x16x32_bf16 v[240:243], v[40:43], v[64:67], v[112:115]\n\t"
  "ds_read_b128 v[32:35], v16 offset:41216\n\t"
  "v_cvt_pk_bf16_f32 v3, v254, v255\n\t"
  "v_mfma_f32_16x16x32_bf16 v[240:243], v[44:47], v[96:99], v[240:243]\n\t"
  "v_exp_f32 v208, v208\n\t"
  "v_mfma_f32_16x16x32_bf16 v[244:247], v[44:47], v[100:103], v[116:119]\n\t"
  "v_exp_f32 v209, v209\n\t"
  "v_mfma_f32_16x16x32_bf16 v[244:247], v[40:43], v[68:71], v[244:247]\n\t"
  "v_accvgpr_write_b32 a159, v3\n\t"
  "v_mfma_f32_16x16x32_bf16 v[248:251], v[40:43], v[72:75], v[120:123]\n\t"
  "ds_read_b128 v[36:39], v17 offset:41216\n\t"
  "v_cvt_pk_bf16_f32 v0, v208, v209\n\t"
  "v_mfma_f32_16x16x32_bf16 v[248:251], v[44:47], v[104:107], v[248:251]\n\t"
  "v_exp_f32 v210, v210\n\t"
  "v_mfma_f32_16x16x32_bf16 v[252:255], v[44:47], v[108:111], v[124:127]\n\t"
  "v_exp_f32 v211, v211\n\t"
  "v_mfma_f32_16x16x32_bf16 v[252:255], v[40:43], v[76:79], v[252:255]\n\t"
  "v_accvgpr_write_b32 a160, v0\n\t"
  "s_waitcnt lgkmcnt(2)\n\t"
  "v_mfma_f32_16x16x32_bf16 v[240:243], v[48:51], v[128:131], v[240:243]\n\t"
  "ds_read_b128 v[40:43], v18 offset:41216\n\t"
  "v_cvt_pk_bf16_f32 v1, v210, v211\n\t"
  "v_mfma_f32_16x16x32_bf16 v[240:243], v[52:55], v[160:163], v[240:243]\n\t"
  "v_exp_f32 v212, v212\n\t"
  "v_mfma_f32_16x16x32_bf16 v[244:247], v[52:55], v[164:167], v[244:247]\n\t"
  "v_exp_f32 v213, v213\n\t"
  "v_mfma_f32_16x16x32_bf16 v[244:247], v[48:51], v[132:135], v[244:247]\n\t"
  "v_accvgpr_write_b32 a161, v1\n\t"
  "s_waitcnt vmcnt(23)\n\t"
  "s_barrier\n\t"
  "v_mfma_f32_16x16x32_bf16 v[248:251], v[48:51], v[136:139], v[248:251]\n\t"
  "ds_read_b128 v[44:47], v19 offset:41216\n\t"
  "v_cvt_pk_bf16_f32 v2, v212, v213\n\t"
  "v_mfma_f32_16x16x32_bf16 v[248:251], v[52:55], v[168:171], v[248:251]\n\t"
  "v_exp_f32 v214, v214\n\t"
  "v_mfma_f32_16x16x32_bf16 v[252:255], v[52:55], v[172:175], v[252:255]\n\t"
  "v_exp_f32 v215, v215\n\t"
  "v_mfma_f32_16x16x32_bf16 v[252:255], v[48:51], v[140:143], v[252:255]\n\t"
  "v_accvgpr_write_b32 a164, v2\n\t"
  "s_waitcnt lgkmcnt(2)\n\t"
  "v_mfma_f32_16x16x32_bf16 v[240:243], v[32:35], v[192:195], v[240:243]\n\t"
  "ds_read_b128 v[48:51], v16 offset:49152\n\t"
  "v_cvt_pk_bf16_f32 v3, v214, v215\n\t"
  "v_mfma_f32_16x16x32_bf16 v[240:243], v[36:39], v[224:227], v[240:243]\n\t"
  "v_exp_f32 v216, v216\n\t"
  "v_mfma_f32_16x16x32_bf16 v[244:247], v[36:39], v[228:231], v[244:247]\n\t"
  "v_exp_f32 v217, v217\n\t"
  "v_mfma_f32_16x16x32_bf16 v[244:247], v[32:35], v[196:199], v[244:247]\n\t"
  "v_accvgpr_write_b32 a165, v3\n\t"
  "s_add_u32 m0, s69, 0\n\t"
  "v_mfma_f32_16x16x32_bf16 v[248:251], v[32:35], v[200:203], v[248:251]\n\t"
  "ds_read_b128 v[52:55], v17 offset:49152\n\t"
  "v_cvt_pk_bf16_f32 v0, v216, v217\n\t"
  "v_mfma_f32_16x16x32_bf16 v[248:251], v[36:39], v[232:235], v[248:251]\n\t"
  "v_exp_f32 v218, v218\n\t"
  "v_mfma_f32_16x16x32_bf16 v[252:255], v[36:39], v[236:239], v[252:255]\n\t"
  "v_exp_f32 v219, v219\n\t"
  "v_mfma_f32_16x16x32_bf16 v[252:255], v[32:35], v[204:207], v[252:255]\n\t"
  "v_accvgpr_write_b32 a168, v0\n\t"
  "s_waitcnt lgkmcnt(2)\n\t"
  "v_mfma_f32_16x16x32_bf16 v[240:243], v[40:43], v[80:83], v[240:243]\n\t"
  "ds_read_b128 v[32:35], v18 offset:49152\n\t"
  "v_cvt_pk_bf16_f32 v1, v218, v219\n\t"
  "v_mfma_f32_16x16x32_bf16 v[240:243], v[44:47], v[144:147], v[240:243]\n\t"
  "v_exp_f32 v220, v220\n\t"
  "v_mfma_f32_16x16x32_bf16 v[244:247], v[44:47], v[148:151], v[244:247]\n\t"
  "v_exp_f32 v221, v221\n\t"
  "v_mfma_f32_16x16x32_bf16 v[244:247], v[40:43], v[84:87], v[244:247]\n\t"
  "v_accvgpr_write_b32 a169, v1\n\t"
  "s_add_u32 s70, s75, 131072\n\t"
  "v_mfma_f32_16x16x32_bf16 v[248:251], v[40:43], v[88:91], v[248:251]\n\t"
  "ds_read_b128 v[36:39], v19 offset:49152\n\t"
  "v_cvt_pk_bf16_f32 v2, v220, v221\n\t"
  "v_mfma_f32_16x16x32_bf16 v[248:251], v[44:47], v[152:155], v[248:251]\n\t"
  "v_exp_f32 v222, v222\n\t"
  "v_mfma_f32_16x16x32_bf16 v[252:255], v[44:47], v[156:159], v[252:255]\n\t"
  "v_exp_f32 v223, v223\n\t"
  "v_mfma_f32_16x16x32_bf16 v[252:255], v[40:43], v[92:95], v[252:255]\n\t"
  "v_accvgpr_write_b32 a172, v2\n\t"
  "s_waitcnt lgkmcnt(2)\n\t"
  "v_mfma_f32_16x16x32_bf16 v[208:211], v[48:51], v[64:67], v[112:115]\n\t"
  "ds_read_b128 v[40:43], v16 offset:49408\n\t"
  "v_cvt_pk_bf16_f32 v3, v222, v223\n\t"
  "v_mfma_f32_16x16x32_bf16 v[208:211], v[52:55], v[96:99], v[208:211]\n\t"
  "v_exp_f32 v240, v240\n\t"
  "v_mfma_f32_16x16x32_bf16 v[212:215], v[52:55], v[100:103], v[116:119]\n\t"
  "v_exp_f32 v241, v241\n\t"
  "v_mfma_f32_16x16x32_bf16 v[212:215], v[48:51], v[68:71], v[212:215]\n\t"
  "buffer_load_dwordx4 v4, s[52:55], s70 offen offset:0 nt lds\n\t"
  "v_accvgpr_write_b32 a173, v3\n\t"
  "v_mfma_f32_16x16x32_bf16 v[216:219], v[48:51], v[72:75], v[120:123]\n\t"
  "ds_read_b128 v[44:47], v17 offset:49408\n\t"
  "v_cvt_pk_bf16_f32 v0, v240, v241\n\t"
  "v_mfma_f32_16x16x32_bf16 v[216:219], v[52:55], v[104:107], v[216:219]\n\t"
  "v_exp_f32 v242, v242\n\t"
  "v_mfma_f32_16x16x32_bf16 v[220:223], v[52:55], v[108:111], v[124:127]\n\t"
  "v_exp_f32 v243, v243\n\t"
  "v_mfma_f32_16x16x32_bf16 v[220:223], v[48:51], v[76:79], v[220:223]\n\t"
  "v_accvgpr_write_b32 a162, v0\n\t"
  "s_waitcnt lgkmcnt(2)\n\t"
  "v_mfma_f32_16x16x32_bf16 v[208:211], v[32:35], v[128:131], v[208:211]\n\t"
  "ds_read_b128 v[48:51], v18 offset:49408\n\t"
  "v_cvt_pk_bf16_f32 v1, v242, v243\n\t"
  "v_mfma_f32_16x16x32_bf16 v[208:211], v[36:39], v[160:163], v[208:211]\n\t"
  "v_exp_f32 v244, v244\n\t"
  "v_mfma_f32_16x16x32_bf16 v[212:215], v[36:39], v[164:167], v[212:215]\n\t"
  "v_exp_f32 v245, v245\n\t"
  "v_mfma_f32_16x16x32_bf16 v[212:215], v[32:35], v[132:135], v[212:215]\n\t"
  "buffer_load_dwordx4 v5, s[52:55], s70 offen offset:1024 nt lds\n\t"
  "v_accvgpr_write_b32 a163, v1\n\t"
  "v_mfma_f32_16x16x32_bf16 v[216:219], v[32:35], v[136:139], v[216:219]\n\t"
  "ds_read_b128 v[52:55], v19 offset:49408\n\t"
  "v_cvt_pk_bf16_f32 v2, v244, v245\n\t"
  "v_mfma_f32_16x16x32_bf16 v[216:219], v[36:39], v[168:171], v[216:219]\n\t"
  "v_exp_f32 v246, v246\n\t"
  "v_mfma_f32_16x16x32_bf16 v[220:223], v[36:39], v[172:175], v[220:223]\n\t"
  "v_exp_f32 v247, v247\n\t"
  "v_mfma_f32_16x16x32_bf16 v[220:223], v[32:35], v[140:143], v[220:223]\n\t"
  "v_accvgpr_write_b32 a166, v2\n\t"
  "s_waitcnt lgkmcnt(2)\n\t"
  "v_mfma_f32_16x16x32_bf16 v[208:211], v[40:43], v[192:195], v[208:211]\n\t"
  "ds_read_b128 v[32:35], v16 offset:57344\n\t"
  "v_cvt_pk_bf16_f32 v3, v246, v247\n\t"
  "v_mfma_f32_16x16x32_bf16 v[208:211], v[44:47], v[224:227], v[208:211]\n\t"
  "v_exp_f32 v248, v248\n\t"
  "v_mfma_f32_16x16x32_bf16 v[212:215], v[44:47], v[228:231], v[212:215]\n\t"
  "v_exp_f32 v249, v249\n\t"
  "v_mfma_f32_16x16x32_bf16 v[212:215], v[40:43], v[196:199], v[212:215]\n\t"
  "buffer_load_dwordx4 v60, s[52:55], s70 offen offset:2048 nt lds\n\t"
  "v_accvgpr_write_b32 a167, v3\n\t"
  "v_mfma_f32_16x16x32_bf16 v[216:219], v[40:43], v[200:203], v[216:219]\n\t"
  "ds_read_b128 v[36:39], v17 offset:57344\n\t"
  "v_cvt_pk_bf16_f32 v0, v248, v249\n\t"
  "v_mfma_f32_16x16x32_bf16 v[216:219], v[44:47], v[232:235], v[216:219]\n\t"
  "v_exp_f32 v250, v250\n\t"
  "v_mfma_f32_16x16x32_bf16 v[220:223], v[44:47], v[236:239], v[220:223]\n\t"
  "v_exp_f32 v251, v251\n\t"
  "v_mfma_f32_16x16x32_bf16 v[220:223], v[40:43], v[204:207], v[220:223]\n\t"
  "v_accvgpr_write_b32 a170, v0\n\t"
  "s_waitcnt lgkmcnt(2)\n\t"
  "v_mfma_f32_16x16x32_bf16 v[208:211], v[48:51], v[80:83], v[208:211]\n\t"
  "ds_read_b128 v[40:43], v18 offset:57344\n\t"
  "v_cvt_pk_bf16_f32 v1, v250, v251\n\t"
  "v_mfma_f32_16x16x32_bf16 v[208:211], v[52:55], v[144:147], v[208:211]\n\t"
  "v_exp_f32 v252, v252\n\t"
  "v_mfma_f32_16x16x32_bf16 v[212:215], v[52:55], v[148:151], v[212:215]\n\t"
  "v_exp_f32 v253, v253\n\t"
  "v_mfma_f32_16x16x32_bf16 v[212:215], v[48:51], v[84:87], v[212:215]\n\t"
  "buffer_load_dwordx4 v61, s[52:55], s70 offen offset:3072 nt lds\n\t"
  "v_accvgpr_write_b32 a171, v1\n\t"
  "v_mfma_f32_16x16x32_bf16 v[216:219], v[48:51], v[88:91], v[216:219]\n\t"
  "ds_read_b128 v[44:47], v19 offset:57344\n\t"
  "v_cvt_pk_bf16_f32 v2, v252, v253\n\t"
  "v_mfma_f32_16x16x32_bf16 v[216:219], v[52:55], v[152:155], v[216:219]\n\t"
  "v_exp_f32 v254, v254\n\t"
  "v_mfma_f32_16x16x32_bf16 v[220:223], v[52:55], v[156:159], v[220:223]\n\t"
  "v_exp_f32 v255, v255\n\t"
  "v_mfma_f32_16x16x32_bf16 v[220:223], v[48:51], v[92:95], v[220:223]\n\t"
  "v_accvgpr_write_b32 a174, v2\n\t"
  "s_waitcnt lgkmcnt(2)\n\t"
  "v_mfma_f32_16x16x32_bf16 v[240:243], v[32:35], v[64:67], v[112:115]\n\t"
  "ds_read_b128 v[48:51], v16 offset:57600\n\t"
  "v_cvt_pk_bf16_f32 v3, v254, v255\n\t"
  "v_mfma_f32_16x16x32_bf16 v[240:243], v[36:39], v[96:99], v[240:243]\n\t"
  "v_exp_f32 v208, v208\n\t"
  "v_mfma_f32_16x16x32_bf16 v[244:247], v[36:39], v[100:103], v[116:119]\n\t"
  "v_exp_f32 v209, v209\n\t"
  "v_mfma_f32_16x16x32_bf16 v[244:247], v[32:35], v[68:71], v[244:247]\n\t"
  "v_accvgpr_write_b32 a175, v3\n\t"
  "s_add_u32 m0, s69, 16384\n\t"
  "v_mfma_f32_16x16x32_bf16 v[248:251], v[32:35], v[72:75], v[120:123]\n\t"
  "ds_read_b128 v[52:55], v17 offset:57600\n\t"
  "v_cvt_pk_bf16_f32 v0, v208, v209\n\t"
  "v_mfma_f32_16x16x32_bf16 v[248:251], v[36:39], v[104:107], v[248:251]\n\t"
  "v_exp_f32 v210, v210\n\t"
  "v_mfma_f32_16x16x32_bf16 v[252:255], v[36:39], v[108:111], v[124:127]\n\t"
  "v_exp_f32 v211, v211\n\t"
  "v_mfma_f32_16x16x32_bf16 v[252:255], v[32:35], v[76:79], v[252:255]\n\t"
  "v_accvgpr_write_b32 a176, v0\n\t"
  "s_waitcnt lgkmcnt(2)\n\t"
  "v_mfma_f32_16x16x32_bf16 v[240:243], v[40:43], v[128:131], v[240:243]\n\t"
  "ds_read_b128 v[32:35], v18 offset:57600\n\t"
  "v_cvt_pk_bf16_f32 v1, v210, v211\n\t"
  "v_mfma_f32_16x16x32_bf16 v[240:243], v[44:47], v[160:163], v[240:243]\n\t"
  "v_exp_f32 v212, v212\n\t"
  "v_mfma_f32_16x16x32_bf16 v[244:247], v[44:47], v[164:167], v[244:247]\n\t"
  "v_exp_f32 v213, v213\n\t"
  "v_mfma_f32_16x16x32_bf16 v[244:247], v[40:43], v[132:135], v[244:247]\n\t"
  "v_accvgpr_write_b32 a177, v1\n\t"
  "s_add_u32 s70, s75, 196608\n\t"
  "v_mfma_f32_16x16x32_bf16 v[248:251], v[40:43], v[136:139], v[248:251]\n\t"
  "ds_read_b128 v[36:39], v19 offset:57600\n\t"
  "v_cvt_pk_bf16_f32 v2, v212, v213\n\t"
  "v_add_u32 v16, 0x10000, v16\n\t"
  "v_mfma_f32_16x16x32_bf16 v[248:251], v[44:47], v[168:171], v[248:251]\n\t"
  "v_exp_f32 v214, v214\n\t"
  "v_add_u32 v17, 0x10000, v17\n\t"
  "v_mfma_f32_16x16x32_bf16 v[252:255], v[44:47], v[172:175], v[252:255]\n\t"
  "v_exp_f32 v215, v215\n\t"
  "v_add_u32 v18, 0x10000, v18\n\t"
  "v_mfma_f32_16x16x32_bf16 v[252:255], v[40:43], v[140:143], v[252:255]\n\t"
  "v_accvgpr_write_b32 a180, v2\n\t"
  "v_add_u32 v19, 0x10000, v19\n\t"
  "s_waitcnt lgkmcnt(2)\n\t"
  "v_mfma_f32_16x16x32_bf16 v[240:243], v[48:51], v[192:195], v[240:243]\n\t"
  "ds_read_b128 v[40:43], v16 offset:0\n\t"
  "v_cvt_pk_bf16_f32 v3, v214, v215\n\t"
  "v_mfma_f32_16x16x32_bf16 v[240:243], v[52:55], v[224:227], v[240:243]\n\t"
  "v_exp_f32 v216, v216\n\t"
  "v_mfma_f32_16x16x32_bf16 v[244:247], v[52:55], v[228:231], v[244:247]\n\t"
  "v_exp_f32 v217, v217\n\t"
  "v_mfma_f32_16x16x32_bf16 v[244:247], v[48:51], v[196:199], v[244:247]\n\t"
  "buffer_load_dwordx4 v4, s[52:55], s70 offen offset:0 nt lds\n\t"
  "v_accvgpr_write_b32 a181, v3\n\t"
  "v_mfma_f32_16x16x32_bf16 v[248:251], v[48:51], v[200:203], v[248:251]\n\t"
  "ds_read_b128 v[44:47], v17 offset:0\n\t"
  "v_cvt_pk_bf16_f32 v0, v216, v217\n\t"
  "v_mfma_f32_16x16x32_bf16 v[248:251], v[52:55], v[232:235], v[248:251]\n\t"
  "v_exp_f32 v218, v218\n\t"
  "v_mfma_f32_16x16x32_bf16 v[252:255], v[52:55], v[236:239], v[252:255]\n\t"
  "v_exp_f32 v219, v219\n\t"
  "v_mfma_f32_16x16x32_bf16 v[252:255], v[48:51], v[204:207], v[252:255]\n\t"
  "v_accvgpr_write_b32 a184, v0\n\t"
  "s_waitcnt lgkmcnt(2)\n\t"
  "v_mfma_f32_16x16x32_bf16 v[240:243], v[32:35], v[80:83], v[240:243]\n\t"
  "ds_read_b128 v[48:51], v18 offset:0\n\t"
  "v_cvt_pk_bf16_f32 v1, v218, v219\n\t"
  "v_mfma_f32_16x16x32_bf16 v[240:243], v[36:39], v[144:147], v[240:243]\n\t"
  "v_exp_f32 v220, v220\n\t"
  "v_mfma_f32_16x16x32_bf16 v[244:247], v[36:39], v[148:151], v[244:247]\n\t"
  "v_exp_f32 v221, v221\n\t"
  "v_mfma_f32_16x16x32_bf16 v[244:247], v[32:35], v[84:87], v[244:247]\n\t"
  "buffer_load_dwordx4 v5, s[52:55], s70 offen offset:1024 nt lds\n\t"
  "v_accvgpr_write_b32 a185, v1\n\t"
  "v_mfma_f32_16x16x32_bf16 v[248:251], v[32:35], v[88:91], v[248:251]\n\t"
  "ds_read_b128 v[52:55], v19 offset:0\n\t"
  "v_cvt_pk_bf16_f32 v2, v220, v221\n\t"
  "v_mfma_f32_16x16x32_bf16 v[248:251], v[36:39], v[152:155], v[248:251]\n\t"
  "v_exp_f32 v222, v222\n\t"
  "v_mfma_f32_16x16x32_bf16 v[252:255], v[36:39], v[156:159], v[252:255]\n\t"
  "v_exp_f32 v223, v223\n\t"
  "v_mfma_f32_16x16x32_bf16 v[252:255], v[32:35], v[92:95], v[252:255]\n\t"
  "v_accvgpr_write_b32 a188, v2\n\t"
  "s_waitcnt lgkmcnt(2)\n\t"
  "v_mfma_f32_16x16x32_bf16 v[208:211], v[40:43], v[64:67], v[112:115]\n\t"
  "ds_read_b128 v[32:35], v16 offset:256\n\t"
  "v_cvt_pk_bf16_f32 v3, v222, v223\n\t"
  "v_mfma_f32_16x16x32_bf16 v[208:211], v[44:47], v[96:99], v[208:211]\n\t"
  "v_exp_f32 v240, v240\n\t"
  "v_mfma_f32_16x16x32_bf16 v[212:215], v[44:47], v[100:103], v[116:119]\n\t"
  "v_exp_f32 v241, v241\n\t"
  "v_mfma_f32_16x16x32_bf16 v[212:215], v[40:43], v[68:71], v[212:215]\n\t"
  "buffer_load_dwordx4 v60, s[52:55], s70 offen offset:2048 nt lds\n\t"
  "v_accvgpr_write_b32 a189, v3\n\t"
  "v_mfma_f32_16x16x32_bf16 v[216:219], v[40:43], v[72:75], v[120:123]\n\t"
  "ds_read_b128 v[36:39], v17 offset:256\n\t"
  "v_cvt_pk_bf16_f32 v0, v240, v241\n\t"
  "v_mfma_f32_16x16x32_bf16 v[216:219], v[44:47], v[104:107], v[216:219]\n\t"
  "v_exp_f32 v242, v242\n\t"
  "v_mfma_f32_16x16x32_bf16 v[220:223], v[44:47], v[108:111], v[124:127]\n\t"
  "v_exp_f32 v243, v243\n\t"
  "v_mfma_f32_16x16x32_bf16 v[220:223], v[40:43], v[76:79], v[220:223]\n\t"
  "v_accvgpr_write_b32 a178, v0\n\t"
  "s_waitcnt lgkmcnt(2)\n\t"
  "v_mfma_f32_16x16x32_bf16 v[208:211], v[48:51], v[128:131], v[208:211]\n\t"
  "ds_read_b128 v[40:43], v18 offset:256\n\t"
  "v_cvt_pk_bf16_f32 v1, v242, v243\n\t"
  "v_mfma_f32_16x16x32_bf16 v[208:211], v[52:55], v[160:163], v[208:211]\n\t"
  "v_exp_f32 v244, v244\n\t"
  "v_mfma_f32_16x16x32_bf16 v[212:215], v[52:55], v[164:167], v[212:215]\n\t"
  "v_exp_f32 v245, v245\n\t"
  "v_mfma_f32_16x16x32_bf16 v[212:215], v[48:51], v[132:135], v[212:215]\n\t"
  "buffer_load_dwordx4 v61, s[52:55], s70 offen offset:3072 nt lds\n\t"
  "v_accvgpr_write_b32 a179, v1\n\t"
  "v_mfma_f32_16x16x32_bf16 v[216:219], v[48:51], v[136:139], v[216:219]\n\t"
  "ds_read_b128 v[44:47], v19 offset:256\n\t"
  "v_cvt_pk_bf16_f32 v2, v244, v245\n\t"
  "v_mfma_f32_16x16x32_bf16 v[216:219], v[52:55], v[168:171], v[216:219]\n\t"
  "v_exp_f32 v246, v246\n\t"
  "v_mfma_f32_16x16x32_bf16 v[220:223], v[52:55], v[172:175], v[220:223]\n\t"
  "v_exp_f32 v247, v247\n\t"
  "v_mfma_f32_16x16x32_bf16 v[220:223], v[48:51], v[140:143], v[220:223]\n\t"
  "v_accvgpr_write_b32 a182, v2\n\t"
  "s_waitcnt lgkmcnt(2)\n\t"
  "v_mfma_f32_16x16x32_bf16 v[208:211], v[32:35], v[192:195], v[208:211]\n\t"
  "ds_read_b128 v[48:51], v16 offset:8192\n\t"
  "v_cvt_pk_bf16_f32 v3, v246, v247\n\t"
  "v_mfma_f32_16x16x32_bf16 v[208:211], v[36:39], v[224:227], v[208:211]\n\t"
  "v_exp_f32 v248, v248\n\t"
  "v_mfma_f32_16x16x32_bf16 v[212:215], v[36:39], v[228:231], v[212:215]\n\t"
  "v_exp_f32 v249, v249\n\t"
  "v_mfma_f32_16x16x32_bf16 v[212:215], v[32:35], v[196:199], v[212:215]\n\t"
  "buffer_load_dword v11, v10, s[60:63], 0 offen offset:1024\n\t"
  "v_accvgpr_write_b32 a183, v3\n\t"
  "v_mfma_f32_16x16x32_bf16 v[216:219], v[32:35], v[200:203], v[216:219]\n\t"
  "ds_read_b128 v[52:55], v17 offset:8192\n\t"
  "v_cvt_pk_bf16_f32 v0, v248, v249\n\t"
  "v_mfma_f32_16x16x32_bf16 v[216:219], v[36:39], v[232:235], v[216:219]\n\t"
  "v_exp_f32 v250, v250\n\t"
  "v_mfma_f32_16x16x32_bf16 v[220:223], v[36:39], v[236:239], v[220:223]\n\t"
  "v_exp_f32 v251, v251\n\t"
  "v_mfma_f32_16x16x32_bf16 v[220:223], v[32:35], v[204:207], v[220:223]\n\t"
  "v_accvgpr_write_b32 a186, v0\n\t"
  "s_waitcnt lgkmcnt(2)\n\t"
  "v_mfma_f32_16x16x32_bf16 v[208:211], v[40:43], v[80:83], v[208:211]\n\t"
  "ds_read_b128 v[32:35], v18 offset:8192\n\t"
  "v_cvt_pk_bf16_f32 v1, v250, v251\n\t"
  "v_mfma_f32_16x16x32_bf16 v[208:211], v[44:47], v[144:147], v[208:211]\n\t"
  "v_exp_f32 v252, v252\n\t"
  "v_mfma_f32_16x16x32_bf16 v[212:215], v[44:47], v[148:151], v[212:215]\n\t"
  "v_exp_f32 v253, v253\n\t"
  "v_mfma_f32_16x16x32_bf16 v[212:215], v[40:43], v[84:87], v[212:215]\n\t"
  "v_accvgpr_write_b32 a187, v1\n\t"
  "v_mfma_f32_16x16x32_bf16 v[216:219], v[40:43], v[88:91], v[216:219]\n\t"
  "ds_read_b128 v[36:39], v19 offset:8192\n\t"
  "v_cvt_pk_bf16_f32 v2, v252, v253\n\t"
  "v_mfma_f32_16x16x32_bf16 v[216:219], v[44:47], v[152:155], v[216:219]\n\t"
  "v_exp_f32 v254, v254\n\t"
  "v_mfma_f32_16x16x32_bf16 v[220:223], v[44:47], v[156:159], v[220:223]\n\t"
  "v_exp_f32 v255, v255\n\t"
  "v_mfma_f32_16x16x32_bf16 v[220:223], v[40:43], v[92:95], v[220:223]\n\t"
  "v_accvgpr_write_b32 a190, v2\n\t"
  "s_waitcnt lgkmcnt(2)\n\t"
  "v_mfma_f32_16x16x32_bf16 v[240:243], v[48:51], v[64:67], v[112:115]\n\t"
  "ds_read_b128 v[40:43], v16 offset:8448\n\t"
  "v_cvt_pk_bf16_f32 v3, v254, v255\n\t"
  "v_mfma_f32_16x16x32_bf16 v[240:243], v[52:55], v[96:99], v[240:243]\n\t"
  "v_exp_f32 v208, v208\n\t"
  "v_mfma_f32_16x16x32_bf16 v[244:247], v[52:55], v[100:103], v[116:119]\n\t"
  "v_exp_f32 v209, v209\n\t"
  "v_mfma_f32_16x16x32_bf16 v[244:247], v[48:51], v[68:71], v[244:247]\n\t"
  "v_accvgpr_write_b32 a191, v3\n\t"
  "v_mfma_f32_16x16x32_bf16 v[248:251], v[48:51], v[72:75], v[120:123]\n\t"
  "ds_read_b128 v[44:47], v17 offset:8448\n\t"
  "v_cvt_pk_bf16_f32 v0, v208, v209\n\t"
  "v_mfma_f32_16x16x32_bf16 v[248:251], v[52:55], v[104:107], v[248:251]\n\t"
  "v_exp_f32 v210, v210\n\t"
  "v_mfma_f32_16x16x32_bf16 v[252:255], v[52:55], v[108:111], v[124:127]\n\t"
  "v_exp_f32 v211, v211\n\t"
  "v_mfma_f32_16x16x32_bf16 v[252:255], v[48:51], v[76:79], v[252:255]\n\t"
  "v_accvgpr_write_b32 a192, v0\n\t"
  "s_waitcnt lgkmcnt(2)\n\t"
  "v_mfma_f32_16x16x32_bf16 v[240:243], v[32:35], v[128:131], v[240:243]\n\t"
  "ds_read_b128 v[48:51], v18 offset:8448\n\t"
  "v_cvt_pk_bf16_f32 v1, v210, v211\n\t"
  "v_mfma_f32_16x16x32_bf16 v[240:243], v[36:39], v[160:163], v[240:243]\n\t"
  "v_exp_f32 v212, v212\n\t"
  "v_mfma_f32_16x16x32_bf16 v[244:247], v[36:39], v[164:167], v[244:247]\n\t"
  "v_exp_f32 v213, v213\n\t"
  "v_mfma_f32_16x16x32_bf16 v[244:247], v[32:35], v[132:135], v[244:247]\n\t"
  "v_accvgpr_write_b32 a193, v1\n\t"
  "s_waitcnt vmcnt(23)\n\t"
  "s_barrier\n\t"
  "v_mfma_f32_16x16x32_bf16 v[248:251], v[32:35], v[136:139], v[248:251]\n\t"
  "ds_read_b128 v[52:55], v19 offset:8448\n\t"
  "v_cvt_pk_bf16_f32 v2, v212, v213\n\t"
  "v_mfma_f32_16x16x32_bf16 v[248:251], v[36:39], v[168:171], v[248:251]\n\t"
  "v_exp_f32 v214, v214\n\t"
  "v_mfma_f32_16x16x32_bf16 v[252:255], v[36:39], v[172:175], v[252:255]\n\t"
  "v_exp_f32 v215, v215\n\t"
  "v_mfma_f32_16x16x32_bf16 v[252:255], v[32:35], v[140:143], v[252:255]\n\t"
  "v_accvgpr_write_b32 a196, v2\n\t"
  "s_waitcnt lgkmcnt(2)\n\t"
  "v_mfma_f32_16x16x32_bf16 v[240:243], v[40:43], v[192:195], v[240:243]\n\t"
  "ds_read_b128 v[32:35], v16 offset:16384\n\t"
  "v_cvt_pk_bf16_f32 v3, v214, v215\n\t"
  "v_mfma_f32_16x16x32_bf16 v[240:243], v[44:47], v[224:227], v[240:243]\n\t"
  "v_exp_f32 v216, v216\n\t"
  "v_mfma_f32_16x16x32_bf16 v[244:247], v[44:47], v[228:231], v[244:247]\n\t"
  "v_exp_f32 v217, v217\n\t"
  "v_mfma_f32_16x16x32_bf16 v[244:247], v[40:43], v[196:199], v[244:247]\n\t"
  "v_accvgpr_write_b32 a197, v3\n\t"
  "s_add_u32 m0, s69, 98304\n\t"
  "v_mfma_f32_16x16x32_bf16 v[248:251], v[40:43], v[200:203], v[248:251]\n\t"
  "ds_read_b128 v[36:39], v17 offset:16384\n\t"
  "v_cvt_pk_bf16_f32 v0, v216, v217\n\t"
  "v_mfma_f32_16x16x32_bf16 v[248:251], v[44:47], v[232:235], v[248:251]\n\t"
  "v_exp_f32 v218, v218\n\t"
  "v_mfma_f32_16x16x32_bf16 v[252:255], v[44:47], v[236:239], v[252:255]\n\t"
  "v_exp_f32 v219, v219\n\t"
  "v_mfma_f32_16x16x32_bf16 v[252:255], v[40:43], v[204:207], v[252:255]\n\t"
  "v_accvgpr_write_b32 a200, v0\n\t"
  "s_waitcnt lgkmcnt(2)\n\t"
  "v_mfma_f32_16x16x32_bf16 v[240:243], v[48:51], v[80:83], v[240:243]\n\t"
  "ds_read_b128 v[40:43], v18 offset:16384\n\t"
  "v_cvt_pk_bf16_f32 v1, v218, v219\n\t"
  "v_mfma_f32_16x16x32_bf16 v[240:243], v[52:55], v[144:147], v[240:243]\n\t"
  "v_exp_f32 v220, v220\n\t"
  "v_mfma_f32_16x16x32_bf16 v[244:247], v[52:55], v[148:151], v[244:247]\n\t"
  "v_exp_f32 v221, v221\n\t"
  "v_mfma_f32_16x16x32_bf16 v[244:247], v[48:51], v[84:87], v[244:247]\n\t"
  "v_accvgpr_write_b32 a201, v1\n\t"
  "s_add_u32 s70, s76, 0\n\t"
  "v_mfma_f32_16x16x32_bf16 v[248:251], v[48:51], v[88:91], v[248:251]\n\t"
  "ds_read_b128 v[44:47], v19 offset:16384\n\t"
  "v_cvt_pk_bf16_f32 v2, v220, v221\n\t"
  "v_mfma_f32_16x16x32_bf16 v[248:251], v[52:55], v[152:155], v[248:251]\n\t"
  "v_exp_f32 v222, v222\n\t"
  "v_mfma_f32_16x16x32_bf16 v[252:255], v[52:55], v[156:159], v[252:255]\n\t"
  "v_exp_f32 v223, v223\n\t"
  "v_mfma_f32_16x16x32_bf16 v[252:255], v[48:51], v[92:95], v[252:255]\n\t"
  "v_accvgpr_write_b32 a204, v2\n\t"
  "s_waitcnt lgkmcnt(2)\n\t"
  "v_mfma_f32_16x16x32_bf16 v[208:211], v[32:35], v[64:67], v[112:115]\n\t"
  "ds_read_b128 v[48:51], v16 offset:16640\n\t"
  "v_cvt_pk_bf16_f32 v3, v222, v223\n\t"
  "v_mfma_f32_16x16x32_bf16 v[208:211], v[36:39], v[96:99], v[208:211]\n\t"
  "v_exp_f32 v240, v240\n\t"
  "v_mfma_f32_16x16x32_bf16 v[212:215], v[36:39], v[100:103], v[116:119]\n\t"
  "v_exp_f32 v241, v241\n\t"
  "v_mfma_f32_16x16x32_bf16 v[212:215], v[32:35], v[68:71], v[212:215]\n\t"
  "buffer_load_dwordx4 v4, s[52:55], s70 offen offset:0 nt lds\n\t"
  "v_accvgpr_write_b32 a205, v3\n\t"
  "v_mfma_f32_16x16x32_bf16 v[216:219], v[32:35], v[72:75], v[120:123]\n\t"
  "ds_read_b128 v[52:55], v17 offset:16640\n\t"
  "v_cvt_pk_bf16_f32 v0, v240, v241\n\t"
  "v_mfma_f32_16x16x32_bf16 v[216:219], v[36:39], v[104:107], v[216:219]\n\t"
  "v_exp_f32 v242, v242\n\t"
  "v_mfma_f32_16x16x32_bf16 v[220:223], v[36:39], v[108:111], v[124:127]\n\t"
  "v_exp_f32 v243, v243\n\t"
  "v_mfma_f32_16x16x32_bf16 v[220:223], v[32:35], v[76:79], v[220:223]\n\t"
  "v_accvgpr_write_b32 a194, v0\n\t"
  "s_waitcnt lgkmcnt(2)\n\t"
  "v_mfma_f32_16x16x32_bf16 v[208:211], v[40:43], v[128:131], v[208:211]\n\t"
  "ds_read_b128 v[32:35], v18 offset:16640\n\t"
  "v_cvt_pk_bf16_f32 v1, v242, v243\n\t"
  "v_mfma_f32_16x16x32_bf16 v[208:211], v[44:47], v[160:163], v[208:211]\n\t"
  "v_exp_f32 v244, v244\n\t"
  "v_mfma_f32_16x16x32_bf16 v[212:215], v[44:47], v[164:167], v[212:215]\n\t"
  "v_exp_f32 v245, v245\n\t"
  "v_mfma_f32_16x16x32_bf16 v[212:215], v[40:43], v[132:135], v[212:215]\n\t"
  "buffer_load_dwordx4 v5, s[52:55], s70 offen offset:1024 nt lds\n\t"
  "v_accvgpr_write_b32 a195, v1\n\t"
  "v_mfma_f32_16x16x32_bf16 v[216:219], v[40:43], v[136:139], v[216:219]\n\t"
  "ds_read_b128 v[36:39], v19 offset:16640\n\t"
  "v_cvt_pk_bf16_f32 v2, v244, v245\n\t"
  "v_mfma_f32_16x16x32_bf16 v[216:219], v[44:47], v[168:171], v[216:219]\n\t"
  "v_exp_f32 v246, v246\n\t"
  "v_mfma_f32_16x16x32_bf16 v[220:223], v[44:47], v[172:175], v[220:223]\n\t"
  "v_exp_f32 v247, v247\n\t"
  "v_mfma_f32_16x16x32_bf16 v[220:223], v[40:43], v[140:143], v[220:223]\n\t"
  "v_accvgpr_write_b32 a198, v2\n\t"
  "s_waitcnt lgkmcnt(2)\n\t"
  "v_mfma_f32_16x16x32_bf16 v[208:211], v[48:51], v[192:195], v[208:211]\n\t"
  "ds_read_b128 v[40:43], v16 offset:24576\n\t"
  "v_cvt_pk_bf16_f32 v3, v246, v247\n\t"
  "v_mfma_f32_16x16x32_bf16 v[208:211], v[52:55], v[224:227], v[208:211]\n\t"
  "v_exp_f32 v248, v248\n\t"
  "v_mfma_f32_16x16x32_bf16 v[212:215], v[52:55], v[228:231], v[212:215]\n\t"
  "v_exp_f32 v249, v249\n\t"
  "v_mfma_f32_16x16x32_bf16 v[212:215], v[48:51], v[196:199], v[212:215]\n\t"
  "buffer_load_dwordx4 v60, s[52:55], s70 offen offset:2048 nt lds\n\t"
  "v_accvgpr_write_b32 a199, v3\n\t"
  "v_mfma_f32_16x16x32_bf16 v[216:219], v[48:51], v[200:203], v[216:219]\n\t"
  "ds_read_b128 v[44:47], v17 offset:24576\n\t"
  "v_cvt_pk_bf16_f32 v0, v248, v249\n\t"
  "v_mfma_f32_16x16x32_bf16 v[216:219], v[52:55], v[232:235], v[216:219]\n\t"
  "v_exp_f32 v250, v250\n\t"
  "v_mfma_f32_16x16x32_bf16 v[220:223], v[52:55], v[236:239], v[220:223]\n\t"
  "v_exp_f32 v251, v251\n\t"
  "v_mfma_f32_16x16x32_bf16 v[220:223], v[48:51], v[204:207], v[220:223]\n\t"
  "v_accvgpr_write_b32 a202, v0\n\t"
  "s_waitcnt lgkmcnt(2)\n\t"
  "v_mfma_f32_16x16x32_bf16 v[208:211], v[32:35], v[80:83], v[208:211]\n\t"
  "ds_read_b128 v[48:51], v18 offset:24576\n\t"
  "v_cvt_pk_bf16_f32 v1, v250, v251\n\t"
  "v_mfma_f32_16x16x32_bf16 v[208:211], v[36:39], v[144:147], v[208:211]\n\t"
  "v_exp_f32 v252, v252\n\t"
  "v_mfma_f32_16x16x32_bf16 v[212:215], v[36:39], v[148:151], v[212:215]\n\t"
  "v_exp_f32 v253, v253\n\t"
  "v_mfma_f32_16x16x32_bf16 v[212:215], v[32:35], v[84:87], v[212:215]\n\t"
  "buffer_load_dwordx4 v61, s[52:55], s70 offen offset:3072 nt lds\n\t"
  "v_accvgpr_write_b32 a203, v1\n\t"
  "v_mfma_f32_16x16x32_bf16 v[216:219], v[32:35], v[88:91], v[216:219]\n\t"
  "ds_read_b128 v[52:55], v19 offset:24576\n\t"
  "v_cvt_pk_bf16_f32 v2, v252, v253\n\t"
  "v_mfma_f32_16x16x32_bf16 v[216:219], v[36:39], v[152:155], v[216:219]\n\t"
  "v_exp_f32 v254, v254\n\t"
  "v_mfma_f32_16x16x32_bf16 v[220:223], v[36:39], v[156:159], v[220:223]\n\t"
  "v_exp_f32 v255, v255\n\t"
  "v_mfma_f32_16x16x32_bf16 v[220:223], v[32:35], v[92:95], v[220:223]\n\t"
  "v_accvgpr_write_b32 a206, v2\n\t"
  "s_waitcnt lgkmcnt(2)\n\t"
  "v_mfma_f32_16x16x32_bf16 v[240:243], v[40:43], v[64:67], v[112:115]\n\t"
  "ds_read_b128 v[32:35], v16 offset:24832\n\t"
  "v_cvt_pk_bf16_f32 v3, v254, v255\n\t"
  "v_mfma_f32_16x16x32_bf16 v[240:243], v[44:47], v[96:99], v[240:243]\n\t"
  "v_exp_f32 v208, v208\n\t"
  "v_mfma_f32_16x16x32_bf16 v[244:247], v[44:47], v[100:103], v[116:119]\n\t"
  "v_exp_f32 v209, v209\n\t"
  "v_mfma_f32_16x16x32_bf16 v[244:247], v[40:43], v[68:71], v[244:247]\n\t"
  "v_accvgpr_write_b32 a207, v3\n\t"
  "s_add_u32 m0, s69, 114688\n\t"
  "v_mfma_f32_16x16x32_bf16 v[248:251], v[40:43], v[72:75], v[120:123]\n\t"
  "ds_read_b128 v[36:39], v17 offset:24832\n\t"
  "v_cvt_pk_bf16_f32 v0, v208, v209\n\t"
  "v_mfma_f32_16x16x32_bf16 v[248:251], v[44:47], v[104:107], v[248:251]\n\t"
  "v_exp_f32 v210, v210\n\t"
  "v_mfma_f32_16x16x32_bf16 v[252:255], v[44:47], v[108:111], v[124:127]\n\t"
  "v_exp_f32 v211, v211\n\t"
  "v_mfma_f32_16x16x32_bf16 v[252:255], v[40:43], v[76:79], v[252:255]\n\t"
  "v_accvgpr_write_b32 a208, v0\n\t"
  "s_waitcnt lgkmcnt(2)\n\t"
  "v_mfma_f32_16x16x32_bf16 v[240:243], v[48:51], v[128:131], v[240:243]\n\t"
  "ds_read_b128 v[40:43], v18 offset:24832\n\t"
  "v_cvt_pk_bf16_f32 v1, v210, v211\n\t"
  "v_mfma_f32_16x16x32_bf16 v[240:243], v[52:55], v[160:163], v[240:243]\n\t"
  "v_exp_f32 v212, v212\n\t"
  "v_mfma_f32_16x16x32_bf16 v[244:247], v[52:55], v[164:167], v[244:247]\n\t"
  "v_exp_f32 v213, v213\n\t"
  "v_mfma_f32_16x16x32_bf16 v[244:247], v[48:51], v[132:135], v[244:247]\n\t"
  "v_accvgpr_write_b32 a209, v1\n\t"
  "s_add_u32 s70, s76, 65536\n\t"
  "v_mfma_f32_16x16x32_bf16 v[248:251], v[48:51], v[136:139], v[248:251]\n\t"
  "ds_read_b128 v[44:47], v19 offset:24832\n\t"
  "v_cvt_pk_bf16_f32 v2, v212, v213\n\t"
  "v_add_u32 v16, 0xffff0000, v16\n\t"
  "v_mfma_f32_16x16x32_bf16 v[248:251], v[52:55], v[168:171], v[248:251]\n\t"
  "v_exp_f32 v214, v214\n\t"
  "v_add_u32 v17, 0xffff0000, v17\n\t"
  "v_mfma_f32_16x16x32_bf16 v[252:255], v[52:55], v[172:175], v[252:255]\n\t"
  "v_exp_f32 v215, v215\n\t"
  "v_add_u32 v18, 0xffff0000, v18\n\t"
  "v_mfma_f32_16x16x32_bf16 v[252:255], v[48:51], v[140:143], v[252:255]\n\t"
  "v_accvgpr_write_b32 a212, v2\n\t"
  "v_add_u32 v19, 0xffff0000, v19\n\t"
  "s_waitcnt lgkmcnt(2)\n\t"
  "v_mfma_f32_16x16x32_bf16 v[240:243], v[32:35], v[192:195], v[240:243]\n\t"
  "ds_read_b128 v[48:51], v16 offset:0\n\t"
  "v_cvt_pk_bf16_f32 v3, v214, v215\n\t"
  "v_mfma_f32_16x16x32_bf16 v[240:243], v[36:39], v[224:227], v[240:243]\n\t"
  "v_exp_f32 v216, v216\n\t"
  "v_mfma_f32_16x16x32_bf16 v[244:247], v[36:39], v[228:231], v[244:247]\n\t"
  "v_exp_f32 v217, v217\n\t"
  "v_mfma_f32_16x16x32_bf16 v[244:247], v[32:35], v[196:199], v[244:247]\n\t"
  "buffer_load_dwordx4 v4, s[52:55], s70 offen offset:0 nt lds\n\t"
  "v_accvgpr_write_b32 a213, v3\n\t"
  "v_mfma_f32_16x16x32_bf16 v[248:251], v[32:35], v[200:203], v[248:251]\n\t"
  "ds_read_b128 v[52:55], v17 offset:0\n\t"
  "v_cvt_pk_bf16_f32 v0, v216, v217\n\t"
  "v_mfma_f32_16x16x32_bf16 v[248:251], v[36:39], v[232:235], v[248:251]\n\t"
  "v_exp_f32 v218, v218\n\t"
  "v_mfma_f32_16x16x32_bf16 v[252:255], v[36:39], v[236:239], v[252:255]\n\t"
  "v_exp_f32 v219, v219\n\t"
  "v_mfma_f32_16x16x32_bf16 v[252:255], v[32:35], v[204:207], v[252:255]\n\t"
  "v_accvgpr_write_b32 a216, v0\n\t"
  "s_waitcnt lgkmcnt(2)\n\t"
  "v_mfma_f32_16x16x32_bf16 v[240:243], v[40:43], v[80:83], v[240:243]\n\t"
  "ds_read_b128 v[32:35], v18 offset:0\n\t"
  "v_cvt_pk_bf16_f32 v1, v218, v219\n\t"
  "v_mfma_f32_16x16x32_bf16 v[240:243], v[44:47], v[144:147], v[240:243]\n\t"
  "v_exp_f32 v220, v220\n\t"
  "v_mfma_f32_16x16x32_bf16 v[244:247], v[44:47], v[148:151], v[244:247]\n\t"
  "v_exp_f32 v221, v221\n\t"
  "v_mfma_f32_16x16x32_bf16 v[244:247], v[40:43], v[84:87], v[244:247]\n\t"
  "buffer_load_dwordx4 v5, s[52:55], s70 offen offset:1024 nt lds\n\t"
  "v_accvgpr_write_b32 a217, v1\n\t"
  "v_mfma_f32_16x16x32_bf16 v[248:251], v[40:43], v[88:91], v[248:251]\n\t"
  "ds_read_b128 v[36:39], v19 offset:0\n\t"
  "v_cvt_pk_bf16_f32 v2, v220, v221\n\t"
  "v_mfma_f32_16x16x32_bf16 v[248:251], v[44:47], v[152:155], v[248:251]\n\t"
  "v_exp_f32 v222, v222\n\t"
  "v_mfma_f32_16x16x32_bf16 v[252:255], v[44:47], v[156:159], v[252:255]\n\t"
  "v_exp_f32 v223, v223\n\t"
  "v_mfma_f32_16x16x32_bf16 v[252:255], v[40:43], v[92:95], v[252:255]\n\t"
  "v_accvgpr_write_b32 a220, v2\n\t"
  "s_waitcnt lgkmcnt(2)\n\t"
  "v_mfma_f32_16x16x32_bf16 v[208:211], v[48:51], v[64:67], v[112:115]\n\t"
  "ds_read_b128 v[40:43], v16 offset:256\n\t"
  "v_cvt_pk_bf16_f32 v3, v222, v223\n\t"
  "v_mfma_f32_16x16x32_bf16 v[208:211], v[52:55], v[96:99], v[208:211]\n\t"
  "v_exp_f32 v240, v240\n\t"
  "v_mfma_f32_16x16x32_bf16 v[212:215], v[52:55], v[100:103], v[116:119]\n\t"
  "v_exp_f32 v241, v241\n\t"
  "v_mfma_f32_16x16x32_bf16 v[212:215], v[48:51], v[68:71], v[212:215]\n\t"
  "buffer_load_dwordx4 v60, s[52:55], s70 offen offset:2048 nt lds\n\t"
  "v_accvgpr_write_b32 a221, v3\n\t"
  "v_mfma_f32_16x16x32_bf16 v[216:219], v[48:51], v[72:75], v[120:123]\n\t"
  "ds_read_b128 v[44:47], v17 offset:256\n\t"
  "v_cvt_pk_bf16_f32 v0, v240, v241\n\t"
  "v_mfma_f32_16x16x32_bf16 v[216:219], v[52:55], v[104:107], v[216:219]\n\t"
  "v_exp_f32 v242, v242\n\t"
  "v_mfma_f32_16x16x32_bf16 v[220:223], v[52:55], v[108:111], v[124:127]\n\t"
  "v_exp_f32 v243, v243\n\t"
  "v_mfma_f32_16x16x32_bf16 v[220:223], v[48:51], v[76:79], v[220:223]\n\t"
  "v_accvgpr_write_b32 a210, v0\n\t"
  "s_waitcnt lgkmcnt(2)\n\t"
  "v_mfma_f32_16x16x32_bf16 v[208:211], v[32:35], v[128:131], v[208:211]\n\t"
  "ds_read_b128 v[48:51], v18 offset:256\n\t"
  "v_cvt_pk_bf16_f32 v1, v242, v243\n\t"
  "v_mfma_f32_16x16x32_bf16 v[208:211], v[36:39], v[160:163], v[208:211]\n\t"
  "v_exp_f32 v244, v244\n\t"
  "v_mfma_f32_16x16x32_bf16 v[212:215], v[36:39], v[164:167], v[212:215]\n\t"
  "v_exp_f32 v245, v245\n\t"
  "v_mfma_f32_16x16x32_bf16 v[212:215], v[32:35], v[132:135], v[212:215]\n\t"
  "buffer_load_dwordx4 v61, s[52:55], s70 offen offset:3072 nt lds\n\t"
  "v_accvgpr_write_b32 a211, v1\n\t"
  "v_mfma_f32_16x16x32_bf16 v[216:219], v[32:35], v[136:139], v[216:219]\n\t"
  "ds_read_b128 v[52:55], v19 offset:256\n\t"
  "v_cvt_pk_bf16_f32 v2, v244, v245\n\t"
  "v_mfma_f32_16x16x32_bf16 v[216:219], v[36:39], v[168:171], v[216:219]\n\t"
  "v_exp_f32 v246, v246\n\t"
  "v_mfma_f32_16x16x32_bf16 v[220:223], v[36:39], v[172:175], v[220:223]\n\t"
  "v_exp_f32 v247, v247\n\t"
  "v_mfma_f32_16x16x32_bf16 v[220:223], v[32:35], v[140:143], v[220:223]\n\t"
  "v_accvgpr_write_b32 a214, v2\n\t"
  "s_waitcnt lgkmcnt(2)\n\t"
  "v_mfma_f32_16x16x32_bf16 v[208:211], v[40:43], v[192:195], v[208:211]\n\t"
  "ds_read_b128 v[32:35], v16 offset:8192\n\t"
  "v_cvt_pk_bf16_f32 v3, v246, v247\n\t"
  "v_mfma_f32_16x16x32_bf16 v[208:211], v[44:47], v[224:227], v[208:211]\n\t"
  "v_exp_f32 v248, v248\n\t"
  "v_mfma_f32_16x16x32_bf16 v[212:215], v[44:47], v[228:231], v[212:215]\n\t"
  "v_exp_f32 v249, v249\n\t"
  "v_mfma_f32_16x16x32_bf16 v[212:215], v[40:43], v[196:199], v[212:215]\n\t"
  "buffer_load_dword v11, v10, s[60:63], 0 offen offset:2048\n\t"
  "v_accvgpr_write_b32 a215, v3\n\t"
  "v_mfma_f32_16x16x32_bf16 v[216:219], v[40:43], v[200:203], v[216:219]\n\t"
  "ds_read_b128 v[36:39], v17 offset:8192\n\t"
  "v_cvt_pk_bf16_f32 v0, v248, v249\n\t"
  "v_mfma_f32_16x16x32_bf16 v[216:219], v[44:47], v[232:235], v[216:219]\n\t"
  "v_exp_f32 v250, v250\n\t"
  "v_mfma_f32_16x16x32_bf16 v[220:223], v[44:47], v[236:239], v[220:223]\n\t"
  "v_exp_f32 v251, v251\n\t"
  "v_mfma_f32_16x16x32_bf16 v[220:223], v[40:43], v[204:207], v[220:223]\n\t"
  "v_accvgpr_write_b32 a218, v0\n\t"
  "s_waitcnt lgkmcnt(2)\n\t"
  "v_mfma_f32_16x16x32_bf16 v[208:211], v[48:51], v[80:83], v[208:211]\n\t"
  "ds_read_b128 v[40:43], v18 offset:8192\n\t"
  "v_cvt_pk_bf16_f32 v1, v250, v251\n\t"
  "v_mfma_f32_16x16x32_bf16 v[208:211], v[52:55], v[144:147], v[208:211]\n\t"
  "v_exp_f32 v252, v252\n\t"
  "v_mfma_f32_16x16x32_bf16 v[212:215], v[52:55], v[148:151], v[212:215]\n\t"
  "v_exp_f32 v253, v253\n\t"
  "v_mfma_f32_16x16x32_bf16 v[212:215], v[48:51], v[84:87], v[212:215]\n\t"
  "v_accvgpr_write_b32 a219, v1\n\t"
  "v_mfma_f32_16x16x32_bf16 v[216:219], v[48:51], v[88:91], v[216:219]\n\t"
  "ds_read_b128 v[44:47], v19 offset:8192\n\t"
  "v_cvt_pk_bf16_f32 v2, v252, v253\n\t"
  "v_mfma_f32_16x16x32_bf16 v[216:219], v[52:55], v[152:155], v[216:219]\n\t"
  "v_exp_f32 v254, v254\n\t"
  "v_mfma_f32_16x16x32_bf16 v[220:223], v[52:55], v[156:159], v[220:223]\n\t"
  "v_exp_f32 v255, v255\n\t"
  "v_mfma_f32_16x16x32_bf16 v[220:223], v[48:51], v[92:95], v[220:223]\n\t"
  "v_accvgpr_write_b32 a222, v2\n\t"
  "s_waitcnt lgkmcnt(2)\n\t"
  "v_mfma_f32_16x16x32_bf16 v[240:243], v[32:35], v[64:67], v[112:115]\n\t"
  "ds_read_b128 v[48:51], v16 offset:8448\n\t"
  "v_cvt_pk_bf16_f32 v3, v254, v255\n\t"
  "v_mfma_f32_16x16x32_bf16 v[240:243], v[36:39], v[96:99], v[240:243]\n\t"
  "v_exp_f32 v208, v208\n\t"
  "v_mfma_f32_16x16x32_bf16 v[244:247], v[36:39], v[100:103], v[116:119]\n\t"
  "v_exp_f32 v209, v209\n\t"
  "v_mfma_f32_16x16x32_bf16 v[244:247], v[32:35], v[68:71], v[244:247]\n\t"
  "v_accvgpr_write_b32 a223, v3\n\t"
  "v_mfma_f32_16x16x32_bf16 v[248:251], v[32:35], v[72:75], v[120:123]\n\t"
  "ds_read_b128 v[52:55], v17 offset:8448\n\t"
  "v_cvt_pk_bf16_f32 v0, v208, v209\n\t"
  "v_mfma_f32_16x16x32_bf16 v[248:251], v[36:39], v[104:107], v[248:251]\n\t"
  "v_exp_f32 v210, v210\n\t"
  "v_mfma_f32_16x16x32_bf16 v[252:255], v[36:39], v[108:111], v[124:127]\n\t"
  "v_exp_f32 v211, v211\n\t"
  "v_mfma_f32_16x16x32_bf16 v[252:255], v[32:35], v[76:79], v[252:255]\n\t"
  "v_accvgpr_write_b32 a224, v0\n\t"
  "s_waitcnt lgkmcnt(2)\n\t"
  "v_mfma_f32_16x16x32_bf16 v[240:243], v[40:43], v[128:131], v[240:243]\n\t"
  "ds_read_b128 v[32:35], v18 offset:8448\n\t"
  "v_cvt_pk_bf16_f32 v1, v210, v211\n\t"
  "v_mfma_f32_16x16x32_bf16 v[240:243], v[44:47], v[160:163], v[240:243]\n\t"
  "v_exp_f32 v212, v212\n\t"
  "v_mfma_f32_16x16x32_bf16 v[244:247], v[44:47], v[164:167], v[244:247]\n\t"
  "v_exp_f32 v213, v213\n\t"
  "v_mfma_f32_16x16x32_bf16 v[244:247], v[40:43], v[132:135], v[244:247]\n\t"
  "v_accvgpr_write_b32 a225, v1\n\t"
  "s_waitcnt vmcnt(23)\n\t"
  "s_barrier\n\t"
  "v_mfma_f32_16x16x32_bf16 v[248:251], v[40:43], v[136:139], v[248:251]\n\t"
  "ds_read_b128 v[36:39], v19 offset:8448\n\t"
  "v_cvt_pk_bf16_f32 v2, v212, v213\n\t"
  "v_mfma_f32_16x16x32_bf16 v[248:251], v[44:47], v[168:171], v[248:251]\n\t"
  "v_exp_f32 v214, v214\n\t"
  "v_mfma_f32_16x16x32_bf16 v[252:255], v[44:47], v[172:175], v[252:255]\n\t"
  "v_exp_f32 v215, v215\n\t"
  "v_mfma_f32_16x16x32_bf16 v[252:255], v[40:43], v[140:143], v[252:255]\n\t"
  "v_accvgpr_write_b32 a228, v2\n\t"
  "s_waitcnt lgkmcnt(2)\n\t"
  "v_mfma_f32_16x16x32_bf16 v[240:243], v[48:51], v[192:195], v[240:243]\n\t"
  "ds_read_b128 v[40:43], v16 offset:16384\n\t"
  "v_cvt_pk_bf16_f32 v3, v214, v215\n\t"
  "v_mfma_f32_16x16x32_bf16 v[240:243], v[52:55], v[224:227], v[240:243]\n\t"
  "v_exp_f32 v216, v216\n\t"
  "v_mfma_f32_16x16x32_bf16 v[244:247], v[52:55], v[228:231], v[244:247]\n\t"
  "v_exp_f32 v217, v217\n\t"
  "v_mfma_f32_16x16x32_bf16 v[244:247], v[48:51], v[196:199], v[244:247]\n\t"
  "v_accvgpr_write_b32 a229, v3\n\t"
  "s_add_u32 m0, s69, 131072\n\t"
  "v_mfma_f32_16x16x32_bf16 v[248:251], v[48:51], v[200:203], v[248:251]\n\t"
  "ds_read_b128 v[44:47], v17 offset:16384\n\t"
  "v_cvt_pk_bf16_f32 v0, v216, v217\n\t"
  "v_mfma_f32_16x16x32_bf16 v[248:251], v[52:55], v[232:235], v[248:251]\n\t"
  "v_exp_f32 v218, v218\n\t"
  "v_mfma_f32_16x16x32_bf16 v[252:255], v[52:55], v[236:239], v[252:255]\n\t"
  "v_exp_f32 v219, v219\n\t"
  "v_mfma_f32_16x16x32_bf16 v[252:255], v[48:51], v[204:207], v[252:255]\n\t"
  "v_accvgpr_write_b32 a232, v0\n\t"
  "s_waitcnt lgkmcnt(2)\n\t"
  "v_mfma_f32_16x16x32_bf16 v[240:243], v[32:35], v[80:83], v[240:243]\n\t"
  "ds_read_b128 v[48:51], v18 offset:16384\n\t"
  "v_cvt_pk_bf16_f32 v1, v218, v219\n\t"
  "v_mfma_f32_16x16x32_bf16 v[240:243], v[36:39], v[144:147], v[240:243]\n\t"
  "v_exp_f32 v220, v220\n\t"
  "v_mfma_f32_16x16x32_bf16 v[244:247], v[36:39], v[148:151], v[244:247]\n\t"
  "v_exp_f32 v221, v221\n\t"
  "v_mfma_f32_16x16x32_bf16 v[244:247], v[32:35], v[84:87], v[244:247]\n\t"
  "v_accvgpr_write_b32 a233, v1\n\t"
  "s_add_u32 s70, s76, 131072\n\t"
  "v_mfma_f32_16x16x32_bf16 v[248:251], v[32:35], v[88:91], v[248:251]\n\t"
  "ds_read_b128 v[52:55], v19 offset:16384\n\t"
  "v_cvt_pk_bf16_f32 v2, v220, v221\n\t"
  "v_mfma_f32_16x16x32_bf16 v[248:251], v[36:39], v[152:155], v[248:251]\n\t"
  "v_exp_f32 v222, v222\n\t"
  "v_mfma_f32_16x16x32_bf16 v[252:255], v[36:39], v[156:159], v[252:255]\n\t"
  "v_exp_f32 v223, v223\n\t"
  "v_mfma_f32_16x16x32_bf16 v[252:255], v[32:35], v[92:95], v[252:255]\n\t"
  "v_accvgpr_write_b32 a236, v2\n\t"
  "s_waitcnt lgkmcnt(2)\n\t"
  "v_mfma_f32_16x16x32_bf16 v[208:211], v[40:43], v[64:67], v[112:115]\n\t"
  "ds_read_b128 v[32:35], v16 offset:16640\n\t"
  "v_cvt_pk_bf16_f32 v3, v222, v223\n\t"
  "v_mfma_f32_16x16x32_bf16 v[208:211], v[44:47], v[96:99], v[208:211]\n\t"
  "v_exp_f32 v240, v240\n\t"
  "v_mfma_f32_16x16x32_bf16 v[212:215], v[44:47], v[100:103], v[116:119]\n\t"
  "v_exp_f32 v241, v241\n\t"
  "v_mfma_f32_16x16x32_bf16 v[212:215], v[40:43], v[68:71], v[212:215]\n\t"
  "buffer_load_dwordx4 v4, s[52:55], s70 offen offset:0 nt lds\n\t"
  "v_accvgpr_write_b32 a237, v3\n\t"
  "v_mfma_f32_16x16x32_bf16 v[216:219], v[40:43], v[72:75], v[120:123]\n\t"
  "ds_read_b128 v[36:39], v17 offset:16640\n\t"
  "v_cvt_pk_bf16_f32 v0, v240, v241\n\t"
  "v_mfma_f32_16x16x32_bf16 v[216:219], v[44:47], v[104:107], v[216:219]\n\t"
  "v_exp_f32 v242, v242\n\t"
  "v_mfma_f32_16x16x32_bf16 v[220:223], v[44:47], v[108:111], v[124:127]\n\t"
  "v_exp_f32 v243, v243\n\t"
  "v_mfma_f32_16x16x32_bf16 v[220:223], v[40:43], v[76:79], v[220:223]\n\t"
  "v_accvgpr_write_b32 a226, v0\n\t"
  "s_waitcnt lgkmcnt(2)\n\t"
  "v_mfma_f32_16x16x32_bf16 v[208:211], v[48:51], v[128:131], v[208:211]\n\t"
  "ds_read_b128 v[40:43], v18 offset:16640\n\t"
  "v_cvt_pk_bf16_f32 v1, v242, v243\n\t"
  "v_mfma_f32_16x16x32_bf16 v[208:211], v[52:55], v[160:163], v[208:211]\n\t"
  "v_exp_f32 v244, v244\n\t"
  "v_mfma_f32_16x16x32_bf16 v[212:215], v[52:55], v[164:167], v[212:215]\n\t"
  "v_exp_f32 v245, v245\n\t"
  "v_mfma_f32_16x16x32_bf16 v[212:215], v[48:51], v[132:135], v[212:215]\n\t"
  "buffer_load_dwordx4 v5, s[52:55], s70 offen offset:1024 nt lds\n\t"
  "v_accvgpr_write_b32 a227, v1\n\t"
  "v_mfma_f32_16x16x32_bf16 v[216:219], v[48:51], v[136:139], v[216:219]\n\t"
  "ds_read_b128 v[44:47], v19 offset:16640\n\t"
  "v_cvt_pk_bf16_f32 v2, v244, v245\n\t"
  "v_mfma_f32_16x16x32_bf16 v[216:219], v[52:55], v[168:171], v[216:219]\n\t"
  "v_exp_f32 v246, v246\n\t"
  "v_mfma_f32_16x16x32_bf16 v[220:223], v[52:55], v[172:175], v[220:223]\n\t"
  "v_exp_f32 v247, v247\n\t"
  "v_mfma_f32_16x16x32_bf16 v[220:223], v[48:51], v[140:143], v[220:223]\n\t"
  "v_accvgpr_write_b32 a230, v2\n\t"
  "s_waitcnt lgkmcnt(2)\n\t"
  "v_mfma_f32_16x16x32_bf16 v[208:211], v[32:35], v[192:195], v[208:211]\n\t"
  "ds_read_b128 v[48:51], v16 offset:24576\n\t"
  "v_cvt_pk_bf16_f32 v3, v246, v247\n\t"
  "v_mfma_f32_16x16x32_bf16 v[208:211], v[36:39], v[224:227], v[208:211]\n\t"
  "v_exp_f32 v248, v248\n\t"
  "v_mfma_f32_16x16x32_bf16 v[212:215], v[36:39], v[228:231], v[212:215]\n\t"
  "v_exp_f32 v249, v249\n\t"
  "v_mfma_f32_16x16x32_bf16 v[212:215], v[32:35], v[196:199], v[212:215]\n\t"
  "buffer_load_dwordx4 v60, s[52:55], s70 offen offset:2048 nt lds\n\t"
  "v_accvgpr_write_b32 a231, v3\n\t"
  "v_mfma_f32_16x16x32_bf16 v[216:219], v[32:35], v[200:203], v[216:219]\n\t"
  "ds_read_b128 v[52:55], v17 offset:24576\n\t"
  "v_cvt_pk_bf16_f32 v0, v248, v249\n\t"
  "v_mfma_f32_16x16x32_bf16 v[216:219], v[36:39], v[232:235], v[216:219]\n\t"
  "v_exp_f32 v250, v250\n\t"
  "v_mfma_f32_16x16x32_bf16 v[220:223], v[36:39], v[236:239], v[220:223]\n\t"
  "v_exp_f32 v251, v251\n\t"
  "v_mfma_f32_16x16x32_bf16 v[220:223], v[32:35], v[204:207], v[220:223]\n\t"
  "v_accvgpr_write_b32 a234, v0\n\t"
  "s_waitcnt lgkmcnt(2)\n\t"
  "v_mfma_f32_16x16x32_bf16 v[208:211], v[40:43], v[80:83], v[208:211]\n\t"
  "ds_read_b128 v[32:35], v18 offset:24576\n\t"
  "v_cvt_pk_bf16_f32 v1, v250, v251\n\t"
  "v_mfma_f32_16x16x32_bf16 v[208:211], v[44:47], v[144:147], v[208:211]\n\t"
  "v_exp_f32 v252, v252\n\t"
  "v_mfma_f32_16x16x32_bf16 v[212:215], v[44:47], v[148:151], v[212:215]\n\t"
  "v_exp_f32 v253, v253\n\t"
  "v_mfma_f32_16x16x32_bf16 v[212:215], v[40:43], v[84:87], v[212:215]\n\t"
  "buffer_load_dwordx4 v61, s[52:55], s70 offen offset:3072 nt lds\n\t"
  "v_accvgpr_write_b32 a235, v1\n\t"
  "v_mfma_f32_16x16x32_bf16 v[216:219], v[40:43], v[88:91], v[216:219]\n\t"
  "ds_read_b128 v[36:39], v19 offset:24576\n\t"
  "v_cvt_pk_bf16_f32 v2, v252, v253\n\t"
  "v_mfma_f32_16x16x32_bf16 v[216:219], v[44:47], v[152:155], v[216:219]\n\t"
  "v_exp_f32 v254, v254\n\t"
  "v_mfma_f32_16x16x32_bf16 v[220:223], v[44:47], v[156:159], v[220:223]\n\t"
  "v_exp_f32 v255, v255\n\t"
  "v_mfma_f32_16x16x32_bf16 v[220:223], v[40:43], v[92:95], v[220:223]\n\t"
  "v_accvgpr_write_b32 a238, v2\n\t"
  "s_waitcnt lgkmcnt(2)\n\t"
  "v_mfma_f32_16x16x32_bf16 v[240:243], v[48:51], v[64:67], v[112:115]\n\t"
  "ds_read_b128 v[40:43], v16 offset:24832\n\t"
  "v_cvt_pk_bf16_f32 v3, v254, v255\n\t"
  "v_mfma_f32_16x16x32_bf16 v[240:243], v[52:55], v[96:99], v[240:243]\n\t"
  "v_exp_f32 v208, v208\n\t"
  "v_mfma_f32_16x16x32_bf16 v[244:247], v[52:55], v[100:103], v[116:119]\n\t"
  "v_exp_f32 v209, v209\n\t"
  "v_mfma_f32_16x16x32_bf16 v[244:247], v[48:51], v[68:71], v[244:247]\n\t"
  "v_accvgpr_write_b32 a239, v3\n\t"
  "s_add_u32 m0, s69, 147456\n\t"
  "v_mfma_f32_16x16x32_bf16 v[248:251], v[48:51], v[72:75], v[120:123]\n\t"
  "ds_read_b128 v[44:47], v17 offset:24832\n\t"
  "v_cvt_pk_bf16_f32 v0, v208, v209\n\t"
  "v_mfma_f32_16x16x32_bf16 v[248:251], v[52:55], v[104:107], v[248:251]\n\t"
  "v_exp_f32 v210, v210\n\t"
  "v_mfma_f32_16x16x32_bf16 v[252:255], v[52:55], v[108:111], v[124:127]\n\t"
  "v_exp_f32 v211, v211\n\t"
  "v_mfma_f32_16x16x32_bf16 v[252:255], v[48:51], v[76:79], v[252:255]\n\t"
  "v_accvgpr_write_b32 a240, v0\n\t"
  "s_waitcnt lgkmcnt(2)\n\t"
  "v_mfma_f32_16x16x32_bf16 v[240:243], v[32:35], v[128:131], v[240:243]\n\t"
  "ds_read_b128 v[48:51], v18 offset:24832\n\t"
  "v_cvt_pk_bf16_f32 v1, v210, v211\n\t"
  "v_mfma_f32_16x16x32_bf16 v[240:243], v[36:39], v[160:163], v[240:243]\n\t"
  "v_exp_f32 v212, v212\n\t"
  "v_mfma_f32_16x16x32_bf16 v[244:247], v[36:39], v[164:167], v[244:247]\n\t"
  "v_exp_f32 v213, v213\n\t"
  "v_mfma_f32_16x16x32_bf16 v[244:247], v[32:35], v[132:135], v[244:247]\n\t"
  "v_accvgpr_write_b32 a241, v1\n\t"
  "s_add_u32 s70, s76, 196608\n\t"
  "v_mfma_f32_16x16x32_bf16 v[248:251], v[32:35], v[136:139], v[248:251]\n\t"
  "ds_read_b128 v[52:55], v19 offset:24832\n\t"
  "v_cvt_pk_bf16_f32 v2, v212, v213\n\t"
  "v_mfma_f32_16x16x32_bf16 v[248:251], v[36:39], v[168:171], v[248:251]\n\t"
  "v_exp_f32 v214, v214\n\t"
  "v_mfma_f32_16x16x32_bf16 v[252:255], v[36:39], v[172:175], v[252:255]\n\t"
  "v_exp_f32 v215, v215\n\t"
  "v_mfma_f32_16x16x32_bf16 v[252:255], v[32:35], v[140:143], v[252:255]\n\t"
  "v_accvgpr_write_b32 a244, v2\n\t"
  "s_waitcnt lgkmcnt(2)\n\t"
  "v_mfma_f32_16x16x32_bf16 v[240:243], v[40:43], v[192:195], v[240:243]\n\t"
  "ds_read_b64_tr_b16 v[32:33], v12 offset:32768\n\t"
  "v_cvt_pk_bf16_f32 v3, v214, v215\n\t"
  "v_mfma_f32_16x16x32_bf16 v[240:243], v[44:47], v[224:227], v[240:243]\n\t"
  "ds_read_b64_tr_b16 v[34:35], v12 offset:34816\n\t"
  "v_exp_f32 v216, v216\n\t"
  "v_mfma_f32_16x16x32_bf16 v[244:247], v[44:47], v[228:231], v[244:247]\n\t"
  "v_exp_f32 v217, v217\n\t"
  "v_mfma_f32_16x16x32_bf16 v[244:247], v[40:43], v[196:199], v[244:247]\n\t"
  "buffer_load_dwordx4 v4, s[52:55], s70 offen offset:0 nt lds\n\t"
  "v_accvgpr_write_b32 a245, v3\n\t"
  "v_mfma_f32_16x16x32_bf16 v[248:251], v[40:43], v[200:203], v[248:251]\n\t"
  "ds_read_b64_tr_b16 v[36:37], v12 offset:36864\n\t"
  "v_cvt_pk_bf16_f32 v0, v216, v217\n\t"
  "v_mfma_f32_16x16x32_bf16 v[248:251], v[44:47], v[232:235], v[248:251]\n\t"
  "ds_read_b64_tr_b16 v[38:39], v12 offset:38912\n\t"
  "v_exp_f32 v218, v218\n\t"
  "v_mfma_f32_16x16x32_bf16 v[252:255], v[44:47], v[236:239], v[252:255]\n\t"
  "v_exp_f32 v219, v219\n\t"
  "v_mfma_f32_16x16x32_bf16 v[252:255], v[40:43], v[204:207], v[252:255]\n\t"
  "v_accvgpr_write_b32 a248, v0\n\t"
  "s_waitcnt lgkmcnt(4)\n\t"
  "v_mfma_f32_16x16x32_bf16 v[240:243], v[48:51], v[80:83], v[240:243]\n\t"
  "ds_read_b64_tr_b16 v[40:41], v13 offset:32768\n\t"
  "v_cvt_pk_bf16_f32 v1, v218, v219\n\t"
  "v_mfma_f32_16x16x32_bf16 v[240:243], v[52:55], v[144:147], v[240:243]\n\t"
  "ds_read_b64_tr_b16 v[42:43], v13 offset:34816\n\t"
  "v_exp_f32 v220, v220\n\t"
  "v_mfma_f32_16x16x32_bf16 v[244:247], v[52:55], v[148:151], v[244:247]\n\t"
  "v_exp_f32 v221, v221\n\t"
  "v_mfma_f32_16x16x32_bf16 v[244:247], v[48:51], v[84:87], v[244:247]\n\t"
  "buffer_load_dwordx4 v5, s[52:55], s70 offen offset:1024 nt lds\n\t"
  "v_accvgpr_write_b32 a249, v1\n\t"
  "v_mfma_f32_16x16x32_bf16 v[248:251], v[48:51], v[88:91], v[248:251]\n\t"
  "ds_read_b64_tr_b16 v[44:45], v13 offset:36864\n\t"
  "v_cvt_pk_bf16_f32 v2, v220, v221\n\t"
  "v_mfma_f32_16x16x32_bf16 v[248:251], v[52:55], v[152:155], v[248:251]\n\t"
  "ds_read_b64_tr_b16 v[46:47], v13 offset:38912\n\t"
  "v_exp_f32 v222, v222\n\t"
  "v_mfma_f32_16x16x32_bf16 v[252:255], v[52:55], v[156:159], v[252:255]\n\t"
  "v_exp_f32 v223, v223\n\t"
  "v_mfma_f32_16x16x32_bf16 v[252:255], v[48:51], v[92:95], v[252:255]\n\t"
  "v_accvgpr_write_b32 a252, v2\n\t"
  "s_waitcnt lgkmcnt(4)\n\t"
  "v_mfma_f32_16x16x32_bf16 v[64:67], v[32:35], a[0:3], 0\n\t"
  "ds_read_b64_tr_b16 v[48:49], v14 offset:32768\n\t"
  "v_cvt_pk_bf16_f32 v3, v222, v223\n\t"
  "v_mfma_f32_16x16x32_bf16 v[64:67], v[36:39], a[16:19], v[64:67]\n\t"
  "ds_read_b64_tr_b16 v[50:51], v14 offset:34816\n\t"
  "v_mfma_f32_16x16x32_bf16 v[68:71], v[36:39], a[20:23], 0\n\t"
  "buffer_load_dwordx4 v60, s[52:55], s70 offen offset:2048 nt lds\n\t"
  "v_mfma_f32_16x16x32_bf16 v[68:71], v[32:35], a[4:7], v[68:71]\n\t"
  "v_accvgpr_write_b32 a253, v3\n\t"
  "v_mfma_f32_16x16x32_bf16 v[72:75], v[32:35], a[8:11], 0\n\t"
  "ds_read_b64_tr_b16 v[52:53], v14 offset:36864\n\t"
  "v_mfma_f32_16x16x32_bf16 v[72:75], v[36:39], a[24:27], v[72:75]\n\t"
  "ds_read_b64_tr_b16 v[54:55], v14 offset:38912\n\t"
  "v_mfma_f32_16x16x32_bf16 v[76:79], v[36:39], a[28:31], 0\n\t"
  "v_exp_f32 v240, v240\n\t"
  "v_mfma_f32_16x16x32_bf16 v[76:79], v[32:35], a[12:15], v[76:79]\n\t"
  "buffer_load_dwordx4 v61, s[52:55], s70 offen offset:3072 nt lds\n\t"
  "s_waitcnt lgkmcnt(4)\n\t"
  "v_mfma_f32_16x16x32_bf16 v[96:99], v[40:43], a[0:3], 0\n\t"
  "ds_read_b64_tr_b16 v[32:33], v15 offset:32768\n\t"
  "v_mfma_f32_16x16x32_bf16 v[96:99], v[44:47], a[16:19], v[96:99]\n\t"
  "ds_read_b64_tr_b16 v[34:35], v15 offset:34816\n\t"
  "v_mfma_f32_16x16x32_bf16 v[100:103], v[44:47], a[20:23], 0\n\t"
  "v_exp_f32 v241, v241\n\t"
  "v_mfma_f32_16x16x32_bf16 v[100:103], v[40:43], a[4:7], v[100:103]\n\t"
  "v_cvt_pk_bf16_f32 v0, v240, v241\n\t"
  "v_accvgpr_write_b32 a242, v0\n\t"
  "v_mfma_f32_16x16x32_bf16 v[104:107], v[40:43], a[8:11], 0\n\t"
  "ds_read_b64_tr_b16 v[36:37], v15 offset:36864\n\t"
  "v_mfma_f32_16x16x32_bf16 v[104:107], v[44:47], a[24:27], v[104:107]\n\t"
  "ds_read_b64_tr_b16 v[38:39], v15 offset:38912\n\t"
  "v_mfma_f32_16x16x32_bf16 v[108:111], v[44:47], a[28:31], 0\n\t"
  "v_exp_f32 v242, v242\n\t"
  "v_mfma_f32_16x16x32_bf16 v[108:111], v[40:43], a[12:15], v[108:111]\n\t"
  "s_waitcnt lgkmcnt(4)\n\t"
  "v_mfma_f32_16x16x32_bf16 v[128:131], v[48:51], a[0:3], 0\n\t"
  "ds_read_b64_tr_b16 v[40:41], v12 offset:40960\n\t"
  "v_mfma_f32_16x16x32_bf16 v[128:131], v[52:55], a[16:19], v[128:131]\n\t"
  "ds_read_b64_tr_b16 v[42:43], v12 offset:43008\n\t"
  "v_mfma_f32_16x16x32_bf16 v[132:135], v[52:55], a[20:23], 0\n\t"
  "v_exp_f32 v243, v243\n\t"
  "v_mfma_f32_16x16x32_bf16 v[132:135], v[48:51], a[4:7], v[132:135]\n\t"
  "v_cvt_pk_bf16_f32 v1, v242, v243\n\t"
  "v_accvgpr_write_b32 a243, v1\n\t"
  "v_mfma_f32_16x16x32_bf16 v[136:139], v[48:51], a[8:11], 0\n\t"
  "ds_read_b64_tr_b16 v[44:45], v12 offset:45056\n\t"
  "v_mfma_f32_16x16x32_bf16 v[136:139], v[52:55], a[24:27], v[136:139]\n\t"
  "ds_read_b64_tr_b16 v[46:47], v12 offset:47104\n\t"
  "v_mfma_f32_16x16x32_bf16 v[140:143], v[52:55], a[28:31], 0\n\t"
  "v_exp_f32 v244, v244\n\t"
  "v_mfma_f32_16x16x32_bf16 v[140:143], v[48:51], a[12:15], v[140:143]\n\t"
  "s_waitcnt lgkmcnt(4)\n\t"
  "v_mfma_f32_16x16x32_bf16 v[160:163], v[32:35], a[0:3], 0\n\t"
  "ds_read_b64_tr_b16 v[48:49], v13 offset:40960\n\t"
  "v_mfma_f32_16x16x32_bf16 v[160:163], v[36:39], a[16:19], v[160:163]\n\t"
  "ds_read_b64_tr_b16 v[50:51], v13 offset:43008\n\t"
  "v_mfma_f32_16x16x32_bf16 v[164:167], v[36:39], a[20:23], 0\n\t"
  "v_exp_f32 v245, v245\n\t"
  "v_mfma_f32_16x16x32_bf16 v[164:167], v[32:35], a[4:7], v[164:167]\n\t"
  "v_cvt_pk_bf16_f32 v2, v244, v245\n\t"
  "v_accvgpr_write_b32 a246, v2\n\t"
  "v_mfma_f32_16x16x32_bf16 v[168:171], v[32:35], a[8:11], 0\n\t"
  "ds_read_b64_tr_b16 v[52:53], v13 offset:45056\n\t"
  "v_mfma_f32_16x16x32_bf16 v[168:171], v[36:39], a[24:27], v[168:171]\n\t"
  "ds_read_b64_tr_b16 v[54:55], v13 offset:47104\n\t"
  "v_mfma_f32_16x16x32_bf16 v[172:175], v[36:39], a[28:31], 0\n\t"
  "v_exp_f32 v246, v246\n\t"
  "v_mfma_f32_16x16x32_bf16 v[172:175], v[32:35], a[12:15], v[172:175]\n\t"
  "v_exp_f32 v247, v247\n\t"
  "v_mfma_f32_16x16x32_bf16 v[176:179], v[56:59], a[0:3], 0\n\t"
  "v_cvt_pk_bf16_f32 v3, v246, v247\n\t"
  "v_accvgpr_write_b32 a247, v3\n\t"
  "v_mfma_f32_16x16x32_bf16 v[176:179], v[56:59], a[16:19], v[176:179]\n\t"
  "v_exp_f32 v248, v248\n\t"
  "v_mfma_f32_16x16x32_bf16 v[180:183], v[56:59], a[4:7], 0\n\t"
  "v_exp_f32 v249, v249\n\t"
  "v_mfma_f32_16x16x32_bf16 v[180:183], v[56:59], a[20:23], v[180:183]\n\t"
  "v_cvt_pk_bf16_f32 v0, v248, v249\n\t"
  "v_accvgpr_write_b32 a250, v0\n\t"
  "v_mfma_f32_16x16x32_bf16 v[184:187], v[56:59], a[8:11], 0\n\t"
  "v_exp_f32 v250, v250\n\t"
  "v_mfma_f32_16x16x32_bf16 v[184:187], v[56:59], a[24:27], v[184:187]\n\t"
  "v_exp_f32 v251, v251\n\t"
  "v_mfma_f32_16x16x32_bf16 v[188:191], v[56:59], a[12:15], 0\n\t"
  "v_cvt_pk_bf16_f32 v1, v250, v251\n\t"
  "v_accvgpr_write_b32 a251, v1\n\t"
  "v_mfma_f32_16x16x32_bf16 v[188:191], v[56:59], a[28:31], v[188:191]\n\t"
  "s_waitcnt lgkmcnt(4)\n\t"
  "v_mfma_f32_16x16x32_bf16 v[64:67], v[40:43], a[32:35], v[64:67]\n\t"
  "ds_read_b64_tr_b16 v[32:33], v14 offset:40960\n\t"
  "s_add_u32 m0, s69, 65536\n\t"
  "v_mfma_f32_16x16x32_bf16 v[64:67], v[44:47], a[48:51], v[64:67]\n\t"
  "ds_read_b64_tr_b16 v[34:35], v14 offset:43008\n\t"
  "s_add_u32 s70, s77, 0\n\t"
  "v_mfma_f32_16x16x32_bf16 v[68:71], v[44:47], a[52:55], v[68:71]\n\t"
  "v_exp_f32 v252, v252\n\t"
  "v_mfma_f32_16x16x32_bf16 v[68:71], v[40:43], a[36:39], v[68:71]\n\t"
  "s_waitcnt vmcnt(22)\n\t"
  "s_barrier\n\t"
  "v_mfma_f32_16x16x32_bf16 v[72:75], v[40:43], a[40:43], v[72:75]\n\t"
  "ds_read_b64_tr_b16 v[36:37], v14 offset:45056\n\t"
  "v_mfma_f32_16x16x32_bf16 v[72:75], v[44:47], a[56:59], v[72:75]\n\t"
  "ds_read_b64_tr_b16 v[38:39], v14 offset:47104\n\t"
  "v_mfma_f32_16x16x32_bf16 v[76:79], v[44:47], a[60:63], v[76:79]\n\t"
  "buffer_load_dwordx4 v4, s[52:55], s70 offen offset:0 nt lds\n\t"
  "v_mfma_f32_16x16x32_bf16 v[76:79], v[40:43], a[44:47], v[76:79]\n\t"
  "s_waitcnt lgkmcnt(4)\n\t"
  "v_mfma_f32_16x16x32_bf16 v[96:99], v[48:51], a[32:35], v[96:99]\n\t"
  "ds_read_b64_tr_b16 v[40:41], v15 offset:40960\n\t"
  "v_mfma_f32_16x16x32_bf16 v[96:99], v[52:55], a[48:51], v[96:99]\n\t"
  "ds_read_b64_tr_b16 v[42:43], v15 offset:43008\n\t"
  "v_mfma_f32_16x16x32_bf16 v[100:103], v[52:55], a[52:55], v[100:103]\n\t"
  "v_exp_f32 v253, v253\n\t"
  "v_mfma_f32_16x16x32_bf16 v[100:103], v[48:51], a[36:39], v[100:103]\n\t"
  "buffer_load_dwordx4 v5, s[52:55], s70 offen offset:1024 nt lds\n\t"
  "v_cvt_pk_bf16_f32 v2, v252, v253\n\t"
  "v_mfma_f32_16x16x32_bf16 v[104:107], v[48:51], a[40:43], v[104:107]\n\t"
  "ds_read_b64_tr_b16 v[44:45], v15 offset:45056\n\t"
  "v_accvgpr_write_b32 a254, v2\n\t"
  "v_mfma_f32_16x16x32_bf16 v[104:107], v[52:55], a[56:59], v[104:107]\n\t"
  "ds_read_b64_tr_b16 v[46:47], v15 offset:47104\n\t"
  "v_mfma_f32_16x16x32_bf16 v[108:111], v[52:55], a[60:63], v[108:111]\n\t"
  "v_exp_f32 v254, v254\n\t"
  "v_mfma_f32_16x16x32_bf16 v[108:111], v[48:51], a[44:47], v[108:111]\n\t"
  "s_waitcnt lgkmcnt(4)\n\t"
  "v_mfma_f32_16x16x32_bf16 v[128:131], v[32:35], a[32:35], v[128:131]\n\t"
  "ds_read_b64_tr_b16 v[48:49], v12 offset:49152\n\t"
  "v_mfma_f32_16x16x32_bf16 v[128:131], v[36:39], a[48:51], v[128:131]\n\t"
  "ds_read_b64_tr_b16 v[50:51], v12 offset:51200\n\t"
  "v_mfma_f32_16x16x32_bf16 v[132:135], v[36:39], a[52:55], v[132:135]\n\t"
  "buffer_load_dwordx4 v60, s[52:55], s70 offen offset:2048 nt lds\n\t"
  "v_mfma_f32_16x16x32_bf16 v[132:135], v[32:35], a[36:39], v[132:135]\n\t"
  "v_exp_f32 v255, v255\n\t"
  "v_mfma_f32_16x16x32_bf16 v[136:139], v[32:35], a[40:43], v[136:139]\n\t"
  "ds_read_b64_tr_b16 v[52:53], v12 offset:53248\n\t"
  "v_cvt_pk_bf16_f32 v3, v254, v255\n\t"
  "v_mfma_f32_16x16x32_bf16 v[136:139], v[36:39], a[56:59], v[136:139]\n\t"
  "ds_read_b64_tr_b16 v[54:55], v12 offset:55296\n\t"
  "v_accvgpr_write_b32 a255, v3\n\t"
  "v_mfma_f32_16x16x32_bf16 v[140:143], v[36:39], a[60:63], v[140:143]\n\t"
  "v_mfma_f32_16x16x32_bf16 v[140:143], v[32:35], a[44:47], v[140:143]\n\t"
  "buffer_load_dwordx4 v61, s[52:55], s70 offen offset:3072 nt lds\n\t"
  "s_waitcnt lgkmcnt(4)\n\t"
  "v_mfma_f32_16x16x32_bf16 v[160:163], v[40:43], a[32:35], v[160:163]\n\t"
  "ds_read_b64_tr_b16 v[32:33], v13 offset:49152\n\t"
  "s_add_u32 m0, s69, 81920\n\t"
  "v_mfma_f32_16x16x32_bf16 v[160:163], v[44:47], a[48:51], v[160:163]\n\t"
  "ds_read_b64_tr_b16 v[34:35], v13 offset:51200\n\t"
  "s_add_u32 s70, s77, 65536\n\t"
  "v_mfma_f32_16x16x32_bf16 v[164:167], v[44:47], a[52:55], v[164:167]\n\t"
  "v_mfma_f32_16x16x32_bf16 v[164:167], v[40:43], a[36:39], v[164:167]\n\t"
  "v_mfma_f32_16x16x32_bf16 v[168:171], v[40:43], a[40:43], v[168:171]\n\t"
  "ds_read_b64_tr_b16 v[36:37], v13 offset:53248\n\t"
  "v_mfma_f32_16x16x32_bf16 v[168:171], v[44:47], a[56:59], v[168:171]\n\t"
  "ds_read_b64_tr_b16 v[38:39], v13 offset:55296\n\t"
  "v_mfma_f32_16x16x32_bf16 v[172:175], v[44:47], a[60:63], v[172:175]\n\t"
  "buffer_load_dwordx4 v4, s[52:55], s70 offen offset:0 nt lds\n\t"
  "v_mfma_f32_16x16x32_bf16 v[172:175], v[40:43], a[44:47], v[172:175]\n\t"
  "v_mfma_f32_16x16x32_bf16 v[176:179], v[56:59], a[32:35], v[176:179]\n\t"
  "v_mfma_f32_16x16x32_bf16 v[176:179], v[56:59], a[48:51], v[176:179]\n\t"
  "v_mfma_f32_16x16x32_bf16 v[180:183], v[56:59], a[36:39], v[180:183]\n\t"
  "v_mfma_f32_16x16x32_bf16 v[180:183], v[56:59], a[52:55], v[180:183]\n\t"
  "buffer_load_dwordx4 v5, s[52:55], s70 offen offset:1024 nt lds\n\t"
  "v_mfma_f32_16x16x32_bf16 v[184:187], v[56:59], a[40:43], v[184:187]\n\t"
  "v_mfma_f32_16x16x32_bf16 v[184:187], v[56:59], a[56:59], v[184:187]\n\t"
  "v_mfma_f32_16x16x32_bf16 v[188:191], v[56:59], a[44:47], v[188:191]\n\t"
  "v_mfma_f32_16x16x32_bf16 v[188:191], v[56:59], a[60:63], v[188:191]\n\t"
  "s_waitcnt lgkmcnt(4)\n\t"
  "v_mfma_f32_16x16x32_bf16 v[64:67], v[48:51], a[64:67], v[64:67]\n\t"
  "ds_read_b64_tr_b16 v[40:41], v14 offset:49152\n\t"
  "v_mfma_f32_16x16x32_bf16 v[64:67], v[52:55], a[80:83], v[64:67]\n\t"
  "ds_read_b64_tr_b16 v[42:43], v14 offset:51200\n\t"
  "v_mfma_f32_16x16x32_bf16 v[68:71], v[52:55], a[84:87], v[68:71]\n\t"
  "buffer_load_dwordx4 v60, s[52:55], s70 offen offset:2048 nt lds\n\t"
  "v_mfma_f32_16x16x32_bf16 v[68:71], v[48:51], a[68:71], v[68:71]\n\t"
  "v_mfma_f32_16x16x32_bf16 v[72:75], v[48:51], a[72:75], v[72:75]\n\t"
  "ds_read_b64_tr_b16 v[44:45], v14 offset:53248\n\t"
  "v_mfma_f32_16x16x32_bf16 v[72:75], v[52:55], a[88:91], v[72:75]\n\t"
  "ds_read_b64_tr_b16 v[46:47], v14 offset:55296\n\t"
  "v_mfma_f32_16x16x32_bf16 v[76:79], v[52:55], a[92:95], v[76:79]\n\t"
  "v_mfma_f32_16x16x32_bf16 v[76:79], v[48:51], a[76:79], v[76:79]\n\t"
  "buffer_load_dwordx4 v61, s[52:55], s70 offen offset:3072 nt lds\n\t"
  "s_waitcnt lgkmcnt(4)\n\t"
  "v_mfma_f32_16x16x32_bf16 v[96:99], v[32:35], a[64:67], v[96:99]\n\t"
  "ds_read_b64_tr_b16 v[48:49], v15 offset:49152\n\t"
  "v_mfma_f32_16x16x32_bf16 v[96:99], v[36:39], a[80:83], v[96:99]\n\t"
  "ds_read_b64_tr_b16 v[50:51], v15 offset:51200\n\t"
  "v_mfma_f32_16x16x32_bf16 v[100:103], v[36:39], a[84:87], v[100:103]\n\t"
  "v_mfma_f32_16x16x32_bf16 v[100:103], v[32:35], a[68:71], v[100:103]\n\t"
  "v_mfma_f32_16x16x32_bf16 v[104:107], v[32:35], a[72:75], v[104:107]\n\t"
  "ds_read_b64_tr_b16 v[52:53], v15 offset:53248\n\t"
  "v_mfma_f32_16x16x32_bf16 v[104:107], v[36:39], a[88:91], v[104:107]\n\t"
  "ds_read_b64_tr_b16 v[54:55], v15 offset:55296\n\t"
  "v_mfma_f32_16x16x32_bf16 v[108:111], v[36:39], a[92:95], v[108:111]\n\t"
  "v_mfma_f32_16x16x32_bf16 v[108:111], v[32:35], a[76:79], v[108:111]\n\t"
  "s_waitcnt lgkmcnt(4)\n\t"
  "v_mfma_f32_16x16x32_bf16 v[128:131], v[40:43], a[64:67], v[128:131]\n\t"
  "ds_read_b64_tr_b16 v[32:33], v12 offset:57344\n\t"
  "v_mfma_f32_16x16x32_bf16 v[128:131], v[44:47], a[80:83], v[128:131]\n\t"
  "ds_read_b64_tr_b16 v[34:35], v12 offset:59392\n\t"
  "v_mfma_f32_16x16x32_bf16 v[132:135], v[44:47], a[84:87], v[132:135]\n\t"
  "v_mfma_f32_16x16x32_bf16 v[132:135], v[40:43], a[68:71], v[132:135]\n\t"
  "v_mfma_f32_16x16x32_bf16 v[136:139], v[40:43], a[72:75], v[136:139]\n\t"
  "ds_read_b64_tr_b16 v[36:37], v12 offset:61440\n\t"
  "v_mfma_f32_16x16x32_bf16 v[136:139], v[44:47], a[88:91], v[136:139]\n\t"
  "ds_read_b64_tr_b16 v[38:39], v12 offset:63488\n\t"
  "v_mfma_f32_16x16x32_bf16 v[140:143], v[44:47], a[92:95], v[140:143]\n\t"
  "v_mfma_f32_16x16x32_bf16 v[140:143], v[40:43], a[76:79], v[140:143]\n\t"
  "s_waitcnt lgkmcnt(4)\n\t"
  "v_mfma_f32_16x16x32_bf16 v[160:163], v[48:51], a[64:67], v[160:163]\n\t"
  "ds_read_b64_tr_b16 v[40:41], v13 offset:57344\n\t"
  "v_mfma_f32_16x16x32_bf16 v[160:163], v[52:55], a[80:83], v[160:163]\n\t"
  "ds_read_b64_tr_b16 v[42:43], v13 offset:59392\n\t"
  "v_mfma_f32_16x16x32_bf16 v[164:167], v[52:55], a[84:87], v[164:167]\n\t"
  "v_mfma_f32_16x16x32_bf16 v[164:167], v[48:51], a[68:71], v[164:167]\n\t"
  "v_mfma_f32_16x16x32_bf16 v[168:171], v[48:51], a[72:75], v[168:171]\n\t"
  "ds_read_b64_tr_b16 v[44:45], v13 offset:61440\n\t"
  "v_mfma_f32_16x16x32_bf16 v[168:171], v[52:55], a[88:91], v[168:171]\n\t"
  "ds_read_b64_tr_b16 v[46:47], v13 offset:63488\n\t"
  "v_mfma_f32_16x16x32_bf16 v[172:175], v[52:55], a[92:95], v[172:175]\n\t"
  "v_readfirstlane_b32 s72, v31\n\t"
  "s_nop 1\n\t"
  "s_add_u32 s72, s72, 1\n\t"
  "v_mfma_f32_16x16x32_bf16 v[172:175], v[48:51], a[76:79], v[172:175]\n\t"
  "s_lshl_b32 s72, s72, 2\n\t"
  "s_add_u32 s73, s72, 1\n\t"
  "v_mfma_f32_16x16x32_bf16 v[176:179], v[56:59], a[64:67], v[176:179]\n\t"
  "s_add_u32 s74, s72, 2\n\t"
  "s_add_u32 s91, s72, 3\n\t"
  "v_mfma_f32_16x16x32_bf16 v[176:179], v[56:59], a[80:83], v[176:179]\n\t"
  "v_mfma_f32_16x16x32_bf16 v[180:183], v[56:59], a[68:71], v[180:183]\n\t"
  "v_mfma_f32_16x16x32_bf16 v[180:183], v[56:59], a[84:87], v[180:183]\n\t"
  "v_mfma_f32_16x16x32_bf16 v[184:187], v[56:59], a[72:75], v[184:187]\n\t"
  "v_mfma_f32_16x16x32_bf16 v[184:187], v[56:59], a[88:91], v[184:187]\n\t"
  "v_mfma_f32_16x16x32_bf16 v[188:191], v[56:59], a[76:79], v[188:191]\n\t"
  "v_mfma_f32_16x16x32_bf16 v[188:191], v[56:59], a[92:95], v[188:191]\n\t"
  "s_waitcnt lgkmcnt(4)\n\t"
  "v_mfma_f32_16x16x32_bf16 v[64:67], v[32:35], a[96:99], v[64:67]\n\t"
  "ds_read_b64_tr_b16 v[48:49], v14 offset:57344\n\t"
  "v_mfma_f32_16x16x32_bf16 v[64:67], v[36:39], a[112:115], v[64:67]\n\t"
  "ds_read_b64_tr_b16 v[50:51], v14 offset:59392\n\t"
  "v_mfma_f32_16x16x32_bf16 v[68:71], v[36:39], a[116:119], v[68:71]\n\t"
  "v_mfma_f32_16x16x32_bf16 v[68:71], v[32:35], a[100:103], v[68:71]\n\t"
  "v_mfma_f32_16x16x32_bf16 v[72:75], v[32:35], a[104:107], v[72:75]\n\t"
  "ds_read_b64_tr_b16 v[52:53], v14 offset:61440\n\t"
  "v_mfma_f32_16x16x32_bf16 v[72:75], v[36:39], a[120:123], v[72:75]\n\t"
  "ds_read_b64_tr_b16 v[54:55], v14 offset:63488\n\t"
  "v_mfma_f32_16x16x32_bf16 v[76:79], v[36:39], a[124:127], v[76:79]\n\t"
  "v_mfma_f32_16x16x32_bf16 v[76:79], v[32:35], a[108:111], v[76:79]\n\t"
  "s_waitcnt lgkmcnt(4)\n\t"
  "v_mfma_f32_16x16x32_bf16 v[96:99], v[40:43], a[96:99], v[96:99]\n\t"
  "ds_read_b64_tr_b16 v[32:33], v15 offset:57344\n\t"
  "v_mfma_f32_16x16x32_bf16 v[96:99], v[44:47], a[112:115], v[96:99]\n\t"
  "ds_read_b64_tr_b16 v[34:35], v15 offset:59392\n\t"
  "v_mfma_f32_16x16x32_bf16 v[100:103], v[44:47], a[116:119], v[100:103]\n\t"
  "v_mfma_f32_16x16x32_bf16 v[100:103], v[40:43], a[100:103], v[100:103]\n\t"
  "v_mfma_f32_16x16x32_bf16 v[104:107], v[40:43], a[104:107], v[104:107]\n\t"
  "ds_read_b64_tr_b16 v[36:37], v15 offset:61440\n\t"
  "v_mfma_f32_16x16x32_bf16 v[104:107], v[44:47], a[120:123], v[104:107]\n\t"
  "ds_read_b64_tr_b16 v[38:39], v15 offset:63488\n\t"
  "v_mfma_f32_16x16x32_bf16 v[108:111], v[44:47], a[124:127], v[108:111]\n\t"
  "v_mfma_f32_16x16x32_bf16 v[108:111], v[40:43], a[108:111], v[108:111]\n\t"
  "s_waitcnt lgkmcnt(4)\n\t"
  "v_mfma_f32_16x16x32_bf16 v[128:131], v[48:51], a[96:99], v[128:131]\n\t"
  "ds_read_b64_tr_b16 v[40:41], v12 offset:0\n\t"
  "v_mfma_f32_16x16x32_bf16 v[128:131], v[52:55], a[112:115], v[128:131]\n\t"
  "ds_read_b64_tr_b16 v[42:43], v12 offset:2048\n\t"
  "v_mfma_f32_16x16x32_bf16 v[132:135], v[52:55], a[116:119], v[132:135]\n\t"
  "v_mfma_f32_16x16x32_bf16 v[132:135], v[48:51], a[100:103], v[132:135]\n\t"
  "v_mfma_f32_16x16x32_bf16 v[136:139], v[48:51], a[104:107], v[136:139]\n\t"
  "ds_read_b64_tr_b16 v[44:45], v12 offset:4096\n\t"
  "v_mov_b32 v31, 0x4b800000\n\t"
  "v_mfma_f32_16x16x32_bf16 v[136:139], v[52:55], a[120:123], v[136:139]\n\t"
  "ds_read_b64_tr_b16 v[46:47], v12 offset:6144\n\t"
  "s_lshr_b32 s87, s96, 2\n\t"
  "v_mfma_f32_16x16x32_bf16 v[140:143], v[52:55], a[124:127], v[140:143]\n\t"
  "s_add_u32 s87, s87, 0\n\t"
  "s_and_b32 s87, s87, 3\n\t"
  "v_mfma_f32_16x16x32_bf16 v[140:143], v[48:51], a[108:111], v[140:143]\n\t"
  "s_lshl_b32 s86, s90, 2\n\t"
  "s_waitcnt lgkmcnt(4)\n\t"
  "v_mfma_f32_16x16x32_bf16 v[160:163], v[32:35], a[96:99], v[160:163]\n\t"
  "ds_read_b64_tr_b16 v[48:49], v13 offset:0\n\t"
  "s_add_u32 s87, s87, s86\n\t"
  "v_mfma_f32_16x16x32_bf16 v[160:163], v[36:39], a[112:115], v[160:163]\n\t"
  "ds_read_b64_tr_b16 v[50:51], v13 offset:2048\n\t"
  "s_lshl_b32 s87, s87, 17\n\t"
  "v_mfma_f32_16x16x32_bf16 v[164:167], v[36:39], a[116:119], v[164:167]\n\t"
  "v_add_u32 v16, s87, v20\n\t"
  "s_lshr_b32 s87, s96, 2\n\t"
  "v_mfma_f32_16x16x32_bf16 v[164:167], v[32:35], a[100:103], v[164:167]\n\t"
  "s_add_u32 s87, s87, 1\n\t"
  "s_and_b32 s87, s87, 3\n\t"
  "v_mfma_f32_16x16x32_bf16 v[168:171], v[32:35], a[104:107], v[168:171]\n\t"
  "ds_read_b64_tr_b16 v[52:53], v13 offset:4096\n\t"
  "s_lshl_b32 s86, s90, 2\n\t"
  "v_mfma_f32_16x16x32_bf16 v[168:171], v[36:39], a[120:123], v[168:171]\n\t"
  "ds_read_b64_tr_b16 v[54:55], v13 offset:6144\n\t"
  "s_add_u32 s87, s87, s86\n\t"
  "v_mfma_f32_16x16x32_bf16 v[172:175], v[36:39], a[124:127], v[172:175]\n\t"
  "s_lshl_b32 s87, s87, 17\n\t"
  "v_add_u32 v17, s87, v20\n\t"
  "v_mfma_f32_16x16x32_bf16 v[172:175], v[32:35], a[108:111], v[172:175]\n\t"
  "s_lshr_b32 s87, s96, 2\n\t"
  "s_add_u32 s87, s87, 2\n\t"
  "v_mfma_f32_16x16x32_bf16 v[176:179], v[56:59], a[96:99], v[176:179]\n\t"
  "s_and_b32 s87, s87, 3\n\t"
  "s_lshl_b32 s86, s90, 2\n\t"
  "v_mfma_f32_16x16x32_bf16 v[176:179], v[56:59], a[112:115], v[176:179]\n\t"
  "s_add_u32 s87, s87, s86\n\t"
  "s_lshl_b32 s87, s87, 17\n\t"
  "v_mfma_f32_16x16x32_bf16 v[180:183], v[56:59], a[100:103], v[180:183]\n\t"
  "v_add_u32 v18, s87, v20\n\t"
  "s_lshr_b32 s87, s96, 2\n\t"
  "v_mfma_f32_16x16x32_bf16 v[180:183], v[56:59], a[116:119], v[180:183]\n\t"
  "s_add_u32 s87, s87, 3\n\t"
  "s_and_b32 s87, s87, 3\n\t"
  "v_mfma_f32_16x16x32_bf16 v[184:187], v[56:59], a[104:107], v[184:187]\n\t"
  "s_lshl_b32 s86, s90, 2\n\t"
  "s_add_u32 s87, s87, s86\n\t"
  "v_mfma_f32_16x16x32_bf16 v[184:187], v[56:59], a[120:123], v[184:187]\n\t"
  "s_lshl_b32 s87, s87, 17\n\t"
  "v_add_u32 v19, s87, v20\n\t"
  "v_mfma_f32_16x16x32_bf16 v[188:191], v[56:59], a[108:111], v[188:191]\n\t"
  "v_mfma_f32_16x16x32_bf16 v[188:191], v[56:59], a[124:127], v[188:191]\n\t"
  "s_waitcnt lgkmcnt(4)\n\t"
  "v_mfma_f32_16x16x32_bf16 v[64:67], v[40:43], a[128:131], v[64:67]\n\t"
  "ds_read_b64_tr_b16 v[32:33], v14 offset:0\n\t"
  "v_mfma_f32_16x16x32_bf16 v[64:67], v[44:47], a[144:147], v[64:67]\n\t"
  "ds_read_b64_tr_b16 v[34:35], v14 offset:2048\n\t"
  "v_mfma_f32_16x16x32_bf16 v[68:71], v[44:47], a[148:151], v[68:71]\n\t"
  "v_mfma_f32_16x16x32_bf16 v[68:71], v[40:43], a[132:135], v[68:71]\n\t"
  "v_mfma_f32_16x16x32_bf16 v[72:75], v[40:43], a[136:139], v[72:75]\n\t"
  "ds_read_b64_tr_b16 v[36:37], v14 offset:4096\n\t"
  "v_mfma_f32_16x16x32_bf16 v[72:75], v[44:47], a[152:155], v[72:75]\n\t"
  "ds_read_b64_tr_b16 v[38:39], v14 offset:6144\n\t"
  "v_mfma_f32_16x16x32_bf16 v[76:79], v[44:47], a[156:159], v[76:79]\n\t"
  "v_mfma_f32_16x16x32_bf16 v[76:79], v[40:43], a[140:143], v[76:79]\n\t"
  "s_waitcnt lgkmcnt(4)\n\t"
  "v_mfma_f32_16x16x32_bf16 v[96:99], v[48:51], a[128:131], v[96:99]\n\t"
  "ds_read_b64_tr_b16 v[40:41], v15 offset:0\n\t"
  "v_mfma_f32_16x16x32_bf16 v[96:99], v[52:55], a[144:147], v[96:99]\n\t"
  "ds_read_b64_tr_b16 v[42:43], v15 offset:2048\n\t"
  "v_mfma_f32_16x16x32_bf16 v[100:103], v[52:55], a[148:151], v[100:103]\n\t"
  "v_mfma_f32_16x16x32_bf16 v[100:103], v[48:51], a[132:135], v[100:103]\n\t"
  "v_mfma_f32_16x16x32_bf16 v[104:107], v[48:51], a[136:139], v[104:107]\n\t"
  "ds_read_b64_tr_b16 v[44:45], v15 offset:4096\n\t"
  "v_mfma_f32_16x16x32_bf16 v[104:107], v[52:55], a[152:155], v[104:107]\n\t"
  "ds_read_b64_tr_b16 v[46:47], v15 offset:6144\n\t"
  "v_mfma_f32_16x16x32_bf16 v[108:111], v[52:55], a[156:159], v[108:111]\n\t"
  "v_mfma_f32_16x16x32_bf16 v[108:111], v[48:51], a[140:143], v[108:111]\n\t"
  "s_waitcnt lgkmcnt(4)\n\t"
  "v_mfma_f32_16x16x32_bf16 v[128:131], v[32:35], a[128:131], v[128:131]\n\t"
  "ds_read_b64_tr_b16 v[48:49], v12 offset:8192\n\t"
  "v_mfma_f32_16x16x32_bf16 v[128:131], v[36:39], a[144:147], v[128:131]\n\t"
  "ds_read_b64_tr_b16 v[50:51], v12 offset:10240\n\t"
  "v_mfma_f32_16x16x32_bf16 v[132:135], v[36:39], a[148:151], v[132:135]\n\t"
  "v_mfma_f32_16x16x32_bf16 v[132:135], v[32:35], a[132:135], v[132:135]\n\t"
  "v_mfma_f32_16x16x32_bf16 v[136:139], v[32:35], a[136:139], v[136:139]\n\t"
  "ds_read_b64_tr_b16 v[52:53], v12 offset:12288\n\t"
  "v_mfma_f32_16x16x32_bf16 v[136:139], v[36:39], a[152:155], v[136:139]\n\t"
  "ds_read_b64_tr_b16 v[54:55], v12 offset:14336\n\t"
  "v_mfma_f32_16x16x32_bf16 v[140:143], v[36:39], a[156:159], v[140:143]\n\t"
  "v_mfma_f32_16x16x32_bf16 v[140:143], v[32:35], a[140:143], v[140:143]\n\t"
  "s_waitcnt lgkmcnt(4)\n\t"
  "v_mfma_f32_16x16x32_bf16 v[160:163], v[40:43], a[128:131], v[160:163]\n\t"
  "ds_read_b64_tr_b16 v[32:33], v13 offset:8192\n\t"
  "v_mfma_f32_16x16x32_bf16 v[160:163], v[44:47], a[144:147], v[160:163]\n\t"
  "ds_read_b64_tr_b16 v[34:35], v13 offset:10240\n\t"
  "v_mfma_f32_16x16x32_bf16 v[164:167], v[44:47], a[148:151], v[164:167]\n\t"
  "v_mfma_f32_16x16x32_bf16 v[164:167], v[40:43], a[132:135], v[164:167]\n\t"
  "v_mfma_f32_16x16x32_bf16 v[168:171], v[40:43], a[136:139], v[168:171]\n\t"
  "ds_read_b64_tr_b16 v[36:37], v13 offset:12288\n\t"
  "v_mfma_f32_16x16x32_bf16 v[168:171], v[44:47], a[152:155], v[168:171]\n\t"
  "ds_read_b64_tr_b16 v[38:39], v13 offset:14336\n\t"
  "v_mfma_f32_16x16x32_bf16 v[172:175], v[44:47], a[156:159], v[172:175]\n\t"
  "v_mfma_f32_16x16x32_bf16 v[172:175], v[40:43], a[140:143], v[172:175]\n\t"
  "v_mfma_f32_16x16x32_bf16 v[176:179], v[56:59], a[128:131], v[176:179]\n\t"
  "v_mfma_f32_16x16x32_bf16 v[176:179], v[56:59], a[144:147], v[176:179]\n\t"
  "v_mfma_f32_16x16x32_bf16 v[180:183], v[56:59], a[132:135], v[180:183]\n\t"
  "v_mfma_f32_16x16x32_bf16 v[180:183], v[56:59], a[148:151], v[180:183]\n\t"
  "v_mfma_f32_16x16x32_bf16 v[184:187], v[56:59], a[136:139], v[184:187]\n\t"
  "v_mfma_f32_16x16x32_bf16 v[184:187], v[56:59], a[152:155], v[184:187]\n\t"
  "v_mfma_f32_16x16x32_bf16 v[188:191], v[56:59], a[140:143], v[188:191]\n\t"
  "v_mfma_f32_16x16x32_bf16 v[188:191], v[56:59], a[156:159], v[188:191]\n\t"
  "s_waitcnt lgkmcnt(4)\n\t"
  "v_mfma_f32_16x16x32_bf16 v[64:67], v[48:51], a[160:163], v[64:67]\n\t"
  "ds_read_b64_tr_b16 v[40:41], v14 offset:8192\n\t"
  "s_add_u32 m0, s69, 32768\n\t"
  "v_mfma_f32_16x16x32_bf16 v[64:67], v[52:55], a[176:179], v[64:67]\n\t"
  "ds_read_b64_tr_b16 v[42:43], v14 offset:10240\n\t"
  "s_add_u32 s70, s77, 131072\n\t"
  "v_mfma_f32_16x16x32_bf16 v[68:71], v[52:55], a[180:183], v[68:71]\n\t"
  "v_mfma_f32_16x16x32_bf16 v[68:71], v[48:51], a[164:167], v[68:71]\n\t"
  "s_waitcnt vmcnt(21)\n\t"
  "s_barrier\n\t"
  "v_mfma_f32_16x16x32_bf16 v[72:75], v[48:51], a[168:171], v[72:75]\n\t"
  "ds_read_b64_tr_b16 v[44:45], v14 offset:12288\n\t"
  "v_mfma_f32_16x16x32_bf16 v[72:75], v[52:55], a[184:187], v[72:75]\n\t"
  "ds_read_b64_tr_b16 v[46:47], v14 offset:14336\n\t"
  "v_mfma_f32_16x16x32_bf16 v[76:79], v[52:55], a[188:191], v[76:79]\n\t"
  "buffer_load_dwordx4 v4, s[52:55], s70 offen offset:0 nt lds\n\t"
  "v_mfma_f32_16x16x32_bf16 v[76:79], v[48:51], a[172:175], v[76:79]\n\t"
  "s_waitcnt lgkmcnt(4)\n\t"
  "v_mfma_f32_16x16x32_bf16 v[96:99], v[32:35], a[160:163], v[96:99]\n\t"
  "ds_read_b64_tr_b16 v[48:49], v15 offset:8192\n\t"
  "v_mfma_f32_16x16x32_bf16 v[96:99], v[36:39], a[176:179], v[96:99]\n\t"
  "ds_read_b64_tr_b16 v[50:51], v15 offset:10240\n\t"
  "v_mfma_f32_16x16x32_bf16 v[100:103], v[36:39], a[180:183], v[100:103]\n\t"
  "v_mfma_f32_16x16x32_bf16 v[100:103], v[32:35], a[164:167], v[100:103]\n\t"
  "buffer_load_dwordx4 v5, s[52:55], s70 offen offset:1024 nt lds\n\t"
  "v_mfma_f32_16x16x32_bf16 v[104:107], v[32:35], a[168:171], v[104:107]\n\t"
  "ds_read_b64_tr_b16 v[52:53], v15 offset:12288\n\t"
  "v_mfma_f32_16x16x32_bf16 v[104:107], v[36:39], a[184:187], v[104:107]\n\t"
  "ds_read_b64_tr_b16 v[54:55], v15 offset:14336\n\t"
  "v_mfma_f32_16x16x32_bf16 v[108:111], v[36:39], a[188:191], v[108:111]\n\t"
  "v_mfma_f32_16x16x32_bf16 v[108:111], v[32:35], a[172:175], v[108:111]\n\t"
  "s_waitcnt lgkmcnt(4)\n\t"
  "v_mfma_f32_16x16x32_bf16 v[128:131], v[40:43], a[160:163], v[128:131]\n\t"
  "ds_read_b64_tr_b16 v[32:33], v12 offset:16384\n\t"
  "v_mfma_f32_16x16x32_bf16 v[128:131], v[44:47], a[176:179], v[128:131]\n\t"
  "ds_read_b64_tr_b16 v[34:35], v12 offset:18432\n\t"
  "v_mfma_f32_16x16x32_bf16 v[132:135], v[44:47], a[180:183], v[132:135]\n\t"
  "buffer_load_dwordx4 v60, s[52:55], s70 offen offset:2048 nt lds\n\t"
  "v_mfma_f32_16x16x32_bf16 v[132:135], v[40:43], a[164:167], v[132:135]\n\t"
  "v_mfma_f32_16x16x32_bf16 v[136:139], v[40:43], a[168:171], v[136:139]\n\t"
  "ds_read_b64_tr_b16 v[36:37], v12 offset:20480\n\t"
  "v_mfma_f32_16x16x32_bf16 v[136:139], v[44:47], a[184:187], v[136:139]\n\t"
  "ds_read_b64_tr_b16 v[38:39], v12 offset:22528\n\t"
  "v_mfma_f32_16x16x32_bf16 v[140:143], v[44:47], a[188:191], v[140:143]\n\t"
  "v_mfma_f32_16x16x32_bf16 v[140:143], v[40:43], a[172:175], v[140:143]\n\t"
  "buffer_load_dwordx4 v61, s[52:55], s70 offen offset:3072 nt lds\n\t"
  "s_waitcnt lgkmcnt(4)\n\t"
  "v_mfma_f32_16x16x32_bf16 v[160:163], v[48:51], a[160:163], v[160:163]\n\t"
  "ds_read_b64_tr_b16 v[40:41], v13 offset:16384\n\t"
  "s_add_u32 m0, s69, 49152\n\t"
  "v_mfma_f32_16x16x32_bf16 v[160:163], v[52:55], a[176:179], v[160:163]\n\t"
  "ds_read_b64_tr_b16 v[42:43], v13 offset:18432\n\t"
  "s_add_u32 s70, s77, 196608\n\t"
  "v_mfma_f32_16x16x32_bf16 v[164:167], v[52:55], a[180:183], v[164:167]\n\t"
  "v_mfma_f32_16x16x32_bf16 v[164:167], v[48:51], a[164:167], v[164:167]\n\t"
  "v_mfma_f32_16x16x32_bf16 v[168:171], v[48:51], a[168:171], v[168:171]\n\t"
  "ds_read_b64_tr_b16 v[44:45], v13 offset:20480\n\t"
  "v_mfma_f32_16x16x32_bf16 v[168:171], v[52:55], a[184:187], v[168:171]\n\t"
  "ds_read_b64_tr_b16 v[46:47], v13 offset:22528\n\t"
  "v_mfma_f32_16x16x32_bf16 v[172:175], v[52:55], a[188:191], v[172:175]\n\t"
  "buffer_load_dwordx4 v4, s[52:55], s70 offen offset:0 nt lds\n\t"
  "v_mfma_f32_16x16x32_bf16 v[172:175], v[48:51], a[172:175], v[172:175]\n\t"
  "v_mfma_f32_16x16x32_bf16 v[176:179], v[56:59], a[160:163], v[176:179]\n\t"
  "v_mfma_f32_16x16x32_bf16 v[176:179], v[56:59], a[176:179], v[176:179]\n\t"
  "v_mfma_f32_16x16x32_bf16 v[180:183], v[56:59], a[164:167], v[180:183]\n\t"
  "v_mfma_f32_16x16x32_bf16 v[180:183], v[56:59], a[180:183], v[180:183]\n\t"
  "buffer_load_dwordx4 v5, s[52:55], s70 offen offset:1024 nt lds\n\t"
  "v_mfma_f32_16x16x32_bf16 v[184:187], v[56:59], a[168:171], v[184:187]\n\t"
  "v_mfma_f32_16x16x32_bf16 v[184:187], v[56:59], a[184:187], v[184:187]\n\t"
  "v_mfma_f32_16x16x32_bf16 v[188:191], v[56:59], a[172:175], v[188:191]\n\t"
  "v_mfma_f32_16x16x32_bf16 v[188:191], v[56:59], a[188:191], v[188:191]\n\t"
  "s_waitcnt lgkmcnt(4)\n\t"
  "v_mfma_f32_16x16x32_bf16 v[64:67], v[32:35], a[192:195], v[64:67]\n\t"
  "ds_read_b64_tr_b16 v[48:49], v14 offset:16384\n\t"
  "v_mfma_f32_16x16x32_bf16 v[64:67], v[36:39], a[208:211], v[64:67]\n\t"
  "ds_read_b64_tr_b16 v[50:51], v14 offset:18432\n\t"
  "v_mfma_f32_16x16x32_bf16 v[68:71], v[36:39], a[212:215], v[68:71]\n\t"
  "buffer_load_dwordx4 v60, s[52:55], s70 offen offset:2048 nt lds\n\t"
  "v_mfma_f32_16x16x32_bf16 v[68:71], v[32:35], a[196:199], v[68:71]\n\t"
  "v_mfma_f32_16x16x32_bf16 v[72:75], v[32:35], a[200:203], v[72:75]\n\t"
  "ds_read_b64_tr_b16 v[52:53], v14 offset:20480\n\t"
  "v_mfma_f32_16x16x32_bf16 v[72:75], v[36:39], a[216:219], v[72:75]\n\t"
  "ds_read_b64_tr_b16 v[54:55], v14 offset:22528\n\t"
  "v_mfma_f32_16x16x32_bf16 v[76:79], v[36:39], a[220:223], v[76:79]\n\t"
  "v_mfma_f32_16x16x32_bf16 v[76:79], v[32:35], a[204:207], v[76:79]\n\t"
  "buffer_load_dwordx4 v61, s[52:55], s70 offen offset:3072 nt lds\n\t"
  "s_waitcnt lgkmcnt(4)\n\t"
  "v_mfma_f32_16x16x32_bf16 v[96:99], v[40:43], a[192:195], v[96:99]\n\t"
  "ds_read_b64_tr_b16 v[32:33], v15 offset:16384\n\t"
  "v_mfma_f32_16x16x32_bf16 v[96:99], v[44:47], a[208:211], v[96:99]\n\t"
  "ds_read_b64_tr_b16 v[34:35], v15 offset:18432\n\t"
  "v_mfma_f32_16x16x32_bf16 v[100:103], v[44:47], a[212:215], v[100:103]\n\t"
  "v_mfma_f32_16x16x32_bf16 v[100:103], v[40:43], a[196:199], v[100:103]\n\t"
  "v_mfma_f32_16x16x32_bf16 v[104:107], v[40:43], a[200:203], v[104:107]\n\t"
  "ds_read_b64_tr_b16 v[36:37], v15 offset:20480\n\t"
  "v_mfma_f32_16x16x32_bf16 v[104:107], v[44:47], a[216:219], v[104:107]\n\t"
  "ds_read_b64_tr_b16 v[38:39], v15 offset:22528\n\t"
  "v_mfma_f32_16x16x32_bf16 v[108:111], v[44:47], a[220:223], v[108:111]\n\t"
  "v_mfma_f32_16x16x32_bf16 v[108:111], v[40:43], a[204:207], v[108:111]\n\t"
  "s_waitcnt lgkmcnt(4)\n\t"
  "v_mfma_f32_16x16x32_bf16 v[128:131], v[48:51], a[192:195], v[128:131]\n\t"
  "ds_read_b64_tr_b16 v[40:41], v12 offset:24576\n\t"
  "v_mfma_f32_16x16x32_bf16 v[128:131], v[52:55], a[208:211], v[128:131]\n\t"
  "ds_read_b64_tr_b16 v[42:43], v12 offset:26624\n\t"
  "v_mfma_f32_16x16x32_bf16 v[132:135], v[52:55], a[212:215], v[132:135]\n\t"
  "v_mfma_f32_16x16x32_bf16 v[132:135], v[48:51], a[196:199], v[132:135]\n\t"
  "v_mfma_f32_16x16x32_bf16 v[136:139], v[48:51], a[200:203], v[136:139]\n\t"
  "ds_read_b64_tr_b16 v[44:45], v12 offset:28672\n\t"
  "v_mfma_f32_16x16x32_bf16 v[136:139], v[52:55], a[216:219], v[136:139]\n\t"
  "ds_read_b64_tr_b16 v[46:47], v12 offset:30720\n\t"
  "v_mfma_f32_16x16x32_bf16 v[140:143], v[52:55], a[220:223], v[140:143]\n\t"
  "v_mfma_f32_16x16x32_bf16 v[140:143], v[48:51], a[204:207], v[140:143]\n\t"
  "s_waitcnt lgkmcnt(4)\n\t"
  "v_mfma_f32_16x16x32_bf16 v[160:163], v[32:35], a[192:195], v[160:163]\n\t"
  "ds_read_b64_tr_b16 v[48:49], v13 offset:24576\n\t"
  "v_mfma_f32_16x16x32_bf16 v[160:163], v[36:39], a[208:211], v[160:163]\n\t"
  "ds_read_b64_tr_b16 v[50:51], v13 offset:26624\n\t"
  "v_mfma_f32_16x16x32_bf16 v[164:167], v[36:39], a[212:215], v[164:167]\n\t"
  "v_mfma_f32_16x16x32_bf16 v[164:167], v[32:35], a[196:199], v[164:167]\n\t"
  "v_mfma_f32_16x16x32_bf16 v[168:171], v[32:35], a[200:203], v[168:171]\n\t"
  "ds_read_b64_tr_b16 v[52:53], v13 offset:28672\n\t"
  "v_mfma_f32_16x16x32_bf16 v[168:171], v[36:39], a[216:219], v[168:171]\n\t"
  "ds_read_b64_tr_b16 v[54:55], v13 offset:30720\n\t"
  "v_mfma_f32_16x16x32_bf16 v[172:175], v[36:39], a[220:223], v[172:175]\n\t"
  "v_mfma_f32_16x16x32_bf16 v[172:175], v[32:35], a[204:207], v[172:175]\n\t"
  "v_mfma_f32_16x16x32_bf16 v[176:179], v[56:59], a[192:195], v[176:179]\n\t"
  "v_mfma_f32_16x16x32_bf16 v[176:179], v[56:59], a[208:211], v[176:179]\n\t"
  "v_mfma_f32_16x16x32_bf16 v[180:183], v[56:59], a[196:199], v[180:183]\n\t"
  "v_mfma_f32_16x16x32_bf16 v[180:183], v[56:59], a[212:215], v[180:183]\n\t"
  "v_mfma_f32_16x16x32_bf16 v[184:187], v[56:59], a[200:203], v[184:187]\n\t"
  "v_mfma_f32_16x16x32_bf16 v[184:187], v[56:59], a[216:219], v[184:187]\n\t"
  "v_mfma_f32_16x16x32_bf16 v[188:191], v[56:59], a[204:207], v[188:191]\n\t"
  "v_mfma_f32_16x16x32_bf16 v[188:191], v[56:59], a[220:223], v[188:191]\n\t"
  "s_waitcnt lgkmcnt(4)\n\t"
  "v_mfma_f32_16x16x32_bf16 v[64:67], v[40:43], a[224:227], v[64:67]\n\t"
  "ds_read_b64_tr_b16 v[32:33], v14 offset:24576\n\t"
  "v_mfma_f32_16x16x32_bf16 v[64:67], v[44:47], a[240:243], v[64:67]\n\t"
  "ds_read_b64_tr_b16 v[34:35], v14 offset:26624\n\t"
  "v_mfma_f32_16x16x32_bf16 v[68:71], v[44:47], a[244:247], v[68:71]\n\t"
  "v_mfma_f32_16x16x32_bf16 v[68:71], v[40:43], a[228:231], v[68:71]\n\t"
  "v_mfma_f32_16x16x32_bf16 v[72:75], v[40:43], a[232:235], v[72:75]\n\t"
  "ds_read_b64_tr_b16 v[36:37], v14 offset:28672\n\t"
  "v_mfma_f32_16x16x32_bf16 v[72:75], v[44:47], a[248:251], v[72:75]\n\t"
  "ds_read_b64_tr_b16 v[38:39], v14 offset:30720\n\t"
  "v_mfma_f32_16x16x32_bf16 v[76:79], v[44:47], a[252:255], v[76:79]\n\t"
  "v_mfma_f32_16x16x32_bf16 v[76:79], v[40:43], a[236:239], v[76:79]\n\t"
  "s_waitcnt lgkmcnt(4)\n\t"
  "v_mfma_f32_16x16x32_bf16 v[96:99], v[48:51], a[224:227], v[96:99]\n\t"
  "ds_read_b64_tr_b16 v[40:41], v15 offset:24576\n\t"
  "v_mfma_f32_16x16x32_bf16 v[96:99], v[52:55], a[240:243], v[96:99]\n\t"
  "ds_read_b64_tr_b16 v[42:43], v15 offset:26624\n\t"
  "v_mfma_f32_16x16x32_bf16 v[100:103], v[52:55], a[244:247], v[100:103]\n\t"
  "v_mfma_f32_16x16x32_bf16 v[100:103], v[48:51], a[228:231], v[100:103]\n\t"
  "v_mfma_f32_16x16x32_bf16 v[104:107], v[48:51], a[232:235], v[104:107]\n\t"
  "ds_read_b64_tr_b16 v[44:45], v15 offset:28672\n\t"
  "v_mfma_f32_16x16x32_bf16 v[104:107], v[52:55], a[248:251], v[104:107]\n\t"
  "ds_read_b64_tr_b16 v[46:47], v15 offset:30720\n\t"
  "v_add_u32 v12, 0x10000, v12\n\t"
  "v_mfma_f32_16x16x32_bf16 v[108:111], v[52:55], a[252:255], v[108:111]\n\t"
  "v_add_u32 v13, 0x10000, v13\n\t"
  "v_add_u32 v14, 0x10000, v14\n\t"
  "v_mfma_f32_16x16x32_bf16 v[108:111], v[48:51], a[236:239], v[108:111]\n\t"
  "v_add_u32 v15, 0x10000, v15\n\t"
  "s_waitcnt lgkmcnt(4)\n\t"
  "v_mfma_f32_16x16x32_bf16 v[128:131], v[32:35], a[224:227], v[128:131]\n\t"
  "ds_read_b64_tr_b16 v[48:49], v12 offset:32768\n\t"
  "v_mfma_f32_16x16x32_bf16 v[128:131], v[36:39], a[240:243], v[128:131]\n\t"
  "ds_read_b64_tr_b16 v[50:51], v12 offset:34816\n\t"
  "v_mfma_f32_16x16x32_bf16 v[132:135], v[36:39], a[244:247], v[132:135]\n\t"
  "v_mfma_f32_16x16x32_bf16 v[132:135], v[32:35], a[228:231], v[132:135]\n\t"
  "v_mfma_f32_16x16x32_bf16 v[136:139], v[32:35], a[232:235], v[136:139]\n\t"
  "ds_read_b64_tr_b16 v[52:53], v12 offset:36864\n\t"
  "v_mfma_f32_16x16x32_bf16 v[136:139], v[36:39], a[248:251], v[136:139]\n\t"
  "ds_read_b64_tr_b16 v[54:55], v12 offset:38912\n\t"
  "v_mfma_f32_16x16x32_bf16 v[140:143], v[36:39], a[252:255], v[140:143]\n\t"
  "v_mfma_f32_16x16x32_bf16 v[140:143], v[32:35], a[236:239], v[140:143]\n\t"
  "s_waitcnt lgkmcnt(4)\n\t"
  "v_mfma_f32_16x16x32_bf16 v[160:163], v[40:43], a[224:227], v[160:163]\n\t"
  "ds_read_b64_tr_b16 v[32:33], v13 offset:32768\n\t"
  "v_mfma_f32_16x16x32_bf16 v[160:163], v[44:47], a[240:243], v[160:163]\n\t"
  "ds_read_b64_tr_b16 v[34:35], v13 offset:34816\n\t"
  "v_mfma_f32_16x16x32_bf16 v[164:167], v[44:47], a[244:247], v[164:167]\n\t"
  "v_mfma_f32_16x16x32_bf16 v[164:167], v[40:43], a[228:231], v[164:167]\n\t"
  "v_mfma_f32_16x16x32_bf16 v[168:171], v[40:43], a[232:235], v[168:171]\n\t"
  "ds_read_b64_tr_b16 v[36:37], v13 offset:36864\n\t"
  "v_mfma_f32_16x16x32_bf16 v[168:171], v[44:47], a[248:251], v[168:171]\n\t"
  "ds_read_b64_tr_b16 v[38:39], v13 offset:38912\n\t"
  "v_mfma_f32_16x16x32_bf16 v[172:175], v[44:47], a[252:255], v[172:175]\n\t"
  "v_mfma_f32_16x16x32_bf16 v[172:175], v[40:43], a[236:239], v[172:175]\n\t"
  "v_mfma_f32_16x16x32_bf16 v[176:179], v[56:59], a[224:227], v[176:179]\n\t"
  "v_mfma_f32_16x16x32_bf16 v[176:179], v[56:59], a[240:243], v[176:179]\n\t"
  "v_mfma_f32_16x16x32_bf16 v[180:183], v[56:59], a[228:231], v[180:183]\n\t"
  "v_mfma_f32_16x16x32_bf16 v[180:183], v[56:59], a[244:247], v[180:183]\n\t"
  "v_mfma_f32_16x16x32_bf16 v[184:187], v[56:59], a[232:235], v[184:187]\n\t"
  "v_mfma_f32_16x16x32_bf16 v[184:187], v[56:59], a[248:251], v[184:187]\n\t"
  "v_mfma_f32_16x16x32_bf16 v[188:191], v[56:59], a[236:239], v[188:191]\n\t"
  "v_mfma_f32_16x16x32_bf16 v[188:191], v[56:59], a[252:255], v[188:191]\n\t"
  "s_waitcnt lgkmcnt(4)\n\t"
  "v_mfma_f32_16x16x32_bf16 v[192:195], v[48:51], a[0:3], 0\n\t"
  "ds_read_b64_tr_b16 v[40:41], v14 offset:32768\n\t"
  "v_mfma_f32_16x16x32_bf16 v[192:195], v[52:55], a[16:19], v[192:195]\n\t"
  "ds_read_b64_tr_b16 v[42:43], v14 offset:34816\n\t"
  "v_mfma_f32_16x16x32_bf16 v[196:199], v[52:55], a[20:23], 0\n\t"
  "v_mfma_f32_16x16x32_bf16 v[196:199], v[48:51], a[4:7], v[196:199]\n\t"
  "v_cmp_nlt_f32 vcc, v176, v31\n\t"
  "v_mfma_f32_16x16x32_bf16 v[200:203], v[48:51], a[8:11], 0\n\t"
  "ds_read_b64_tr_b16 v[44:45], v14 offset:36864\n\t"
  "v_mfma_f32_16x16x32_bf16 v[200:203], v[52:55], a[24:27], v[200:203]\n\t"
  "ds_read_b64_tr_b16 v[46:47], v14 offset:38912\n\t"
  "v_mfma_f32_16x16x32_bf16 v[204:207], v[52:55], a[28:31], 0\n\t"
  "v_rcp_f32 v22, v176\n\t"
  "v_mfma_f32_16x16x32_bf16 v[204:207], v[48:51], a[12:15], v[204:207]\n\t"
  "s_or_b64 s[84:85], s[84:85], vcc\n\t"
  "s_waitcnt lgkmcnt(4)\n\t"
  "v_mfma_f32_16x16x32_bf16 v[224:227], v[32:35], a[0:3], 0\n\t"
  "ds_read_b64_tr_b16 v[48:49], v15 offset:32768\n\t"
  "v_cmp_nlt_f32 vcc, v180, v31\n\t"
  "v_mfma_f32_16x16x32_bf16 v[224:227], v[36:39], a[16:19], v[224:227]\n\t"
  "ds_read_b64_tr_b16 v[50:51], v15 offset:34816\n\t"
  "v_mfma_f32_16x16x32_bf16 v[228:231], v[36:39], a[20:23], 0\n\t"
  "v_rcp_f32 v23, v180\n\t"
  "v_mfma_f32_16x16x32_bf16 v[228:231], v[32:35], a[4:7], v[228:231]\n\t"
  "s_or_b64 s[84:85], s[84:85], vcc\n\t"
  "v_cmp_nlt_f32 vcc, v184, v31\n\t"
  "v_mfma_f32_16x16x32_bf16 v[232:235], v[32:35], a[8:11], 0\n\t"
  "ds_read_b64_tr_b16 v[52:53], v15 offset:36864\n\t"
  "v_mfma_f32_16x16x32_bf16 v[232:235], v[36:39], a[24:27], v[232:235]\n\t"
  "ds_read_b64_tr_b16 v[54:55], v15 offset:38912\n\t"
  "v_mfma_f32_16x16x32_bf16 v[236:239], v[36:39], a[28:31], 0\n\t"
  "v_rcp_f32 v24, v184\n\t"
  "v_mfma_f32_16x16x32_bf16 v[236:239], v[32:35], a[12:15], v[236:239]\n\t"
  "s_or_b64 s[84:85], s[84:85], vcc\n\t"
  "s_waitcnt lgkmcnt(4)\n\t"
  "v_mfma_f32_16x16x32_bf16 v[80:83], v[40:43], a[0:3], 0\n\t"
  "ds_read_b64_tr_b16 v[32:33], v12 offset:40960\n\t"
  "v_cmp_nlt_f32 vcc, v188, v31\n\t"
  "v_mfma_f32_16x16x32_bf16 v[80:83], v[44:47], a[16:19], v[80:83]\n\t"
  "ds_read_b64_tr_b16 v[34:35], v12 offset:43008\n\t"
  "v_mfma_f32_16x16x32_bf16 v[84:87], v[44:47], a[20:23], 0\n\t"
  "v_rcp_f32 v25, v188\n\t"
  "v_mfma_f32_16x16x32_bf16 v[84:87], v[40:43], a[4:7], v[84:87]\n\t"
  "s_or_b64 s[84:85], s[84:85], vcc\n\t"
  "v_mfma_f32_16x16x32_bf16 v[88:91], v[40:43], a[8:11], 0\n\t"
  "ds_read_b64_tr_b16 v[36:37], v12 offset:45056\n\t"
  "v_mul_f32 v64, v22, v64\n\t"
  "v_mfma_f32_16x16x32_bf16 v[88:91], v[44:47], a[24:27], v[88:91]\n\t"
  "ds_read_b64_tr_b16 v[38:39], v12 offset:47104\n\t"
  "v_mul_f32 v65, v22, v65\n\t"
  "v_mfma_f32_16x16x32_bf16 v[92:95], v[44:47], a[28:31], 0\n\t"
  "s_cmp_lg_u64 s[84:85], 0\n\ts_cselect_b32 s87, 2, 0\n\t"
  "v_mfma_f32_16x16x32_bf16 v[92:95], v[40:43], a[12:15], v[92:95]\n\t"
  "v_mov_b32 v0, s87\n\t"
  "s_waitcnt lgkmcnt(4)\n\t"
  "v_mfma_f32_16x16x32_bf16 v[144:147], v[48:51], a[0:3], 0\n\t"
  "ds_read_b64_tr_b16 v[40:41], v13 offset:40960\n\t"
  "s_lshl_b32 s87, s90, 2\n\t"
  "v_mfma_f32_16x16x32_bf16 v[144:147], v[52:55], a[16:19], v[144:147]\n\t"
  "ds_read_b64_tr_b16 v[42:43], v13 offset:43008\n\t"
  "s_add_u32 s87, s87, s89\n\t"
  "v_mfma_f32_16x16x32_bf16 v[148:151], v[52:55], a[20:23], 0\n\t"
  "s_add_u32 s87, s87, 16384\n\t"
  "v_mov_b32 v1, s87\n\t"
  "v_mfma_f32_16x16x32_bf16 v[148:151], v[48:51], a[4:7], v[148:151]\n\t"
  "s_add_u32 s87, s89, 16384\n\t"
  "v_mov_b32 v27, s87\n\t"
  "v_mfma_f32_16x16x32_bf16 v[152:155], v[48:51], a[8:11], 0\n\t"
  "ds_read_b64_tr_b16 v[44:45], v13 offset:45056\n\t"
  "v_mul_f32 v66, v22, v66\n\t"
  "v_mfma_f32_16x16x32_bf16 v[152:155], v[52:55], a[24:27], v[152:155]\n\t"
  "ds_read_b64_tr_b16 v[46:47], v13 offset:47104\n\t"
  "v_mul_f32 v67, v22, v67\n\t"
  "v_mfma_f32_16x16x32_bf16 v[156:159], v[52:55], a[28:31], 0\n\t"
  "v_mul_f32 v96, v22, v96\n\t"
  "v_mul_f32 v97, v22, v97\n\t"
  "v_mfma_f32_16x16x32_bf16 v[156:159], v[48:51], a[12:15], v[156:159]\n\t"
  "v_mul_f32 v98, v22, v98\n\t"
  "s_waitcnt lgkmcnt(4)\n\t"
  "v_mfma_f32_16x16x32_bf16 v[192:195], v[32:35], a[32:35], v[192:195]\n\t"
  "ds_read_b64_tr_b16 v[48:49], v14 offset:40960\n\t"
  "v_mul_f32 v99, v22, v99\n\t"
  "v_mfma_f32_16x16x32_bf16 v[192:195], v[36:39], a[48:51], v[192:195]\n\t"
  "ds_read_b64_tr_b16 v[50:51], v14 offset:43008\n\t"
  "v_cvt_pk_bf16_f32 v208, v64, v65\n\t"
  "v_mfma_f32_16x16x32_bf16 v[196:199], v[36:39], a[52:55], v[196:199]\n\t"
  "v_cvt_pk_bf16_f32 v209, v66, v67\n\t"
  "v_cvt_pk_bf16_f32 v210, v96, v97\n\t"
  "v_mfma_f32_16x16x32_bf16 v[196:199], v[32:35], a[36:39], v[196:199]\n\t"
  "v_cvt_pk_bf16_f32 v211, v98, v99\n\t"
  "s_and_b32 s87, s96, 3\n\t"
  "v_mfma_f32_16x16x32_bf16 v[200:203], v[32:35], a[40:43], v[200:203]\n\t"
  "ds_read_b64_tr_b16 v[52:53], v14 offset:45056\n\t"
  "s_mul_i32 s87, s87, 224\n\t"
  "v_mfma_f32_16x16x32_bf16 v[200:203], v[36:39], a[56:59], v[200:203]\n\t"
  "ds_read_b64_tr_b16 v[54:55], v14 offset:47104\n\t"
  "s_lshr_b32 s86, s96, 2\n\t"
  "v_mfma_f32_16x16x32_bf16 v[204:207], v[36:39], a[60:63], v[204:207]\n\t"
  "buffer_store_dwordx4 v[208:211], v16, s[56:59], s79 offen offset:0 sc1\n\t"
  "v_mul_f32 v68, v23, v68\n\t"
  "v_mfma_f32_16x16x32_bf16 v[204:207], v[32:35], a[44:47], v[204:207]\n\t"
  "v_mul_f32 v69, v23, v69\n\t"
  "s_waitcnt lgkmcnt(4)\n\t"
  "v_mfma_f32_16x16x32_bf16 v[224:227], v[40:43], a[32:35], v[224:227]\n\t"
  "ds_read_b64_tr_b16 v[32:33], v15 offset:40960\n\t"
  "s_add_u32 m0, s69, 0\n\t"
  "v_mfma_f32_16x16x32_bf16 v[224:227], v[44:47], a[48:51], v[224:227]\n\t"
  "ds_read_b64_tr_b16 v[34:35], v15 offset:43008\n\t"
  "s_add_u32 s70, s78, 0\n\t"
  "v_mfma_f32_16x16x32_bf16 v[228:231], v[44:47], a[52:55], v[228:231]\n\t"
  "v_mul_f32 v70, v23, v70\n\t"
  "v_mul_f32 v71, v23, v71\n\t"
  "v_mfma_f32_16x16x32_bf16 v[228:231], v[40:43], a[36:39], v[228:231]\n\t"
  "s_waitcnt vmcnt(26)\n\t"
  "s_barrier\n\t"
  "v_mfma_f32_16x16x32_bf16 v[232:235], v[40:43], a[40:43], v[232:235]\n\t"
  "ds_read_b64_tr_b16 v[36:37], v15 offset:45056\n\t"
  "v_mul_f32 v100, v23, v100\n\t"
  "v_mfma_f32_16x16x32_bf16 v[232:235], v[44:47], a[56:59], v[232:235]\n\t"
  "ds_read_b64_tr_b16 v[38:39], v15 offset:47104\n\t"
  "v_mul_f32 v101, v23, v101\n\t"
  "v_mfma_f32_16x16x32_bf16 v[236:239], v[44:47], a[60:63], v[236:239]\n\t"
  "buffer_load_dwordx4 v4, s[52:55], s70 offen offset:0 nt lds\n\t"
  "v_mul_f32 v102, v23, v102\n\t"
  "v_mfma_f32_16x16x32_bf16 v[236:239], v[40:43], a[44:47], v[236:239]\n\t"
  "ds_write_b32 v1, v0\n\t"
  "s_waitcnt lgkmcnt(5)\n\t"
  "v_mfma_f32_16x16x32_bf16 v[80:83], v[48:51], a[32:35], v[80:83]\n\t"
  "ds_read_b64_tr_b16 v[40:41], v12 offset:49152\n\t"
  "v_mul_f32 v103, v23, v103\n\t"
  "v_mfma_f32_16x16x32_bf16 v[80:83], v[52:55], a[48:51], v[80:83]\n\t"
  "ds_read_b64_tr_b16 v[42:43], v12 offset:51200\n\t"
  "v_cvt_pk_bf16_f32 v212, v68, v69\n\t"
  "v_mfma_f32_16x16x32_bf16 v[84:87], v[52:55], a[52:55], v[84:87]\n\t"
  "v_cvt_pk_bf16_f32 v213, v70, v71\n\t"
  "v_cvt_pk_bf16_f32 v214, v100, v101\n\t"
  "v_mfma_f32_16x16x32_bf16 v[84:87], v[48:51], a[36:39], v[84:87]\n\t"
  "buffer_load_dwordx4 v5, s[52:55], s70 offen offset:1024 nt lds\n\t"
  "v_cvt_pk_bf16_f32 v215, v102, v103\n\t"
  "v_mfma_f32_16x16x32_bf16 v[88:91], v[48:51], a[40:43], v[88:91]\n\t"
  "ds_read_b64_tr_b16 v[44:45], v12 offset:53248\n\t"
  "s_mul_i32 s86, s86, 917504\n\t"
  "v_mfma_f32_16x16x32_bf16 v[88:91], v[52:55], a[56:59], v[88:91]\n\t"
  "ds_read_b64_tr_b16 v[46:47], v12 offset:55296\n\t"
  "s_add_u32 s87, s87, s86\n\t"
  "v_mfma_f32_16x16x32_bf16 v[92:95], v[52:55], a[60:63], v[92:95]\n\t"
  "s_sub_u32 s86, 3670016, s87\n\t"
  "s_mov_b64 s[60:61], %7\n\t"
  "v_mfma_f32_16x16x32_bf16 v[92:95], v[48:51], a[44:47], v[92:95]\n\t"
  "s_waitcnt lgkmcnt(5)\n\t"
  "v_mfma_f32_16x16x32_bf16 v[144:147], v[32:35], a[32:35], v[144:147]\n\t"
  "ds_read_b64_tr_b16 v[48:49], v13 offset:49152\n\t"
  "v_mfma_f32_16x16x32_bf16 v[144:147], v[36:39], a[48:51], v[144:147]\n\t"
  "ds_read_b64_tr_b16 v[50:51], v13 offset:51200\n\t"
  "v_mfma_f32_16x16x32_bf16 v[148:151], v[36:39], a[52:55], v[148:151]\n\t"
  "buffer_load_dwordx4 v60, s[52:55], s70 offen offset:2048 nt lds\n\t"
  "v_mfma_f32_16x16x32_bf16 v[148:151], v[32:35], a[36:39], v[148:151]\n\t"
  "s_add_u32 s60, s60, s87\n\ts_addc_u32 s61, s61, 0\n\t"
  "v_mfma_f32_16x16x32_bf16 v[152:155], v[32:35], a[40:43], v[152:155]\n\t"
  "ds_read_b64_tr_b16 v[52:53], v13 offset:53248\n\t"
  "s_mov_b32 s62, s86\n\t"
  "v_mfma_f32_16x16x32_bf16 v[152:155], v[36:39], a[56:59], v[152:155]\n\t"
  "ds_read_b64_tr_b16 v[54:55], v13 offset:55296\n\t"
  "s_mov_b32 s63, 0x20000\n\t"
  "v_mfma_f32_16x16x32_bf16 v[156:159], v[36:39], a[60:63], v[156:159]\n\t"
  "buffer_store_dwordx4 v[212:215], v17, s[56:59], s79 offen offset:0 sc1\n\t"
  "v_mul_f32 v72, v24, v72\n\t"
  "v_mfma_f32_16x16x32_bf16 v[156:159], v[32:35], a[44:47], v[156:159]\n\t"
  "buffer_load_dwordx4 v61, s[52:55], s70 offen offset:3072 nt lds\n\t"
  "s_waitcnt lgkmcnt(4)\n\t"
  "v_mfma_f32_16x16x32_bf16 v[192:195], v[40:43], a[64:67], v[192:195]\n\t"
  "ds_read_b64_tr_b16 v[32:33], v14 offset:49152\n\t"
  "v_mul_f32 v73, v24, v73\n\t"
  "v_mfma_f32_16x16x32_bf16 v[192:195], v[44:47], a[80:83], v[192:195]\n\t"
  "ds_read_b64_tr_b16 v[34:35], v14 offset:51200\n\t"
  "v_mul_f32 v74, v24, v74\n\t"
  "v_mfma_f32_16x16x32_bf16 v[196:199], v[44:47], a[84:87], v[196:199]\n\t"
  "v_mul_f32 v75, v24, v75\n\t"
  "v_mul_f32 v104, v24, v104\n\t"
  "v_mfma_f32_16x16x32_bf16 v[196:199], v[40:43], a[68:71], v[196:199]\n\t"
  "v_mul_f32 v105, v24, v105\n\t"
  "v_mul_f32 v106, v24, v106\n\t"
  "v_mfma_f32_16x16x32_bf16 v[200:203], v[40:43], a[72:75], v[200:203]\n\t"
  "ds_read_b64_tr_b16 v[36:37], v14 offset:53248\n\t"
  "v_mul_f32 v107, v24, v107\n\t"
  "v_mfma_f32_16x16x32_bf16 v[200:203], v[44:47], a[88:91], v[200:203]\n\t"
  "ds_read_b64_tr_b16 v[38:39], v14 offset:55296\n\t"
  "v_cvt_pk_bf16_f32 v216, v72, v73\n\t"
  "v_mfma_f32_16x16x32_bf16 v[204:207], v[44:47], a[92:95], v[204:207]\n\t"
  "v_cvt_pk_bf16_f32 v217, v74, v75\n\t"
  "v_cvt_pk_bf16_f32 v218, v104, v105\n\t"
  "v_mfma_f32_16x16x32_bf16 v[204:207], v[40:43], a[76:79], v[204:207]\n\t"
  "v_cvt_pk_bf16_f32 v219, v106, v107\n\t"
  "s_waitcnt lgkmcnt(4)\n\t"
  "v_mfma_f32_16x16x32_bf16 v[224:227], v[48:51], a[64:67], v[224:227]\n\t"
  "ds_read_b64_tr_b16 v[40:41], v15 offset:49152\n\t"
  "s_and_b32 s86, s96, 3\n\t"
  "v_mfma_f32_16x16x32_bf16 v[224:227], v[52:55], a[80:83], v[224:227]\n\t"
  "ds_read_b64_tr_b16 v[42:43], v15 offset:51200\n\t"
  "s_mov_b32 s84, 0xcb61fa50\n\t"
  "v_mfma_f32_16x16x32_bf16 v[228:231], v[52:55], a[84:87], v[228:231]\n\t"
  "s_cmp_eq_u32 s86, 1\n\ts_cselect_b32 s84, 0x1cb60fa5, s84\n\t"
  "v_mfma_f32_16x16x32_bf16 v[228:231], v[48:51], a[68:71], v[228:231]\n\t"
  "s_cmp_eq_u32 s86, 2\n\ts_cselect_b32 s84, 0x61cb50fa, s84\n\t"
  "v_mfma_f32_16x16x32_bf16 v[232:235], v[48:51], a[72:75], v[232:235]\n\t"
  "ds_read_b64_tr_b16 v[44:45], v15 offset:53248\n\t"
  "v_mfma_f32_16x16x32_bf16 v[232:235], v[52:55], a[88:91], v[232:235]\n\t"
  "ds_read_b64_tr_b16 v[46:47], v15 offset:55296\n\t"
  "v_mfma_f32_16x16x32_bf16 v[236:239], v[52:55], a[92:95], v[236:239]\n\t"
  "s_cmp_eq_u32 s86, 3\n\ts_cselect_b32 s84, 0xb61ca50f, s84\n\t"
  "v_mfma_f32_16x16x32_bf16 v[236:239], v[48:51], a[76:79], v[236:239]\n\t"
  "s_mov_b32 s85, 0xe943d872\n\t"
  "s_waitcnt lgkmcnt(4)\n\t"
  "v_mfma_f32_16x16x32_bf16 v[80:83], v[32:35], a[64:67], v[80:83]\n\t"
  "ds_read_b64_tr_b16 v[48:49], v12 offset:57344\n\t"
  "v_mfma_f32_16x16x32_bf16 v[80:83], v[36:39], a[80:83], v[80:83]\n\t"
  "ds_read_b64_tr_b16 v[50:51], v12 offset:59392\n\t"
  "v_mfma_f32_16x16x32_bf16 v[84:87], v[36:39], a[84:87], v[84:87]\n\t"
  "s_cmp_eq_u32 s86, 1\n\ts_cselect_b32 s85, 0x3e942d87, s85\n\t"
  "v_mfma_f32_16x16x32_bf16 v[84:87], v[32:35], a[68:71], v[84:87]\n\t"
  "s_cmp_eq_u32 s86, 2\n\ts_cselect_b32 s85, 0x43e972d8, s85\n\t"
  "v_mfma_f32_16x16x32_bf16 v[88:91], v[32:35], a[72:75], v[88:91]\n\t"
  "ds_read_b64_tr_b16 v[52:53], v12 offset:61440\n\t"
  "v_mfma_f32_16x16x32_bf16 v[88:91], v[36:39], a[88:91], v[88:91]\n\t"
  "ds_read_b64_tr_b16 v[54:55], v12 offset:63488\n\t"
  "v_mfma_f32_16x16x32_bf16 v[92:95], v[36:39], a[92:95], v[92:95]\n\t"
  "s_cmp_eq_u32 s86, 3\n\ts_cselect_b32 s85, 0x943e872d, s85\n\t"
  "v_mfma_f32_16x16x32_bf16 v[92:95], v[32:35], a[76:79], v[92:95]\n\t"
  "s_mul_i32 s87, s97, 1966080\n\t"
  "s_waitcnt lgkmcnt(4)\n\t"
  "v_mfma_f32_16x16x32_bf16 v[144:147], v[40:43], a[64:67], v[144:147]\n\t"
  "ds_read_b64_tr_b16 v[32:33], v13 offset:57344\n\t"
  "s_mov_b64 s[40:41], %6\n\t"
  "v_mfma_f32_16x16x32_bf16 v[144:147], v[44:47], a[80:83], v[144:147]\n\t"
  "ds_read_b64_tr_b16 v[34:35], v13 offset:59392\n\t"
  "v_mfma_f32_16x16x32_bf16 v[148:151], v[44:47], a[84:87], v[148:151]\n\t"
  "buffer_store_dwordx4 v[216:219], v18, s[56:59], s79 offen offset:0 sc1\n\t"
  "v_mul_f32 v76, v25, v76\n\t"
  "v_mfma_f32_16x16x32_bf16 v[148:151], v[40:43], a[68:71], v[148:151]\n\t"
  "v_mul_f32 v77, v25, v77\n\t"
  "v_mul_f32 v78, v25, v78\n\t"
  "v_mfma_f32_16x16x32_bf16 v[152:155], v[40:43], a[72:75], v[152:155]\n\t"
  "ds_read_b64_tr_b16 v[36:37], v13 offset:61440\n\t"
  "v_mul_f32 v79, v25, v79\n\t"
  "v_mfma_f32_16x16x32_bf16 v[152:155], v[44:47], a[88:91], v[152:155]\n\t"
  "ds_read_b64_tr_b16 v[38:39], v13 offset:63488\n\t"
  "v_mul_f32 v108, v25, v108\n\t"
  "v_mfma_f32_16x16x32_bf16 v[156:159], v[44:47], a[92:95], v[156:159]\n\t"
  "v_mul_f32 v109, v25, v109\n\t"
  "v_mul_f32 v110, v25, v110\n\t"
  "v_mfma_f32_16x16x32_bf16 v[156:159], v[40:43], a[76:79], v[156:159]\n\t"
  "v_mul_f32 v111, v25, v111\n\t"
  "s_waitcnt lgkmcnt(4)\n\t"
  "v_mfma_f32_16x16x32_bf16 v[192:195], v[48:51], a[96:99], v[192:195]\n\t"
  "ds_read_b64_tr_b16 v[40:41], v14 offset:57344\n\t"
  "v_cvt_pk_bf16_f32 v220, v76, v77\n\t"
  "v_mfma_f32_16x16x32_bf16 v[192:195], v[52:55], a[112:115], v[192:195]\n\t"
  "ds_read_b64_tr_b16 v[42:43], v14 offset:59392\n\t"
  "v_cvt_pk_bf16_f32 v221, v78, v79\n\t"
  "v_mfma_f32_16x16x32_bf16 v[196:199], v[52:55], a[116:119], v[196:199]\n\t"
  "v_cvt_pk_bf16_f32 v222, v108, v109\n\t"
  "v_cvt_pk_bf16_f32 v223, v110, v111\n\t"
  "v_mfma_f32_16x16x32_bf16 v[196:199], v[48:51], a[100:103], v[196:199]\n\t"
  "s_add_u32 s40, s40, s87\n\ts_addc_u32 s41, s41, 0\n\t"
  "v_mfma_f32_16x16x32_bf16 v[200:203], v[48:51], a[104:107], v[200:203]\n\t"
  "ds_read_b64_tr_b16 v[44:45], v14 offset:61440\n\t"
  "s_mov_b32 s42, 1966080\n\t"
  "v_mfma_f32_16x16x32_bf16 v[200:203], v[52:55], a[120:123], v[200:203]\n\t"
  "ds_read_b64_tr_b16 v[46:47], v14 offset:63488\n\t"
  "s_mov_b32 s43, 0x20000\n\t"
  "v_mfma_f32_16x16x32_bf16 v[204:207], v[52:55], a[124:127], v[204:207]\n\t"
  "s_mov_b64 s[44:45], %8\n\t"
  "s_mov_b32 s46, 215040\n\t"
  "v_mfma_f32_16x16x32_bf16 v[204:207], v[48:51], a[108:111], v[204:207]\n\t"
  "s_mov_b32 s47, 0x20000\n\t"
  "s_waitcnt lgkmcnt(4)\n\t"
  "v_mfma_f32_16x16x32_bf16 v[224:227], v[32:35], a[96:99], v[224:227]\n\t"
  "ds_read_b64_tr_b16 v[48:49], v15 offset:57344\n\t"
  "s_add_u32 m0, s69, 98304\n\t"
  "v_mfma_f32_16x16x32_bf16 v[224:227], v[36:39], a[112:115], v[224:227]\n\t"
  "ds_read_b64_tr_b16 v[50:51], v15 offset:59392\n\t"
  "s_add_u32 s70, s78, 131072\n\t"
  "v_mfma_f32_16x16x32_bf16 v[228:231], v[36:39], a[116:119], v[228:231]\n\t"
  "v_lshrrev_b32 v59, 4, v20\n\t"
  "v_subrev_u32 v59, 0, v59\n\t"
  "v_mfma_f32_16x16x32_bf16 v[228:231], v[32:35], a[100:103], v[228:231]\n\t"
  "s_waitcnt vmcnt(27)\n\t"
  "s_barrier\n\t"
  "v_mfma_f32_16x16x32_bf16 v[232:235], v[32:35], a[104:107], v[232:235]\n\t"
  "ds_read_b64_tr_b16 v[52:53], v15 offset:61440\n\t"
  "v_mul_u32_u24 v62, 4682, v59\n\t"
  "v_mfma_f32_16x16x32_bf16 v[232:235], v[36:39], a[120:123], v[232:235]\n\t"
  "ds_read_b64_tr_b16 v[54:55], v15 offset:63488\n\t"
  "v_add_u32 v12, 0x10000, v12\n\t"
  "v_add_u32 v13, 0x10000, v13\n\t"
  "v_mfma_f32_16x16x32_bf16 v[236:239], v[36:39], a[124:127], v[236:239]\n\t"
  "buffer_load_dwordx4 v4, s[52:55], s70 offen offset:0 nt lds\n\t"
  "v_add_u32 v14, 0x10000, v14\n\t"
  "v_mfma_f32_16x16x32_bf16 v[236:239], v[32:35], a[108:111], v[236:239]\n\t"
  "v_add_u32 v15, 0x10000, v15\n\t"
  "s_waitcnt lgkmcnt(4)\n\t"
  "v_mfma_f32_16x16x32_bf16 v[80:83], v[40:43], a[96:99], v[80:83]\n\t"
  "ds_read_b64_tr_b16 v[32:33], v12 offset:0\n\t"
  "v_lshrrev_b32 v62, 16, v62\n\t"
  "v_mfma_f32_16x16x32_bf16 v[80:83], v[44:47], a[112:115], v[80:83]\n\t"
  "ds_read_b64_tr_b16 v[34:35], v12 offset:2048\n\t"
  "v_mul_u32_u24 v63, 672, v62\n\t"
  "v_mfma_f32_16x16x32_bf16 v[84:87], v[44:47], a[116:119], v[84:87]\n\t"
  "ds_read_b128 v[0:3], v27\n\t"
  "v_lshl_add_u32 v63, v59, 4, v63\n\t"
  "v_mfma_f32_16x16x32_bf16 v[84:87], v[40:43], a[100:103], v[84:87]\n\t"
  "buffer_load_dwordx4 v5, s[52:55], s70 offen offset:1024 nt lds\n\t"
  "v_cmp_lt_u32 vcc, 55, v59\n\t"
  "v_mfma_f32_16x16x32_bf16 v[88:91], v[40:43], a[104:107], v[88:91]\n\t"
  "ds_read_b64_tr_b16 v[36:37], v12 offset:4096\n\t"
  "v_mov_b32 v62, 0x40000000\n\t"
  "v_mfma_f32_16x16x32_bf16 v[88:91], v[44:47], a[120:123], v[88:91]\n\t"
  "ds_read_b64_tr_b16 v[38:39], v12 offset:6144\n\t"
  "v_cndmask_b32 v63, v63, v62, vcc\n\t"
  "v_mfma_f32_16x16x32_bf16 v[92:95], v[44:47], a[124:127], v[92:95]\n\t"
  "buffer_store_dwordx4 v[220:223], v19, s[56:59], s79 offen offset:0 sc1\n\t"
  "v_mul_f32 v128, v22, v128\n\t"
  "v_mfma_f32_16x16x32_bf16 v[92:95], v[40:43], a[108:111], v[92:95]\n\t"
  "v_mul_f32 v129, v22, v129\n\t"
  "s_waitcnt lgkmcnt(5)\n\t"
  "v_mfma_f32_16x16x32_bf16 v[144:147], v[48:51], a[96:99], v[144:147]\n\t"
  "ds_read_b64_tr_b16 v[40:41], v13 offset:0\n\t"
  "v_mul_f32 v130, v22, v130\n\t"
  "v_mfma_f32_16x16x32_bf16 v[144:147], v[52:55], a[112:115], v[144:147]\n\t"
  "ds_read_b64_tr_b16 v[42:43], v13 offset:2048\n\t"
  "v_mul_f32 v131, v22, v131\n\t"
  "v_mfma_f32_16x16x32_bf16 v[148:151], v[52:55], a[116:119], v[148:151]\n\t"
  "buffer_load_dwordx4 v60, s[52:55], s70 offen offset:2048 nt lds\n\t"
  "v_mul_f32 v160, v22, v160\n\t"
  "v_mfma_f32_16x16x32_bf16 v[148:151], v[48:51], a[100:103], v[148:151]\n\t"
  "v_mul_f32 v161, v22, v161\n\t"
  "v_mul_f32 v162, v22, v162\n\t"
  "v_mfma_f32_16x16x32_bf16 v[152:155], v[48:51], a[104:107], v[152:155]\n\t"
  "ds_read_b64_tr_b16 v[44:45], v13 offset:4096\n\t"
  "s_waitcnt lgkmcnt(5)\n\t"
  "v_or3_b32 v0, v0, v1, v2\n\t"
  "v_mfma_f32_16x16x32_bf16 v[152:155], v[52:55], a[120:123], v[152:155]\n\t"
  "ds_read_b64_tr_b16 v[46:47], v13 offset:6144\n\t"
  "v_or_b32 v0, v0, v3\n\t"
  "v_mfma_f32_16x16x32_bf16 v[156:159], v[52:55], a[124:127], v[156:159]\n\t"
  "v_readfirstlane_b32 s98, v0\n\t"
  "v_mul_f32 v163, v22, v163\n\t"
  "v_mfma_f32_16x16x32_bf16 v[156:159], v[48:51], a[108:111], v[156:159]\n\t"
  "buffer_load_dwordx4 v61, s[52:55], s70 offen offset:3072 nt lds\n\t"
  "s_waitcnt lgkmcnt(4)\n\t"
  "v_mfma_f32_16x16x32_bf16 v[192:195], v[32:35], a[128:131], v[192:195]\n\t"
  "ds_read_b64_tr_b16 v[48:49], v14 offset:0\n\t"
  "v_cvt_pk_bf16_f32 v240, v128, v129\n\t"
  "v_mfma_f32_16x16x32_bf16 v[192:195], v[36:39], a[144:147], v[192:195]\n\t"
  "ds_read_b64_tr_b16 v[50:51], v14 offset:2048\n\t"
  "v_cvt_pk_bf16_f32 v241, v130, v131\n\t"
  "v_mfma_f32_16x16x32_bf16 v[196:199], v[36:39], a[148:151], v[196:199]\n\t"
  "s_cmp_lg_u32 s98, 0\n\ts_cbranch_scc1 L_k1_exit\n\t"
  "v_mfma_f32_16x16x32_bf16 v[196:199], v[32:35], a[132:135], v[196:199]\n\t"
  "v_cvt_pk_bf16_f32 v242, v160, v161\n\t"
  "v_cvt_pk_bf16_f32 v243, v162, v163\n\t"
  "v_mfma_f32_16x16x32_bf16 v[200:203], v[32:35], a[136:139], v[200:203]\n\t"
  "ds_read_b64_tr_b16 v[52:53], v14 offset:4096\n\t"
  "s_mul_i32 s87, s90, 14336\n\t"
  "v_mfma_f32_16x16x32_bf16 v[200:203], v[36:39], a[152:155], v[200:203]\n\t"
  "ds_read_b64_tr_b16 v[54:55], v14 offset:6144\n\t"
  "v_add_u32 v63, s87, v63\n\t"
  "v_mfma_f32_16x16x32_bf16 v[204:207], v[36:39], a[156:159], v[204:207]\n\t"
  "v_add_u32 v6, 0, v63\n\t"
  "v_add_u32 v8, 5120, v63\n\t"
  "v_mfma_f32_16x16x32_bf16 v[204:207], v[32:35], a[140:143], v[204:207]\n\t"
  "v_lshrrev_b32 v59, 4, v20\n\t"
  "s_waitcnt lgkmcnt(4)\n\t"
  "v_mfma_f32_16x16x32_bf16 v[224:227], v[40:43], a[128:131], v[224:227]\n\t"
  "ds_read_b64_tr_b16 v[32:33], v15 offset:0\n\t"
  "v_subrev_u32 v59, 8, v59\n\t"
  "v_mfma_f32_16x16x32_bf16 v[224:227], v[44:47], a[144:147], v[224:227]\n\t"
  "ds_read_b64_tr_b16 v[34:35], v15 offset:2048\n\t"
  "v_mul_u32_u24 v62, 4682, v59\n\t"
  "v_mfma_f32_16x16x32_bf16 v[228:231], v[44:47], a[148:151], v[228:231]\n\t"
  "v_lshrrev_b32 v62, 16, v62\n\t"
  "v_mul_u32_u24 v63, 672, v62\n\t"
  "v_mfma_f32_16x16x32_bf16 v[228:231], v[40:43], a[132:135], v[228:231]\n\t"
  "v_lshl_add_u32 v63, v59, 4, v63\n\t"
  "v_cmp_lt_u32 vcc, 55, v59\n\t"
  "v_mfma_f32_16x16x32_bf16 v[232:235], v[40:43], a[136:139], v[232:235]\n\t"
  "ds_read_b64_tr_b16 v[36:37], v15 offset:4096\n\t"
  "v_mov_b32 v62, 0x40000000\n\t"
  "v_mfma_f32_16x16x32_bf16 v[232:235], v[44:47], a[152:155], v[232:235]\n\t"
  "ds_read_b64_tr_b16 v[38:39], v15 offset:6144\n\t"
  "v_cndmask_b32 v63, v63, v62, vcc\n\t"
  "v_mfma_f32_16x16x32_bf16 v[236:239], v[44:47], a[156:159], v[236:239]\n\t"
  "s_mul_i32 s87, s90, 14336\n\t"
  "v_add_u32 v63, s87, v63\n\t"
  "v_mfma_f32_16x16x32_bf16 v[236:239], v[40:43], a[140:143], v[236:239]\n\t"
  "v_add_u32 v7, 2560, v63\n\t"
  "s_waitcnt lgkmcnt(4)\n\t"
  "v_mfma_f32_16x16x32_bf16 v[80:83], v[48:51], a[128:131], v[80:83]\n\t"
  "ds_read_b64_tr_b16 v[40:41], v12 offset:8192\n\t"
  "v_add_u32 v9, 7680, v63\n\t"
  "v_mfma_f32_16x16x32_bf16 v[80:83], v[52:55], a[144:147], v[80:83]\n\t"
  "ds_read_b64_tr_b16 v[42:43], v12 offset:10240\n\t"
  "v_lshrrev_b32 v59, 8, v20\n\t"
  "v_mfma_f32_16x16x32_bf16 v[84:87], v[52:55], a[148:151], v[84:87]\n\t"
  "buffer_store_dwordx4 v[240:243], v16, s[56:59], s79 offen offset:1024 sc1\n\t"
  "v_mul_f32 v132, v23, v132\n\t"
  "v_mfma_f32_16x16x32_bf16 v[84:87], v[48:51], a[132:135], v[84:87]\n\t"
  "v_mul_f32 v133, v23, v133\n\t"
  "v_mul_f32 v134, v23, v134\n\t"
  "v_mfma_f32_16x16x32_bf16 v[88:91], v[48:51], a[136:139], v[88:91]\n\t"
  "ds_read_b64_tr_b16 v[44:45], v12 offset:12288\n\t"
  "v_mul_f32 v135, v23, v135\n\t"
  "v_mfma_f32_16x16x32_bf16 v[88:91], v[52:55], a[152:155], v[88:91]\n\t"
  "ds_read_b64_tr_b16 v[46:47], v12 offset:14336\n\t"
  "v_mul_f32 v164, v23, v164\n\t"
  "v_mfma_f32_16x16x32_bf16 v[92:95], v[52:55], a[156:159], v[92:95]\n\t"
  "v_mul_f32 v165, v23, v165\n\t"
  "v_mul_f32 v166, v23, v166\n\t"
  "v_mfma_f32_16x16x32_bf16 v[92:95], v[48:51], a[140:143], v[92:95]\n\t"
  "v_mul_f32 v167, v23, v167\n\t"
  "s_waitcnt lgkmcnt(4)\n\t"
  "v_mfma_f32_16x16x32_bf16 v[144:147], v[32:35], a[128:131], v[144:147]\n\t"
  "ds_read_b64_tr_b16 v[48:49], v13 offset:8192\n\t"
  "v_cvt_pk_bf16_f32 v244, v132, v133\n\t"
  "v_mfma_f32_16x16x32_bf16 v[144:147], v[36:39], a[144:147], v[144:147]\n\t"
  "ds_read_b64_tr_b16 v[50:51], v13 offset:10240\n\t"
  "v_cvt_pk_bf16_f32 v245, v134, v135\n\t"
  "v_mfma_f32_16x16x32_bf16 v[148:151], v[36:39], a[148:151], v[148:151]\n\t"
  "v_cvt_pk_bf16_f32 v246, v164, v165\n\t"
  "v_cvt_pk_bf16_f32 v247, v166, v167\n\t"
  "v_mfma_f32_16x16x32_bf16 v[148:151], v[32:35], a[132:135], v[148:151]\n\t"
  "v_lshlrev_b32 v62, 10, v59\n\t"
  "v_and_b32 v59, 1, v59\n\t"
  "v_mfma_f32_16x16x32_bf16 v[152:155], v[32:35], a[136:139], v[152:155]\n\t"
  "ds_read_b64_tr_b16 v[52:53], v13 offset:12288\n\t"
  "v_lshl_add_u32 v62, v59, 7, v62\n\t"
  "v_mfma_f32_16x16x32_bf16 v[152:155], v[36:39], a[152:155], v[152:155]\n\t"
  "ds_read_b64_tr_b16 v[54:55], v13 offset:14336\n\t"
  "v_bfe_u32 v59, v20, 6, 2\n\t"
  "v_mfma_f32_16x16x32_bf16 v[156:159], v[36:39], a[156:159], v[156:159]\n\t"
  "v_mul_u32_u24 v59, 224, v59\n\t"
  "v_add_u32 v62, v59, v62\n\t"
  "v_mfma_f32_16x16x32_bf16 v[156:159], v[32:35], a[140:143], v[156:159]\n\t"
  "v_bfe_u32 v59, v20, 4, 2\n\t"
  "s_waitcnt lgkmcnt(4)\n\t"
  "v_mfma_f32_16x16x32_bf16 v[192:195], v[40:43], a[160:163], v[192:195]\n\t"
  "ds_read_b64_tr_b16 v[32:33], v14 offset:8192\n\t"
  "v_lshl_add_u32 v62, v59, 3, v62\n\t"
  "v_mfma_f32_16x16x32_bf16 v[192:195], v[44:47], a[176:179], v[192:195]\n\t"
  "ds_read_b64_tr_b16 v[34:35], v14 offset:10240\n\t"
  "v_add_u32 v28, s89, v62\n\t"
  "v_mfma_f32_16x16x32_bf16 v[196:199], v[44:47], a[180:183], v[196:199]\n\t"
  "v_add_u32 v29, 0x10000, v28\n\t"
  "v_add_u32 v58, 0x20000, v28\n\t"
  "v_mfma_f32_16x16x32_bf16 v[196:199], v[40:43], a[164:167], v[196:199]\n\t"
  "s_lshr_b32 s86, s96, 2\n\t"
  "s_add_u32 s87, s86, 0\n\t"
  "v_mfma_f32_16x16x32_bf16 v[200:203], v[40:43], a[168:171], v[200:203]\n\t"
  "ds_read_b64_tr_b16 v[36:37], v14 offset:12288\n\t"
  "s_and_b32 s87, s87, 3\n\t"
  "v_mfma_f32_16x16x32_bf16 v[200:203], v[44:47], a[184:187], v[200:203]\n\t"
  "ds_read_b64_tr_b16 v[38:39], v14 offset:14336\n\t"
  "s_lshl_b32 s87, s87, 17\n\t"
  "v_mfma_f32_16x16x32_bf16 v[204:207], v[44:47], a[188:191], v[204:207]\n\t"
  "s_lshl_b32 s86, s86, 15\n\t"
  "s_add_u32 s87, s87, s86\n\t"
  "v_mfma_f32_16x16x32_bf16 v[204:207], v[40:43], a[172:175], v[204:207]\n\t"
  "s_lshl_b32 s86, s90, 19\n\t"
  "s_waitcnt lgkmcnt(4)\n\t"
  "v_mfma_f32_16x16x32_bf16 v[224:227], v[48:51], a[160:163], v[224:227]\n\t"
  "ds_read_b64_tr_b16 v[40:41], v15 offset:8192\n\t"
  "s_add_u32 m0, s69, 16384\n\t"
  "v_mfma_f32_16x16x32_bf16 v[224:227], v[52:55], a[176:179], v[224:227]\n\t"
  "ds_read_b64_tr_b16 v[42:43], v15 offset:10240\n\t"
  "s_add_u32 s70, s78, 65536\n\t"
  "v_mfma_f32_16x16x32_bf16 v[228:231], v[52:55], a[180:183], v[228:231]\n\t"
  "s_add_u32 s87, s87, s86\n\t"
  "v_add_u32 v10, s87, v20\n\t"
  "v_mfma_f32_16x16x32_bf16 v[228:231], v[48:51], a[164:167], v[228:231]\n\t"
  "s_waitcnt vmcnt(25)\n\t"
  "s_barrier\n\t"
  "v_mfma_f32_16x16x32_bf16 v[232:235], v[48:51], a[168:171], v[232:235]\n\t"
  "ds_read_b64_tr_b16 v[44:45], v15 offset:12288\n\t"
  "s_lshr_b32 s86, s96, 2\n\t"
  "v_mfma_f32_16x16x32_bf16 v[232:235], v[52:55], a[184:187], v[232:235]\n\t"
  "ds_read_b64_tr_b16 v[46:47], v15 offset:14336\n\t"
  "s_add_u32 s87, s86, 1\n\t"
  "v_mfma_f32_16x16x32_bf16 v[236:239], v[52:55], a[188:191], v[236:239]\n\t"
  "buffer_load_dwordx4 v4, s[52:55], s70 offen offset:0 nt lds\n\t"
  "s_and_b32 s87, s87, 3\n\t"
  "v_mfma_f32_16x16x32_bf16 v[236:239], v[48:51], a[172:175], v[236:239]\n\t"
  "buffer_store_dwordx4 v[244:247], v17, s[56:59], s79 offen offset:1024 sc1\n\t"
  "s_waitcnt lgkmcnt(4)\n\t"
  "v_mfma_f32_16x16x32_bf16 v[80:83], v[32:35], a[160:163], v[80:83]\n\t"
  "ds_read_b64_tr_b16 v[48:49], v12 offset:16384\n\t"
  "v_mul_f32 v136, v24, v136\n\t"
  "v_mfma_f32_16x16x32_bf16 v[80:83], v[36:39], a[176:179], v[80:83]\n\t"
  "ds_read_b64_tr_b16 v[50:51], v12 offset:18432\n\t"
  "v_mul_f32 v137, v24, v137\n\t"
  "v_mfma_f32_16x16x32_bf16 v[84:87], v[36:39], a[180:183], v[84:87]\n\t"
  "v_mul_f32 v138, v24, v138\n\t"
  "v_mul_f32 v139, v24, v139\n\t"
  "v_mfma_f32_16x16x32_bf16 v[84:87], v[32:35], a[164:167], v[84:87]\n\t"
  "buffer_load_dwordx4 v5, s[52:55], s70 offen offset:1024 nt lds\n\t"
  "v_mul_f32 v168, v24, v168\n\t"
  "v_mfma_f32_16x16x32_bf16 v[88:91], v[32:35], a[168:171], v[88:91]\n\t"
  "ds_read_b64_tr_b16 v[52:53], v12 offset:20480\n\t"
  "v_mul_f32 v169, v24, v169\n\t"
  "v_mfma_f32_16x16x32_bf16 v[88:91], v[36:39], a[184:187], v[88:91]\n\t"
  "ds_read_b64_tr_b16 v[54:55], v12 offset:22528\n\t"
  "v_mul_f32 v170, v24, v170\n\t"
  "v_mfma_f32_16x16x32_bf16 v[92:95], v[36:39], a[188:191], v[92:95]\n\t"
  "v_mul_f32 v171, v24, v171\n\t"
  "v_cvt_pk_bf16_f32 v248, v136, v137\n\t"
  "v_mfma_f32_16x16x32_bf16 v[92:95], v[32:35], a[172:175], v[92:95]\n\t"
  "v_cvt_pk_bf16_f32 v249, v138, v139\n\t"
  "s_waitcnt lgkmcnt(4)\n\t"
  "v_mfma_f32_16x16x32_bf16 v[144:147], v[40:43], a[160:163], v[144:147]\n\t"
  "ds_read_b64_tr_b16 v[32:33], v13 offset:16384\n\t"
  "v_cvt_pk_bf16_f32 v250, v168, v169\n\t"
  "v_mfma_f32_16x16x32_bf16 v[144:147], v[44:47], a[176:179], v[144:147]\n\t"
  "ds_read_b64_tr_b16 v[34:35], v13 offset:18432\n\t"
  "v_cvt_pk_bf16_f32 v251, v170, v171\n\t"
  "v_mfma_f32_16x16x32_bf16 v[148:151], v[44:47], a[180:183], v[148:151]\n\t"
  "buffer_load_dwordx4 v60, s[52:55], s70 offen offset:2048 nt lds\n\t"
  "s_lshl_b32 s87, s87, 17\n\t"
  "v_mfma_f32_16x16x32_bf16 v[148:151], v[40:43], a[164:167], v[148:151]\n\t"
  "s_lshl_b32 s86, s86, 15\n\t"
  "s_add_u32 s87, s87, s86\n\t"
  "v_mfma_f32_16x16x32_bf16 v[152:155], v[40:43], a[168:171], v[152:155]\n\t"
  "ds_read_b64_tr_b16 v[36:37], v13 offset:20480\n\t"
  "s_lshl_b32 s86, s90, 19\n\t"
  "v_mfma_f32_16x16x32_bf16 v[152:155], v[44:47], a[184:187], v[152:155]\n\t"
  "ds_read_b64_tr_b16 v[38:39], v13 offset:22528\n\t"
  "s_add_u32 s87, s87, s86\n\t"
  "v_mfma_f32_16x16x32_bf16 v[156:159], v[44:47], a[188:191], v[156:159]\n\t"
  "v_add_u32 v11, s87, v20\n\t"
  "s_lshr_b32 s86, s96, 2\n\t"
  "v_mfma_f32_16x16x32_bf16 v[156:159], v[40:43], a[172:175], v[156:159]\n\t"
  "buffer_load_dwordx4 v61, s[52:55], s70 offen offset:3072 nt lds\n\t"
  "s_waitcnt lgkmcnt(4)\n\t"
  "v_mfma_f32_16x16x32_bf16 v[192:195], v[48:51], a[192:195], v[192:195]\n\t"
  "ds_read_b64_tr_b16 v[40:41], v14 offset:16384\n\t"
  "s_add_u32 m0, s69, 114688\n\t"
  "v_mfma_f32_16x16x32_bf16 v[192:195], v[52:55], a[208:211], v[192:195]\n\t"
  "ds_read_b64_tr_b16 v[42:43], v14 offset:18432\n\t"
  "s_add_u32 s70, s78, 196608\n\t"
  "v_mfma_f32_16x16x32_bf16 v[196:199], v[52:55], a[212:215], v[196:199]\n\t"
  "s_add_u32 s87, s86, 2\n\t"
  "s_and_b32 s87, s87, 3\n\t"
  "v_mfma_f32_16x16x32_bf16 v[196:199], v[48:51], a[196:199], v[196:199]\n\t"
  "s_lshl_b32 s87, s87, 17\n\t"
  "s_lshl_b32 s86, s86, 15\n\t"
  "v_mfma_f32_16x16x32_bf16 v[200:203], v[48:51], a[200:203], v[200:203]\n\t"
  "ds_read_b64_tr_b16 v[44:45], v14 offset:20480\n\t"
  "s_add_u32 s87, s87, s86\n\t"
  "v_mfma_f32_16x16x32_bf16 v[200:203], v[52:55], a[216:219], v[200:203]\n\t"
  "ds_read_b64_tr_b16 v[46:47], v14 offset:22528\n\t"
  "s_lshl_b32 s86, s90, 19\n\t"
  "v_mfma_f32_16x16x32_bf16 v[204:207], v[52:55], a[220:223], v[204:207]\n\t"
  "buffer_load_dwordx4 v4, s[52:55], s70 offen offset:0 nt lds\n\t"
  "s_add_u32 s87, s87, s86\n\t"
  "v_mfma_f32_16x16x32_bf16 v[204:207], v[48:51], a[204:207], v[204:207]\n\t"
  "v_add_u32 v56, s87, v20\n\t"
  "s_waitcnt lgkmcnt(4)\n\t"
  "v_mfma_f32_16x16x32_bf16 v[224:227], v[32:35], a[192:195], v[224:227]\n\t"
  "ds_read_b64_tr_b16 v[48:49], v15 offset:16384\n\t"
  "s_lshr_b32 s86, s96, 2\n\t"
  "v_mfma_f32_16x16x32_bf16 v[224:227], v[36:39], a[208:211], v[224:227]\n\t"
  "ds_read_b64_tr_b16 v[50:51], v15 offset:18432\n\t"
  "s_add_u32 s87, s86, 3\n\t"
  "v_mfma_f32_16x16x32_bf16 v[228:231], v[36:39], a[212:215], v[228:231]\n\t"
  "buffer_store_dwordx4 v[248:251], v18, s[56:59], s79 offen offset:1024 sc1\n\t"
  "v_mul_f32 v140, v25, v140\n\t"
  "v_mfma_f32_16x16x32_bf16 v[228:231], v[32:35], a[196:199], v[228:231]\n\t"
  "buffer_load_dwordx4 v5, s[52:55], s70 offen offset:1024 nt lds\n\t"
  "v_mul_f32 v141, v25, v141\n\t"
  "v_mfma_f32_16x16x32_bf16 v[232:235], v[32:35], a[200:203], v[232:235]\n\t"
  "ds_read_b64_tr_b16 v[52:53], v15 offset:20480\n\t"
  "v_mul_f32 v142, v25, v142\n\t"
  "v_mfma_f32_16x16x32_bf16 v[232:235], v[36:39], a[216:219], v[232:235]\n\t"
  "ds_read_b64_tr_b16 v[54:55], v15 offset:22528\n\t"
  "v_mul_f32 v143, v25, v143\n\t"
  "v_mfma_f32_16x16x32_bf16 v[236:239], v[36:39], a[220:223], v[236:239]\n\t"
  "v_mul_f32 v172, v25, v172\n\t"
  "v_mul_f32 v173, v25, v173\n\t"
  "v_mfma_f32_16x16x32_bf16 v[236:239], v[32:35], a[204:207], v[236:239]\n\t"
  "v_mul_f32 v174, v25, v174\n\t"
  "s_waitcnt lgkmcnt(4)\n\t"
  "v_mfma_f32_16x16x32_bf16 v[80:83], v[40:43], a[192:195], v[80:83]\n\t"
  "ds_read_b64_tr_b16 v[32:33], v12 offset:24576\n\t"
  "v_mul_f32 v175, v25, v175\n\t"
  "v_mfma_f32_16x16x32_bf16 v[80:83], v[44:47], a[208:211], v[80:83]\n\t"
  "ds_read_b64_tr_b16 v[34:35], v12 offset:26624\n\t"
  "v_cvt_pk_bf16_f32 v252, v140, v141\n\t"
  "v_mfma_f32_16x16x32_bf16 v[84:87], v[44:47], a[212:215], v[84:87]\n\t"
  "buffer_load_dwordx4 v60, s[52:55], s70 offen offset:2048 nt lds\n\t"
  "v_cvt_pk_bf16_f32 v253, v142, v143\n\t"
  "v_mfma_f32_16x16x32_bf16 v[84:87], v[40:43], a[196:199], v[84:87]\n\t"
  "v_cvt_pk_bf16_f32 v254, v172, v173\n\t"
  "v_cvt_pk_bf16_f32 v255, v174, v175\n\t"
  "v_mfma_f32_16x16x32_bf16 v[88:91], v[40:43], a[200:203], v[88:91]\n\t"
  "ds_read_b64_tr_b16 v[36:37], v12 offset:28672\n\t"
  "s_and_b32 s87, s87, 3\n\t"
  "v_mfma_f32_16x16x32_bf16 v[88:91], v[44:47], a[216:219], v[88:91]\n\t"
  "ds_read_b64_tr_b16 v[38:39], v12 offset:30720\n\t"
  "s_lshl_b32 s87, s87, 17\n\t"
  "v_mfma_f32_16x16x32_bf16 v[92:95], v[44:47], a[220:223], v[92:95]\n\t"
  "s_lshl_b32 s86, s86, 15\n\t"
  "s_add_u32 s87, s87, s86\n\t"
  "v_mfma_f32_16x16x32_bf16 v[92:95], v[40:43], a[204:207], v[92:95]\n\t"
  "buffer_load_dwordx4 v61, s[52:55], s70 offen offset:3072 nt lds\n\t"
  "s_waitcnt lgkmcnt(4)\n\t"
  "v_mfma_f32_16x16x32_bf16 v[144:147], v[48:51], a[192:195], v[144:147]\n\t"
  "ds_read_b64_tr_b16 v[40:41], v13 offset:24576\n\t"
  "s_lshl_b32 s86, s90, 19\n\t"
  "v_mfma_f32_16x16x32_bf16 v[144:147], v[52:55], a[208:211], v[144:147]\n\t"
  "ds_read_b64_tr_b16 v[42:43], v13 offset:26624\n\t"
  "s_add_u32 s87, s87, s86\n\t"
  "v_mfma_f32_16x16x32_bf16 v[148:151], v[52:55], a[212:215], v[148:151]\n\t"
  "v_add_u32 v57, s87, v20\n\t"
  "s_lshl_b32 s87, s97, 2\n\t"
  "v_mfma_f32_16x16x32_bf16 v[148:151], v[48:51], a[196:199], v[148:151]\n\t"
  "s_add_u32 s87, s87, s90\n\t"
  "s_lshl_b32 s87, s87, 4\n\t"
  "v_mfma_f32_16x16x32_bf16 v[152:155], v[48:51], a[200:203], v[152:155]\n\t"
  "ds_read_b64_tr_b16 v[44:45], v13 offset:28672\n\t"
  "s_add_u32 s86, s87, s96\n\t"
  "v_mfma_f32_16x16x32_bf16 v[152:155], v[52:55], a[216:219], v[152:155]\n\t"
  "ds_read_b64_tr_b16 v[46:47], v13 offset:30720\n\t"
  "s_lshl_b32 s86, s86, 2\n\t"
  "v_mfma_f32_16x16x32_bf16 v[156:159], v[52:55], a[220:223], v[156:159]\n\t"
  "v_lshlrev_b32 v21, 12, v20\n\t"
  "v_add_u32 v21, s86, v21\n\t"
  "v_mfma_f32_16x16x32_bf16 v[156:159], v[48:51], a[204:207], v[156:159]\n\t"
  "s_and_b32 s86, s86, 0xfffffff0\n\t"
  "s_waitcnt lgkmcnt(4)\n\t"
  "v_mfma_f32_16x16x32_bf16 v[192:195], v[32:35], a[224:227], v[192:195]\n\t"
  "ds_read_b64_tr_b16 v[48:49], v14 offset:24576\n\t"
  "v_bfe_u32 v30, v20, 4, 2\n\t"
  "v_mfma_f32_16x16x32_bf16 v[192:195], v[36:39], a[240:243], v[192:195]\n\t"
  "ds_read_b64_tr_b16 v[50:51], v14 offset:26624\n\t"
  "v_lshlrev_b32 v30, 2, v30\n\t"
  "v_mfma_f32_16x16x32_bf16 v[196:199], v[36:39], a[244:247], v[196:199]\n\t"
  "v_add_u32 v30, s86, v30\n\t"
  "v_mfma_f32_16x16x32_bf16 v[196:199], v[32:35], a[228:231], v[196:199]\n\t"
  "v_mfma_f32_16x16x32_bf16 v[200:203], v[32:35], a[232:235], v[200:203]\n\t"
  "ds_read_b64_tr_b16 v[52:53], v14 offset:28672\n\t"
  "v_mfma_f32_16x16x32_bf16 v[200:203], v[36:39], a[248:251], v[200:203]\n\t"
  "ds_read_b64_tr_b16 v[54:55], v14 offset:30720\n\t"
  "v_mfma_f32_16x16x32_bf16 v[204:207], v[36:39], a[252:255], v[204:207]\n\t"
  "buffer_store_dwordx4 v[252:255], v19, s[56:59], s79 offen offset:1024 sc1\n\t"
  "v_mfma_f32_16x16x32_bf16 v[204:207], v[32:35], a[236:239], v[204:207]\n\t"
  "s_waitcnt lgkmcnt(4)\n\t"
  "v_mfma_f32_16x16x32_bf16 v[224:227], v[40:43], a[224:227], v[224:227]\n\t"
  "ds_read_b64_tr_b16 v[32:33], v15 offset:24576\n\t"
  "v_mfma_f32_16x16x32_bf16 v[224:227], v[44:47], a[240:243], v[224:227]\n\t"
  "ds_read_b64_tr_b16 v[34:35], v15 offset:26624\n\t"
  "v_mfma_f32_16x16x32_bf16 v[228:231], v[44:47], a[244:247], v[228:231]\n\t"
  "v_mfma_f32_16x16x32_bf16 v[228:231], v[40:43], a[228:231], v[228:231]\n\t"
  "v_mfma_f32_16x16x32_bf16 v[232:235], v[40:43], a[232:235], v[232:235]\n\t"
  "ds_read_b64_tr_b16 v[36:37], v15 offset:28672\n\t"
  "v_mfma_f32_16x16x32_bf16 v[232:235], v[44:47], a[248:251], v[232:235]\n\t"
  "ds_read_b64_tr_b16 v[38:39], v15 offset:30720\n\t"
  "v_add_u32 v12, 0xffff0000, v12\n\t"
  "v_mfma_f32_16x16x32_bf16 v[236:239], v[44:47], a[252:255], v[236:239]\n\t"
  "v_add_u32 v13, 0xffff0000, v13\n\t"
  "v_add_u32 v14, 0xffff0000, v14\n\t"
  "v_mfma_f32_16x16x32_bf16 v[236:239], v[40:43], a[236:239], v[236:239]\n\t"
  "v_add_u32 v15, 0xffff0000, v15\n\t"
  "s_waitcnt lgkmcnt(4)\n\t"
  "v_mfma_f32_16x16x32_bf16 v[80:83], v[48:51], a[224:227], v[80:83]\n\t"
  "ds_read_b64_tr_b16 v[40:41], v12 offset:0\n\t"
  "v_mfma_f32_16x16x32_bf16 v[80:83], v[52:55], a[240:243], v[80:83]\n\t"
  "ds_read_b64_tr_b16 v[42:43], v12 offset:2048\n\t"
  "v_mfma_f32_16x16x32_bf16 v[84:87], v[52:55], a[244:247], v[84:87]\n\t"
  "v_mfma_f32_16x16x32_bf16 v[84:87], v[48:51], a[228:231], v[84:87]\n\t"
  "v_mfma_f32_16x16x32_bf16 v[88:91], v[48:51], a[232:235], v[88:91]\n\t"
  "ds_read_b64_tr_b16 v[44:45], v12 offset:4096\n\t"
  "v_mfma_f32_16x16x32_bf16 v[88:91], v[52:55], a[248:251], v[88:91]\n\t"
  "ds_read_b64_tr_b16 v[46:47], v12 offset:6144\n\t"
  "v_mfma_f32_16x16x32_bf16 v[92:95], v[52:55], a[252:255], v[92:95]\n\t"
  "v_mfma_f32_16x16x32_bf16 v[92:95], v[48:51], a[236:239], v[92:95]\n\t"
  "s_waitcnt lgkmcnt(4)\n\t"
  "v_mfma_f32_16x16x32_bf16 v[144:147], v[32:35], a[224:227], v[144:147]\n\t"
  "ds_read_b64_tr_b16 v[48:49], v13 offset:0\n\t"
  "v_mfma_f32_16x16x32_bf16 v[144:147], v[36:39], a[240:243], v[144:147]\n\t"
  "ds_read_b64_tr_b16 v[50:51], v13 offset:2048\n\t"
  "v_mfma_f32_16x16x32_bf16 v[148:151], v[36:39], a[244:247], v[148:151]\n\t"
  "v_mfma_f32_16x16x32_bf16 v[148:151], v[32:35], a[228:231], v[148:151]\n\t"
  "v_mfma_f32_16x16x32_bf16 v[152:155], v[32:35], a[232:235], v[152:155]\n\t"
  "ds_read_b64_tr_b16 v[52:53], v13 offset:4096\n\t"
  "v_mfma_f32_16x16x32_bf16 v[152:155], v[36:39], a[248:251], v[152:155]\n\t"
  "ds_read_b64_tr_b16 v[54:55], v13 offset:6144\n\t"
  "v_mfma_f32_16x16x32_bf16 v[156:159], v[36:39], a[252:255], v[156:159]\n\t"
  "v_mfma_f32_16x16x32_bf16 v[156:159], v[32:35], a[236:239], v[156:159]\n\t"
  "s_waitcnt lgkmcnt(4)\n\t"
  "v_mfma_f32_16x16x32_bf16 v[64:67], v[40:43], a[0:3], 0\n\t"
  "ds_read_b64_tr_b16 v[32:33], v14 offset:0\n\t"
  "v_mfma_f32_16x16x32_bf16 v[64:67], v[44:47], a[16:19], v[64:67]\n\t"
  "ds_read_b64_tr_b16 v[34:35], v14 offset:2048\n\t"
  "v_mfma_f32_16x16x32_bf16 v[68:71], v[44:47], a[20:23], 0\n\t"
  "v_mfma_f32_16x16x32_bf16 v[68:71], v[40:43], a[4:7], v[68:71]\n\t"
  "v_mul_f32 v192, v22, v192\n\t"
  "v_mul_f32 v193, v22, v193\n\t"
  "v_mfma_f32_16x16x32_bf16 v[72:75], v[40:43], a[8:11], 0\n\t"
  "ds_read_b64_tr_b16 v[36:37], v14 offset:4096\n\t"
  "v_mul_f32 v194, v22, v194\n\t"
  "v_mfma_f32_16x16x32_bf16 v[72:75], v[44:47], a[24:27], v[72:75]\n\t"
  "ds_read_b64_tr_b16 v[38:39], v14 offset:6144\n\t"
  "v_mul_f32 v195, v22, v195\n\t"
  "v_mfma_f32_16x16x32_bf16 v[76:79], v[44:47], a[28:31], 0\n\t"
  "v_mul_f32 v224, v22, v224\n\t"
  "v_mul_f32 v225, v22, v225\n\t"
  "v_mfma_f32_16x16x32_bf16 v[76:79], v[40:43], a[12:15], v[76:79]\n\t"
  "v_mul_f32 v226, v22, v226\n\t"
  "s_waitcnt lgkmcnt(4)\n\t"
  "v_mfma_f32_16x16x32_bf16 v[96:99], v[48:51], a[0:3], 0\n\t"
  "ds_read_b64_tr_b16 v[40:41], v15 offset:0\n\t"
  "v_mul_f32 v227, v22, v227\n\t"
  "v_mfma_f32_16x16x32_bf16 v[96:99], v[52:55], a[16:19], v[96:99]\n\t"
  "ds_read_b64_tr_b16 v[42:43], v15 offset:2048\n\t"
  "v_cvt_pk_bf16_f32 v112, v192, v193\n\t"
  "v_mfma_f32_16x16x32_bf16 v[100:103], v[52:55], a[20:23], 0\n\t"
  "v_cvt_pk_bf16_f32 v113, v194, v195\n\t"
  "v_cvt_pk_bf16_f32 v114, v224, v225\n\t"
  "v_mfma_f32_16x16x32_bf16 v[100:103], v[48:51], a[4:7], v[100:103]\n\t"
  "v_cvt_pk_bf16_f32 v115, v226, v227\n\t"
  "v_mfma_f32_16x16x32_bf16 v[104:107], v[48:51], a[8:11], 0\n\t"
  "ds_read_b64_tr_b16 v[44:45], v15 offset:4096\n\t"
  "v_mfma_f32_16x16x32_bf16 v[104:107], v[52:55], a[24:27], v[104:107]\n\t"
  "ds_read_b64_tr_b16 v[46:47], v15 offset:6144\n\t"
  "v_mfma_f32_16x16x32_bf16 v[108:111], v[52:55], a[28:31], 0\n\t"
  "v_mfma_f32_16x16x32_bf16 v[108:111], v[48:51], a[12:15], v[108:111]\n\t"
  "buffer_store_dwordx4 v[112:115], v16, s[56:59], s80 offen offset:0 sc1\n\t"
  "s_waitcnt lgkmcnt(4)\n\t"
  "v_mfma_f32_16x16x32_bf16 v[128:131], v[32:35], a[0:3], 0\n\t"
  "ds_read_b64_tr_b16 v[48:49], v12 offset:8192\n\t"
  "v_mul_f32 v196, v23, v196\n\t"
  "v_mfma_f32_16x16x32_bf16 v[128:131], v[36:39], a[16:19], v[128:131]\n\t"
  "ds_read_b64_tr_b16 v[50:51], v12 offset:10240\n\t"
  "v_mul_f32 v197, v23, v197\n\t"
  "v_mfma_f32_16x16x32_bf16 v[132:135], v[36:39], a[20:23], 0\n\t"
  "v_mul_f32 v198, v23, v198\n\t"
  "v_mul_f32 v199, v23, v199\n\t"
  "v_mfma_f32_16x16x32_bf16 v[132:135], v[32:35], a[4:7], v[132:135]\n\t"
  "v_mul_f32 v228, v23, v228\n\t"
  "v_mul_f32 v229, v23, v229\n\t"
  "v_mfma_f32_16x16x32_bf16 v[136:139], v[32:35], a[8:11], 0\n\t"
  "ds_read_b64_tr_b16 v[52:53], v12 offset:12288\n\t"
  "v_mul_f32 v230, v23, v230\n\t"
  "v_mfma_f32_16x16x32_bf16 v[136:139], v[36:39], a[24:27], v[136:139]\n\t"
  "ds_read_b64_tr_b16 v[54:55], v12 offset:14336\n\t"
  "v_mul_f32 v231, v23, v231\n\t"
  "v_mfma_f32_16x16x32_bf16 v[140:143], v[36:39], a[28:31], 0\n\t"
  "v_cvt_pk_bf16_f32 v116, v196, v197\n\t"
  "v_cvt_pk_bf16_f32 v117, v198, v199\n\t"
  "v_mfma_f32_16x16x32_bf16 v[140:143], v[32:35], a[12:15], v[140:143]\n\t"
  "v_cvt_pk_bf16_f32 v118, v228, v229\n\t"
  "s_waitcnt lgkmcnt(4)\n\t"
  "v_mfma_f32_16x16x32_bf16 v[160:163], v[40:43], a[0:3], 0\n\t"
  "ds_read_b64_tr_b16 v[32:33], v13 offset:8192\n\t"
  "v_cvt_pk_bf16_f32 v119, v230, v231\n\t"
  "v_mfma_f32_16x16x32_bf16 v[160:163], v[44:47], a[16:19], v[160:163]\n\t"
  "ds_read_b64_tr_b16 v[34:35], v13 offset:10240\n\t"
  "v_mfma_f32_16x16x32_bf16 v[164:167], v[44:47], a[20:23], 0\n\t"
  "v_mfma_f32_16x16x32_bf16 v[164:167], v[40:43], a[4:7], v[164:167]\n\t"
  "v_mfma_f32_16x16x32_bf16 v[168:171], v[40:43], a[8:11], 0\n\t"
  "ds_read_b64_tr_b16 v[36:37], v13 offset:12288\n\t"
  "v_mfma_f32_16x16x32_bf16 v[168:171], v[44:47], a[24:27], v[168:171]\n\t"
  "ds_read_b64_tr_b16 v[38:39], v13 offset:14336\n\t"
  "v_mfma_f32_16x16x32_bf16 v[172:175], v[44:47], a[28:31], 0\n\t"
  "v_mfma_f32_16x16x32_bf16 v[172:175], v[40:43], a[12:15], v[172:175]\n\t"
  "s_waitcnt lgkmcnt(4)\n\t"
  "v_mfma_f32_16x16x32_bf16 v[64:67], v[48:51], a[32:35], v[64:67]\n\t"
  "ds_read_b64_tr_b16 v[40:41], v14 offset:8192\n\t"
  "v_mfma_f32_16x16x32_bf16 v[64:67], v[52:55], a[48:51], v[64:67]\n\t"
  "ds_read_b64_tr_b16 v[42:43], v14 offset:10240\n\t"
  "v_mfma_f32_16x16x32_bf16 v[68:71], v[52:55], a[52:55], v[68:71]\n\t"
  "v_mfma_f32_16x16x32_bf16 v[68:71], v[48:51], a[36:39], v[68:71]\n\t"
  "v_mfma_f32_16x16x32_bf16 v[72:75], v[48:51], a[40:43], v[72:75]\n\t"
  "ds_read_b64_tr_b16 v[44:45], v14 offset:12288\n\t"
  "v_mfma_f32_16x16x32_bf16 v[72:75], v[52:55], a[56:59], v[72:75]\n\t"
  "ds_read_b64_tr_b16 v[46:47], v14 offset:14336\n\t"
  "v_mfma_f32_16x16x32_bf16 v[76:79], v[52:55], a[60:63], v[76:79]\n\t"
  "v_mfma_f32_16x16x32_bf16 v[76:79], v[48:51], a[44:47], v[76:79]\n\t"
  "s_waitcnt lgkmcnt(4)\n\t"
  "v_mfma_f32_16x16x32_bf16 v[96:99], v[32:35], a[32:35], v[96:99]\n\t"
  "ds_read_b64_tr_b16 v[48:49], v15 offset:8192\n\t"
  "s_add_u32 m0, s69, 131072\n\t"
  "v_mfma_f32_16x16x32_bf16 v[96:99], v[36:39], a[48:51], v[96:99]\n\t"
  "ds_read_b64_tr_b16 v[50:51], v15 offset:10240\n\t"
  "s_bfe_u32 s70, s84, 0x40000\n\t"
  "v_mfma_f32_16x16x32_bf16 v[100:103], v[36:39], a[52:55], v[100:103]\n\t"
  "s_mul_i32 s70, s70, 57344\n\t"
  "v_mfma_f32_16x16x32_bf16 v[100:103], v[32:35], a[36:39], v[100:103]\n\t"
  "s_waitcnt vmcnt(29)\n\t"
  "s_barrier\n\t"
  "v_mfma_f32_16x16x32_bf16 v[104:107], v[32:35], a[40:43], v[104:107]\n\t"
  "ds_read_b64_tr_b16 v[52:53], v15 offset:12288\n\t"
  "v_mfma_f32_16x16x32_bf16 v[104:107], v[36:39], a[56:59], v[104:107]\n\t"
  "ds_read_b64_tr_b16 v[54:55], v15 offset:14336\n\t"
  "v_mfma_f32_16x16x32_bf16 v[108:111], v[36:39], a[60:63], v[108:111]\n\t"
  "buffer_load_dwordx4 v6, s[60:63], s70 offen offset:0  lds\n\t"
  "v_mfma_f32_16x16x32_bf16 v[108:111], v[32:35], a[44:47], v[108:111]\n\t"
  "buffer_store_dwordx4 v[116:119], v17, s[56:59], s80 offen offset:0 sc1\n\t"
  "s_waitcnt lgkmcnt(4)\n\t"
  "v_mfma_f32_16x16x32_bf16 v[128:131], v[40:43], a[32:35], v[128:131]\n\t"
  "ds_read_b64_tr_b16 v[32:33], v12 offset:16384\n\t"
  "v_mul_f32 v200, v24, v200\n\t"
  "v_mfma_f32_16x16x32_bf16 v[128:131], v[44:47], a[48:51], v[128:131]\n\t"
  "ds_read_b64_tr_b16 v[34:35], v12 offset:18432\n\t"
  "v_mul_f32 v201, v24, v201\n\t"
  "v_mfma_f32_16x16x32_bf16 v[132:135], v[44:47], a[52:55], v[132:135]\n\t"
  "v_mul_f32 v202, v24, v202\n\t"
  "v_mul_f32 v203, v24, v203\n\t"
  "v_mfma_f32_16x16x32_bf16 v[132:135], v[40:43], a[36:39], v[132:135]\n\t"
  "buffer_load_dwordx4 v7, s[60:63], s70 offen offset:1024  lds\n\t"
  "v_mul_f32 v232, v24, v232\n\t"
  "v_mfma_f32_16x16x32_bf16 v[136:139], v[40:43], a[40:43], v[136:139]\n\t"
  "ds_read_b64_tr_b16 v[36:37], v12 offset:20480\n\t"
  "v_mul_f32 v233, v24, v233\n\t"
  "v_mfma_f32_16x16x32_bf16 v[136:139], v[44:47], a[56:59], v[136:139]\n\t"
  "ds_read_b64_tr_b16 v[38:39], v12 offset:22528\n\t"
  "v_mul_f32 v234, v24, v234\n\t"
  "v_mfma_f32_16x16x32_bf16 v[140:143], v[44:47], a[60:63], v[140:143]\n\t"
  "v_mul_f32 v235, v24, v235\n\t"
  "v_cvt_pk_bf16_f32 v120, v200, v201\n\t"
  "v_mfma_f32_16x16x32_bf16 v[140:143], v[40:43], a[44:47], v[140:143]\n\t"
  "v_cvt_pk_bf16_f32 v121, v202, v203\n\t"
  "s_waitcnt lgkmcnt(4)\n\t"
  "v_mfma_f32_16x16x32_bf16 v[160:163], v[48:51], a[32:35], v[160:163]\n\t"
  "ds_read_b64_tr_b16 v[40:41], v13 offset:16384\n\t"
  "v_cvt_pk_bf16_f32 v122, v232, v233\n\t"
  "v_mfma_f32_16x16x32_bf16 v[160:163], v[52:55], a[48:51], v[160:163]\n\t"
  "ds_read_b64_tr_b16 v[42:43], v13 offset:18432\n\t"
  "v_cvt_pk_bf16_f32 v123, v234, v235\n\t"
  "v_mfma_f32_16x16x32_bf16 v[164:167], v[52:55], a[52:55], v[164:167]\n\t"
  "buffer_load_dwordx4 v8, s[60:63], s70 offen offset:2048  lds\n\t"
  "v_mfma_f32_16x16x32_bf16 v[164:167], v[48:51], a[36:39], v[164:167]\n\t"
  "v_mfma_f32_16x16x32_bf16 v[168:171], v[48:51], a[40:43], v[168:171]\n\t"
  "ds_read_b64_tr_b16 v[44:45], v13 offset:20480\n\t"
  "v_mfma_f32_16x16x32_bf16 v[168:171], v[52:55], a[56:59], v[168:171]\n\t"
  "ds_read_b64_tr_b16 v[46:47], v13 offset:22528\n\t"
  "v_mfma_f32_16x16x32_bf16 v[172:175], v[52:55], a[60:63], v[172:175]\n\t"
  "v_mfma_f32_16x16x32_bf16 v[172:175], v[48:51], a[44:47], v[172:175]\n\t"
  "buffer_load_dwordx4 v9, s[60:63], s70 offen offset:3072  lds\n\t"
  "s_waitcnt lgkmcnt(4)\n\t"
  "v_mfma_f32_16x16x32_bf16 v[64:67], v[32:35], a[64:67], v[64:67]\n\t"
  "ds_read_b64_tr_b16 v[48:49], v14 offset:16384\n\t"
  "s_add_u32 m0, s69, 147456\n\t"
  "v_mfma_f32_16x16x32_bf16 v[64:67], v[36:39], a[80:83], v[64:67]\n\t"
  "ds_read_b64_tr_b16 v[50:51], v14 offset:18432\n\t"
  "s_bfe_u32 s70, s84, 0x40004\n\t"
  "v_mfma_f32_16x16x32_bf16 v[68:71], v[36:39], a[84:87], v[68:71]\n\t"
  "s_mul_i32 s70, s70, 57344\n\t"
  "v_mfma_f32_16x16x32_bf16 v[68:71], v[32:35], a[68:71], v[68:71]\n\t"
  "v_mfma_f32_16x16x32_bf16 v[72:75], v[32:35], a[72:75], v[72:75]\n\t"
  "ds_read_b64_tr_b16 v[52:53], v14 offset:20480\n\t"
  "v_mfma_f32_16x16x32_bf16 v[72:75], v[36:39], a[88:91], v[72:75]\n\t"
  "ds_read_b64_tr_b16 v[54:55], v14 offset:22528\n\t"
  "v_mfma_f32_16x16x32_bf16 v[76:79], v[36:39], a[92:95], v[76:79]\n\t"
  "buffer_load_dwordx4 v6, s[60:63], s70 offen offset:0  lds\n\t"
  "v_mfma_f32_16x16x32_bf16 v[76:79], v[32:35], a[76:79], v[76:79]\n\t"
  "buffer_store_dwordx4 v[120:123], v18, s[56:59], s80 offen offset:0 sc1\n\t"
  "s_waitcnt lgkmcnt(4)\n\t"
  "v_mfma_f32_16x16x32_bf16 v[96:99], v[40:43], a[64:67], v[96:99]\n\t"
  "ds_read_b64_tr_b16 v[32:33], v15 offset:16384\n\t"
  "v_mul_f32 v204, v25, v204\n\t"
  "v_mfma_f32_16x16x32_bf16 v[96:99], v[44:47], a[80:83], v[96:99]\n\t"
  "ds_read_b64_tr_b16 v[34:35], v15 offset:18432\n\t"
  "v_mul_f32 v205, v25, v205\n\t"
  "v_mfma_f32_16x16x32_bf16 v[100:103], v[44:47], a[84:87], v[100:103]\n\t"
  "v_mul_f32 v206, v25, v206\n\t"
  "v_mul_f32 v207, v25, v207\n\t"
  "v_mfma_f32_16x16x32_bf16 v[100:103], v[40:43], a[68:71], v[100:103]\n\t"
  "buffer_load_dwordx4 v7, s[60:63], s70 offen offset:1024  lds\n\t"
  "v_mul_f32 v236, v25, v236\n\t"
  "v_mfma_f32_16x16x32_bf16 v[104:107], v[40:43], a[72:75], v[104:107]\n\t"
  "ds_read_b64_tr_b16 v[36:37], v15 offset:20480\n\t"
  "v_mul_f32 v237, v25, v237\n\t"
  "v_mfma_f32_16x16x32_bf16 v[104:107], v[44:47], a[88:91], v[104:107]\n\t"
  "ds_read_b64_tr_b16 v[38:39], v15 offset:22528\n\t"
  "v_mul_f32 v238, v25, v238\n\t"
  "v_mfma_f32_16x16x32_bf16 v[108:111], v[44:47], a[92:95], v[108:111]\n\t"
  "v_mul_f32 v239, v25, v239\n\t"
  "v_cvt_pk_bf16_f32 v124, v204, v205\n\t"
  "v_mfma_f32_16x16x32_bf16 v[108:111], v[40:43], a[76:79], v[108:111]\n\t"
  "v_cvt_pk_bf16_f32 v125, v206, v207\n\t"
  "s_waitcnt lgkmcnt(4)\n\t"
  "v_mfma_f32_16x16x32_bf16 v[128:131], v[48:51], a[64:67], v[128:131]\n\t"
  "ds_read_b64_tr_b16 v[40:41], v12 offset:24576\n\t"
  "v_cvt_pk_bf16_f32 v126, v236, v237\n\t"
  "v_mfma_f32_16x16x32_bf16 v[128:131], v[52:55], a[80:83], v[128:131]\n\t"
  "ds_read_b64_tr_b16 v[42:43], v12 offset:26624\n\t"
  "v_cvt_pk_bf16_f32 v127, v238, v239\n\t"
  "v_mfma_f32_16x16x32_bf16 v[132:135], v[52:55], a[84:87], v[132:135]\n\t"
  "buffer_load_dwordx4 v8, s[60:63], s70 offen offset:2048  lds\n\t"
  "v_mfma_f32_16x16x32_bf16 v[132:135], v[48:51], a[68:71], v[132:135]\n\t"
  "v_mfma_f32_16x16x32_bf16 v[136:139], v[48:51], a[72:75], v[136:139]\n\t"
  "ds_read_b64_tr_b16 v[44:45], v12 offset:28672\n\t"
  "v_mfma_f32_16x16x32_bf16 v[136:139], v[52:55], a[88:91], v[136:139]\n\t"
  "ds_read_b64_tr_b16 v[46:47], v12 offset:30720\n\t"
  "v_mfma_f32_16x16x32_bf16 v[140:143], v[52:55], a[92:95], v[140:143]\n\t"
  "v_mfma_f32_16x16x32_bf16 v[140:143], v[48:51], a[76:79], v[140:143]\n\t"
  "buffer_load_dwordx4 v9, s[60:63], s70 offen offset:3072  lds\n\t"
  "s_waitcnt lgkmcnt(4)\n\t"
  "v_mfma_f32_16x16x32_bf16 v[160:163], v[32:35], a[64:67], v[160:163]\n\t"
  "ds_read_b64_tr_b16 v[48:49], v13 offset:24576\n\t"
  "v_mfma_f32_16x16x32_bf16 v[160:163], v[36:39], a[80:83], v[160:163]\n\t"
  "ds_read_b64_tr_b16 v[50:51], v13 offset:26624\n\t"
  "v_mfma_f32_16x16x32_bf16 v[164:167], v[36:39], a[84:87], v[164:167]\n\t"
  "v_mfma_f32_16x16x32_bf16 v[164:167], v[32:35], a[68:71], v[164:167]\n\t"
  "v_mfma_f32_16x16x32_bf16 v[168:171], v[32:35], a[72:75], v[168:171]\n\t"
  "ds_read_b64_tr_b16 v[52:53], v13 offset:28672\n\t"
  "v_mfma_f32_16x16x32_bf16 v[168:171], v[36:39], a[88:91], v[168:171]\n\t"
  "ds_read_b64_tr_b16 v[54:55], v13 offset:30720\n\t"
  "v_mfma_f32_16x16x32_bf16 v[172:175], v[36:39], a[92:95], v[172:175]\n\t"
  "v_mfma_f32_16x16x32_bf16 v[172:175], v[32:35], a[76:79], v[172:175]\n\t"
  "s_waitcnt lgkmcnt(4)\n\t"
  "v_mfma_f32_16x16x32_bf16 v[64:67], v[40:43], a[96:99], v[64:67]\n\t"
  "ds_read_b64_tr_b16 v[32:33], v14 offset:24576\n\t"
  "v_mfma_f32_16x16x32_bf16 v[64:67], v[44:47], a[112:115], v[64:67]\n\t"
  "ds_read_b64_tr_b16 v[34:35], v14 offset:26624\n\t"
  "v_mfma_f32_16x16x32_bf16 v[68:71], v[44:47], a[116:119], v[68:71]\n\t"
  "v_mfma_f32_16x16x32_bf16 v[68:71], v[40:43], a[100:103], v[68:71]\n\t"
  "buffer_store_dwordx4 v[124:127], v19, s[56:59], s80 offen offset:0 sc1\n\t"
  "v_mul_f32 v80, v22, v80\n\t"
  "v_mfma_f32_16x16x32_bf16 v[72:75], v[40:43], a[104:107], v[72:75]\n\t"
  "ds_read_b64_tr_b16 v[36:37], v14 offset:28672\n\t"
  "v_mul_f32 v81, v22, v81\n\t"
  "v_mfma_f32_16x16x32_bf16 v[72:75], v[44:47], a[120:123], v[72:75]\n\t"
  "ds_read_b64_tr_b16 v[38:39], v14 offset:30720\n\t"
  "v_mul_f32 v82, v22, v82\n\t"
  "v_mfma_f32_16x16x32_bf16 v[76:79], v[44:47], a[124:127], v[76:79]\n\t"
  "v_mul_f32 v83, v22, v83\n\t"
  "v_mul_f32 v144, v22, v144\n\t"
  "v_mfma_f32_16x16x32_bf16 v[76:79], v[40:43], a[108:111], v[76:79]\n\t"
  "v_mul_f32 v145, v22, v145\n\t"
  "s_waitcnt lgkmcnt(4)\n\t"
  "v_mfma_f32_16x16x32_bf16 v[96:99], v[48:51], a[96:99], v[96:99]\n\t"
  "ds_read_b64_tr_b16 v[40:41], v15 offset:24576\n\t"
  "v_mul_f32 v146, v22, v146\n\t"
  "v_mfma_f32_16x16x32_bf16 v[96:99], v[52:55], a[112:115], v[96:99]\n\t"
  "ds_read_b64_tr_b16 v[42:43], v15 offset:26624\n\t"
  "v_mul_f32 v147, v22, v147\n\t"
  "v_mfma_f32_16x16x32_bf16 v[100:103], v[52:55], a[116:119], v[100:103]\n\t"
  "v_cvt_pk_bf16_f32 v176, v80, v81\n\t"
  "v_cvt_pk_bf16_f32 v177, v82, v83\n\t"
  "v_mfma_f32_16x16x32_bf16 v[100:103], v[48:51], a[100:103], v[100:103]\n\t"
  "v_cvt_pk_bf16_f32 v178, v144, v145\n\t"
  "v_cvt_pk_bf16_f32 v179, v146, v147\n\t"
  "v_mfma_f32_16x16x32_bf16 v[104:107], v[48:51], a[104:107], v[104:107]\n\t"
  "ds_read_b64_tr_b16 v[44:45], v15 offset:28672\n\t"
  "v_mfma_f32_16x16x32_bf16 v[104:107], v[52:55], a[120:123], v[104:107]\n\t"
  "ds_read_b64_tr_b16 v[46:47], v15 offset:30720\n\t"
  "v_add_u32 v12, 0xffff0000, v12\n\t"
  "v_mfma_f32_16x16x32_bf16 v[108:111], v[52:55], a[124:127], v[108:111]\n\t"
  "v_add_u32 v13, 0xffff0000, v13\n\t"
  "v_add_u32 v14, 0xffff0000, v14\n\t"
  "v_mfma_f32_16x16x32_bf16 v[108:111], v[48:51], a[108:111], v[108:111]\n\t"
  "v_add_u32 v15, 0xffff0000, v15\n\t"
  "s_waitcnt lgkmcnt(4)\n\t"
  "v_mfma_f32_16x16x32_bf16 v[128:131], v[32:35], a[96:99], v[128:131]\n\t"
  "ds_read_b64_tr_b16 v[48:49], v12 offset:32768\n\t"
  "v_mfma_f32_16x16x32_bf16 v[128:131], v[36:39], a[112:115], v[128:131]\n\t"
  "ds_read_b64_tr_b16 v[50:51], v12 offset:34816\n\t"
  "v_mfma_f32_16x16x32_bf16 v[132:135], v[36:39], a[116:119], v[132:135]\n\t"
  "v_mfma_f32_16x16x32_bf16 v[132:135], v[32:35], a[100:103], v[132:135]\n\t"
  "v_mfma_f32_16x16x32_bf16 v[136:139], v[32:35], a[104:107], v[136:139]\n\t"
  "ds_read_b64_tr_b16 v[52:53], v12 offset:36864\n\t"
  "v_mfma_f32_16x16x32_bf16 v[136:139], v[36:39], a[120:123], v[136:139]\n\t"
  "ds_read_b64_tr_b16 v[54:55], v12 offset:38912\n\t"
  "v_mfma_f32_16x16x32_bf16 v[140:143], v[36:39], a[124:127], v[140:143]\n\t"
  "v_mfma_f32_16x16x32_bf16 v[140:143], v[32:35], a[108:111], v[140:143]\n\t"
  "s_waitcnt lgkmcnt(4)\n\t"
  "v_mfma_f32_16x16x32_bf16 v[160:163], v[40:43], a[96:99], v[160:163]\n\t"
  "ds_read_b64_tr_b16 v[32:33], v13 offset:32768\n\t"
  "v_mfma_f32_16x16x32_bf16 v[160:163], v[44:47], a[112:115], v[160:163]\n\t"
  "ds_read_b64_tr_b16 v[34:35], v13 offset:34816\n\t"
  "v_mfma_f32_16x16x32_bf16 v[164:167], v[44:47], a[116:119], v[164:167]\n\t"
  "v_mfma_f32_16x16x32_bf16 v[164:167], v[40:43], a[100:103], v[164:167]\n\t"
  "v_mfma_f32_16x16x32_bf16 v[168:171], v[40:43], a[104:107], v[168:171]\n\t"
  "ds_read_b64_tr_b16 v[36:37], v13 offset:36864\n\t"
  "v_mfma_f32_16x16x32_bf16 v[168:171], v[44:47], a[120:123], v[168:171]\n\t"
  "ds_read_b64_tr_b16 v[38:39], v13 offset:38912\n\t"
  "v_mfma_f32_16x16x32_bf16 v[172:175], v[44:47], a[124:127], v[172:175]\n\t"
  "v_mfma_f32_16x16x32_bf16 v[172:175], v[40:43], a[108:111], v[172:175]\n\t"
  "buffer_store_dwordx4 v[176:179], v16, s[56:59], s80 offen offset:1024 sc1\n\t"
  "s_waitcnt lgkmcnt(4)\n\t"
  "v_mfma_f32_16x16x32_bf16 v[64:67], v[48:51], a[128:131], v[64:67]\n\t"
  "ds_read_b64_tr_b16 v[40:41], v14 offset:32768\n\t"
  "v_mul_f32 v84, v23, v84\n\t"
  "v_mfma_f32_16x16x32_bf16 v[64:67], v[52:55], a[144:147], v[64:67]\n\t"
  "ds_read_b64_tr_b16 v[42:43], v14 offset:34816\n\t"
  "v_mul_f32 v85, v23, v85\n\t"
  "v_mfma_f32_16x16x32_bf16 v[68:71], v[52:55], a[148:151], v[68:71]\n\t"
  "v_mul_f32 v86, v23, v86\n\t"
  "v_mul_f32 v87, v23, v87\n\t"
  "v_mfma_f32_16x16x32_bf16 v[68:71], v[48:51], a[132:135], v[68:71]\n\t"
  "v_mul_f32 v148, v23, v148\n\t"
  "v_mul_f32 v149, v23, v149\n\t"
  "v_mfma_f32_16x16x32_bf16 v[72:75], v[48:51], a[136:139], v[72:75]\n\t"
  "ds_read_b64_tr_b16 v[44:45], v14 offset:36864\n\t"
  "v_mul_f32 v150, v23, v150\n\t"
  "v_mfma_f32_16x16x32_bf16 v[72:75], v[52:55], a[152:155], v[72:75]\n\t"
  "ds_read_b64_tr_b16 v[46:47], v14 offset:38912\n\t"
  "v_mul_f32 v151, v23, v151\n\t"
  "v_mfma_f32_16x16x32_bf16 v[76:79], v[52:55], a[156:159], v[76:79]\n\t"
  "v_cvt_pk_bf16_f32 v180, v84, v85\n\t"
  "v_cvt_pk_bf16_f32 v181, v86, v87\n\t"
  "v_mfma_f32_16x16x32_bf16 v[76:79], v[48:51], a[140:143], v[76:79]\n\t"
  "v_cvt_pk_bf16_f32 v182, v148, v149\n\t"
  "s_waitcnt lgkmcnt(4)\n\t"
  "v_mfma_f32_16x16x32_bf16 v[96:99], v[32:35], a[128:131], v[96:99]\n\t"
  "ds_read_b64_tr_b16 v[48:49], v15 offset:32768\n\t"
  "v_cvt_pk_bf16_f32 v183, v150, v151\n\t"
  "v_mfma_f32_16x16x32_bf16 v[96:99], v[36:39], a[144:147], v[96:99]\n\t"
  "ds_read_b64_tr_b16 v[50:51], v15 offset:34816\n\t"
  "v_mfma_f32_16x16x32_bf16 v[100:103], v[36:39], a[148:151], v[100:103]\n\t"
  "v_mfma_f32_16x16x32_bf16 v[100:103], v[32:35], a[132:135], v[100:103]\n\t"
  "v_mfma_f32_16x16x32_bf16 v[104:107], v[32:35], a[136:139], v[104:107]\n\t"
  "ds_read_b64_tr_b16 v[52:53], v15 offset:36864\n\t"
  "v_mfma_f32_16x16x32_bf16 v[104:107], v[36:39], a[152:155], v[104:107]\n\t"
  "ds_read_b64_tr_b16 v[54:55], v15 offset:38912\n\t"
  "v_mfma_f32_16x16x32_bf16 v[108:111], v[36:39], a[156:159], v[108:111]\n\t"
  "v_mfma_f32_16x16x32_bf16 v[108:111], v[32:35], a[140:143], v[108:111]\n\t"
  "s_waitcnt lgkmcnt(4)\n\t"
  "v_mfma_f32_16x16x32_bf16 v[128:131], v[40:43], a[128:131], v[128:131]\n\t"
  "ds_read_b64_tr_b16 v[32:33], v12 offset:40960\n\t"
  "v_mfma_f32_16x16x32_bf16 v[128:131], v[44:47], a[144:147], v[128:131]\n\t"
  "ds_read_b64_tr_b16 v[34:35], v12 offset:43008\n\t"
  "v_mfma_f32_16x16x32_bf16 v[132:135], v[44:47], a[148:151], v[132:135]\n\t"
  "v_mfma_f32_16x16x32_bf16 v[132:135], v[40:43], a[132:135], v[132:135]\n\t"
  "v_mfma_f32_16x16x32_bf16 v[136:139], v[40:43], a[136:139], v[136:139]\n\t"
  "ds_read_b64_tr_b16 v[36:37], v12 offset:45056\n\t"
  "v_mfma_f32_16x16x32_bf16 v[136:139], v[44:47], a[152:155], v[136:139]\n\t"
  "ds_read_b64_tr_b16 v[38:39], v12 offset:47104\n\t"
  "v_mfma_f32_16x16x32_bf16 v[140:143], v[44:47], a[156:159], v[140:143]\n\t"
  "v_mfma_f32_16x16x32_bf16 v[140:143], v[40:43], a[140:143], v[140:143]\n\t"
  "s_waitcnt lgkmcnt(4)\n\t"
  "v_mfma_f32_16x16x32_bf16 v[160:163], v[48:51], a[128:131], v[160:163]\n\t"
  "ds_read_b64_tr_b16 v[40:41], v13 offset:40960\n\t"
  "v_mfma_f32_16x16x32_bf16 v[160:163], v[52:55], a[144:147], v[160:163]\n\t"
  "ds_read_b64_tr_b16 v[42:43], v13 offset:43008\n\t"
  "v_mfma_f32_16x16x32_bf16 v[164:167], v[52:55], a[148:151], v[164:167]\n\t"
  "v_mfma_f32_16x16x32_bf16 v[164:167], v[48:51], a[132:135], v[164:167]\n\t"
  "buffer_store_dwordx4 v[180:183], v17, s[56:59], s80 offen offset:1024 sc1\n\t"
  "v_mul_f32 v88, v24, v88\n\t"
  "v_mfma_f32_16x16x32_bf16 v[168:171], v[48:51], a[136:139], v[168:171]\n\t"
  "ds_read_b64_tr_b16 v[44:45], v13 offset:45056\n\t"
  "v_mul_f32 v89, v24, v89\n\t"
  "v_mfma_f32_16x16x32_bf16 v[168:171], v[52:55], a[152:155], v[168:171]\n\t"
  "ds_read_b64_tr_b16 v[46:47], v13 offset:47104\n\t"
  "v_mul_f32 v90, v24, v90\n\t"
  "v_mfma_f32_16x16x32_bf16 v[172:175], v[52:55], a[156:159], v[172:175]\n\t"
  "v_mul_f32 v91, v24, v91\n\t"
  "v_mul_f32 v152, v24, v152\n\t"
  "v_mfma_f32_16x16x32_bf16 v[172:175], v[48:51], a[140:143], v[172:175]\n\t"
  "v_mul_f32 v153, v24, v153\n\t"
  "s_waitcnt lgkmcnt(4)\n\t"
  "v_mfma_f32_16x16x32_bf16 v[64:67], v[32:35], a[160:163], v[64:67]\n\t"
  "ds_read_b64_tr_b16 v[48:49], v14 offset:40960\n\t"
  "v_mul_f32 v154, v24, v154\n\t"
  "v_mfma_f32_16x16x32_bf16 v[64:67], v[36:39], a[176:179], v[64:67]\n\t"
  "ds_read_b64_tr_b16 v[50:51], v14 offset:43008\n\t"
  "v_mul_f32 v155, v24, v155\n\t"
  "v_mfma_f32_16x16x32_bf16 v[68:71], v[36:39], a[180:183], v[68:71]\n\t"
  "v_cvt_pk_bf16_f32 v184, v88, v89\n\t"
  "v_cvt_pk_bf16_f32 v185, v90, v91\n\t"
  "v_mfma_f32_16x16x32_bf16 v[68:71], v[32:35], a[164:167], v[68:71]\n\t"
  "v_cvt_pk_bf16_f32 v186, v152, v153\n\t"
  "v_cvt_pk_bf16_f32 v187, v154, v155\n\t"
  "v_mfma_f32_16x16x32_bf16 v[72:75], v[32:35], a[168:171], v[72:75]\n\t"
  "ds_read_b64_tr_b16 v[52:53], v14 offset:45056\n\t"
  "v_mfma_f32_16x16x32_bf16 v[72:75], v[36:39], a[184:187], v[72:75]\n\t"
  "ds_read_b64_tr_b16 v[54:55], v14 offset:47104\n\t"
  "v_mfma_f32_16x16x32_bf16 v[76:79], v[36:39], a[188:191], v[76:79]\n\t"
  "v_mfma_f32_16x16x32_bf16 v[76:79], v[32:35], a[172:175], v[76:79]\n\t"
  "s_waitcnt lgkmcnt(4)\n\t"
  "v_mfma_f32_16x16x32_bf16 v[96:99], v[40:43], a[160:163], v[96:99]\n\t"
  "ds_read_b64_tr_b16 v[32:33], v15 offset:40960\n\t"
  "s_add_u32 m0, s69, 65536\n\t"
  "v_mfma_f32_16x16x32_bf16 v[96:99], v[44:47], a[176:179], v[96:99]\n\t"
  "ds_read_b64_tr_b16 v[34:35], v15 offset:43008\n\t"
  "s_bfe_u32 s70, s84, 0x40008\n\t"
  "v_mfma_f32_16x16x32_bf16 v[100:103], v[44:47], a[180:183], v[100:103]\n\t"
  "s_mul_i32 s70, s70, 57344\n\t"
  "v_mfma_f32_16x16x32_bf16 v[100:103], v[40:43], a[164:167], v[100:103]\n\t"
  "s_waitcnt vmcnt(32)\n\t"
  "s_barrier\n\t"
  "v_mfma_f32_16x16x32_bf16 v[104:107], v[40:43], a[168:171], v[104:107]\n\t"
  "ds_read_b64_tr_b16 v[36:37], v15 offset:45056\n\t"
  "v_mfma_f32_16x16x32_bf16 v[104:107], v[44:47], a[184:187], v[104:107]\n\t"
  "ds_read_b64_tr_b16 v[38:39], v15 offset:47104\n\t"
  "v_mfma_f32_16x16x32_bf16 v[108:111], v[44:47], a[188:191], v[108:111]\n\t"
  "buffer_load_dwordx4 v6, s[60:63], s70 offen offset:0  lds\n\t"
  "v_mfma_f32_16x16x32_bf16 v[108:111], v[40:43], a[172:175], v[108:111]\n\t"
  "s_waitcnt lgkmcnt(4)\n\t"
  "v_mfma_f32_16x16x32_bf16 v[128:131], v[48:51], a[160:163], v[128:131]\n\t"
  "ds_read_b64_tr_b16 v[40:41], v12 offset:49152\n\t"
  "v_mfma_f32_16x16x32_bf16 v[128:131], v[52:55], a[176:179], v[128:131]\n\t"
  "ds_read_b64_tr_b16 v[42:43], v12 offset:51200\n\t"
  "v_mfma_f32_16x16x32_bf16 v[132:135], v[52:55], a[180:183], v[132:135]\n\t"
  "v_mfma_f32_16x16x32_bf16 v[132:135], v[48:51], a[164:167], v[132:135]\n\t"
  "buffer_load_dwordx4 v7, s[60:63], s70 offen offset:1024  lds\n\t"
  "v_mfma_f32_16x16x32_bf16 v[136:139], v[48:51], a[168:171], v[136:139]\n\t"
  "ds_read_b64_tr_b16 v[44:45], v12 offset:53248\n\t"
  "v_mfma_f32_16x16x32_bf16 v[136:139], v[52:55], a[184:187], v[136:139]\n\t"
  "ds_read_b64_tr_b16 v[46:47], v12 offset:55296\n\t"
  "v_mfma_f32_16x16x32_bf16 v[140:143], v[52:55], a[188:191], v[140:143]\n\t"
  "v_mfma_f32_16x16x32_bf16 v[140:143], v[48:51], a[172:175], v[140:143]\n\t"
  "buffer_store_dwordx4 v[184:187], v18, s[56:59], s80 offen offset:1024 sc1\n\t"
  "s_waitcnt lgkmcnt(4)\n\t"
  "v_mfma_f32_16x16x32_bf16 v[160:163], v[32:35], a[160:163], v[160:163]\n\t"
  "ds_read_b64_tr_b16 v[48:49], v13 offset:49152\n\t"
  "v_mul_f32 v92, v25, v92\n\t"
  "v_mfma_f32_16x16x32_bf16 v[160:163], v[36:39], a[176:179], v[160:163]\n\t"
  "ds_read_b64_tr_b16 v[50:51], v13 offset:51200\n\t"
  "v_mul_f32 v93, v25, v93\n\t"
  "v_mfma_f32_16x16x32_bf16 v[164:167], v[36:39], a[180:183], v[164:167]\n\t"
  "buffer_load_dwordx4 v8, s[60:63], s70 offen offset:2048  lds\n\t"
  "v_mul_f32 v94, v25, v94\n\t"
  "v_mfma_f32_16x16x32_bf16 v[164:167], v[32:35], a[164:167], v[164:167]\n\t"
  "v_mul_f32 v95, v25, v95\n\t"
  "v_mul_f32 v156, v25, v156\n\t"
  "v_mfma_f32_16x16x32_bf16 v[168:171], v[32:35], a[168:171], v[168:171]\n\t"
  "ds_read_b64_tr_b16 v[52:53], v13 offset:53248\n\t"
  "v_mul_f32 v157, v25, v157\n\t"
  "v_mfma_f32_16x16x32_bf16 v[168:171], v[36:39], a[184:187], v[168:171]\n\t"
  "ds_read_b64_tr_b16 v[54:55], v13 offset:55296\n\t"
  "v_mul_f32 v158, v25, v158\n\t"
  "v_mfma_f32_16x16x32_bf16 v[172:175], v[36:39], a[188:191], v[172:175]\n\t"
  "v_mul_f32 v159, v25, v159\n\t"
  "v_cvt_pk_bf16_f32 v188, v92, v93\n\t"
  "v_mfma_f32_16x16x32_bf16 v[172:175], v[32:35], a[172:175], v[172:175]\n\t"
  "buffer_load_dwordx4 v9, s[60:63], s70 offen offset:3072  lds\n\t"
  "s_waitcnt lgkmcnt(4)\n\t"
  "v_mfma_f32_16x16x32_bf16 v[64:67], v[40:43], a[192:195], v[64:67]\n\t"
  "ds_read_b64_tr_b16 v[32:33], v14 offset:49152\n\t"
  "s_add_u32 m0, s69, 81920\n\t"
  "v_mfma_f32_16x16x32_bf16 v[64:67], v[44:47], a[208:211], v[64:67]\n\t"
  "ds_read_b64_tr_b16 v[34:35], v14 offset:51200\n\t"
  "s_bfe_u32 s70, s84, 0x4000c\n\t"
  "v_mfma_f32_16x16x32_bf16 v[68:71], v[44:47], a[212:215], v[68:71]\n\t"
  "s_mul_i32 s70, s70, 57344\n\t"
  "v_cvt_pk_bf16_f32 v189, v94, v95\n\t"
  "v_mfma_f32_16x16x32_bf16 v[68:71], v[40:43], a[196:199], v[68:71]\n\t"
  "v_cvt_pk_bf16_f32 v190, v156, v157\n\t"
  "v_cvt_pk_bf16_f32 v191, v158, v159\n\t"
  "v_mfma_f32_16x16x32_bf16 v[72:75], v[40:43], a[200:203], v[72:75]\n\t"
  "ds_read_b64_tr_b16 v[36:37], v14 offset:53248\n\t"
  "v_mfma_f32_16x16x32_bf16 v[72:75], v[44:47], a[216:219], v[72:75]\n\t"
  "ds_read_b64_tr_b16 v[38:39], v14 offset:55296\n\t"
  "v_mfma_f32_16x16x32_bf16 v[76:79], v[44:47], a[220:223], v[76:79]\n\t"
  "buffer_load_dwordx4 v6, s[60:63], s70 offen offset:0  lds\n\t"
  "v_mfma_f32_16x16x32_bf16 v[76:79], v[40:43], a[204:207], v[76:79]\n\t"
  "s_waitcnt lgkmcnt(4)\n\t"
  "v_mfma_f32_16x16x32_bf16 v[96:99], v[48:51], a[192:195], v[96:99]\n\t"
  "ds_read_b64_tr_b16 v[40:41], v15 offset:49152\n\t"
  "v_mfma_f32_16x16x32_bf16 v[96:99], v[52:55], a[208:211], v[96:99]\n\t"
  "ds_read_b64_tr_b16 v[42:43], v15 offset:51200\n\t"
  "v_mfma_f32_16x16x32_bf16 v[100:103], v[52:55], a[212:215], v[100:103]\n\t"
  "v_mfma_f32_16x16x32_bf16 v[100:103], v[48:51], a[196:199], v[100:103]\n\t"
  "buffer_load_dwordx4 v7, s[60:63], s70 offen offset:1024  lds\n\t"
  "v_mfma_f32_16x16x32_bf16 v[104:107], v[48:51], a[200:203], v[104:107]\n\t"
  "ds_read_b64_tr_b16 v[44:45], v15 offset:53248\n\t"
  "v_mfma_f32_16x16x32_bf16 v[104:107], v[52:55], a[216:219], v[104:107]\n\t"
  "ds_read_b64_tr_b16 v[46:47], v15 offset:55296\n\t"
  "v_mfma_f32_16x16x32_bf16 v[108:111], v[52:55], a[220:223], v[108:111]\n\t"
  "v_mfma_f32_16x16x32_bf16 v[108:111], v[48:51], a[204:207], v[108:111]\n\t"
  "s_waitcnt lgkmcnt(4)\n\t"
  "v_mfma_f32_16x16x32_bf16 v[128:131], v[32:35], a[192:195], v[128:131]\n\t"
  "ds_read_b64_tr_b16 v[48:49], v12 offset:57344\n\t"
  "v_mfma_f32_16x16x32_bf16 v[128:131], v[36:39], a[208:211], v[128:131]\n\t"
  "ds_read_b64_tr_b16 v[50:51], v12 offset:59392\n\t"
  "v_mfma_f32_16x16x32_bf16 v[132:135], v[36:39], a[212:215], v[132:135]\n\t"
  "buffer_load_dwordx4 v8, s[60:63], s70 offen offset:2048  lds\n\t"
  "v_mfma_f32_16x16x32_bf16 v[132:135], v[32:35], a[196:199], v[132:135]\n\t"
  "buffer_store_dwordx4 v[188:191], v19, s[56:59], s80 offen offset:1024 sc1\n\t"
  "v_mfma_f32_16x16x32_bf16 v[136:139], v[32:35], a[200:203], v[136:139]\n\t"
  "ds_read_b64_tr_b16 v[52:53], v12 offset:61440\n\t"
  "v_mfma_f32_16x16x32_bf16 v[136:139], v[36:39], a[216:219], v[136:139]\n\t"
  "ds_read_b64_tr_b16 v[54:55], v12 offset:63488\n\t"
  "v_mfma_f32_16x16x32_bf16 v[140:143], v[36:39], a[220:223], v[140:143]\n\t"
  "v_mfma_f32_16x16x32_bf16 v[140:143], v[32:35], a[204:207], v[140:143]\n\t"
  "buffer_load_dwordx4 v9, s[60:63], s70 offen offset:3072  lds\n\t"
  "s_waitcnt lgkmcnt(4)\n\t"
  "v_mfma_f32_16x16x32_bf16 v[160:163], v[40:43], a[192:195], v[160:163]\n\t"
  "ds_read_b64_tr_b16 v[32:33], v13 offset:57344\n\t"
  "v_mfma_f32_16x16x32_bf16 v[160:163], v[44:47], a[208:211], v[160:163]\n\t"
  "ds_read_b64_tr_b16 v[34:35], v13 offset:59392\n\t"
  "v_mfma_f32_16x16x32_bf16 v[164:167], v[44:47], a[212:215], v[164:167]\n\t"
  "v_mfma_f32_16x16x32_bf16 v[164:167], v[40:43], a[196:199], v[164:167]\n\t"
  "v_mfma_f32_16x16x32_bf16 v[168:171], v[40:43], a[200:203], v[168:171]\n\t"
  "ds_read_b64_tr_b16 v[36:37], v13 offset:61440\n\t"
  "v_mfma_f32_16x16x32_bf16 v[168:171], v[44:47], a[216:219], v[168:171]\n\t"
  "ds_read_b64_tr_b16 v[38:39], v13 offset:63488\n\t"
  "v_mfma_f32_16x16x32_bf16 v[172:175], v[44:47], a[220:223], v[172:175]\n\t"
  "v_mfma_f32_16x16x32_bf16 v[172:175], v[40:43], a[204:207], v[172:175]\n\t"
  "s_waitcnt lgkmcnt(4)\n\t"
  "v_mfma_f32_16x16x32_bf16 v[64:67], v[48:51], a[224:227], v[64:67]\n\t"
  "ds_read_b64_tr_b16 v[40:41], v14 offset:57344\n\t"
  "v_mfma_f32_16x16x32_bf16 v[64:67], v[52:55], a[240:243], v[64:67]\n\t"
  "ds_read_b64_tr_b16 v[42:43], v14 offset:59392\n\t"
  "v_mfma_f32_16x16x32_bf16 v[68:71], v[52:55], a[244:247], v[68:71]\n\t"
  "v_mfma_f32_16x16x32_bf16 v[68:71], v[48:51], a[228:231], v[68:71]\n\t"
  "v_mfma_f32_16x16x32_bf16 v[72:75], v[48:51], a[232:235], v[72:75]\n\t"
  "ds_read_b64_tr_b16 v[44:45], v14 offset:61440\n\t"
  "v_mfma_f32_16x16x32_bf16 v[72:75], v[52:55], a[248:251], v[72:75]\n\t"
  "ds_read_b64_tr_b16 v[46:47], v14 offset:63488\n\t"
  "v_mfma_f32_16x16x32_bf16 v[76:79], v[52:55], a[252:255], v[76:79]\n\t"
  "v_mfma_f32_16x16x32_bf16 v[76:79], v[48:51], a[236:239], v[76:79]\n\t"
  "s_waitcnt lgkmcnt(4)\n\t"
  "v_mfma_f32_16x16x32_bf16 v[96:99], v[32:35], a[224:227], v[96:99]\n\t"
  "ds_read_b64_tr_b16 v[48:49], v15 offset:57344\n\t"
  "v_mfma_f32_16x16x32_bf16 v[96:99], v[36:39], a[240:243], v[96:99]\n\t"
  "ds_read_b64_tr_b16 v[50:51], v15 offset:59392\n\t"
  "v_mfma_f32_16x16x32_bf16 v[100:103], v[36:39], a[244:247], v[100:103]\n\t"
  "v_mfma_f32_16x16x32_bf16 v[100:103], v[32:35], a[228:231], v[100:103]\n\t"
  "v_mfma_f32_16x16x32_bf16 v[104:107], v[32:35], a[232:235], v[104:107]\n\t"
  "ds_read_b64_tr_b16 v[52:53], v15 offset:61440\n\t"
  "v_mfma_f32_16x16x32_bf16 v[104:107], v[36:39], a[248:251], v[104:107]\n\t"
  "ds_read_b64_tr_b16 v[54:55], v15 offset:63488\n\t"
  "v_mfma_f32_16x16x32_bf16 v[108:111], v[36:39], a[252:255], v[108:111]\n\t"
  "v_mfma_f32_16x16x32_bf16 v[108:111], v[32:35], a[236:239], v[108:111]\n\t"
  "s_waitcnt lgkmcnt(4)\n\t"
  "v_mfma_f32_16x16x32_bf16 v[128:131], v[40:43], a[224:227], v[128:131]\n\t"
  "ds_read_b64_tr_b16 v[32:33], v12 offset:0\n\t"
  "v_mfma_f32_16x16x32_bf16 v[128:131], v[44:47], a[240:243], v[128:131]\n\t"
  "ds_read_b64_tr_b16 v[34:35], v12 offset:2048\n\t"
  "v_mfma_f32_16x16x32_bf16 v[132:135], v[44:47], a[244:247], v[132:135]\n\t"
  "v_mfma_f32_16x16x32_bf16 v[132:135], v[40:43], a[228:231], v[132:135]\n\t"
  "v_mfma_f32_16x16x32_bf16 v[136:139], v[40:43], a[232:235], v[136:139]\n\t"
  "ds_read_b64_tr_b16 v[36:37], v12 offset:4096\n\t"
  "v_mfma_f32_16x16x32_bf16 v[136:139], v[44:47], a[248:251], v[136:139]\n\t"
  "ds_read_b64_tr_b16 v[38:39], v12 offset:6144\n\t"
  "v_mfma_f32_16x16x32_bf16 v[140:143], v[44:47], a[252:255], v[140:143]\n\t"
  "v_mfma_f32_16x16x32_bf16 v[140:143], v[40:43], a[236:239], v[140:143]\n\t"
  "s_waitcnt lgkmcnt(4)\n\t"
  "v_mfma_f32_16x16x32_bf16 v[160:163], v[48:51], a[224:227], v[160:163]\n\t"
  "ds_read_b64_tr_b16 v[40:41], v13 offset:0\n\t"
  "v_mfma_f32_16x16x32_bf16 v[160:163], v[52:55], a[240:243], v[160:163]\n\t"
  "ds_read_b64_tr_b16 v[42:43], v13 offset:2048\n\t"
  "v_mfma_f32_16x16x32_bf16 v[164:167], v[52:55], a[244:247], v[164:167]\n\t"
  "v_mfma_f32_16x16x32_bf16 v[164:167], v[48:51], a[228:231], v[164:167]\n\t"
  "v_mfma_f32_16x16x32_bf16 v[168:171], v[48:51], a[232:235], v[168:171]\n\t"
  "ds_read_b64_tr_b16 v[44:45], v13 offset:4096\n\t"
  "v_mfma_f32_16x16x32_bf16 v[168:171], v[52:55], a[248:251], v[168:171]\n\t"
  "ds_read_b64_tr_b16 v[46:47], v13 offset:6144\n\t"
  "v_mfma_f32_16x16x32_bf16 v[172:175], v[52:55], a[252:255], v[172:175]\n\t"
  "v_mfma_f32_16x16x32_bf16 v[172:175], v[48:51], a[236:239], v[172:175]\n\t"
  "s_waitcnt lgkmcnt(4)\n\t"
  "v_mfma_f32_16x16x32_bf16 v[192:195], v[32:35], a[0:3], 0\n\t"
  "ds_read_b64_tr_b16 v[48:49], v14 offset:0\n\t"
  "v_mfma_f32_16x16x32_bf16 v[192:195], v[36:39], a[16:19], v[192:195]\n\t"
  "ds_read_b64_tr_b16 v[50:51], v14 offset:2048\n\t"
  "v_mfma_f32_16x16x32_bf16 v[196:199], v[36:39], a[20:23], 0\n\t"
  "v_mfma_f32_16x16x32_bf16 v[196:199], v[32:35], a[4:7], v[196:199]\n\t"
  "v_mul_f32 v64, v22, v64\n\t"
  "v_mul_f32 v65, v22, v65\n\t"
  "v_mfma_f32_16x16x32_bf16 v[200:203], v[32:35], a[8:11], 0\n\t"
  "ds_read_b64_tr_b16 v[52:53], v14 offset:4096\n\t"
  "v_mul_f32 v66, v22, v66\n\t"
  "v_mfma_f32_16x16x32_bf16 v[200:203], v[36:39], a[24:27], v[200:203]\n\t"
  "ds_read_b64_tr_b16 v[54:55], v14 offset:6144\n\t"
  "v_mul_f32 v67, v22, v67\n\t"
  "v_mfma_f32_16x16x32_bf16 v[204:207], v[36:39], a[28:31], 0\n\t"
  "v_mul_f32 v96, v22, v96\n\t"
  "v_mul_f32 v97, v22, v97\n\t"
  "v_mfma_f32_16x16x32_bf16 v[204:207], v[32:35], a[12:15], v[204:207]\n\t"
  "v_mul_f32 v98, v22, v98\n\t"
  "s_waitcnt lgkmcnt(4)\n\t"
  "v_mfma_f32_16x16x32_bf16 v[224:227], v[40:43], a[0:3], 0\n\t"
  "ds_read_b64_tr_b16 v[32:33], v15 offset:0\n\t"
  "v_mul_f32 v99, v22, v99\n\t"
  "v_mfma_f32_16x16x32_bf16 v[224:227], v[44:47], a[16:19], v[224:227]\n\t"
  "ds_read_b64_tr_b16 v[34:35], v15 offset:2048\n\t"
  "v_cvt_pk_bf16_f32 v64, v64, v65\n\t"
  "v_mfma_f32_16x16x32_bf16 v[228:231], v[44:47], a[20:23], 0\n\t"
  "v_cvt_pk_bf16_f32 v65, v66, v67\n\t"
  "v_cvt_pk_bf16_f32 v66, v96, v97\n\t"
  "v_mfma_f32_16x16x32_bf16 v[228:231], v[40:43], a[4:7], v[228:231]\n\t"
  "v_cvt_pk_bf16_f32 v67, v98, v99\n\t"
  "v_mfma_f32_16x16x32_bf16 v[232:235], v[40:43], a[8:11], 0\n\t"
  "ds_read_b64_tr_b16 v[36:37], v15 offset:4096\n\t"
  "v_mfma_f32_16x16x32_bf16 v[232:235], v[44:47], a[24:27], v[232:235]\n\t"
  "ds_read_b64_tr_b16 v[38:39], v15 offset:6144\n\t"
  "v_mfma_f32_16x16x32_bf16 v[236:239], v[44:47], a[28:31], 0\n\t"
  "v_mfma_f32_16x16x32_bf16 v[236:239], v[40:43], a[12:15], v[236:239]\n\t"
  "buffer_store_dwordx4 v[64:67], v16, s[56:59], s81 offen offset:0 sc1\n\t"
  "s_waitcnt lgkmcnt(4)\n\t"
  "v_mfma_f32_16x16x32_bf16 v[80:83], v[48:51], a[0:3], 0\n\t"
  "ds_read_b64_tr_b16 v[40:41], v12 offset:8192\n\t"
  "v_mul_f32 v68, v23, v68\n\t"
  "v_mfma_f32_16x16x32_bf16 v[80:83], v[52:55], a[16:19], v[80:83]\n\t"
  "ds_read_b64_tr_b16 v[42:43], v12 offset:10240\n\t"
  "v_mul_f32 v69, v23, v69\n\t"
  "v_mfma_f32_16x16x32_bf16 v[84:87], v[52:55], a[20:23], 0\n\t"
  "v_mul_f32 v70, v23, v70\n\t"
  "v_mul_f32 v71, v23, v71\n\t"
  "v_mfma_f32_16x16x32_bf16 v[84:87], v[48:51], a[4:7], v[84:87]\n\t"
  "v_mul_f32 v100, v23, v100\n\t"
  "v_mul_f32 v101, v23, v101\n\t"
  "v_mfma_f32_16x16x32_bf16 v[88:91], v[48:51], a[8:11], 0\n\t"
  "ds_read_b64_tr_b16 v[44:45], v12 offset:12288\n\t"
  "v_mul_f32 v102, v23, v102\n\t"
  "v_mfma_f32_16x16x32_bf16 v[88:91], v[52:55], a[24:27], v[88:91]\n\t"
  "ds_read_b64_tr_b16 v[46:47], v12 offset:14336\n\t"
  "v_mul_f32 v103, v23, v103\n\t"
  "v_mfma_f32_16x16x32_bf16 v[92:95], v[52:55], a[28:31], 0\n\t"
  "v_cvt_pk_bf16_f32 v68, v68, v69\n\t"
  "v_cvt_pk_bf16_f32 v69, v70, v71\n\t"
  "v_mfma_f32_16x16x32_bf16 v[92:95], v[48:51], a[12:15], v[92:95]\n\t"
  "v_cvt_pk_bf16_f32 v70, v100, v101\n\t"
  "s_waitcnt lgkmcnt(4)\n\t"
  "v_mfma_f32_16x16x32_bf16 v[144:147], v[32:35], a[0:3], 0\n\t"
  "ds_read_b64_tr_b16 v[48:49], v13 offset:8192\n\t"
  "v_cvt_pk_bf16_f32 v71, v102, v103\n\t"
  "v_mfma_f32_16x16x32_bf16 v[144:147], v[36:39], a[16:19], v[144:147]\n\t"
  "ds_read_b64_tr_b16 v[50:51], v13 offset:10240\n\t"
  "v_mfma_f32_16x16x32_bf16 v[148:151], v[36:39], a[20:23], 0\n\t"
  "v_mfma_f32_16x16x32_bf16 v[148:151], v[32:35], a[4:7], v[148:151]\n\t"
  "v_mfma_f32_16x16x32_bf16 v[152:155], v[32:35], a[8:11], 0\n\t"
  "ds_read_b64_tr_b16 v[52:53], v13 offset:12288\n\t"
  "v_mfma_f32_16x16x32_bf16 v[152:155], v[36:39], a[24:27], v[152:155]\n\t"
  "ds_read_b64_tr_b16 v[54:55], v13 offset:14336\n\t"
  "v_mfma_f32_16x16x32_bf16 v[156:159], v[36:39], a[28:31], 0\n\t"
  "v_mfma_f32_16x16x32_bf16 v[156:159], v[32:35], a[12:15], v[156:159]\n\t"
  "s_waitcnt lgkmcnt(4)\n\t"
  "v_mfma_f32_16x16x32_bf16 v[192:195], v[40:43], a[32:35], v[192:195]\n\t"
  "ds_read_b64_tr_b16 v[32:33], v14 offset:8192\n\t"
  "v_accvgpr_write_b32 a0, v208\n\t"
  "v_mfma_f32_16x16x32_bf16 v[192:195], v[44:47], a[48:51], v[192:195]\n\t"
  "ds_read_b64_tr_b16 v[34:35], v14 offset:10240\n\t"
  "v_accvgpr_write_b32 a1, v209\n\t"
  "v_mfma_f32_16x16x32_bf16 v[196:199], v[44:47], a[52:55], v[196:199]\n\t"
  "v_accvgpr_write_b32 a2, v210\n\t"
  "v_accvgpr_write_b32 a3, v211\n\t"
  "v_mfma_f32_16x16x32_bf16 v[196:199], v[40:43], a[36:39], v[196:199]\n\t"
  "v_accvgpr_write_b32 a4, v212\n\t"
  "v_accvgpr_write_b32 a5, v213\n\t"
  "v_mfma_f32_16x16x32_bf16 v[200:203], v[40:43], a[40:43], v[200:203]\n\t"
  "ds_read_b64_tr_b16 v[36:37], v14 offset:12288\n\t"
  "v_accvgpr_write_b32 a6, v214\n\t"
  "v_mfma_f32_16x16x32_bf16 v[200:203], v[44:47], a[56:59], v[200:203]\n\t"
  "ds_read_b64_tr_b16 v[38:39], v14 offset:14336\n\t"
  "v_accvgpr_write_b32 a7, v215\n\t"
  "v_mfma_f32_16x16x32_bf16 v[204:207], v[44:47], a[60:63], v[204:207]\n\t"
  "v_accvgpr_write_b32 a8, v216\n\t"
  "v_accvgpr_write_b32 a9, v217\n\t"
  "v_mfma_f32_16x16x32_bf16 v[204:207], v[40:43], a[44:47], v[204:207]\n\t"
  "v_accvgpr_write_b32 a10, v218\n\t"
  "s_waitcnt lgkmcnt(4)\n\t"
  "v_mfma_f32_16x16x32_bf16 v[224:227], v[48:51], a[32:35], v[224:227]\n\t"
  "ds_read_b64_tr_b16 v[40:41], v15 offset:8192\n\t"
  "s_add_u32 m0, s69, 32768\n\t"
  "v_mfma_f32_16x16x32_bf16 v[224:227], v[52:55], a[48:51], v[224:227]\n\t"
  "ds_read_b64_tr_b16 v[42:43], v15 offset:10240\n\t"
  "s_bfe_u32 s70, s84, 0x40010\n\t"
  "v_mfma_f32_16x16x32_bf16 v[228:231], v[52:55], a[52:55], v[228:231]\n\t"
  "s_mul_i32 s70, s70, 57344\n\t"
  "v_accvgpr_write_b32 a11, v219\n\t"
  "v_mfma_f32_16x16x32_bf16 v[228:231], v[48:51], a[36:39], v[228:231]\n\t"
  "s_waitcnt vmcnt(31)\n\t"
  "s_barrier\n\t"
  "v_mfma_f32_16x16x32_bf16 v[232:235], v[48:51], a[40:43], v[232:235]\n\t"
  "ds_read_b64_tr_b16 v[44:45], v15 offset:12288\n\t"
  "v_accvgpr_write_b32 a12, v220\n\t"
  "v_mfma_f32_16x16x32_bf16 v[232:235], v[52:55], a[56:59], v[232:235]\n\t"
  "ds_read_b64_tr_b16 v[46:47], v15 offset:14336\n\t"
  "v_accvgpr_write_b32 a13, v221\n\t"
  "v_mfma_f32_16x16x32_bf16 v[236:239], v[52:55], a[60:63], v[236:239]\n\t"
  "buffer_load_dwordx4 v6, s[60:63], s70 offen offset:0  lds\n\t"
  "v_accvgpr_write_b32 a14, v222\n\t"
  "v_mfma_f32_16x16x32_bf16 v[236:239], v[48:51], a[44:47], v[236:239]\n\t"
  "buffer_store_dwordx4 v[68:71], v17, s[56:59], s81 offen offset:0 sc1\n\t"
  "s_waitcnt lgkmcnt(4)\n\t"
  "v_mfma_f32_16x16x32_bf16 v[80:83], v[32:35], a[32:35], v[80:83]\n\t"
  "ds_read_b64_tr_b16 v[48:49], v12 offset:16384\n\t"
  "v_mul_f32 v72, v24, v72\n\t"
  "v_mfma_f32_16x16x32_bf16 v[80:83], v[36:39], a[48:51], v[80:83]\n\t"
  "ds_read_b64_tr_b16 v[50:51], v12 offset:18432\n\t"
  "v_mul_f32 v73, v24, v73\n\t"
  "v_mfma_f32_16x16x32_bf16 v[84:87], v[36:39], a[52:55], v[84:87]\n\t"
  "v_mul_f32 v74, v24, v74\n\t"
  "v_mul_f32 v75, v24, v75\n\t"
  "v_mfma_f32_16x16x32_bf16 v[84:87], v[32:35], a[36:39], v[84:87]\n\t"
  "buffer_load_dwordx4 v7, s[60:63], s70 offen offset:1024  lds\n\t"
  "v_mul_f32 v104, v24, v104\n\t"
  "v_mfma_f32_16x16x32_bf16 v[88:91], v[32:35], a[40:43], v[88:91]\n\t"
  "ds_read_b64_tr_b16 v[52:53], v12 offset:20480\n\t"
  "v_mul_f32 v105, v24, v105\n\t"
  "v_mfma_f32_16x16x32_bf16 v[88:91], v[36:39], a[56:59], v[88:91]\n\t"
  "ds_read_b64_tr_b16 v[54:55], v12 offset:22528\n\t"
  "v_mul_f32 v106, v24, v106\n\t"
  "v_mfma_f32_16x16x32_bf16 v[92:95], v[36:39], a[60:63], v[92:95]\n\t"
  "v_mul_f32 v107, v24, v107\n\t"
  "v_cvt_pk_bf16_f32 v72, v72, v73\n\t"
  "v_mfma_f32_16x16x32_bf16 v[92:95], v[32:35], a[44:47], v[92:95]\n\t"
  "v_cvt_pk_bf16_f32 v73, v74, v75\n\t"
  "s_waitcnt lgkmcnt(4)\n\t"
  "v_mfma_f32_16x16x32_bf16 v[144:147], v[40:43], a[32:35], v[144:147]\n\t"
  "ds_read_b64_tr_b16 v[32:33], v13 offset:16384\n\t"
  "v_cvt_pk_bf16_f32 v74, v104, v105\n\t"
  "v_mfma_f32_16x16x32_bf16 v[144:147], v[44:47], a[48:51], v[144:147]\n\t"
  "ds_read_b64_tr_b16 v[34:35], v13 offset:18432\n\t"
  "v_cvt_pk_bf16_f32 v75, v106, v107\n\t"
  "v_mfma_f32_16x16x32_bf16 v[148:151], v[44:47], a[52:55], v[148:151]\n\t"
  "buffer_load_dwordx4 v8, s[60:63], s70 offen offset:2048  lds\n\t"
  "v_mov_b32 v27, s72\n\t"
  "v_mfma_f32_16x16x32_bf16 v[148:151], v[40:43], a[36:39], v[148:151]\n\t"
  "v_accvgpr_write_b32 a15, v223\n\t"
  "v_accvgpr_write_b32 a16, v240\n\t"
  "v_mfma_f32_16x16x32_bf16 v[152:155], v[40:43], a[40:43], v[152:155]\n\t"
  "ds_read_b64_tr_b16 v[36:37], v13 offset:20480\n\t"
  "v_accvgpr_write_b32 a17, v241\n\t"
  "v_mfma_f32_16x16x32_bf16 v[152:155], v[44:47], a[56:59], v[152:155]\n\t"
  "ds_read_b64_tr_b16 v[38:39], v13 offset:22528\n\t"
  "v_accvgpr_write_b32 a18, v242\n\t"
  "v_mfma_f32_16x16x32_bf16 v[156:159], v[44:47], a[60:63], v[156:159]\n\t"
  "v_accvgpr_write_b32 a19, v243\n\t"
  "v_accvgpr_write_b32 a20, v244\n\t"
  "v_mfma_f32_16x16x32_bf16 v[156:159], v[40:43], a[44:47], v[156:159]\n\t"
  "buffer_load_dwordx4 v9, s[60:63], s70 offen offset:3072  lds\n\t"
  "s_waitcnt lgkmcnt(4)\n\t"
  "v_mfma_f32_16x16x32_bf16 v[192:195], v[48:51], a[64:67], v[192:195]\n\t"
  "ds_read_b64_tr_b16 v[40:41], v14 offset:16384\n\t"
  "s_add_u32 m0, s69, 49152\n\t"
  "v_mfma_f32_16x16x32_bf16 v[192:195], v[52:55], a[80:83], v[192:195]\n\t"
  "ds_read_b64_tr_b16 v[42:43], v14 offset:18432\n\t"
  "s_bfe_u32 s70, s84, 0x40014\n\t"
  "s_waitcnt vmcnt(7)\n\t"
  "v_mfma_f32_16x16x32_bf16 v[196:199], v[52:55], a[84:87], v[196:199]\n\t"
  "buffer_store_dword v27, v21, s[92:95], 0 offen offset:2048 sc1\n\t"
  "s_mul_i32 s70, s70, 57344\n\t"
  "v_mfma_f32_16x16x32_bf16 v[196:199], v[48:51], a[68:71], v[196:199]\n\t"
  "v_accvgpr_write_b32 a21, v245\n\t"
  "v_accvgpr_write_b32 a22, v246\n\t"
  "v_mfma_f32_16x16x32_bf16 v[200:203], v[48:51], a[72:75], v[200:203]\n\t"
  "ds_read_b64_tr_b16 v[44:45], v14 offset:20480\n\t"
  "v_accvgpr_write_b32 a23, v247\n\t"
  "v_mfma_f32_16x16x32_bf16 v[200:203], v[52:55], a[88:91], v[200:203]\n\t"
  "ds_read_b64_tr_b16 v[46:47], v14 offset:22528\n\t"
  "v_accvgpr_write_b32 a24, v248\n\t"
  "v_mfma_f32_16x16x32_bf16 v[204:207], v[52:55], a[92:95], v[204:207]\n\t"
  "buffer_load_dwordx4 v6, s[60:63], s70 offen offset:0  lds\n\t"
  "v_accvgpr_write_b32 a25, v249\n\t"
  "v_mfma_f32_16x16x32_bf16 v[204:207], v[48:51], a[76:79], v[204:207]\n\t"
  "buffer_store_dwordx4 v[72:75], v18, s[56:59], s81 offen offset:0 sc1\n\t"
  "s_waitcnt lgkmcnt(4)\n\t"
  "v_mfma_f32_16x16x32_bf16 v[224:227], v[32:35], a[64:67], v[224:227]\n\t"
  "ds_read_b64_tr_b16 v[48:49], v15 offset:16384\n\t"
  "v_mul_f32 v76, v25, v76\n\t"
  "v_mfma_f32_16x16x32_bf16 v[224:227], v[36:39], a[80:83], v[224:227]\n\t"
  "ds_read_b64_tr_b16 v[50:51], v15 offset:18432\n\t"
  "v_mul_f32 v77, v25, v77\n\t"
  "v_mfma_f32_16x16x32_bf16 v[228:231], v[36:39], a[84:87], v[228:231]\n\t"
  "v_mul_f32 v78, v25, v78\n\t"
  "v_mul_f32 v79, v25, v79\n\t"
  "v_mfma_f32_16x16x32_bf16 v[228:231], v[32:35], a[68:71], v[228:231]\n\t"
  "buffer_load_dwordx4 v7, s[60:63], s70 offen offset:1024  lds\n\t"
  "v_mul_f32 v108, v25, v108\n\t"
  "v_mfma_f32_16x16x32_bf16 v[232:235], v[32:35], a[72:75], v[232:235]\n\t"
  "ds_read_b64_tr_b16 v[52:53], v15 offset:20480\n\t"
  "v_mul_f32 v109, v25, v109\n\t"
  "v_mfma_f32_16x16x32_bf16 v[232:235], v[36:39], a[88:91], v[232:235]\n\t"
  "ds_read_b64_tr_b16 v[54:55], v15 offset:22528\n\t"
  "v_mul_f32 v110, v25, v110\n\t"
  "v_mfma_f32_16x16x32_bf16 v[236:239], v[36:39], a[92:95], v[236:239]\n\t"
  "v_mul_f32 v111, v25, v111\n\t"
  "v_cvt_pk_bf16_f32 v76, v76, v77\n\t"
  "v_mfma_f32_16x16x32_bf16 v[236:239], v[32:35], a[76:79], v[236:239]\n\t"
  "v_cvt_pk_bf16_f32 v77, v78, v79\n\t"
  "s_waitcnt lgkmcnt(4)\n\t"
  "v_mfma_f32_16x16x32_bf16 v[80:83], v[40:43], a[64:67], v[80:83]\n\t"
  "ds_read_b64_tr_b16 v[32:33], v12 offset:24576\n\t"
  "v_cvt_pk_bf16_f32 v78, v108, v109\n\t"
  "v_mfma_f32_16x16x32_bf16 v[80:83], v[44:47], a[80:83], v[80:83]\n\t"
  "ds_read_b64_tr_b16 v[34:35], v12 offset:26624\n\t"
  "v_cvt_pk_bf16_f32 v79, v110, v111\n\t"
  "v_mfma_f32_16x16x32_bf16 v[84:87], v[44:47], a[84:87], v[84:87]\n\t"
  "buffer_load_dwordx4 v8, s[60:63], s70 offen offset:2048  lds\n\t"
  "v_accvgpr_write_b32 a26, v250\n\t"
  "v_mfma_f32_16x16x32_bf16 v[84:87], v[40:43], a[68:71], v[84:87]\n\t"
  "v_accvgpr_write_b32 a27, v251\n\t"
  "v_accvgpr_write_b32 a28, v252\n\t"
  "v_mfma_f32_16x16x32_bf16 v[88:91], v[40:43], a[72:75], v[88:91]\n\t"
  "ds_read_b64_tr_b16 v[36:37], v12 offset:28672\n\t"
  "v_accvgpr_write_b32 a29, v253\n\t"
  "v_mfma_f32_16x16x32_bf16 v[88:91], v[44:47], a[88:91], v[88:91]\n\t"
  "ds_read_b64_tr_b16 v[38:39], v12 offset:30720\n\t"
  "v_accvgpr_write_b32 a30, v254\n\t"
  "v_mfma_f32_16x16x32_bf16 v[92:95], v[44:47], a[92:95], v[92:95]\n\t"
  "v_accvgpr_write_b32 a31, v255\n\t"
  "v_accvgpr_write_b32 a32, v112\n\t"
  "v_mfma_f32_16x16x32_bf16 v[92:95], v[40:43], a[76:79], v[92:95]\n\t"
  "buffer_load_dwordx4 v9, s[60:63], s70 offen offset:3072  lds\n\t"
  "s_waitcnt lgkmcnt(4)\n\t"
  "v_mfma_f32_16x16x32_bf16 v[144:147], v[48:51], a[64:67], v[144:147]\n\t"
  "ds_read_b64_tr_b16 v[40:41], v13 offset:24576\n\t"
  "v_accvgpr_write_b32 a33, v113\n\t"
  "v_mfma_f32_16x16x32_bf16 v[144:147], v[52:55], a[80:83], v[144:147]\n\t"
  "ds_read_b64_tr_b16 v[42:43], v13 offset:26624\n\t"
  "v_accvgpr_write_b32 a34, v114\n\t"
  "v_mfma_f32_16x16x32_bf16 v[148:151], v[52:55], a[84:87], v[148:151]\n\t"
  "v_accvgpr_write_b32 a35, v115\n\t"
  "v_accvgpr_write_b32 a36, v116\n\t"
  "v_mfma_f32_16x16x32_bf16 v[148:151], v[48:51], a[68:71], v[148:151]\n\t"
  "v_accvgpr_write_b32 a37, v117\n\t"
  "v_accvgpr_write_b32 a38, v118\n\t"
  "v_mfma_f32_16x16x32_bf16 v[152:155], v[48:51], a[72:75], v[152:155]\n\t"
  "ds_read_b64_tr_b16 v[44:45], v13 offset:28672\n\t"
  "v_accvgpr_write_b32 a39, v119\n\t"
  "v_mfma_f32_16x16x32_bf16 v[152:155], v[52:55], a[88:91], v[152:155]\n\t"
  "ds_read_b64_tr_b16 v[46:47], v13 offset:30720\n\t"
  "v_accvgpr_write_b32 a40, v120\n\t"
  "v_mfma_f32_16x16x32_bf16 v[156:159], v[52:55], a[92:95], v[156:159]\n\t"
  "v_accvgpr_write_b32 a41, v121\n\t"
  "v_accvgpr_write_b32 a42, v122\n\t"
  "v_mfma_f32_16x16x32_bf16 v[156:159], v[48:51], a[76:79], v[156:159]\n\t"
  "v_accvgpr_write_b32 a43, v123\n\t"
  "s_waitcnt lgkmcnt(4)\n\t"
  "v_mfma_f32_16x16x32_bf16 v[192:195], v[32:35], a[96:99], v[192:195]\n\t"
  "ds_read_b64_tr_b16 v[48:49], v14 offset:24576\n\t"
  "v_accvgpr_write_b32 a44, v124\n\t"
  "v_mfma_f32_16x16x32_bf16 v[192:195], v[36:39], a[112:115], v[192:195]\n\t"
  "ds_read_b64_tr_b16 v[50:51], v14 offset:26624\n\t"
  "v_accvgpr_write_b32 a45, v125\n\t"
  "v_mfma_f32_16x16x32_bf16 v[196:199], v[36:39], a[116:119], v[196:199]\n\t"
  "v_accvgpr_write_b32 a46, v126\n\t"
  "v_accvgpr_write_b32 a47, v127\n\t"
  "v_mfma_f32_16x16x32_bf16 v[196:199], v[32:35], a[100:103], v[196:199]\n\t"
  "buffer_store_dwordx4 v[76:79], v19, s[56:59], s81 offen offset:0 sc1\n\t"
  "v_mul_f32 v128, v22, v128\n\t"
  "v_mfma_f32_16x16x32_bf16 v[200:203], v[32:35], a[104:107], v[200:203]\n\t"
  "ds_read_b64_tr_b16 v[52:53], v14 offset:28672\n\t"
  "v_mul_f32 v129, v22, v129\n\t"
  "v_mfma_f32_16x16x32_bf16 v[200:203], v[36:39], a[120:123], v[200:203]\n\t"
  "ds_read_b64_tr_b16 v[54:55], v14 offset:30720\n\t"
  "v_mul_f32 v130, v22, v130\n\t"
  "v_mfma_f32_16x16x32_bf16 v[204:207], v[36:39], a[124:127], v[204:207]\n\t"
  "v_mul_f32 v131, v22, v131\n\t"
  "v_mul_f32 v160, v22, v160\n\t"
  "v_mfma_f32_16x16x32_bf16 v[204:207], v[32:35], a[108:111], v[204:207]\n\t"
  "v_mul_f32 v161, v22, v161\n\t"
  "s_waitcnt lgkmcnt(4)\n\t"
  "v_mfma_f32_16x16x32_bf16 v[224:227], v[40:43], a[96:99], v[224:227]\n\t"
  "ds_read_b64_tr_b16 v[32:33], v15 offset:24576\n\t"
  "v_mul_f32 v162, v22, v162\n\t"
  "v_mfma_f32_16x16x32_bf16 v[224:227], v[44:47], a[112:115], v[224:227]\n\t"
  "ds_read_b64_tr_b16 v[34:35], v15 offset:26624\n\t"
  "v_mul_f32 v163, v22, v163\n\t"
  "v_mfma_f32_16x16x32_bf16 v[228:231], v[44:47], a[116:119], v[228:231]\n\t"
  "v_cvt_pk_bf16_f32 v128, v128, v129\n\t"
  "v_cvt_pk_bf16_f32 v129, v130, v131\n\t"
  "v_mfma_f32_16x16x32_bf16 v[228:231], v[40:43], a[100:103], v[228:231]\n\t"
  "v_cvt_pk_bf16_f32 v130, v160, v161\n\t"
  "v_cvt_pk_bf16_f32 v131, v162, v163\n\t"
  "v_mfma_f32_16x16x32_bf16 v[232:235], v[40:43], a[104:107], v[232:235]\n\t"
  "ds_read_b64_tr_b16 v[36:37], v15 offset:28672\n\t"
  "v_accvgpr_write_b32 a48, v176\n\t"
  "v_mfma_f32_16x16x32_bf16 v[232:235], v[44:47], a[120:123], v[232:235]\n\t"
  "ds_read_b64_tr_b16 v[38:39], v15 offset:30720\n\t"
  "v_add_u32 v12, 0x10000, v12\n\t"
  "v_mfma_f32_16x16x32_bf16 v[236:239], v[44:47], a[124:127], v[236:239]\n\t"
  "v_add_u32 v13, 0x10000, v13\n\t"
  "v_add_u32 v14, 0x10000, v14\n\t"
  "v_mfma_f32_16x16x32_bf16 v[236:239], v[40:43], a[108:111], v[236:239]\n\t"
  "v_add_u32 v15, 0x10000, v15\n\t"
  "s_waitcnt lgkmcnt(4)\n\t"
  "v_mfma_f32_16x16x32_bf16 v[80:83], v[48:51], a[96:99], v[80:83]\n\t"
  "ds_read_b64_tr_b16 v[40:41], v12 offset:32768\n\t"
  "v_accvgpr_write_b32 a49, v177\n\t"
  "v_mfma_f32_16x16x32_bf16 v[80:83], v[52:55], a[112:115], v[80:83]\n\t"
  "ds_read_b64_tr_b16 v[42:43], v12 offset:34816\n\t"
  "v_accvgpr_write_b32 a50, v178\n\t"
  "v_mfma_f32_16x16x32_bf16 v[84:87], v[52:55], a[116:119], v[84:87]\n\t"
  "v_accvgpr_write_b32 a51, v179\n\t"
  "v_accvgpr_write_b32 a52, v180\n\t"
  "v_mfma_f32_16x16x32_bf16 v[84:87], v[48:51], a[100:103], v[84:87]\n\t"
  "v_accvgpr_write_b32 a53, v181\n\t"
  "v_accvgpr_write_b32 a54, v182\n\t"
  "v_mfma_f32_16x16x32_bf16 v[88:91], v[48:51], a[104:107], v[88:91]\n\t"
  "ds_read_b64_tr_b16 v[44:45], v12 offset:36864\n\t"
  "v_accvgpr_write_b32 a55, v183\n\t"
  "v_mfma_f32_16x16x32_bf16 v[88:91], v[52:55], a[120:123], v[88:91]\n\t"
  "ds_read_b64_tr_b16 v[46:47], v12 offset:38912\n\t"
  "v_accvgpr_write_b32 a56, v184\n\t"
  "v_mfma_f32_16x16x32_bf16 v[92:95], v[52:55], a[124:127], v[92:95]\n\t"
  "v_accvgpr_write_b32 a57, v185\n\t"
  "v_accvgpr_write_b32 a58, v186\n\t"
  "v_mfma_f32_16x16x32_bf16 v[92:95], v[48:51], a[108:111], v[92:95]\n\t"
  "v_accvgpr_write_b32 a59, v187\n\t"
  "s_waitcnt lgkmcnt(4)\n\t"
  "v_mfma_f32_16x16x32_bf16 v[144:147], v[32:35], a[96:99], v[144:147]\n\t"
  "ds_read_b64_tr_b16 v[48:49], v13 offset:32768\n\t"
  "v_accvgpr_write_b32 a60, v188\n\t"
  "v_mfma_f32_16x16x32_bf16 v[144:147], v[36:39], a[112:115], v[144:147]\n\t"
  "ds_read_b64_tr_b16 v[50:51], v13 offset:34816\n\t"
  "v_accvgpr_write_b32 a61, v189\n\t"
  "v_mfma_f32_16x16x32_bf16 v[148:151], v[36:39], a[116:119], v[148:151]\n\t"
  "v_accvgpr_write_b32 a62, v190\n\t"
  "v_accvgpr_write_b32 a63, v191\n\t"
  "v_mfma_f32_16x16x32_bf16 v[148:151], v[32:35], a[100:103], v[148:151]\n\t"
  "v_accvgpr_write_b32 a64, v64\n\t"
  "v_accvgpr_write_b32 a65, v65\n\t"
  "v_mfma_f32_16x16x32_bf16 v[152:155], v[32:35], a[104:107], v[152:155]\n\t"
  "ds_read_b64_tr_b16 v[52:53], v13 offset:36864\n\t"
  "v_accvgpr_write_b32 a66, v66\n\t"
  "v_mfma_f32_16x16x32_bf16 v[152:155], v[36:39], a[120:123], v[152:155]\n\t"
  "ds_read_b64_tr_b16 v[54:55], v13 offset:38912\n\t"
  "v_accvgpr_write_b32 a67, v67\n\t"
  "v_mfma_f32_16x16x32_bf16 v[156:159], v[36:39], a[124:127], v[156:159]\n\t"
  "v_accvgpr_write_b32 a68, v68\n\t"
  "v_accvgpr_write_b32 a69, v69\n\t"
  "v_mfma_f32_16x16x32_bf16 v[156:159], v[32:35], a[108:111], v[156:159]\n\t"
  "buffer_store_dwordx4 v[128:131], v16, s[56:59], s81 offen offset:1024 sc1\n\t"
  "s_waitcnt lgkmcnt(4)\n\t"
  "v_mfma_f32_16x16x32_bf16 v[192:195], v[40:43], a[128:131], v[192:195]\n\t"
  "ds_read_b64_tr_b16 v[32:33], v14 offset:32768\n\t"
  "v_mul_f32 v132, v23, v132\n\t"
  "v_mfma_f32_16x16x32_bf16 v[192:195], v[44:47], a[144:147], v[192:195]\n\t"
  "ds_read_b64_tr_b16 v[34:35], v14 offset:34816\n\t"
  "v_mul_f32 v133, v23, v133\n\t"
  "v_mfma_f32_16x16x32_bf16 v[196:199], v[44:47], a[148:151], v[196:199]\n\t"
  "v_mul_f32 v134, v23, v134\n\t"
  "v_mul_f32 v135, v23, v135\n\t"
  "v_mfma_f32_16x16x32_bf16 v[196:199], v[40:43], a[132:135], v[196:199]\n\t"
  "v_mul_f32 v164, v23, v164\n\t"
  "v_mul_f32 v165, v23, v165\n\t"
  "v_mfma_f32_16x16x32_bf16 v[200:203], v[40:43], a[136:139], v[200:203]\n\t"
  "ds_read_b64_tr_b16 v[36:37], v14 offset:36864\n\t"
  "v_mul_f32 v166, v23, v166\n\t"
  "v_mfma_f32_16x16x32_bf16 v[200:203], v[44:47], a[152:155], v[200:203]\n\t"
  "ds_read_b64_tr_b16 v[38:39], v14 offset:38912\n\t"
  "v_mul_f32 v167, v23, v167\n\t"
  "v_mfma_f32_16x16x32_bf16 v[204:207], v[44:47], a[156:159], v[204:207]\n\t"
  "v_cvt_pk_bf16_f32 v132, v132, v133\n\t"
  "v_cvt_pk_bf16_f32 v133, v134, v135\n\t"
  "v_mfma_f32_16x16x32_bf16 v[204:207], v[40:43], a[140:143], v[204:207]\n\t"
  "v_cvt_pk_bf16_f32 v134, v164, v165\n\t"
  "s_waitcnt lgkmcnt(4)\n\t"
  "v_mfma_f32_16x16x32_bf16 v[224:227], v[48:51], a[128:131], v[224:227]\n\t"
  "ds_read_b64_tr_b16 v[40:41], v15 offset:32768\n\t"
  "v_cvt_pk_bf16_f32 v135, v166, v167\n\t"
  "v_mfma_f32_16x16x32_bf16 v[224:227], v[52:55], a[144:147], v[224:227]\n\t"
  "ds_read_b64_tr_b16 v[42:43], v15 offset:34816\n\t"
  "v_accvgpr_write_b32 a70, v70\n\t"
  "v_mfma_f32_16x16x32_bf16 v[228:231], v[52:55], a[148:151], v[228:231]\n\t"
  "buffer_load_dword v26, v30, s[92:95], 0 offen offset:2048 sc1\n\t"
  "v_accvgpr_write_b32 a71, v71\n\t"
  "v_mfma_f32_16x16x32_bf16 v[228:231], v[48:51], a[132:135], v[228:231]\n\t"
  "v_accvgpr_write_b32 a72, v72\n\t"
  "v_accvgpr_write_b32 a73, v73\n\t"
  "v_mfma_f32_16x16x32_bf16 v[232:235], v[48:51], a[136:139], v[232:235]\n\t"
  "ds_read_b64_tr_b16 v[44:45], v15 offset:36864\n\t"
  "v_accvgpr_write_b32 a74, v74\n\t"
  "v_mfma_f32_16x16x32_bf16 v[232:235], v[52:55], a[152:155], v[232:235]\n\t"
  "ds_read_b64_tr_b16 v[46:47], v15 offset:38912\n\t"
  "v_accvgpr_write_b32 a75, v75\n\t"
  "v_mfma_f32_16x16x32_bf16 v[236:239], v[52:55], a[156:159], v[236:239]\n\t"
  "v_accvgpr_write_b32 a76, v76\n\t"
  "v_accvgpr_write_b32 a77, v77\n\t"
  "v_mfma_f32_16x16x32_bf16 v[236:239], v[48:51], a[140:143], v[236:239]\n\t"
  "v_accvgpr_write_b32 a78, v78\n\t"
  "s_waitcnt lgkmcnt(4)\n\t"
  "v_mfma_f32_16x16x32_bf16 v[80:83], v[32:35], a[128:131], v[80:83]\n\t"
  "ds_read_b64_tr_b16 v[48:49], v12 offset:40960\n\t"
  "v_accvgpr_write_b32 a79, v79\n\t"
  "v_mfma_f32_16x16x32_bf16 v[80:83], v[36:39], a[144:147], v[80:83]\n\t"
  "ds_read_b64_tr_b16 v[50:51], v12 offset:43008\n\t"
  "v_accvgpr_write_b32 a80, v128\n\t"
  "v_mfma_f32_16x16x32_bf16 v[84:87], v[36:39], a[148:151], v[84:87]\n\t"
  "v_accvgpr_write_b32 a81, v129\n\t"
  "v_accvgpr_write_b32 a82, v130\n\t"
  "v_mfma_f32_16x16x32_bf16 v[84:87], v[32:35], a[132:135], v[84:87]\n\t"
  "v_accvgpr_write_b32 a83, v131\n\t"
  "v_accvgpr_write_b32 a84, v132\n\t"
  "v_mfma_f32_16x16x32_bf16 v[88:91], v[32:35], a[136:139], v[88:91]\n\t"
  "ds_read_b64_tr_b16 v[52:53], v12 offset:45056\n\t"
  "v_accvgpr_write_b32 a85, v133\n\t"
  "v_mfma_f32_16x16x32_bf16 v[88:91], v[36:39], a[152:155], v[88:91]\n\t"
  "ds_read_b64_tr_b16 v[54:55], v12 offset:47104\n\t"
  "v_accvgpr_write_b32 a86, v134\n\t"
  "v_mfma_f32_16x16x32_bf16 v[92:95], v[36:39], a[156:159], v[92:95]\n\t"
  "v_accvgpr_write_b32 a87, v135\n\t"
  "v_mfma_f32_16x16x32_bf16 v[92:95], v[32:35], a[140:143], v[92:95]\n\t"
  "s_waitcnt lgkmcnt(4)\n\t"
  "v_mfma_f32_16x16x32_bf16 v[144:147], v[40:43], a[128:131], v[144:147]\n\t"
  "ds_read_b64_tr_b16 v[32:33], v13 offset:40960\n\t"
  "v_mfma_f32_16x16x32_bf16 v[144:147], v[44:47], a[144:147], v[144:147]\n\t"
  "ds_read_b64_tr_b16 v[34:35], v13 offset:43008\n\t"
  "v_mfma_f32_16x16x32_bf16 v[148:151], v[44:47], a[148:151], v[148:151]\n\t"
  "v_mfma_f32_16x16x32_bf16 v[148:151], v[40:43], a[132:135], v[148:151]\n\t"
  "buffer_store_dwordx4 v[132:135], v17, s[56:59], s81 offen offset:1024 sc1\n\t"
  "v_mul_f32 v136, v24, v136\n\t"
  "v_mfma_f32_16x16x32_bf16 v[152:155], v[40:43], a[136:139], v[152:155]\n\t"
  "ds_read_b64_tr_b16 v[36:37], v13 offset:45056\n\t"
  "v_mul_f32 v137, v24, v137\n\t"
  "v_mfma_f32_16x16x32_bf16 v[152:155], v[44:47], a[152:155], v[152:155]\n\t"
  "ds_read_b64_tr_b16 v[38:39], v13 offset:47104\n\t"
  "v_mul_f32 v138, v24, v138\n\t"
  "v_mfma_f32_16x16x32_bf16 v[156:159], v[44:47], a[156:159], v[156:159]\n\t"
  "v_mul_f32 v139, v24, v139\n\t"
  "v_mul_f32 v168, v24, v168\n\t"
  "v_mfma_f32_16x16x32_bf16 v[156:159], v[40:43], a[140:143], v[156:159]\n\t"
  "v_mul_f32 v169, v24, v169\n\t"
  "s_waitcnt lgkmcnt(4)\n\t"
  "v_mfma_f32_16x16x32_bf16 v[192:195], v[48:51], a[160:163], v[192:195]\n\t"
  "ds_read_b64_tr_b16 v[40:41], v14 offset:40960\n\t"
  "v_mul_f32 v170, v24, v170\n\t"
  "v_mfma_f32_16x16x32_bf16 v[192:195], v[52:55], a[176:179], v[192:195]\n\t"
  "ds_read_b64_tr_b16 v[42:43], v14 offset:43008\n\t"
  "v_mul_f32 v171, v24, v171\n\t"
  "v_mfma_f32_16x16x32_bf16 v[196:199], v[52:55], a[180:183], v[196:199]\n\t"
  "v_cvt_pk_bf16_f32 v136, v136, v137\n\t"
  "v_cvt_pk_bf16_f32 v137, v138, v139\n\t"
  "v_mfma_f32_16x16x32_bf16 v[196:199], v[48:51], a[164:167], v[196:199]\n\t"
  "v_cvt_pk_bf16_f32 v138, v168, v169\n\t"
  "v_cvt_pk_bf16_f32 v139, v170, v171\n\t"
  "v_mfma_f32_16x16x32_bf16 v[200:203], v[48:51], a[168:171], v[200:203]\n\t"
  "ds_read_b64_tr_b16 v[44:45], v14 offset:45056\n\t"
  "v_accvgpr_write_b32 a88, v136\n\t"
  "v_mfma_f32_16x16x32_bf16 v[200:203], v[52:55], a[184:187], v[200:203]\n\t"
  "ds_read_b64_tr_b16 v[46:47], v14 offset:47104\n\t"
  "v_accvgpr_write_b32 a89, v137\n\t"
  "v_mfma_f32_16x16x32_bf16 v[204:207], v[52:55], a[188:191], v[204:207]\n\t"
  "v_accvgpr_write_b32 a90, v138\n\t"
  "v_accvgpr_write_b32 a91, v139\n\t"
  "v_mfma_f32_16x16x32_bf16 v[204:207], v[48:51], a[172:175], v[204:207]\n\t"
  "s_waitcnt lgkmcnt(4)\n\t"
  "v_mfma_f32_16x16x32_bf16 v[224:227], v[32:35], a[160:163], v[224:227]\n\t"
  "ds_read_b64_tr_b16 v[48:49], v15 offset:40960\n\t"
  "s_add_u32 m0, s69, 0\n\t"
  "v_mfma_f32_16x16x32_bf16 v[224:227], v[36:39], a[176:179], v[224:227]\n\t"
  "ds_read_b64_tr_b16 v[50:51], v15 offset:43008\n\t"
  "s_bfe_u32 s70, s84, 0x40018\n\t"
  "v_mfma_f32_16x16x32_bf16 v[228:231], v[36:39], a[180:183], v[228:231]\n\t"
  "s_mul_i32 s70, s70, 57344\n\t"
  "v_mfma_f32_16x16x32_bf16 v[228:231], v[32:35], a[164:167], v[228:231]\n\t"
  "s_barrier\n\t"
  "v_mfma_f32_16x16x32_bf16 v[232:235], v[32:35], a[168:171], v[232:235]\n\t"
  "ds_read_b64_tr_b16 v[52:53], v15 offset:45056\n\t"
  "v_mfma_f32_16x16x32_bf16 v[232:235], v[36:39], a[184:187], v[232:235]\n\t"
  "ds_read_b64_tr_b16 v[54:55], v15 offset:47104\n\t"
  "v_mfma_f32_16x16x32_bf16 v[236:239], v[36:39], a[188:191], v[236:239]\n\t"
  "buffer_load_dwordx4 v6, s[60:63], s70 offen offset:0  lds\n\t"
  "v_mfma_f32_16x16x32_bf16 v[236:239], v[32:35], a[172:175], v[236:239]\n\t"
  "s_waitcnt lgkmcnt(4)\n\t"
  "v_mfma_f32_16x16x32_bf16 v[80:83], v[40:43], a[160:163], v[80:83]\n\t"
  "ds_read_b64_tr_b16 v[32:33], v12 offset:49152\n\t"
  "v_mfma_f32_16x16x32_bf16 v[80:83], v[44:47], a[176:179], v[80:83]\n\t"
  "ds_read_b64_tr_b16 v[34:35], v12 offset:51200\n\t"
  "v_mfma_f32_16x16x32_bf16 v[84:87], v[44:47], a[180:183], v[84:87]\n\t"
  "v_mfma_f32_16x16x32_bf16 v[84:87], v[40:43], a[164:167], v[84:87]\n\t"
  "buffer_load_dwordx4 v7, s[60:63], s70 offen offset:1024  lds\n\t"
  "s_waitcnt vmcnt(3)\n\t"
  "v_subrev_u32 v26, s72, v26\n\t"
  "v_mfma_f32_16x16x32_bf16 v[88:91], v[40:43], a[168:171], v[88:91]\n\t"
  "ds_read_b64_tr_b16 v[36:37], v12 offset:53248\n\t"
  "v_cmp_lt_u32 vcc, 3, v26\n\t"
  "v_mfma_f32_16x16x32_bf16 v[88:91], v[44:47], a[184:187], v[88:91]\n\t"
  "ds_read_b64_tr_b16 v[38:39], v12 offset:55296\n\t"
  "s_nop 4\n\t"
  "v_mfma_f32_16x16x32_bf16 v[92:95], v[44:47], a[188:191], v[92:95]\n\t"
  "s_cbranch_vccnz L_k1_sp1\n\t"
  "L_k1_rs1:\n\t"
  "v_mfma_f32_16x16x32_bf16 v[92:95], v[40:43], a[172:175], v[92:95]\n\t"
  "buffer_store_dwordx4 v[136:139], v18, s[56:59], s81 offen offset:1024 sc1\n\t"
  "s_waitcnt lgkmcnt(4)\n\t"
  "v_mfma_f32_16x16x32_bf16 v[144:147], v[48:51], a[160:163], v[144:147]\n\t"
  "ds_read_b64_tr_b16 v[40:41], v13 offset:49152\n\t"
  "v_mul_f32 v140, v25, v140\n\t"
  "v_mfma_f32_16x16x32_bf16 v[144:147], v[52:55], a[176:179], v[144:147]\n\t"
  "ds_read_b64_tr_b16 v[42:43], v13 offset:51200\n\t"
  "v_mul_f32 v141, v25, v141\n\t"
  "v_mfma_f32_16x16x32_bf16 v[148:151], v[52:55], a[180:183], v[148:151]\n\t"
  "buffer_load_dwordx4 v8, s[60:63], s70 offen offset:2048  lds\n\t"
  "v_mul_f32 v142, v25, v142\n\t"
  "v_mfma_f32_16x16x32_bf16 v[148:151], v[48:51], a[164:167], v[148:151]\n\t"
  "v_mul_f32 v143, v25, v143\n\t"
  "v_mul_f32 v172, v25, v172\n\t"
  "v_mfma_f32_16x16x32_bf16 v[152:155], v[48:51], a[168:171], v[152:155]\n\t"
  "ds_read_b64_tr_b16 v[44:45], v13 offset:53248\n\t"
  "v_mul_f32 v173, v25, v173\n\t"
  "v_mfma_f32_16x16x32_bf16 v[152:155], v[52:55], a[184:187], v[152:155]\n\t"
  "ds_read_b64_tr_b16 v[46:47], v13 offset:55296\n\t"
  "v_mul_f32 v174, v25, v174\n\t"
  "v_mfma_f32_16x16x32_bf16 v[156:159], v[52:55], a[188:191], v[156:159]\n\t"
  "v_mul_f32 v175, v25, v175\n\t"
  "v_cvt_pk_bf16_f32 v140, v140, v141\n\t"
  "v_mfma_f32_16x16x32_bf16 v[156:159], v[48:51], a[172:175], v[156:159]\n\t"
  "buffer_load_dwordx4 v9, s[60:63], s70 offen offset:3072  lds\n\t"
  "s_waitcnt lgkmcnt(4)\n\t"
  "v_mfma_f32_16x16x32_bf16 v[192:195], v[32:35], a[192:195], v[192:195]\n\t"
  "ds_read_b64_tr_b16 v[48:49], v14 offset:49152\n\t"
  "v_cvt_pk_bf16_f32 v141, v142, v143\n\t"
  "v_mfma_f32_16x16x32_bf16 v[192:195], v[36:39], a[208:211], v[192:195]\n\t"
  "ds_read_b64_tr_b16 v[50:51], v14 offset:51200\n\t"
  "v_cvt_pk_bf16_f32 v142, v172, v173\n\t"
  "v_mfma_f32_16x16x32_bf16 v[196:199], v[36:39], a[212:215], v[196:199]\n\t"
  "v_cvt_pk_bf16_f32 v143, v174, v175\n\t"
  "v_mfma_f32_16x16x32_bf16 v[196:199], v[32:35], a[196:199], v[196:199]\n\t"
  "v_accvgpr_write_b32 a92, v140\n\t"
  "v_accvgpr_write_b32 a93, v141\n\t"
  "v_mfma_f32_16x16x32_bf16 v[200:203], v[32:35], a[200:203], v[200:203]\n\t"
  "ds_read_b64_tr_b16 v[52:53], v14 offset:53248\n\t"
  "v_accvgpr_write_b32 a94, v142\n\t"
  "v_mfma_f32_16x16x32_bf16 v[200:203], v[36:39], a[216:219], v[200:203]\n\t"
  "ds_read_b64_tr_b16 v[54:55], v14 offset:55296\n\t"
  "v_accvgpr_write_b32 a95, v143\n\t"
  "s_bfe_u32 s71, s84, 0x40004\n\t"
  "s_lshl_b32 s71, s71, 11\n\t"
  "v_mfma_f32_16x16x32_bf16 v[204:207], v[36:39], a[220:223], v[204:207]\n\t"
  "buffer_load_dwordx4 a[96:99], v10, s[40:43], s71 offen offset:0 sc1\n\t"
  "v_mfma_f32_16x16x32_bf16 v[204:207], v[32:35], a[204:207], v[204:207]\n\t"
  "s_waitcnt lgkmcnt(4)\n\t"
  "v_mfma_f32_16x16x32_bf16 v[224:227], v[40:43], a[192:195], v[224:227]\n\t"
  "ds_read_b64_tr_b16 v[32:33], v15 offset:49152\n\t"
  "v_mfma_f32_16x16x32_bf16 v[224:227], v[44:47], a[208:211], v[224:227]\n\t"
  "ds_read_b64_tr_b16 v[34:35], v15 offset:51200\n\t"
  "v_mfma_f32_16x16x32_bf16 v[228:231], v[44:47], a[212:215], v[228:231]\n\t"
  "v_mfma_f32_16x16x32_bf16 v[228:231], v[40:43], a[196:199], v[228:231]\n\t"
  "buffer_load_dwordx4 a[100:103], v11, s[40:43], s71 offen offset:0 sc1\n\t"
  "v_mfma_f32_16x16x32_bf16 v[232:235], v[40:43], a[200:203], v[232:235]\n\t"
  "ds_read_b64_tr_b16 v[36:37], v15 offset:53248\n\t"
  "v_mfma_f32_16x16x32_bf16 v[232:235], v[44:47], a[216:219], v[232:235]\n\t"
  "ds_read_b64_tr_b16 v[38:39], v15 offset:55296\n\t"
  "v_mfma_f32_16x16x32_bf16 v[236:239], v[44:47], a[220:223], v[236:239]\n\t"
  "v_mfma_f32_16x16x32_bf16 v[236:239], v[40:43], a[204:207], v[236:239]\n\t"
  "s_waitcnt lgkmcnt(4)\n\t"
  "v_mfma_f32_16x16x32_bf16 v[80:83], v[48:51], a[192:195], v[80:83]\n\t"
  "ds_read_b64_tr_b16 v[40:41], v12 offset:57344\n\t"
  "v_mfma_f32_16x16x32_bf16 v[80:83], v[52:55], a[208:211], v[80:83]\n\t"
  "ds_read_b64_tr_b16 v[42:43], v12 offset:59392\n\t"
  "v_mfma_f32_16x16x32_bf16 v[84:87], v[52:55], a[212:215], v[84:87]\n\t"
  "v_mfma_f32_16x16x32_bf16 v[84:87], v[48:51], a[196:199], v[84:87]\n\t"
  "buffer_store_dwordx4 v[140:143], v19, s[56:59], s81 offen offset:1024 sc1\n\t"
  "v_mfma_f32_16x16x32_bf16 v[88:91], v[48:51], a[200:203], v[88:91]\n\t"
  "ds_read_b64_tr_b16 v[44:45], v12 offset:61440\n\t"
  "v_mfma_f32_16x16x32_bf16 v[88:91], v[52:55], a[216:219], v[88:91]\n\t"
  "ds_read_b64_tr_b16 v[46:47], v12 offset:63488\n\t"
  "v_mfma_f32_16x16x32_bf16 v[92:95], v[52:55], a[220:223], v[92:95]\n\t"
  "v_mfma_f32_16x16x32_bf16 v[92:95], v[48:51], a[204:207], v[92:95]\n\t"
  "s_waitcnt lgkmcnt(4)\n\t"
  "v_mfma_f32_16x16x32_bf16 v[144:147], v[32:35], a[192:195], v[144:147]\n\t"
  "ds_read_b64_tr_b16 v[48:49], v13 offset:57344\n\t"
  "v_mfma_f32_16x16x32_bf16 v[144:147], v[36:39], a[208:211], v[144:147]\n\t"
  "ds_read_b64_tr_b16 v[50:51], v13 offset:59392\n\t"
  "v_mfma_f32_16x16x32_bf16 v[148:151], v[36:39], a[212:215], v[148:151]\n\t"
  "buffer_load_dwordx4 a[104:107], v56, s[40:43], s71 offen offset:0 sc1\n\t"
  "v_mfma_f32_16x16x32_bf16 v[148:151], v[32:35], a[196:199], v[148:151]\n\t"
  "v_mfma_f32_16x16x32_bf16 v[152:155], v[32:35], a[200:203], v[152:155]\n\t"
  "ds_read_b64_tr_b16 v[52:53], v13 offset:61440\n\t"
  "v_mfma_f32_16x16x32_bf16 v[152:155], v[36:39], a[216:219], v[152:155]\n\t"
  "ds_read_b64_tr_b16 v[54:55], v13 offset:63488\n\t"
  "v_mfma_f32_16x16x32_bf16 v[156:159], v[36:39], a[220:223], v[156:159]\n\t"
  "buffer_load_dwordx4 a[108:111], v57, s[40:43], s71 offen offset:0 sc1\n\t"
  "v_mfma_f32_16x16x32_bf16 v[156:159], v[32:35], a[204:207], v[156:159]\n\t"
  "s_waitcnt lgkmcnt(4)\n\t"
  "v_mfma_f32_16x16x32_bf16 v[192:195], v[40:43], a[224:227], v[192:195]\n\t"
  "ds_read_b64_tr_b16 v[32:33], v14 offset:57344\n\t"
  "v_mfma_f32_16x16x32_bf16 v[192:195], v[44:47], a[240:243], v[192:195]\n\t"
  "ds_read_b64_tr_b16 v[34:35], v14 offset:59392\n\t"
  "v_mfma_f32_16x16x32_bf16 v[196:199], v[44:47], a[244:247], v[196:199]\n\t"
  "buffer_load_dwordx4 a[112:115], v10, s[40:43], s71 offen offset:1024 sc1\n\t"
  "v_mfma_f32_16x16x32_bf16 v[196:199], v[40:43], a[228:231], v[196:199]\n\t"
  "v_mfma_f32_16x16x32_bf16 v[200:203], v[40:43], a[232:235], v[200:203]\n\t"
  "ds_read_b64_tr_b16 v[36:37], v14 offset:61440\n\t"
  "v_mfma_f32_16x16x32_bf16 v[200:203], v[44:47], a[248:251], v[200:203]\n\t"
  "ds_read_b64_tr_b16 v[38:39], v14 offset:63488\n\t"
  "v_mfma_f32_16x16x32_bf16 v[204:207], v[44:47], a[252:255], v[204:207]\n\t"
  "buffer_load_dwordx4 a[116:119], v11, s[40:43], s71 offen offset:1024 sc1\n\t"
  "v_mfma_f32_16x16x32_bf16 v[204:207], v[40:43], a[236:239], v[204:207]\n\t"
  "s_waitcnt lgkmcnt(4)\n\t"
  "v_mfma_f32_16x16x32_bf16 v[224:227], v[48:51], a[224:227], v[224:227]\n\t"
  "ds_read_b64_tr_b16 v[40:41], v15 offset:57344\n\t"
  "v_mfma_f32_16x16x32_bf16 v[224:227], v[52:55], a[240:243], v[224:227]\n\t"
  "ds_read_b64_tr_b16 v[42:43], v15 offset:59392\n\t"
  "v_mfma_f32_16x16x32_bf16 v[228:231], v[52:55], a[244:247], v[228:231]\n\t"
  "buffer_load_dwordx4 a[120:123], v56, s[40:43], s71 offen offset:1024 sc1\n\t"
  "v_mfma_f32_16x16x32_bf16 v[228:231], v[48:51], a[228:231], v[228:231]\n\t"
  "v_mfma_f32_16x16x32_bf16 v[232:235], v[48:51], a[232:235], v[232:235]\n\t"
  "ds_read_b64_tr_b16 v[44:45], v15 offset:61440\n\t"
  "v_mfma_f32_16x16x32_bf16 v[232:235], v[52:55], a[248:251], v[232:235]\n\t"
  "ds_read_b64_tr_b16 v[46:47], v15 offset:63488\n\t"
  "v_mfma_f32_16x16x32_bf16 v[236:239], v[52:55], a[252:255], v[236:239]\n\t"
  "buffer_load_dwordx4 a[124:127], v57, s[40:43], s71 offen offset:1024 sc1\n\t"
  "v_mfma_f32_16x16x32_bf16 v[236:239], v[48:51], a[236:239], v[236:239]\n\t"
  "s_waitcnt lgkmcnt(4)\n\t"
  "v_mfma_f32_16x16x32_bf16 v[80:83], v[32:35], a[224:227], v[80:83]\n\t"
  "ds_read_b64_tr_b16 v[48:49], v58 offset:0\n\t"
  "v_mfma_f32_16x16x32_bf16 v[80:83], v[36:39], a[240:243], v[80:83]\n\t"
  "ds_read_b64_tr_b16 v[50:51], v58 offset:4096\n\t"
  "v_mfma_f32_16x16x32_bf16 v[84:87], v[36:39], a[244:247], v[84:87]\n\t"
  "s_bfe_u32 s71, s84, 0x40008\n\t"
  "s_lshl_b32 s71, s71, 11\n\t"
  "v_mfma_f32_16x16x32_bf16 v[84:87], v[32:35], a[228:231], v[84:87]\n\t"
  "buffer_load_dwordx4 a[128:131], v10, s[40:43], s71 offen offset:0 sc1\n\t"
  "v_mfma_f32_16x16x32_bf16 v[88:91], v[32:35], a[232:235], v[88:91]\n\t"
  "ds_read_b64_tr_b16 v[52:53], v58 offset:8192\n\t"
  "v_mfma_f32_16x16x32_bf16 v[88:91], v[36:39], a[248:251], v[88:91]\n\t"
  "ds_read_b64_tr_b16 v[54:55], v58 offset:12288\n\t"
  "v_mfma_f32_16x16x32_bf16 v[92:95], v[36:39], a[252:255], v[92:95]\n\t"
  "v_mfma_f32_16x16x32_bf16 v[92:95], v[32:35], a[236:239], v[92:95]\n\t"
  "s_waitcnt lgkmcnt(4)\n\t"
  "v_mfma_f32_16x16x32_bf16 v[144:147], v[40:43], a[224:227], v[144:147]\n\t"
  "ds_read_b64_tr_b16 v[32:33], v58 offset:32\n\t"
  "v_mfma_f32_16x16x32_bf16 v[144:147], v[44:47], a[240:243], v[144:147]\n\t"
  "ds_read_b64_tr_b16 v[34:35], v58 offset:4128\n\t"
  "v_mfma_f32_16x16x32_bf16 v[148:151], v[44:47], a[244:247], v[148:151]\n\t"
  "buffer_load_dwordx4 a[132:135], v11, s[40:43], s71 offen offset:0 sc1\n\t"
  "v_mfma_f32_16x16x32_bf16 v[148:151], v[40:43], a[228:231], v[148:151]\n\t"
  "v_mfma_f32_16x16x32_bf16 v[152:155], v[40:43], a[232:235], v[152:155]\n\t"
  "ds_read_b64_tr_b16 v[36:37], v58 offset:8224\n\t"
  "v_mfma_f32_16x16x32_bf16 v[152:155], v[44:47], a[248:251], v[152:155]\n\t"
  "ds_read_b64_tr_b16 v[38:39], v58 offset:12320\n\t"
  "v_mfma_f32_16x16x32_bf16 v[156:159], v[44:47], a[252:255], v[156:159]\n\t"
  "v_mfma_f32_16x16x32_bf16 v[156:159], v[40:43], a[236:239], v[156:159]\n\t"
  "buffer_load_dwordx4 a[136:139], v56, s[40:43], s71 offen offset:0 sc1\n\t"
  "s_waitcnt lgkmcnt(4)\n\t"
  "v_mfma_f32_16x16x32_bf16 v[208:211], v[48:51], a[0:3], 0\n\t"
  "ds_read_b64_tr_b16 v[40:41], v58 offset:64\n\t"
  "v_mfma_f32_16x16x32_bf16 v[208:211], v[52:55], a[16:19], v[208:211]\n\t"
  "ds_read_b64_tr_b16 v[42:43], v58 offset:4160\n\t"
  "v_mfma_f32_16x16x32_bf16 v[212:215], v[52:55], a[20:23], 0\n\t"
  "v_mfma_f32_16x16x32_bf16 v[212:215], v[48:51], a[4:7], v[212:215]\n\t"
  "v_mul_f32 v192, v22, v192\n\t"
  "v_mul_f32 v193, v22, v193\n\t"
  "v_mfma_f32_16x16x32_bf16 v[216:219], v[48:51], a[8:11], 0\n\t"
  "ds_read_b64_tr_b16 v[44:45], v58 offset:8256\n\t"
  "v_mul_f32 v194, v22, v194\n\t"
  "v_mfma_f32_16x16x32_bf16 v[216:219], v[52:55], a[24:27], v[216:219]\n\t"
  "ds_read_b64_tr_b16 v[46:47], v58 offset:12352\n\t"
  "v_mul_f32 v195, v22, v195\n\t"
  "v_mfma_f32_16x16x32_bf16 v[220:223], v[52:55], a[28:31], 0\n\t"
  "buffer_load_dwordx4 a[140:143], v57, s[40:43], s71 offen offset:0 sc1\n\t"
  "v_mul_f32 v224, v22, v224\n\t"
  "v_mul_f32 v225, v22, v225\n\t"
  "v_mfma_f32_16x16x32_bf16 v[220:223], v[48:51], a[12:15], v[220:223]\n\t"
  "v_mul_f32 v226, v22, v226\n\t"
  "s_waitcnt lgkmcnt(4)\n\t"
  "v_mfma_f32_16x16x32_bf16 v[240:243], v[32:35], a[0:3], 0\n\t"
  "ds_read_b64_tr_b16 v[48:49], v58 offset:96\n\t"
  "v_mul_f32 v227, v22, v227\n\t"
  "v_mfma_f32_16x16x32_bf16 v[240:243], v[36:39], a[16:19], v[240:243]\n\t"
  "ds_read_b64_tr_b16 v[50:51], v58 offset:4192\n\t"
  "v_cvt_pk_bf16_f32 v192, v192, v193\n\t"
  "v_mfma_f32_16x16x32_bf16 v[244:247], v[36:39], a[20:23], 0\n\t"
  "v_cvt_pk_bf16_f32 v193, v194, v195\n\t"
  "v_cvt_pk_bf16_f32 v194, v224, v225\n\t"
  "v_mfma_f32_16x16x32_bf16 v[244:247], v[32:35], a[4:7], v[244:247]\n\t"
  "v_cvt_pk_bf16_f32 v195, v226, v227\n\t"
  "v_mfma_f32_16x16x32_bf16 v[248:251], v[32:35], a[8:11], 0\n\t"
  "ds_read_b64_tr_b16 v[52:53], v58 offset:8288\n\t"
  "v_mfma_f32_16x16x32_bf16 v[248:251], v[36:39], a[24:27], v[248:251]\n\t"
  "ds_read_b64_tr_b16 v[54:55], v58 offset:12384\n\t"
  "v_mfma_f32_16x16x32_bf16 v[252:255], v[36:39], a[28:31], 0\n\t"
  "v_mfma_f32_16x16x32_bf16 v[252:255], v[32:35], a[12:15], v[252:255]\n\t"
  "buffer_store_dwordx4 v[192:195], v16, s[56:59], s82 offen offset:0 sc1\n\t"
  "s_waitcnt lgkmcnt(4)\n\t"
  "v_mfma_f32_16x16x32_bf16 v[112:115], v[40:43], a[0:3], 0\n\t"
  "ds_read_b64_tr_b16 v[32:33], v58 offset:128\n\t"
  "v_mul_f32 v196, v23, v196\n\t"
  "v_mfma_f32_16x16x32_bf16 v[112:115], v[44:47], a[16:19], v[112:115]\n\t"
  "ds_read_b64_tr_b16 v[34:35], v58 offset:4224\n\t"
  "v_mul_f32 v197, v23, v197\n\t"
  "v_mfma_f32_16x16x32_bf16 v[116:119], v[44:47], a[20:23], 0\n\t"
  "v_mul_f32 v198, v23, v198\n\t"
  "v_mul_f32 v199, v23, v199\n\t"
  "v_mfma_f32_16x16x32_bf16 v[116:119], v[40:43], a[4:7], v[116:119]\n\t"
  "v_mul_f32 v228, v23, v228\n\t"
  "v_mul_f32 v229, v23, v229\n\t"
  "v_mfma_f32_16x16x32_bf16 v[120:123], v[40:43], a[8:11], 0\n\t"
  "ds_read_b64_tr_b16 v[36:37], v58 offset:8320\n\t"
  "v_mul_f32 v230, v23, v230\n\t"
  "v_mfma_f32_16x16x32_bf16 v[120:123], v[44:47], a[24:27], v[120:123]\n\t"
  "ds_read_b64_tr_b16 v[38:39], v58 offset:12416\n\t"
  "v_mul_f32 v231, v23, v231\n\t"
  "v_mfma_f32_16x16x32_bf16 v[124:127], v[44:47], a[28:31], 0\n\t"
  "buffer_load_dwordx4 a[144:147], v10, s[40:43], s71 offen offset:1024 sc1\n\t"
  "v_cvt_pk_bf16_f32 v196, v196, v197\n\t"
  "v_cvt_pk_bf16_f32 v197, v198, v199\n\t"
  "v_mfma_f32_16x16x32_bf16 v[124:127], v[40:43], a[12:15], v[124:127]\n\t"
  "v_cvt_pk_bf16_f32 v198, v228, v229\n\t"
  "s_waitcnt lgkmcnt(4)\n\t"
  "v_mfma_f32_16x16x32_bf16 v[176:179], v[48:51], a[0:3], 0\n\t"
  "ds_read_b64_tr_b16 v[40:41], v58 offset:160\n\t"
  "v_cvt_pk_bf16_f32 v199, v230, v231\n\t"
  "v_mfma_f32_16x16x32_bf16 v[176:179], v[52:55], a[16:19], v[176:179]\n\t"
  "ds_read_b64_tr_b16 v[42:43], v58 offset:4256\n\t"
  "v_mfma_f32_16x16x32_bf16 v[180:183], v[52:55], a[20:23], 0\n\t"
  "buffer_load_dwordx4 a[148:151], v11, s[40:43], s71 offen offset:1024 sc1\n\t"
  "v_mfma_f32_16x16x32_bf16 v[180:183], v[48:51], a[4:7], v[180:183]\n\t"
  "v_mfma_f32_16x16x32_bf16 v[184:187], v[48:51], a[8:11], 0\n\t"
  "ds_read_b64_tr_b16 v[44:45], v58 offset:8352\n\t"
  "v_mfma_f32_16x16x32_bf16 v[184:187], v[52:55], a[24:27], v[184:187]\n\t"
  "ds_read_b64_tr_b16 v[46:47], v58 offset:12448\n\t"
  "v_mfma_f32_16x16x32_bf16 v[188:191], v[52:55], a[28:31], 0\n\t"
  "buffer_load_dwordx4 a[152:155], v56, s[40:43], s71 offen offset:1024 sc1\n\t"
  "v_mfma_f32_16x16x32_bf16 v[188:191], v[48:51], a[12:15], v[188:191]\n\t"
  "s_waitcnt lgkmcnt(4)\n\t"
  "v_mfma_f32_16x16x32_bf16 v[64:67], v[32:35], a[0:3], 0\n\t"
  "ds_read_b64_tr_b16 v[48:49], v58 offset:192\n\t"
  "v_mfma_f32_16x16x32_bf16 v[64:67], v[36:39], a[16:19], v[64:67]\n\t"
  "ds_read_b64_tr_b16 v[50:51], v58 offset:4288\n\t"
  "v_mfma_f32_16x16x32_bf16 v[68:71], v[36:39], a[20:23], 0\n\t"
  "buffer_load_dwordx4 a[156:159], v57, s[40:43], s71 offen offset:1024 sc1\n\t"
  "s_barrier\n\t"
  "v_mfma_f32_16x16x32_bf16 v[68:71], v[32:35], a[4:7], v[68:71]\n\t"
  "v_mfma_f32_16x16x32_bf16 v[72:75], v[32:35], a[8:11], 0\n\t"
  "ds_read_b64_tr_b16 v[52:53], v58 offset:8384\n\t"
  "v_mfma_f32_16x16x32_bf16 v[72:75], v[36:39], a[24:27], v[72:75]\n\t"
  "ds_read_b64_tr_b16 v[54:55], v58 offset:12480\n\t"
  "v_mfma_f32_16x16x32_bf16 v[76:79], v[36:39], a[28:31], 0\n\t"
  "v_mfma_f32_16x16x32_bf16 v[76:79], v[32:35], a[12:15], v[76:79]\n\t"
  "buffer_store_dwordx4 v[196:199], v17, s[56:59], s82 offen offset:0 sc1\n\t"
  "s_waitcnt lgkmcnt(4)\n\t"
  "v_mfma_f32_16x16x32_bf16 v[96:99], v[40:43], a[0:3], 0\n\t"
  "ds_read_b64_tr_b16 v[32:33], v58 offset:16384\n\t"
  "v_mul_f32 v200, v24, v200\n\t"
  "v_mfma_f32_16x16x32_bf16 v[96:99], v[44:47], a[16:19], v[96:99]\n\t"
  "ds_read_b64_tr_b16 v[34:35], v58 offset:20480\n\t"
  "v_mul_f32 v201, v24, v201\n\t"
  "v_mfma_f32_16x16x32_bf16 v[100:103], v[44:47], a[20:23], 0\n\t"
  "v_mul_f32 v202, v24, v202\n\t"
  "v_mul_f32 v203, v24, v203\n\t"
  "v_mfma_f32_16x16x32_bf16 v[100:103], v[40:43], a[4:7], v[100:103]\n\t"
  "v_mul_f32 v232, v24, v232\n\t"
  "v_mul_f32 v233, v24, v233\n\t"
  "v_mfma_f32_16x16x32_bf16 v[104:107], v[40:43], a[8:11], 0\n\t"
  "ds_read_b64_tr_b16 v[36:37], v58 offset:24576\n\t"
  "v_mul_f32 v234, v24, v234\n\t"
  "v_mfma_f32_16x16x32_bf16 v[104:107], v[44:47], a[24:27], v[104:107]\n\t"
  "ds_read_b64_tr_b16 v[38:39], v58 offset:28672\n\t"
  "v_mul_f32 v235, v24, v235\n\t"
  "s_bfe_u32 s71, s84, 0x4000c\n\t"
  "s_lshl_b32 s71, s71, 11\n\t"
  "v_mfma_f32_16x16x32_bf16 v[108:111], v[44:47], a[28:31], 0\n\t"
  "buffer_load_dwordx4 a[160:163], v10, s[40:43], s71 offen offset:0 sc1\n\t"
  "v_cvt_pk_bf16_f32 v200, v200, v201\n\t"
  "v_cvt_pk_bf16_f32 v201, v202, v203\n\t"
  "v_mfma_f32_16x16x32_bf16 v[108:111], v[40:43], a[12:15], v[108:111]\n\t"
  "v_cvt_pk_bf16_f32 v202, v232, v233\n\t"
  "s_waitcnt lgkmcnt(4)\n\t"
  "v_mfma_f32_16x16x32_bf16 v[128:131], v[48:51], a[0:3], 0\n\t"
  "ds_read_b64_tr_b16 v[40:41], v58 offset:16416\n\t"
  "v_cvt_pk_bf16_f32 v203, v234, v235\n\t"
  "v_mfma_f32_16x16x32_bf16 v[128:131], v[52:55], a[16:19], v[128:131]\n\t"
  "ds_read_b64_tr_b16 v[42:43], v58 offset:20512\n\t"
  "v_mfma_f32_16x16x32_bf16 v[132:135], v[52:55], a[20:23], 0\n\t"
  "v_mfma_f32_16x16x32_bf16 v[132:135], v[48:51], a[4:7], v[132:135]\n\t"
  "buffer_load_dwordx4 a[164:167], v11, s[40:43], s71 offen offset:0 sc1\n\t"
  "v_mfma_f32_16x16x32_bf16 v[136:139], v[48:51], a[8:11], 0\n\t"
  "ds_read_b64_tr_b16 v[44:45], v58 offset:24608\n\t"
  "v_mov_b32 v27, s73\n\t"
  "v_mfma_f32_16x16x32_bf16 v[136:139], v[52:55], a[24:27], v[136:139]\n\t"
  "ds_read_b64_tr_b16 v[46:47], v58 offset:28704\n\t"
  "v_mfma_f32_16x16x32_bf16 v[140:143], v[52:55], a[28:31], 0\n\t"
  "v_mfma_f32_16x16x32_bf16 v[140:143], v[48:51], a[12:15], v[140:143]\n\t"
  "s_waitcnt vmcnt(12) lgkmcnt(4)\n\t"
  "v_mfma_f32_16x16x32_bf16 v[208:211], v[32:35], a[96:99], v[208:211]\n\t"
  "ds_read_b64_tr_b16 v[48:49], v58 offset:16448\n\t"
  "v_accvgpr_write_b32 a0, v192\n\t"
  "v_mfma_f32_16x16x32_bf16 v[208:211], v[36:39], a[112:115], v[208:211]\n\t"
  "ds_read_b64_tr_b16 v[50:51], v58 offset:20544\n\t"
  "v_accvgpr_write_b32 a1, v193\n\t"
  "v_mfma_f32_16x16x32_bf16 v[212:215], v[36:39], a[116:119], v[212:215]\n\t"
  "v_accvgpr_write_b32 a2, v194\n\t"
  "v_accvgpr_write_b32 a3, v195\n\t"
  "v_mfma_f32_16x16x32_bf16 v[212:215], v[32:35], a[100:103], v[212:215]\n\t"
  "v_accvgpr_write_b32 a4, v196\n\t"
  "v_accvgpr_write_b32 a5, v197\n\t"
  "v_mfma_f32_16x16x32_bf16 v[216:219], v[32:35], a[104:107], v[216:219]\n\t"
  "ds_read_b64_tr_b16 v[52:53], v58 offset:24640\n\t"
  "v_accvgpr_write_b32 a6, v198\n\t"
  "v_mfma_f32_16x16x32_bf16 v[216:219], v[36:39], a[120:123], v[216:219]\n\t"
  "ds_read_b64_tr_b16 v[54:55], v58 offset:28736\n\t"
  "v_mfma_f32_16x16x32_bf16 v[220:223], v[36:39], a[124:127], v[220:223]\n\t"
  "buffer_store_dword v27, v21, s[92:95], 0 offen offset:2048 sc1\n\t"
  "v_accvgpr_write_b32 a7, v199\n\t"
  "v_mfma_f32_16x16x32_bf16 v[220:223], v[32:35], a[108:111], v[220:223]\n\t"
  "buffer_store_dwordx4 v[200:203], v18, s[56:59], s82 offen offset:0 sc1\n\t"
  "s_waitcnt lgkmcnt(4)\n\t"
  "v_mfma_f32_16x16x32_bf16 v[240:243], v[40:43], a[96:99], v[240:243]\n\t"
  "ds_read_b64_tr_b16 v[32:33], v58 offset:16480\n\t"
  "v_mul_f32 v204, v25, v204\n\t"
  "v_mfma_f32_16x16x32_bf16 v[240:243], v[44:47], a[112:115], v[240:243]\n\t"
  "ds_read_b64_tr_b16 v[34:35], v58 offset:20576\n\t"
  "v_mul_f32 v205, v25, v205\n\t"
  "v_mfma_f32_16x16x32_bf16 v[244:247], v[44:47], a[116:119], v[244:247]\n\t"
  "v_mul_f32 v206, v25, v206\n\t"
  "v_mul_f32 v207, v25, v207\n\t"
  "v_mfma_f32_16x16x32_bf16 v[244:247], v[40:43], a[100:103], v[244:247]\n\t"
  "v_mul_f32 v236, v25, v236\n\t"
  "v_mul_f32 v237, v25, v237\n\t"
  "v_mfma_f32_16x16x32_bf16 v[248:251], v[40:43], a[104:107], v[248:251]\n\t"
  "ds_read_b64_tr_b16 v[36:37], v58 offset:24672\n\t"
  "v_mul_f32 v238, v25, v238\n\t"
  "v_mfma_f32_16x16x32_bf16 v[248:251], v[44:47], a[120:123], v[248:251]\n\t"
  "ds_read_b64_tr_b16 v[38:39], v58 offset:28768\n\t"
  "v_mul_f32 v239, v25, v239\n\t"
  "v_mfma_f32_16x16x32_bf16 v[252:255], v[44:47], a[124:127], v[252:255]\n\t"
  "buffer_load_dwordx4 a[168:171], v56, s[40:43], s71 offen offset:0 sc1\n\t"
  "v_cvt_pk_bf16_f32 v204, v204, v205\n\t"
  "v_cvt_pk_bf16_f32 v205, v206, v207\n\t"
  "v_mfma_f32_16x16x32_bf16 v[252:255], v[40:43], a[108:111], v[252:255]\n\t"
  "v_cvt_pk_bf16_f32 v206, v236, v237\n\t"
  "s_waitcnt lgkmcnt(4)\n\t"
  "v_mfma_f32_16x16x32_bf16 v[112:115], v[48:51], a[96:99], v[112:115]\n\t"
  "ds_read_b64_tr_b16 v[40:41], v58 offset:16512\n\t"
  "v_cvt_pk_bf16_f32 v207, v238, v239\n\t"
  "v_mfma_f32_16x16x32_bf16 v[112:115], v[52:55], a[112:115], v[112:115]\n\t"
  "ds_read_b64_tr_b16 v[42:43], v58 offset:20608\n\t"
  "v_accvgpr_write_b32 a8, v200\n\t"
  "v_mfma_f32_16x16x32_bf16 v[116:119], v[52:55], a[116:119], v[116:119]\n\t"
  "v_accvgpr_write_b32 a9, v201\n\t"
  "v_accvgpr_write_b32 a10, v202\n\t"
  "v_mfma_f32_16x16x32_bf16 v[116:119], v[48:51], a[100:103], v[116:119]\n\t"
  "buffer_load_dwordx4 a[172:175], v57, s[40:43], s71 offen offset:0 sc1\n\t"
  "v_accvgpr_write_b32 a11, v203\n\t"
  "v_accvgpr_write_b32 a12, v204\n\t"
  "v_mfma_f32_16x16x32_bf16 v[120:123], v[48:51], a[104:107], v[120:123]\n\t"
  "ds_read_b64_tr_b16 v[44:45], v58 offset:24704\n\t"
  "v_accvgpr_write_b32 a13, v205\n\t"
  "v_mfma_f32_16x16x32_bf16 v[120:123], v[52:55], a[120:123], v[120:123]\n\t"
  "ds_read_b64_tr_b16 v[46:47], v58 offset:28800\n\t"
  "v_accvgpr_write_b32 a14, v206\n\t"
  "v_mfma_f32_16x16x32_bf16 v[124:127], v[52:55], a[124:127], v[124:127]\n\t"
  "v_accvgpr_write_b32 a15, v207\n\t"
  "v_mfma_f32_16x16x32_bf16 v[124:127], v[48:51], a[108:111], v[124:127]\n\t"
  "buffer_load_dwordx4 a[176:179], v10, s[40:43], s71 offen offset:1024 sc1\n\t"
  "s_waitcnt lgkmcnt(4)\n\t"
  "v_mfma_f32_16x16x32_bf16 v[176:179], v[32:35], a[96:99], v[176:179]\n\t"
  "ds_read_b64_tr_b16 v[48:49], v58 offset:16544\n\t"
  "v_mfma_f32_16x16x32_bf16 v[176:179], v[36:39], a[112:115], v[176:179]\n\t"
  "ds_read_b64_tr_b16 v[50:51], v58 offset:20640\n\t"
  "v_mfma_f32_16x16x32_bf16 v[180:183], v[36:39], a[116:119], v[180:183]\n\t"
  "v_mfma_f32_16x16x32_bf16 v[180:183], v[32:35], a[100:103], v[180:183]\n\t"
  "buffer_load_dwordx4 a[180:183], v11, s[40:43], s71 offen offset:1024 sc1\n\t"
  "v_mfma_f32_16x16x32_bf16 v[184:187], v[32:35], a[104:107], v[184:187]\n\t"
  "ds_read_b64_tr_b16 v[52:53], v58 offset:24736\n\t"
  "v_mfma_f32_16x16x32_bf16 v[184:187], v[36:39], a[120:123], v[184:187]\n\t"
  "ds_read_b64_tr_b16 v[54:55], v58 offset:28832\n\t"
  "v_mfma_f32_16x16x32_bf16 v[188:191], v[36:39], a[124:127], v[188:191]\n\t"
  "v_mfma_f32_16x16x32_bf16 v[188:191], v[32:35], a[108:111], v[188:191]\n\t"
  "buffer_store_dwordx4 v[204:207], v19, s[56:59], s82 offen offset:0 sc1\n\t"
  "s_waitcnt lgkmcnt(4)\n\t"
  "v_mfma_f32_16x16x32_bf16 v[64:67], v[40:43], a[96:99], v[64:67]\n\t"
  "ds_read_b64_tr_b16 v[32:33], v58 offset:16576\n\t"
  "v_mul_f32 v80, v22, v80\n\t"
  "v_mfma_f32_16x16x32_bf16 v[64:67], v[44:47], a[112:115], v[64:67]\n\t"
  "ds_read_b64_tr_b16 v[34:35], v58 offset:20672\n\t"
  "v_mul_f32 v81, v22, v81\n\t"
  "v_mfma_f32_16x16x32_bf16 v[68:71], v[44:47], a[116:119], v[68:71]\n\t"
  "v_mul_f32 v82, v22, v82\n\t"
  "v_mul_f32 v83, v22, v83\n\t"
  "v_mfma_f32_16x16x32_bf16 v[68:71], v[40:43], a[100:103], v[68:71]\n\t"
  "buffer_load_dwordx4 a[184:187], v56, s[40:43], s71 offen offset:1024 sc1\n\t"
  "v_mul_f32 v144, v22, v144\n\t"
  "v_mul_f32 v145, v22, v145\n\t"
  "v_mfma_f32_16x16x32_bf16 v[72:75], v[40:43], a[104:107], v[72:75]\n\t"
  "ds_read_b64_tr_b16 v[36:37], v58 offset:24768\n\t"
  "v_mul_f32 v146, v22, v146\n\t"
  "v_mfma_f32_16x16x32_bf16 v[72:75], v[44:47], a[120:123], v[72:75]\n\t"
  "ds_read_b64_tr_b16 v[38:39], v58 offset:28864\n\t"
  "v_mul_f32 v147, v22, v147\n\t"
  "v_mfma_f32_16x16x32_bf16 v[76:79], v[44:47], a[124:127], v[76:79]\n\t"
  "v_cvt_pk_bf16_f32 v80, v80, v81\n\t"
  "v_cvt_pk_bf16_f32 v81, v82, v83\n\t"
  "v_mfma_f32_16x16x32_bf16 v[76:79], v[40:43], a[108:111], v[76:79]\n\t"
  "buffer_load_dwordx4 a[188:191], v57, s[40:43], s71 offen offset:1024 sc1\n\t"
  "v_cvt_pk_bf16_f32 v82, v144, v145\n\t"
  "s_waitcnt lgkmcnt(4)\n\t"
  "v_mfma_f32_16x16x32_bf16 v[96:99], v[48:51], a[96:99], v[96:99]\n\t"
  "ds_read_b64_tr_b16 v[40:41], v29 offset:0\n\t"
  "v_cvt_pk_bf16_f32 v83, v146, v147\n\t"
  "v_mfma_f32_16x16x32_bf16 v[96:99], v[52:55], a[112:115], v[96:99]\n\t"
  "ds_read_b64_tr_b16 v[42:43], v29 offset:4096\n\t"
  "v_accvgpr_write_b32 a16, v80\n\t"
  "v_mfma_f32_16x16x32_bf16 v[100:103], v[52:55], a[116:119], v[100:103]\n\t"
  "v_accvgpr_write_b32 a17, v81\n\t"
  "v_accvgpr_write_b32 a18, v82\n\t"
  "v_mfma_f32_16x16x32_bf16 v[100:103], v[48:51], a[100:103], v[100:103]\n\t"
  "v_accvgpr_write_b32 a19, v83\n\t"
  "v_mfma_f32_16x16x32_bf16 v[104:107], v[48:51], a[104:107], v[104:107]\n\t"
  "ds_read_b64_tr_b16 v[44:45], v29 offset:8192\n\t"
  "v_mfma_f32_16x16x32_bf16 v[104:107], v[52:55], a[120:123], v[104:107]\n\t"
  "ds_read_b64_tr_b16 v[46:47], v29 offset:12288\n\t"
  "v_mfma_f32_16x16x32_bf16 v[108:111], v[52:55], a[124:127], v[108:111]\n\t"
  "v_mfma_f32_16x16x32_bf16 v[108:111], v[48:51], a[108:111], v[108:111]\n\t"
  "s_waitcnt lgkmcnt(4)\n\t"
  "v_mfma_f32_16x16x32_bf16 v[128:131], v[32:35], a[96:99], v[128:131]\n\t"
  "ds_read_b64_tr_b16 v[48:49], v29 offset:32\n\t"
  "v_mfma_f32_16x16x32_bf16 v[128:131], v[36:39], a[112:115], v[128:131]\n\t"
  "ds_read_b64_tr_b16 v[50:51], v29 offset:4128\n\t"
  "v_mfma_f32_16x16x32_bf16 v[132:135], v[36:39], a[116:119], v[132:135]\n\t"
  "v_mfma_f32_16x16x32_bf16 v[132:135], v[32:35], a[100:103], v[132:135]\n\t"
  "v_mfma_f32_16x16x32_bf16 v[136:139], v[32:35], a[104:107], v[136:139]\n\t"
  "ds_read_b64_tr_b16 v[52:53], v29 offset:8224\n\t"
  "v_mfma_f32_16x16x32_bf16 v[136:139], v[36:39], a[120:123], v[136:139]\n\t"
  "ds_read_b64_tr_b16 v[54:55], v29 offset:12320\n\t"
  "v_mfma_f32_16x16x32_bf16 v[140:143], v[36:39], a[124:127], v[140:143]\n\t"
  "v_mfma_f32_16x16x32_bf16 v[140:143], v[32:35], a[108:111], v[140:143]\n\t"
  "buffer_store_dwordx4 v[80:83], v16, s[56:59], s82 offen offset:1024 sc1\n\t"
  "s_waitcnt vmcnt(13) lgkmcnt(4)\n\t"
  "v_mfma_f32_16x16x32_bf16 v[208:211], v[40:43], a[128:131], v[208:211]\n\t"
  "ds_read_b64_tr_b16 v[32:33], v29 offset:64\n\t"
  "v_mul_f32 v84, v23, v84\n\t"
  "v_mfma_f32_16x16x32_bf16 v[208:211], v[44:47], a[144:147], v[208:211]\n\t"
  "ds_read_b64_tr_b16 v[34:35], v29 offset:4160\n\t"
  "v_mul_f32 v85, v23, v85\n\t"
  "v_mfma_f32_16x16x32_bf16 v[212:215], v[44:47], a[148:151], v[212:215]\n\t"
  "v_mul_f32 v86, v23, v86\n\t"
  "v_mul_f32 v87, v23, v87\n\t"
  "v_mfma_f32_16x16x32_bf16 v[212:215], v[40:43], a[132:135], v[212:215]\n\t"
  "v_mul_f32 v148, v23, v148\n\t"
  "v_mul_f32 v149, v23, v149\n\t"
  "v_mfma_f32_16x16x32_bf16 v[216:219], v[40:43], a[136:139], v[216:219]\n\t"
  "ds_read_b64_tr_b16 v[36:37], v29 offset:8256\n\t"
  "v_mul_f32 v150, v23, v150\n\t"
  "v_mfma_f32_16x16x32_bf16 v[216:219], v[44:47], a[152:155], v[216:219]\n\t"
  "ds_read_b64_tr_b16 v[38:39], v29 offset:12352\n\t"
  "v_mul_f32 v151, v23, v151\n\t"
  "s_bfe_u32 s71, s84, 0x40014\n\t"
  "s_lshl_b32 s71, s71, 11\n\t"
  "v_mfma_f32_16x16x32_bf16 v[220:223], v[44:47], a[156:159], v[220:223]\n\t"
  "buffer_load_dwordx4 a[96:99], v10, s[40:43], s71 offen offset:0 sc1\n\t"
  "v_cvt_pk_bf16_f32 v84, v84, v85\n\t"
  "v_cvt_pk_bf16_f32 v85, v86, v87\n\t"
  "v_mfma_f32_16x16x32_bf16 v[220:223], v[40:43], a[140:143], v[220:223]\n\t"
  "v_cvt_pk_bf16_f32 v86, v148, v149\n\t"
  "s_waitcnt lgkmcnt(4)\n\t"
  "v_mfma_f32_16x16x32_bf16 v[240:243], v[48:51], a[128:131], v[240:243]\n\t"
  "ds_read_b64_tr_b16 v[40:41], v29 offset:96\n\t"
  "v_cvt_pk_bf16_f32 v87, v150, v151\n\t"
  "v_mfma_f32_16x16x32_bf16 v[240:243], v[52:55], a[144:147], v[240:243]\n\t"
  "ds_read_b64_tr_b16 v[42:43], v29 offset:4192\n\t"
  "v_accvgpr_write_b32 a20, v84\n\t"
  "v_mfma_f32_16x16x32_bf16 v[244:247], v[52:55], a[148:151], v[244:247]\n\t"
  "v_accvgpr_write_b32 a21, v85\n\t"
  "v_accvgpr_write_b32 a22, v86\n\t"
  "v_mfma_f32_16x16x32_bf16 v[244:247], v[48:51], a[132:135], v[244:247]\n\t"
  "buffer_load_dwordx4 a[100:103], v11, s[40:43], s71 offen offset:0 sc1\n\t"
  "v_accvgpr_write_b32 a23, v87\n\t"
  "v_mfma_f32_16x16x32_bf16 v[248:251], v[48:51], a[136:139], v[248:251]\n\t"
  "ds_read_b64_tr_b16 v[44:45], v29 offset:8288\n\t"
  "v_mfma_f32_16x16x32_bf16 v[248:251], v[52:55], a[152:155], v[248:251]\n\t"
  "ds_read_b64_tr_b16 v[46:47], v29 offset:12384\n\t"
  "v_mfma_f32_16x16x32_bf16 v[252:255], v[52:55], a[156:159], v[252:255]\n\t"
  "v_mfma_f32_16x16x32_bf16 v[252:255], v[48:51], a[140:143], v[252:255]\n\t"
  "s_waitcnt lgkmcnt(4)\n\t"
  "v_mfma_f32_16x16x32_bf16 v[112:115], v[32:35], a[128:131], v[112:115]\n\t"
  "ds_read_b64_tr_b16 v[48:49], v29 offset:128\n\t"
  "v_mfma_f32_16x16x32_bf16 v[112:115], v[36:39], a[144:147], v[112:115]\n\t"
  "ds_read_b64_tr_b16 v[50:51], v29 offset:4224\n\t"
  "v_mfma_f32_16x16x32_bf16 v[116:119], v[36:39], a[148:151], v[116:119]\n\t"
  "v_mfma_f32_16x16x32_bf16 v[116:119], v[32:35], a[132:135], v[116:119]\n\t"
  "v_mfma_f32_16x16x32_bf16 v[120:123], v[32:35], a[136:139], v[120:123]\n\t"
  "ds_read_b64_tr_b16 v[52:53], v29 offset:8320\n\t"
  "v_mfma_f32_16x16x32_bf16 v[120:123], v[36:39], a[152:155], v[120:123]\n\t"
  "ds_read_b64_tr_b16 v[54:55], v29 offset:12416\n\t"
  "v_mfma_f32_16x16x32_bf16 v[124:127], v[36:39], a[156:159], v[124:127]\n\t"
  "buffer_load_dword v26, v30, s[92:95], 0 offen offset:2048 sc1\n\t"
  "v_mfma_f32_16x16x32_bf16 v[124:127], v[32:35], a[140:143], v[124:127]\n\t"
  "buffer_store_dwordx4 v[84:87], v17, s[56:59], s82 offen offset:1024 sc1\n\t"
  "s_waitcnt lgkmcnt(4)\n\t"
  "v_mfma_f32_16x16x32_bf16 v[176:179], v[40:43], a[128:131], v[176:179]\n\t"
  "ds_read_b64_tr_b16 v[32:33], v29 offset:160\n\t"
  "v_mul_f32 v88, v24, v88\n\t"
  "v_mfma_f32_16x16x32_bf16 v[176:179], v[44:47], a[144:147], v[176:179]\n\t"
  "ds_read_b64_tr_b16 v[34:35], v29 offset:4256\n\t"
  "v_mul_f32 v89, v24, v89\n\t"
  "v_mfma_f32_16x16x32_bf16 v[180:183], v[44:47], a[148:151], v[180:183]\n\t"
  "v_mul_f32 v90, v24, v90\n\t"
  "v_mul_f32 v91, v24, v91\n\t"
  "v_mfma_f32_16x16x32_bf16 v[180:183], v[40:43], a[132:135], v[180:183]\n\t"
  "v_mul_f32 v152, v24, v152\n\t"
  "v_mul_f32 v153, v24, v153\n\t"
  "v_mfma_f32_16x16x32_bf16 v[184:187], v[40:43], a[136:139], v[184:187]\n\t"
  "ds_read_b64_tr_b16 v[36:37], v29 offset:8352\n\t"
  "v_mul_f32 v154, v24, v154\n\t"
  "v_mfma_f32_16x16x32_bf16 v[184:187], v[44:47], a[152:155], v[184:187]\n\t"
  "ds_read_b64_tr_b16 v[38:39], v29 offset:12448\n\t"
  "s_add_u32 m0, s69, 16384\n\t"
  "v_mfma_f32_16x16x32_bf16 v[188:191], v[44:47], a[156:159], v[188:191]\n\t"
  "buffer_load_dwordx4 a[104:107], v56, s[40:43], s71 offen offset:0 sc1\n\t"
  "s_bfe_u32 s70, s84, 0x4001c\n\t"
  "s_mul_i32 s70, s70, 57344\n\t"
  "v_mfma_f32_16x16x32_bf16 v[188:191], v[40:43], a[140:143], v[188:191]\n\t"
  "v_mul_f32 v155, v24, v155\n\t"
  "s_waitcnt lgkmcnt(4)\n\t"
  "v_mfma_f32_16x16x32_bf16 v[64:67], v[48:51], a[128:131], v[64:67]\n\t"
  "ds_read_b64_tr_b16 v[40:41], v29 offset:192\n\t"
  "v_cvt_pk_bf16_f32 v88, v88, v89\n\t"
  "v_mfma_f32_16x16x32_bf16 v[64:67], v[52:55], a[144:147], v[64:67]\n\t"
  "ds_read_b64_tr_b16 v[42:43], v29 offset:4288\n\t"
  "v_cvt_pk_bf16_f32 v89, v90, v91\n\t"
  "v_mfma_f32_16x16x32_bf16 v[68:71], v[52:55], a[148:151], v[68:71]\n\t"
  "s_barrier\n\t"
  "v_mfma_f32_16x16x32_bf16 v[68:71], v[48:51], a[132:135], v[68:71]\n\t"
  "buffer_load_dwordx4 v6, s[60:63], s70 offen offset:0  lds\n\t"
  "v_cvt_pk_bf16_f32 v90, v152, v153\n\t"
  "v_mfma_f32_16x16x32_bf16 v[72:75], v[48:51], a[136:139], v[72:75]\n\t"
  "ds_read_b64_tr_b16 v[44:45], v29 offset:8384\n\t"
  "v_cvt_pk_bf16_f32 v91, v154, v155\n\t"
  "v_mfma_f32_16x16x32_bf16 v[72:75], v[52:55], a[152:155], v[72:75]\n\t"
  "ds_read_b64_tr_b16 v[46:47], v29 offset:12480\n\t"
  "v_accvgpr_write_b32 a24, v88\n\t"
  "v_mfma_f32_16x16x32_bf16 v[76:79], v[52:55], a[156:159], v[76:79]\n\t"
  "v_accvgpr_write_b32 a25, v89\n\t"
  "v_accvgpr_write_b32 a26, v90\n\t"
  "v_mfma_f32_16x16x32_bf16 v[76:79], v[48:51], a[140:143], v[76:79]\n\t"
  "v_accvgpr_write_b32 a27, v91\n\t"
  "s_waitcnt lgkmcnt(4)\n\t"
  "v_mfma_f32_16x16x32_bf16 v[96:99], v[32:35], a[128:131], v[96:99]\n\t"
  "ds_read_b64_tr_b16 v[48:49], v29 offset:16384\n\t"
  "v_mfma_f32_16x16x32_bf16 v[96:99], v[36:39], a[144:147], v[96:99]\n\t"
  "ds_read_b64_tr_b16 v[50:51], v29 offset:20480\n\t"
  "v_mfma_f32_16x16x32_bf16 v[100:103], v[36:39], a[148:151], v[100:103]\n\t"
  "buffer_load_dwordx4 v7, s[60:63], s70 offen offset:1024  lds\n\t"
  "v_mfma_f32_16x16x32_bf16 v[100:103], v[32:35], a[132:135], v[100:103]\n\t"
  "v_mfma_f32_16x16x32_bf16 v[104:107], v[32:35], a[136:139], v[104:107]\n\t"
  "ds_read_b64_tr_b16 v[52:53], v29 offset:24576\n\t"
  "v_mfma_f32_16x16x32_bf16 v[104:107], v[36:39], a[152:155], v[104:107]\n\t"
  "ds_read_b64_tr_b16 v[54:55], v29 offset:28672\n\t"
  "v_mfma_f32_16x16x32_bf16 v[108:111], v[36:39], a[156:159], v[108:111]\n\t"
  "v_mfma_f32_16x16x32_bf16 v[108:111], v[32:35], a[140:143], v[108:111]\n\t"
  "buffer_load_dwordx4 v8, s[60:63], s70 offen offset:2048  lds\n\t"
  "s_waitcnt lgkmcnt(4)\n\t"
  "v_mfma_f32_16x16x32_bf16 v[128:131], v[40:43], a[128:131], v[128:131]\n\t"
  "ds_read_b64_tr_b16 v[32:33], v29 offset:16416\n\t"
  "v_mfma_f32_16x16x32_bf16 v[128:131], v[44:47], a[144:147], v[128:131]\n\t"
  "ds_read_b64_tr_b16 v[34:35], v29 offset:20512\n\t"
  "v_mfma_f32_16x16x32_bf16 v[132:135], v[44:47], a[148:151], v[132:135]\n\t"
  "buffer_store_dwordx4 v[88:91], v18, s[56:59], s82 offen offset:1024 sc1\n\t"
  "v_mul_f32 v92, v25, v92\n\t"
  "v_mfma_f32_16x16x32_bf16 v[132:135], v[40:43], a[132:135], v[132:135]\n\t"
  "v_mul_f32 v93, v25, v93\n\t"
  "v_mul_f32 v94, v25, v94\n\t"
  "v_mfma_f32_16x16x32_bf16 v[136:139], v[40:43], a[136:139], v[136:139]\n\t"
  "ds_read_b64_tr_b16 v[36:37], v29 offset:24608\n\t"
  "v_mul_f32 v95, v25, v95\n\t"
  "v_mfma_f32_16x16x32_bf16 v[136:139], v[44:47], a[152:155], v[136:139]\n\t"
  "ds_read_b64_tr_b16 v[38:39], v29 offset:28704\n\t"
  "v_mul_f32 v156, v25, v156\n\t"
  "v_mfma_f32_16x16x32_bf16 v[140:143], v[44:47], a[156:159], v[140:143]\n\t"
  "buffer_load_dwordx4 v9, s[60:63], s70 offen offset:3072  lds\n\t"
  "v_mul_f32 v157, v25, v157\n\t"
  "v_mfma_f32_16x16x32_bf16 v[140:143], v[40:43], a[140:143], v[140:143]\n\t"
  "v_mul_f32 v158, v25, v158\n\t"
  "s_waitcnt vmcnt(11) lgkmcnt(4)\n\t"
  "v_mfma_f32_16x16x32_bf16 v[208:211], v[48:51], a[160:163], v[208:211]\n\t"
  "ds_read_b64_tr_b16 v[40:41], v29 offset:16448\n\t"
  "v_mul_f32 v159, v25, v159\n\t"
  "v_mfma_f32_16x16x32_bf16 v[208:211], v[52:55], a[176:179], v[208:211]\n\t"
  "ds_read_b64_tr_b16 v[42:43], v29 offset:20544\n\t"
  "v_cvt_pk_bf16_f32 v92, v92, v93\n\t"
  "v_mfma_f32_16x16x32_bf16 v[212:215], v[52:55], a[180:183], v[212:215]\n\t"
  "v_cvt_pk_bf16_f32 v93, v94, v95\n\t"
  "v_cvt_pk_bf16_f32 v94, v156, v157\n\t"
  "v_mfma_f32_16x16x32_bf16 v[212:215], v[48:51], a[164:167], v[212:215]\n\t"
  "buffer_load_dwordx4 a[108:111], v57, s[40:43], s71 offen offset:0 sc1\n\t"
  "v_cvt_pk_bf16_f32 v95, v158, v159\n\t"
  "v_mfma_f32_16x16x32_bf16 v[216:219], v[48:51], a[168:171], v[216:219]\n\t"
  "ds_read_b64_tr_b16 v[44:45], v29 offset:24640\n\t"
  "v_accvgpr_write_b32 a28, v92\n\t"
  "v_mfma_f32_16x16x32_bf16 v[216:219], v[52:55], a[184:187], v[216:219]\n\t"
  "ds_read_b64_tr_b16 v[46:47], v29 offset:28736\n\t"
  "v_accvgpr_write_b32 a29, v93\n\t"
  "v_mfma_f32_16x16x32_bf16 v[220:223], v[52:55], a[188:191], v[220:223]\n\t"
  "v_accvgpr_write_b32 a30, v94\n\t"
  "v_accvgpr_write_b32 a31, v95\n\t"
  "v_mfma_f32_16x16x32_bf16 v[220:223], v[48:51], a[172:175], v[220:223]\n\t"
  "s_waitcnt lgkmcnt(4)\n\t"
  "v_mfma_f32_16x16x32_bf16 v[240:243], v[32:35], a[160:163], v[240:243]\n\t"
  "ds_read_b64_tr_b16 v[48:49], v29 offset:16480\n\t"
  "v_mfma_f32_16x16x32_bf16 v[240:243], v[36:39], a[176:179], v[240:243]\n\t"
  "ds_read_b64_tr_b16 v[50:51], v29 offset:20576\n\t"
  "v_mfma_f32_16x16x32_bf16 v[244:247], v[36:39], a[180:183], v[244:247]\n\t"
  "buffer_load_dwordx4 a[112:115], v10, s[40:43], s71 offen offset:1024 sc1\n\t"
  "v_mfma_f32_16x16x32_bf16 v[244:247], v[32:35], a[164:167], v[244:247]\n\t"
  "v_mfma_f32_16x16x32_bf16 v[248:251], v[32:35], a[168:171], v[248:251]\n\t"
  "ds_read_b64_tr_b16 v[52:53], v29 offset:24672\n\t"
  "v_mfma_f32_16x16x32_bf16 v[248:251], v[36:39], a[184:187], v[248:251]\n\t"
  "ds_read_b64_tr_b16 v[54:55], v29 offset:28768\n\t"
  "v_mfma_f32_16x16x32_bf16 v[252:255], v[36:39], a[188:191], v[252:255]\n\t"
  "v_mfma_f32_16x16x32_bf16 v[252:255], v[32:35], a[172:175], v[252:255]\n\t"
  "buffer_store_dwordx4 v[92:95], v19, s[56:59], s82 offen offset:1024 sc1\n\t"
  "s_waitcnt lgkmcnt(4)\n\t"
  "v_mfma_f32_16x16x32_bf16 v[112:115], v[40:43], a[160:163], v[112:115]\n\t"
  "ds_read_b64_tr_b16 v[32:33], v29 offset:16512\n\t"
  "v_mfma_f32_16x16x32_bf16 v[112:115], v[44:47], a[176:179], v[112:115]\n\t"
  "ds_read_b64_tr_b16 v[34:35], v29 offset:20608\n\t"
  "v_mfma_f32_16x16x32_bf16 v[116:119], v[44:47], a[180:183], v[116:119]\n\t"
  "v_mfma_f32_16x16x32_bf16 v[116:119], v[40:43], a[164:167], v[116:119]\n\t"
  "v_mfma_f32_16x16x32_bf16 v[120:123], v[40:43], a[168:171], v[120:123]\n\t"
  "ds_read_b64_tr_b16 v[36:37], v29 offset:24704\n\t"
  "v_mfma_f32_16x16x32_bf16 v[120:123], v[44:47], a[184:187], v[120:123]\n\t"
  "ds_read_b64_tr_b16 v[38:39], v29 offset:28800\n\t"
  "v_mfma_f32_16x16x32_bf16 v[124:127], v[44:47], a[188:191], v[124:127]\n\t"
  "buffer_load_dwordx4 a[116:119], v11, s[40:43], s71 offen offset:1024 sc1\n\t"
  "v_mfma_f32_16x16x32_bf16 v[124:127], v[40:43], a[172:175], v[124:127]\n\t"
  "s_waitcnt lgkmcnt(4)\n\t"
  "v_mfma_f32_16x16x32_bf16 v[176:179], v[48:51], a[160:163], v[176:179]\n\t"
  "ds_read_b64_tr_b16 v[40:41], v29 offset:16544\n\t"
  "v_mfma_f32_16x16x32_bf16 v[176:179], v[52:55], a[176:179], v[176:179]\n\t"
  "ds_read_b64_tr_b16 v[42:43], v29 offset:20640\n\t"
  "v_mfma_f32_16x16x32_bf16 v[180:183], v[52:55], a[180:183], v[180:183]\n\t"
  "buffer_load_dwordx4 a[120:123], v56, s[40:43], s71 offen offset:1024 sc1\n\t"
  "v_mfma_f32_16x16x32_bf16 v[180:183], v[48:51], a[164:167], v[180:183]\n\t"
  "v_mfma_f32_16x16x32_bf16 v[184:187], v[48:51], a[168:171], v[184:187]\n\t"
  "ds_read_b64_tr_b16 v[44:45], v29 offset:24736\n\t"
  "v_mfma_f32_16x16x32_bf16 v[184:187], v[52:55], a[184:187], v[184:187]\n\t"
  "ds_read_b64_tr_b16 v[46:47], v29 offset:28832\n\t"
  "v_mfma_f32_16x16x32_bf16 v[188:191], v[52:55], a[188:191], v[188:191]\n\t"
  "buffer_load_dwordx4 a[124:127], v57, s[40:43], s71 offen offset:1024 sc1\n\t"
  "v_mfma_f32_16x16x32_bf16 v[188:191], v[48:51], a[172:175], v[188:191]\n\t"
  "s_waitcnt lgkmcnt(4)\n\t"
  "v_mfma_f32_16x16x32_bf16 v[64:67], v[32:35], a[160:163], v[64:67]\n\t"
  "ds_read_b64_tr_b16 v[48:49], v29 offset:16576\n\t"
  "v_mfma_f32_16x16x32_bf16 v[64:67], v[36:39], a[176:179], v[64:67]\n\t"
  "ds_read_b64_tr_b16 v[50:51], v29 offset:20672\n\t"
  "v_mfma_f32_16x16x32_bf16 v[68:71], v[36:39], a[180:183], v[68:71]\n\t"
  "s_bfe_u32 s71, s84, 0x40018\n\t"
  "s_lshl_b32 s71, s71, 11\n\t"
  "v_mfma_f32_16x16x32_bf16 v[68:71], v[32:35], a[164:167], v[68:71]\n\t"
  "buffer_load_dwordx4 a[128:131], v10, s[40:43], s71 offen offset:0 sc1\n\t"
  "v_mfma_f32_16x16x32_bf16 v[72:75], v[32:35], a[168:171], v[72:75]\n\t"
  "ds_read_b64_tr_b16 v[52:53], v29 offset:24768\n\t"
  "v_mfma_f32_16x16x32_bf16 v[72:75], v[36:39], a[184:187], v[72:75]\n\t"
  "ds_read_b64_tr_b16 v[54:55], v29 offset:28864\n\t"
  "v_mfma_f32_16x16x32_bf16 v[76:79], v[36:39], a[188:191], v[76:79]\n\t"
  "v_mfma_f32_16x16x32_bf16 v[76:79], v[32:35], a[172:175], v[76:79]\n\t"
  "s_waitcnt lgkmcnt(4)\n\t"
  "v_mfma_f32_16x16x32_bf16 v[96:99], v[40:43], a[160:163], v[96:99]\n\t"
  "ds_read_b64_tr_b16 v[32:33], v28 offset:32768\n\t"
  "v_mfma_f32_16x16x32_bf16 v[96:99], v[44:47], a[176:179], v[96:99]\n\t"
  "ds_read_b64_tr_b16 v[34:35], v28 offset:36864\n\t"
  "v_mfma_f32_16x16x32_bf16 v[100:103], v[44:47], a[180:183], v[100:103]\n\t"
  "buffer_load_dwordx4 a[132:135], v11, s[40:43], s71 offen offset:0 sc1\n\t"
  "v_mfma_f32_16x16x32_bf16 v[100:103], v[40:43], a[164:167], v[100:103]\n\t"
  "v_mfma_f32_16x16x32_bf16 v[104:107], v[40:43], a[168:171], v[104:107]\n\t"
  "ds_read_b64_tr_b16 v[36:37], v28 offset:40960\n\t"
  "v_mfma_f32_16x16x32_bf16 v[104:107], v[44:47], a[184:187], v[104:107]\n\t"
  "ds_read_b64_tr_b16 v[38:39], v28 offset:45056\n\t"
  "v_mfma_f32_16x16x32_bf16 v[108:111], v[44:47], a[188:191], v[108:111]\n\t"
  "v_mfma_f32_16x16x32_bf16 v[108:111], v[40:43], a[172:175], v[108:111]\n\t"
  "buffer_load_dwordx4 a[136:139], v56, s[40:43], s71 offen offset:0 sc1\n\t"
  "s_waitcnt lgkmcnt(4)\n\t"
  "v_mfma_f32_16x16x32_bf16 v[128:131], v[48:51], a[160:163], v[128:131]\n\t"
  "ds_read_b64_tr_b16 v[40:41], v28 offset:32800\n\t"
  "v_mfma_f32_16x16x32_bf16 v[128:131], v[52:55], a[176:179], v[128:131]\n\t"
  "ds_read_b64_tr_b16 v[42:43], v28 offset:36896\n\t"
  "v_mfma_f32_16x16x32_bf16 v[132:135], v[52:55], a[180:183], v[132:135]\n\t"
  "v_mfma_f32_16x16x32_bf16 v[132:135], v[48:51], a[164:167], v[132:135]\n\t"
  "v_mfma_f32_16x16x32_bf16 v[136:139], v[48:51], a[168:171], v[136:139]\n\t"
  "ds_read_b64_tr_b16 v[44:45], v28 offset:40992\n\t"
  "v_mfma_f32_16x16x32_bf16 v[136:139], v[52:55], a[184:187], v[136:139]\n\t"
  "ds_read_b64_tr_b16 v[46:47], v28 offset:45088\n\t"
  "v_mfma_f32_16x16x32_bf16 v[140:143], v[52:55], a[188:191], v[140:143]\n\t"
  "buffer_load_dwordx4 a[140:143], v57, s[40:43], s71 offen offset:0 sc1\n\t"
  "v_mfma_f32_16x16x32_bf16 v[140:143], v[48:51], a[172:175], v[140:143]\n\t"
  "s_waitcnt lgkmcnt(4)\n\t"
  "v_mfma_f32_16x16x32_bf16 v[208:211], v[32:35], a[32:35], v[208:211]\n\t"
  "ds_read_b64_tr_b16 v[48:49], v28 offset:32832\n\t"
  "s_waitcnt vmcnt(17)\n\t"
  "v_subrev_u32 v26, s73, v26\n\t"
  "v_mfma_f32_16x16x32_bf16 v[208:211], v[36:39], a[48:51], v[208:211]\n\t"
  "ds_read_b64_tr_b16 v[50:51], v28 offset:36928\n\t"
  "v_cmp_lt_u32 vcc, 2, v26\n\t"
  "v_mfma_f32_16x16x32_bf16 v[212:215], v[36:39], a[52:55], v[212:215]\n\t"
  "s_nop 4\n\t"
  "s_cbranch_vccnz L_k1_sp2\n\t"
  "L_k1_rs2:\n\t"
  "v_mfma_f32_16x16x32_bf16 v[212:215], v[32:35], a[36:39], v[212:215]\n\t"
  "buffer_load_dwordx4 a[144:147], v10, s[40:43], s71 offen offset:1024 sc1\n\t"
  "v_mfma_f32_16x16x32_bf16 v[216:219], v[32:35], a[40:43], v[216:219]\n\t"
  "ds_read_b64_tr_b16 v[52:53], v28 offset:41024\n\t"
  "v_mfma_f32_16x16x32_bf16 v[216:219], v[36:39], a[56:59], v[216:219]\n\t"
  "ds_read_b64_tr_b16 v[54:55], v28 offset:45120\n\t"
  "v_mfma_f32_16x16x32_bf16 v[220:223], v[36:39], a[60:63], v[220:223]\n\t"
  "v_mfma_f32_16x16x32_bf16 v[220:223], v[32:35], a[44:47], v[220:223]\n\t"
  "s_waitcnt lgkmcnt(4)\n\t"
  "v_mfma_f32_16x16x32_bf16 v[240:243], v[40:43], a[32:35], v[240:243]\n\t"
  "ds_read_b64_tr_b16 v[32:33], v28 offset:32864\n\t"
  "v_mfma_f32_16x16x32_bf16 v[240:243], v[44:47], a[48:51], v[240:243]\n\t"
  "ds_read_b64_tr_b16 v[34:35], v28 offset:36960\n\t"
  "v_mfma_f32_16x16x32_bf16 v[244:247], v[44:47], a[52:55], v[244:247]\n\t"
  "buffer_load_dwordx4 a[148:151], v11, s[40:43], s71 offen offset:1024 sc1\n\t"
  "v_mfma_f32_16x16x32_bf16 v[244:247], v[40:43], a[36:39], v[244:247]\n\t"
  "v_mfma_f32_16x16x32_bf16 v[248:251], v[40:43], a[40:43], v[248:251]\n\t"
  "ds_read_b64_tr_b16 v[36:37], v28 offset:41056\n\t"
  "v_mfma_f32_16x16x32_bf16 v[248:251], v[44:47], a[56:59], v[248:251]\n\t"
  "ds_read_b64_tr_b16 v[38:39], v28 offset:45152\n\t"
  "v_mfma_f32_16x16x32_bf16 v[252:255], v[44:47], a[60:63], v[252:255]\n\t"
  "v_mfma_f32_16x16x32_bf16 v[252:255], v[40:43], a[44:47], v[252:255]\n\t"
  "buffer_load_dwordx4 a[152:155], v56, s[40:43], s71 offen offset:1024 sc1\n\t"
  "s_waitcnt lgkmcnt(4)\n\t"
  "v_mfma_f32_16x16x32_bf16 v[112:115], v[48:51], a[32:35], v[112:115]\n\t"
  "ds_read_b64_tr_b16 v[40:41], v28 offset:32896\n\t"
  "v_mfma_f32_16x16x32_bf16 v[112:115], v[52:55], a[48:51], v[112:115]\n\t"
  "ds_read_b64_tr_b16 v[42:43], v28 offset:36992\n\t"
  "v_mfma_f32_16x16x32_bf16 v[116:119], v[52:55], a[52:55], v[116:119]\n\t"
  "v_mfma_f32_16x16x32_bf16 v[116:119], v[48:51], a[36:39], v[116:119]\n\t"
  "v_mfma_f32_16x16x32_bf16 v[120:123], v[48:51], a[40:43], v[120:123]\n\t"
  "ds_read_b64_tr_b16 v[44:45], v28 offset:41088\n\t"
  "v_mfma_f32_16x16x32_bf16 v[120:123], v[52:55], a[56:59], v[120:123]\n\t"
  "ds_read_b64_tr_b16 v[46:47], v28 offset:45184\n\t"
  "v_mfma_f32_16x16x32_bf16 v[124:127], v[52:55], a[60:63], v[124:127]\n\t"
  "buffer_load_dwordx4 a[156:159], v57, s[40:43], s71 offen offset:1024 sc1\n\t"
  "v_mfma_f32_16x16x32_bf16 v[124:127], v[48:51], a[44:47], v[124:127]\n\t"
  "s_waitcnt lgkmcnt(4)\n\t"
  "v_mfma_f32_16x16x32_bf16 v[176:179], v[32:35], a[32:35], v[176:179]\n\t"
  "ds_read_b64_tr_b16 v[48:49], v28 offset:32928\n\t"
  "v_mfma_f32_16x16x32_bf16 v[176:179], v[36:39], a[48:51], v[176:179]\n\t"
  "ds_read_b64_tr_b16 v[50:51], v28 offset:37024\n\t"
  "v_mfma_f32_16x16x32_bf16 v[180:183], v[36:39], a[52:55], v[180:183]\n\t"
  "s_bfe_u32 s71, s84, 0x4001c\n\t"
  "s_lshl_b32 s71, s71, 11\n\t"
  "v_mfma_f32_16x16x32_bf16 v[180:183], v[32:35], a[36:39], v[180:183]\n\t"
  "buffer_load_dwordx4 a[160:163], v10, s[40:43], s71 offen offset:0 sc1\n\t"
  "v_mfma_f32_16x16x32_bf16 v[184:187], v[32:35], a[40:43], v[184:187]\n\t"
  "ds_read_b64_tr_b16 v[52:53], v28 offset:41120\n\t"
  "v_mov_b32 v27, s74\n\t"
  "v_mfma_f32_16x16x32_bf16 v[184:187], v[36:39], a[56:59], v[184:187]\n\t"
  "ds_read_b64_tr_b16 v[54:55], v28 offset:45216\n\t"
  "s_add_u32 m0, s69, 98304\n\t"
  "v_mfma_f32_16x16x32_bf16 v[188:191], v[36:39], a[60:63], v[188:191]\n\t"
  "s_bfe_u32 s70, s85, 0x40000\n\t"
  "s_mul_i32 s70, s70, 57344\n\t"
  "v_mfma_f32_16x16x32_bf16 v[188:191], v[32:35], a[44:47], v[188:191]\n\t"
  "s_waitcnt lgkmcnt(4)\n\t"
  "v_mfma_f32_16x16x32_bf16 v[64:67], v[40:43], a[32:35], v[64:67]\n\t"
  "ds_read_b64_tr_b16 v[32:33], v28 offset:32960\n\t"
  "v_mfma_f32_16x16x32_bf16 v[64:67], v[44:47], a[48:51], v[64:67]\n\t"
  "ds_read_b64_tr_b16 v[34:35], v28 offset:37056\n\t"
  "v_mfma_f32_16x16x32_bf16 v[68:71], v[44:47], a[52:55], v[68:71]\n\t"
  "s_barrier\n\t"
  "v_mfma_f32_16x16x32_bf16 v[68:71], v[40:43], a[36:39], v[68:71]\n\t"
  "buffer_load_dwordx4 v6, s[60:63], s70 offen offset:0  lds\n\t"
  "v_mfma_f32_16x16x32_bf16 v[72:75], v[40:43], a[40:43], v[72:75]\n\t"
  "ds_read_b64_tr_b16 v[36:37], v28 offset:41152\n\t"
  "v_mfma_f32_16x16x32_bf16 v[72:75], v[44:47], a[56:59], v[72:75]\n\t"
  "ds_read_b64_tr_b16 v[38:39], v28 offset:45248\n\t"
  "s_waitcnt vmcnt(13)\n\t"
  "v_mfma_f32_16x16x32_bf16 v[76:79], v[44:47], a[60:63], v[76:79]\n\t"
  "buffer_store_dword v27, v21, s[92:95], 0 offen offset:2048 sc1\n\t"
  "v_mfma_f32_16x16x32_bf16 v[76:79], v[40:43], a[44:47], v[76:79]\n\t"
  "s_waitcnt lgkmcnt(4)\n\t"
  "v_mfma_f32_16x16x32_bf16 v[96:99], v[48:51], a[32:35], v[96:99]\n\t"
  "ds_read_b64_tr_b16 v[40:41], v28 offset:49152\n\t"
  "v_mfma_f32_16x16x32_bf16 v[96:99], v[52:55], a[48:51], v[96:99]\n\t"
  "ds_read_b64_tr_b16 v[42:43], v28 offset:53248\n\t"
  "v_mfma_f32_16x16x32_bf16 v[100:103], v[52:55], a[52:55], v[100:103]\n\t"
  "buffer_load_dwordx4 v7, s[60:63], s70 offen offset:1024  lds\n\t"
  "v_mfma_f32_16x16x32_bf16 v[100:103], v[48:51], a[36:39], v[100:103]\n\t"
  "v_mfma_f32_16x16x32_bf16 v[104:107], v[48:51], a[40:43], v[104:107]\n\t"
  "ds_read_b64_tr_b16 v[44:45], v28 offset:57344\n\t"
  "v_mfma_f32_16x16x32_bf16 v[104:107], v[52:55], a[56:59], v[104:107]\n\t"
  "ds_read_b64_tr_b16 v[46:47], v28 offset:61440\n\t"
  "v_mfma_f32_16x16x32_bf16 v[108:111], v[52:55], a[60:63], v[108:111]\n\t"
  "v_mfma_f32_16x16x32_bf16 v[108:111], v[48:51], a[44:47], v[108:111]\n\t"
  "buffer_load_dwordx4 v8, s[60:63], s70 offen offset:2048  lds\n\t"
  "s_waitcnt lgkmcnt(4)\n\t"
  "v_mfma_f32_16x16x32_bf16 v[128:131], v[32:35], a[32:35], v[128:131]\n\t"
  "ds_read_b64_tr_b16 v[48:49], v28 offset:49184\n\t"
  "v_mfma_f32_16x16x32_bf16 v[128:131], v[36:39], a[48:51], v[128:131]\n\t"
  "ds_read_b64_tr_b16 v[50:51], v28 offset:53280\n\t"
  "v_mfma_f32_16x16x32_bf16 v[132:135], v[36:39], a[52:55], v[132:135]\n\t"
  "v_mfma_f32_16x16x32_bf16 v[132:135], v[32:35], a[36:39], v[132:135]\n\t"
  "v_mfma_f32_16x16x32_bf16 v[136:139], v[32:35], a[40:43], v[136:139]\n\t"
  "ds_read_b64_tr_b16 v[52:53], v28 offset:57376\n\t"
  "v_mfma_f32_16x16x32_bf16 v[136:139], v[36:39], a[56:59], v[136:139]\n\t"
  "ds_read_b64_tr_b16 v[54:55], v28 offset:61472\n\t"
  "v_mfma_f32_16x16x32_bf16 v[140:143], v[36:39], a[60:63], v[140:143]\n\t"
  "buffer_load_dwordx4 v9, s[60:63], s70 offen offset:3072  lds\n\t"
  "s_add_u32 m0, s69, 114688\n\t"
  "v_mfma_f32_16x16x32_bf16 v[140:143], v[32:35], a[44:47], v[140:143]\n\t"
  "s_bfe_u32 s70, s85, 0x40004\n\t"
  "s_waitcnt vmcnt(14) lgkmcnt(4)\n\t"
  "v_mfma_f32_16x16x32_bf16 v[208:211], v[40:43], a[96:99], v[208:211]\n\t"
  "ds_read_b64_tr_b16 v[32:33], v28 offset:49216\n\t"
  "s_mul_i32 s70, s70, 57344\n\t"
  "v_mfma_f32_16x16x32_bf16 v[208:211], v[44:47], a[112:115], v[208:211]\n\t"
  "ds_read_b64_tr_b16 v[34:35], v28 offset:53312\n\t"
  "v_mfma_f32_16x16x32_bf16 v[212:215], v[44:47], a[116:119], v[212:215]\n\t"
  "v_mfma_f32_16x16x32_bf16 v[212:215], v[40:43], a[100:103], v[212:215]\n\t"
  "buffer_load_dwordx4 v6, s[60:63], s70 offen offset:0  lds\n\t"
  "v_mfma_f32_16x16x32_bf16 v[216:219], v[40:43], a[104:107], v[216:219]\n\t"
  "ds_read_b64_tr_b16 v[36:37], v28 offset:57408\n\t"
  "v_mfma_f32_16x16x32_bf16 v[216:219], v[44:47], a[120:123], v[216:219]\n\t"
  "ds_read_b64_tr_b16 v[38:39], v28 offset:61504\n\t"
  "v_mfma_f32_16x16x32_bf16 v[220:223], v[44:47], a[124:127], v[220:223]\n\t"
  "v_mfma_f32_16x16x32_bf16 v[220:223], v[40:43], a[108:111], v[220:223]\n\t"
  "s_waitcnt lgkmcnt(4)\n\t"
  "v_mfma_f32_16x16x32_bf16 v[240:243], v[48:51], a[96:99], v[240:243]\n\t"
  "ds_read_b64_tr_b16 v[40:41], v28 offset:49248\n\t"
  "v_mfma_f32_16x16x32_bf16 v[240:243], v[52:55], a[112:115], v[240:243]\n\t"
  "ds_read_b64_tr_b16 v[42:43], v28 offset:53344\n\t"
  "v_mfma_f32_16x16x32_bf16 v[244:247], v[52:55], a[116:119], v[244:247]\n\t"
  "buffer_load_dwordx4 v7, s[60:63], s70 offen offset:1024  lds\n\t"
  "v_mfma_f32_16x16x32_bf16 v[244:247], v[48:51], a[100:103], v[244:247]\n\t"
  "v_mfma_f32_16x16x32_bf16 v[248:251], v[48:51], a[104:107], v[248:251]\n\t"
  "ds_read_b64_tr_b16 v[44:45], v28 offset:57440\n\t"
  "v_mfma_f32_16x16x32_bf16 v[248:251], v[52:55], a[120:123], v[248:251]\n\t"
  "ds_read_b64_tr_b16 v[46:47], v28 offset:61536\n\t"
  "v_mfma_f32_16x16x32_bf16 v[252:255], v[52:55], a[124:127], v[252:255]\n\t"
  "v_mfma_f32_16x16x32_bf16 v[252:255], v[48:51], a[108:111], v[252:255]\n\t"
  "buffer_load_dwordx4 v8, s[60:63], s70 offen offset:2048  lds\n\t"
  "s_waitcnt lgkmcnt(4)\n\t"
  "v_mfma_f32_16x16x32_bf16 v[112:115], v[32:35], a[96:99], v[112:115]\n\t"
  "ds_read_b64_tr_b16 v[48:49], v28 offset:49280\n\t"
  "v_mfma_f32_16x16x32_bf16 v[112:115], v[36:39], a[112:115], v[112:115]\n\t"
  "ds_read_b64_tr_b16 v[50:51], v28 offset:53376\n\t"
  "v_mfma_f32_16x16x32_bf16 v[116:119], v[36:39], a[116:119], v[116:119]\n\t"
  "v_mfma_f32_16x16x32_bf16 v[116:119], v[32:35], a[100:103], v[116:119]\n\t"
  "v_mfma_f32_16x16x32_bf16 v[120:123], v[32:35], a[104:107], v[120:123]\n\t"
  "ds_read_b64_tr_b16 v[52:53], v28 offset:57472\n\t"
  "v_mfma_f32_16x16x32_bf16 v[120:123], v[36:39], a[120:123], v[120:123]\n\t"
  "ds_read_b64_tr_b16 v[54:55], v28 offset:61568\n\t"
  "v_mfma_f32_16x16x32_bf16 v[124:127], v[36:39], a[124:127], v[124:127]\n\t"
  "buffer_load_dwordx4 v9, s[60:63], s70 offen offset:3072  lds\n\t"
  "v_mfma_f32_16x16x32_bf16 v[124:127], v[32:35], a[108:111], v[124:127]\n\t"
  "s_waitcnt lgkmcnt(4)\n\t"
  "v_mfma_f32_16x16x32_bf16 v[176:179], v[40:43], a[96:99], v[176:179]\n\t"
  "ds_read_b64_tr_b16 v[32:33], v28 offset:49312\n\t"
  "v_mfma_f32_16x16x32_bf16 v[176:179], v[44:47], a[112:115], v[176:179]\n\t"
  "ds_read_b64_tr_b16 v[34:35], v28 offset:53408\n\t"
  "v_mfma_f32_16x16x32_bf16 v[180:183], v[44:47], a[116:119], v[180:183]\n\t"
  "buffer_load_dwordx4 a[164:167], v11, s[40:43], s71 offen offset:0 sc1\n\t"
  "v_mfma_f32_16x16x32_bf16 v[180:183], v[40:43], a[100:103], v[180:183]\n\t"
  "v_mfma_f32_16x16x32_bf16 v[184:187], v[40:43], a[104:107], v[184:187]\n\t"
  "ds_read_b64_tr_b16 v[36:37], v28 offset:57504\n\t"
  "v_mfma_f32_16x16x32_bf16 v[184:187], v[44:47], a[120:123], v[184:187]\n\t"
  "ds_read_b64_tr_b16 v[38:39], v28 offset:61600\n\t"
  "v_mfma_f32_16x16x32_bf16 v[188:191], v[44:47], a[124:127], v[188:191]\n\t"
  "buffer_load_dwordx4 a[168:171], v56, s[40:43], s71 offen offset:0 sc1\n\t"
  "v_mfma_f32_16x16x32_bf16 v[188:191], v[40:43], a[108:111], v[188:191]\n\t"
  "s_waitcnt lgkmcnt(4)\n\t"
  "v_mfma_f32_16x16x32_bf16 v[64:67], v[48:51], a[96:99], v[64:67]\n\t"
  "ds_read_b64_tr_b16 v[40:41], v28 offset:49344\n\t"
  "v_mfma_f32_16x16x32_bf16 v[64:67], v[52:55], a[112:115], v[64:67]\n\t"
  "ds_read_b64_tr_b16 v[42:43], v28 offset:53440\n\t"
  "v_mfma_f32_16x16x32_bf16 v[68:71], v[52:55], a[116:119], v[68:71]\n\t"
  "buffer_load_dwordx4 a[172:175], v57, s[40:43], s71 offen offset:0 sc1\n\t"
  "v_mfma_f32_16x16x32_bf16 v[68:71], v[48:51], a[100:103], v[68:71]\n\t"
  "v_mfma_f32_16x16x32_bf16 v[72:75], v[48:51], a[104:107], v[72:75]\n\t"
  "ds_read_b64_tr_b16 v[44:45], v28 offset:57536\n\t"
  "v_mfma_f32_16x16x32_bf16 v[72:75], v[52:55], a[120:123], v[72:75]\n\t"
  "ds_read_b64_tr_b16 v[46:47], v28 offset:61632\n\t"
  "v_mfma_f32_16x16x32_bf16 v[76:79], v[52:55], a[124:127], v[76:79]\n\t"
  "buffer_load_dwordx4 a[176:179], v10, s[40:43], s71 offen offset:1024 sc1\n\t"
  "v_mfma_f32_16x16x32_bf16 v[76:79], v[48:51], a[108:111], v[76:79]\n\t"
  "s_waitcnt lgkmcnt(4)\n\t"
  "v_mfma_f32_16x16x32_bf16 v[96:99], v[32:35], a[96:99], v[96:99]\n\t"
  "ds_read_b64_tr_b16 v[48:49], v28 offset:0\n\t"
  "v_mfma_f32_16x16x32_bf16 v[96:99], v[36:39], a[112:115], v[96:99]\n\t"
  "ds_read_b64_tr_b16 v[50:51], v28 offset:4096\n\t"
  "v_mfma_f32_16x16x32_bf16 v[100:103], v[36:39], a[116:119], v[100:103]\n\t"
  "buffer_load_dwordx4 a[180:183], v11, s[40:43], s71 offen offset:1024 sc1\n\t"
  "v_mfma_f32_16x16x32_bf16 v[100:103], v[32:35], a[100:103], v[100:103]\n\t"
  "v_mfma_f32_16x16x32_bf16 v[104:107], v[32:35], a[104:107], v[104:107]\n\t"
  "ds_read_b64_tr_b16 v[52:53], v28 offset:8192\n\t"
  "v_mfma_f32_16x16x32_bf16 v[104:107], v[36:39], a[120:123], v[104:107]\n\t"
  "ds_read_b64_tr_b16 v[54:55], v28 offset:12288\n\t"
  "v_mfma_f32_16x16x32_bf16 v[108:111], v[36:39], a[124:127], v[108:111]\n\t"
  "buffer_load_dwordx4 a[184:187], v56, s[40:43], s71 offen offset:1024 sc1\n\t"
  "v_mfma_f32_16x16x32_bf16 v[108:111], v[32:35], a[108:111], v[108:111]\n\t"
  "s_waitcnt lgkmcnt(4)\n\t"
  "v_mfma_f32_16x16x32_bf16 v[128:131], v[40:43], a[96:99], v[128:131]\n\t"
  "ds_read_b64_tr_b16 v[32:33], v28 offset:32\n\t"
  "v_mfma_f32_16x16x32_bf16 v[128:131], v[44:47], a[112:115], v[128:131]\n\t"
  "ds_read_b64_tr_b16 v[34:35], v28 offset:4128\n\t"
  "v_mfma_f32_16x16x32_bf16 v[132:135], v[44:47], a[116:119], v[132:135]\n\t"
  "buffer_load_dwordx4 a[188:191], v57, s[40:43], s71 offen offset:1024 sc1\n\t"
  "v_mfma_f32_16x16x32_bf16 v[132:135], v[40:43], a[100:103], v[132:135]\n\t"
  "v_mfma_f32_16x16x32_bf16 v[136:139], v[40:43], a[104:107], v[136:139]\n\t"
  "ds_read_b64_tr_b16 v[36:37], v28 offset:8224\n\t"
  "v_mfma_f32_16x16x32_bf16 v[136:139], v[44:47], a[120:123], v[136:139]\n\t"
  "ds_read_b64_tr_b16 v[38:39], v28 offset:12320\n\t"
  "v_mfma_f32_16x16x32_bf16 v[140:143], v[44:47], a[124:127], v[140:143]\n\t"
  "v_mfma_f32_16x16x32_bf16 v[140:143], v[40:43], a[108:111], v[140:143]\n\t"
  "s_waitcnt vmcnt(17) lgkmcnt(4)\n\t"
  "v_mfma_f32_16x16x32_bf16 v[208:211], v[48:51], a[128:131], v[208:211]\n\t"
  "ds_read_b64_tr_b16 v[40:41], v28 offset:64\n\t"
  "v_mfma_f32_16x16x32_bf16 v[208:211], v[52:55], a[144:147], v[208:211]\n\t"
  "ds_read_b64_tr_b16 v[42:43], v28 offset:4160\n\t"
  "v_mfma_f32_16x16x32_bf16 v[212:215], v[52:55], a[148:151], v[212:215]\n\t"
  "buffer_load_dword v26, v30, s[92:95], 0 offen offset:2048 sc1\n\t"
  "v_mfma_f32_16x16x32_bf16 v[212:215], v[48:51], a[132:135], v[212:215]\n\t"
  "v_mfma_f32_16x16x32_bf16 v[216:219], v[48:51], a[136:139], v[216:219]\n\t"
  "ds_read_b64_tr_b16 v[44:45], v28 offset:8256\n\t"
  "v_mfma_f32_16x16x32_bf16 v[216:219], v[52:55], a[152:155], v[216:219]\n\t"
  "ds_read_b64_tr_b16 v[46:47], v28 offset:12352\n\t"
  "v_mfma_f32_16x16x32_bf16 v[220:223], v[52:55], a[156:159], v[220:223]\n\t"
  "s_bfe_u32 s71, s85, 0x40004\n\t"
  "s_lshl_b32 s71, s71, 11\n\t"
  "v_mfma_f32_16x16x32_bf16 v[220:223], v[48:51], a[140:143], v[220:223]\n\t"
  "buffer_load_dwordx4 a[100:103], v11, s[40:43], s71 offen offset:0 sc1\n\t"
  "s_waitcnt lgkmcnt(4)\n\t"
  "v_mfma_f32_16x16x32_bf16 v[240:243], v[32:35], a[128:131], v[240:243]\n\t"
  "ds_read_b64_tr_b16 v[48:49], v28 offset:96\n\t"
  "v_mfma_f32_16x16x32_bf16 v[240:243], v[36:39], a[144:147], v[240:243]\n\t"
  "ds_read_b64_tr_b16 v[50:51], v28 offset:4192\n\t"
  "v_mfma_f32_16x16x32_bf16 v[244:247], v[36:39], a[148:151], v[244:247]\n\t"
  "v_mfma_f32_16x16x32_bf16 v[244:247], v[32:35], a[132:135], v[244:247]\n\t"
  "v_mfma_f32_16x16x32_bf16 v[248:251], v[32:35], a[136:139], v[248:251]\n\t"
  "ds_read_b64_tr_b16 v[52:53], v28 offset:8288\n\t"
  "v_mfma_f32_16x16x32_bf16 v[248:251], v[36:39], a[152:155], v[248:251]\n\t"
  "ds_read_b64_tr_b16 v[54:55], v28 offset:12384\n\t"
  "v_mfma_f32_16x16x32_bf16 v[252:255], v[36:39], a[156:159], v[252:255]\n\t"
  "buffer_load_dwordx4 a[104:107], v56, s[40:43], s71 offen offset:0 sc1\n\t"
  "v_mfma_f32_16x16x32_bf16 v[252:255], v[32:35], a[140:143], v[252:255]\n\t"
  "s_waitcnt lgkmcnt(4)\n\t"
  "v_mfma_f32_16x16x32_bf16 v[112:115], v[40:43], a[128:131], v[112:115]\n\t"
  "ds_read_b64_tr_b16 v[32:33], v28 offset:128\n\t"
  "v_mfma_f32_16x16x32_bf16 v[112:115], v[44:47], a[144:147], v[112:115]\n\t"
  "ds_read_b64_tr_b16 v[34:35], v28 offset:4224\n\t"
  "v_mfma_f32_16x16x32_bf16 v[116:119], v[44:47], a[148:151], v[116:119]\n\t"
  "v_mfma_f32_16x16x32_bf16 v[116:119], v[40:43], a[132:135], v[116:119]\n\t"
  "buffer_load_dwordx4 a[108:111], v57, s[40:43], s71 offen offset:0 sc1\n\t"
  "v_mfma_f32_16x16x32_bf16 v[120:123], v[40:43], a[136:139], v[120:123]\n\t"
  "ds_read_b64_tr_b16 v[36:37], v28 offset:8320\n\t"
  "v_mfma_f32_16x16x32_bf16 v[120:123], v[44:47], a[152:155], v[120:123]\n\t"
  "ds_read_b64_tr_b16 v[38:39], v28 offset:12416\n\t"
  "v_mfma_f32_16x16x32_bf16 v[124:127], v[44:47], a[156:159], v[124:127]\n\t"
  "v_mfma_f32_16x16x32_bf16 v[124:127], v[40:43], a[140:143], v[124:127]\n\t"
  "s_waitcnt lgkmcnt(4)\n\t"
  "v_mfma_f32_16x16x32_bf16 v[176:179], v[48:51], a[128:131], v[176:179]\n\t"
  "ds_read_b64_tr_b16 v[40:41], v28 offset:160\n\t"
  "v_mfma_f32_16x16x32_bf16 v[176:179], v[52:55], a[144:147], v[176:179]\n\t"
  "ds_read_b64_tr_b16 v[42:43], v28 offset:4256\n\t"
  "v_mfma_f32_16x16x32_bf16 v[180:183], v[52:55], a[148:151], v[180:183]\n\t"
  "buffer_load_dwordx4 a[116:119], v11, s[40:43], s71 offen offset:1024 sc1\n\t"
  "v_mfma_f32_16x16x32_bf16 v[180:183], v[48:51], a[132:135], v[180:183]\n\t"
  "v_mfma_f32_16x16x32_bf16 v[184:187], v[48:51], a[136:139], v[184:187]\n\t"
  "ds_read_b64_tr_b16 v[44:45], v28 offset:8352\n\t"
  "v_mfma_f32_16x16x32_bf16 v[184:187], v[52:55], a[152:155], v[184:187]\n\t"
  "ds_read_b64_tr_b16 v[46:47], v28 offset:12448\n\t"
  "s_add_u32 m0, s69, 131072\n\t"
  "v_mfma_f32_16x16x32_bf16 v[188:191], v[52:55], a[156:159], v[188:191]\n\t"
  "s_bfe_u32 s70, s85, 0x40008\n\t"
  "s_mul_i32 s70, s70, 57344\n\t"
  "v_mfma_f32_16x16x32_bf16 v[188:191], v[48:51], a[140:143], v[188:191]\n\t"
  "s_waitcnt lgkmcnt(4)\n\t"
  "v_mfma_f32_16x16x32_bf16 v[64:67], v[32:35], a[128:131], v[64:67]\n\t"
  "ds_read_b64_tr_b16 v[48:49], v28 offset:192\n\t"
  "v_mfma_f32_16x16x32_bf16 v[64:67], v[36:39], a[144:147], v[64:67]\n\t"
  "ds_read_b64_tr_b16 v[50:51], v28 offset:4288\n\t"
  "v_mfma_f32_16x16x32_bf16 v[68:71], v[36:39], a[148:151], v[68:71]\n\t"
  "s_waitcnt vmcnt(16)\n\t"
  "s_barrier\n\t"
  "v_mfma_f32_16x16x32_bf16 v[68:71], v[32:35], a[132:135], v[68:71]\n\t"
  "buffer_load_dwordx4 v6, s[60:63], s70 offen offset:0  lds\n\t"
  "v_mfma_f32_16x16x32_bf16 v[72:75], v[32:35], a[136:139], v[72:75]\n\t"
  "ds_read_b64_tr_b16 v[52:53], v28 offset:8384\n\t"
  "v_mfma_f32_16x16x32_bf16 v[72:75], v[36:39], a[152:155], v[72:75]\n\t"
  "ds_read_b64_tr_b16 v[54:55], v28 offset:12480\n\t"
  "v_mfma_f32_16x16x32_bf16 v[76:79], v[36:39], a[156:159], v[76:79]\n\t"
  "v_mfma_f32_16x16x32_bf16 v[76:79], v[32:35], a[140:143], v[76:79]\n\t"
  "s_waitcnt lgkmcnt(4)\n\t"
  "v_mfma_f32_16x16x32_bf16 v[96:99], v[40:43], a[128:131], v[96:99]\n\t"
  "ds_read_b64_tr_b16 v[32:33], v28 offset:16384\n\t"
  "v_mfma_f32_16x16x32_bf16 v[96:99], v[44:47], a[144:147], v[96:99]\n\t"
  "ds_read_b64_tr_b16 v[34:35], v28 offset:20480\n\t"
  "v_mfma_f32_16x16x32_bf16 v[100:103], v[44:47], a[148:151], v[100:103]\n\t"
  "buffer_load_dwordx4 v7, s[60:63], s70 offen offset:1024  lds\n\t"
  "v_mfma_f32_16x16x32_bf16 v[100:103], v[40:43], a[132:135], v[100:103]\n\t"
  "v_mfma_f32_16x16x32_bf16 v[104:107], v[40:43], a[136:139], v[104:107]\n\t"
  "ds_read_b64_tr_b16 v[36:37], v28 offset:24576\n\t"
  "v_mfma_f32_16x16x32_bf16 v[104:107], v[44:47], a[152:155], v[104:107]\n\t"
  "ds_read_b64_tr_b16 v[38:39], v28 offset:28672\n\t"
  "v_mfma_f32_16x16x32_bf16 v[108:111], v[44:47], a[156:159], v[108:111]\n\t"
  "v_mfma_f32_16x16x32_bf16 v[108:111], v[40:43], a[140:143], v[108:111]\n\t"
  "buffer_load_dwordx4 v8, s[60:63], s70 offen offset:2048  lds\n\t"
  "s_waitcnt lgkmcnt(4)\n\t"
  "v_mfma_f32_16x16x32_bf16 v[128:131], v[48:51], a[128:131], v[128:131]\n\t"
  "ds_read_b64_tr_b16 v[40:41], v28 offset:16416\n\t"
  "v_mfma_f32_16x16x32_bf16 v[128:131], v[52:55], a[144:147], v[128:131]\n\t"
  "ds_read_b64_tr_b16 v[42:43], v28 offset:20512\n\t"
  "v_mfma_f32_16x16x32_bf16 v[132:135], v[52:55], a[148:151], v[132:135]\n\t"
  "v_mfma_f32_16x16x32_bf16 v[132:135], v[48:51], a[132:135], v[132:135]\n\t"
  "v_mfma_f32_16x16x32_bf16 v[136:139], v[48:51], a[136:139], v[136:139]\n\t"
  "ds_read_b64_tr_b16 v[44:45], v28 offset:24608\n\t"
  "v_mfma_f32_16x16x32_bf16 v[136:139], v[52:55], a[152:155], v[136:139]\n\t"
  "ds_read_b64_tr_b16 v[46:47], v28 offset:28704\n\t"
  "v_mfma_f32_16x16x32_bf16 v[140:143], v[52:55], a[156:159], v[140:143]\n\t"
  "buffer_load_dwordx4 v9, s[60:63], s70 offen offset:3072  lds\n\t"
  "s_add_u32 m0, s69, 147456\n\t"
  "v_mfma_f32_16x16x32_bf16 v[140:143], v[48:51], a[140:143], v[140:143]\n\t"
  "s_bfe_u32 s70, s85, 0x4000c\n\t"
  "s_waitcnt vmcnt(9) lgkmcnt(4)\n\t"
  "v_mfma_f32_16x16x32_bf16 v[208:211], v[32:35], a[160:163], v[208:211]\n\t"
  "ds_read_b64_tr_b16 v[48:49], v28 offset:16448\n\t"
  "s_mul_i32 s70, s70, 57344\n\t"
  "v_mfma_f32_16x16x32_bf16 v[208:211], v[36:39], a[176:179], v[208:211]\n\t"
  "ds_read_b64_tr_b16 v[50:51], v28 offset:20544\n\t"
  "v_mfma_f32_16x16x32_bf16 v[212:215], v[36:39], a[180:183], v[212:215]\n\t"
  "v_mfma_f32_16x16x32_bf16 v[212:215], v[32:35], a[164:167], v[212:215]\n\t"
  "buffer_load_dwordx4 v6, s[60:63], s70 offen offset:0  lds\n\t"
  "v_mfma_f32_16x16x32_bf16 v[216:219], v[32:35], a[168:171], v[216:219]\n\t"
  "ds_read_b64_tr_b16 v[52:53], v28 offset:24640\n\t"
  "v_mfma_f32_16x16x32_bf16 v[216:219], v[36:39], a[184:187], v[216:219]\n\t"
  "ds_read_b64_tr_b16 v[54:55], v28 offset:28736\n\t"
  "v_mfma_f32_16x16x32_bf16 v[220:223], v[36:39], a[188:191], v[220:223]\n\t"
  "v_mfma_f32_16x16x32_bf16 v[220:223], v[32:35], a[172:175], v[220:223]\n\t"
  "s_waitcnt lgkmcnt(4)\n\t"
  "v_mfma_f32_16x16x32_bf16 v[240:243], v[40:43], a[160:163], v[240:243]\n\t"
  "ds_read_b64_tr_b16 v[32:33], v28 offset:16480\n\t"
  "v_mfma_f32_16x16x32_bf16 v[240:243], v[44:47], a[176:179], v[240:243]\n\t"
  "ds_read_b64_tr_b16 v[34:35], v28 offset:20576\n\t"
  "v_mfma_f32_16x16x32_bf16 v[244:247], v[44:47], a[180:183], v[244:247]\n\t"
  "buffer_load_dwordx4 v7, s[60:63], s70 offen offset:1024  lds\n\t"
  "v_mfma_f32_16x16x32_bf16 v[244:247], v[40:43], a[164:167], v[244:247]\n\t"
  "v_mfma_f32_16x16x32_bf16 v[248:251], v[40:43], a[168:171], v[248:251]\n\t"
  "ds_read_b64_tr_b16 v[36:37], v28 offset:24672\n\t"
  "v_mfma_f32_16x16x32_bf16 v[248:251], v[44:47], a[184:187], v[248:251]\n\t"
  "ds_read_b64_tr_b16 v[38:39], v28 offset:28768\n\t"
  "v_mfma_f32_16x16x32_bf16 v[252:255], v[44:47], a[188:191], v[252:255]\n\t"
  "v_mfma_f32_16x16x32_bf16 v[252:255], v[40:43], a[172:175], v[252:255]\n\t"
  "buffer_load_dwordx4 v8, s[60:63], s70 offen offset:2048  lds\n\t"
  "s_waitcnt lgkmcnt(4)\n\t"
  "v_mfma_f32_16x16x32_bf16 v[112:115], v[48:51], a[160:163], v[112:115]\n\t"
  "ds_read_b64_tr_b16 v[40:41], v28 offset:16512\n\t"
  "v_mfma_f32_16x16x32_bf16 v[112:115], v[52:55], a[176:179], v[112:115]\n\t"
  "ds_read_b64_tr_b16 v[42:43], v28 offset:20608\n\t"
  "v_mfma_f32_16x16x32_bf16 v[116:119], v[52:55], a[180:183], v[116:119]\n\t"
  "v_mfma_f32_16x16x32_bf16 v[116:119], v[48:51], a[164:167], v[116:119]\n\t"
  "v_mfma_f32_16x16x32_bf16 v[120:123], v[48:51], a[168:171], v[120:123]\n\t"
  "ds_read_b64_tr_b16 v[44:45], v28 offset:24704\n\t"
  "v_mfma_f32_16x16x32_bf16 v[120:123], v[52:55], a[184:187], v[120:123]\n\t"
  "ds_read_b64_tr_b16 v[46:47], v28 offset:28800\n\t"
  "v_mfma_f32_16x16x32_bf16 v[124:127], v[52:55], a[188:191], v[124:127]\n\t"
  "buffer_load_dwordx4 v9, s[60:63], s70 offen offset:3072  lds\n\t"
  "v_mfma_f32_16x16x32_bf16 v[124:127], v[48:51], a[172:175], v[124:127]\n\t"
  "s_waitcnt lgkmcnt(4)\n\t"
  "v_mfma_f32_16x16x32_bf16 v[176:179], v[32:35], a[160:163], v[176:179]\n\t"
  "ds_read_b64_tr_b16 v[48:49], v28 offset:16544\n\t"
  "v_mfma_f32_16x16x32_bf16 v[176:179], v[36:39], a[176:179], v[176:179]\n\t"
  "ds_read_b64_tr_b16 v[50:51], v28 offset:20640\n\t"
  "v_mfma_f32_16x16x32_bf16 v[180:183], v[36:39], a[180:183], v[180:183]\n\t"
  "buffer_load_dwordx4 a[120:123], v56, s[40:43], s71 offen offset:1024 sc1\n\t"
  "v_mfma_f32_16x16x32_bf16 v[180:183], v[32:35], a[164:167], v[180:183]\n\t"
  "v_mfma_f32_16x16x32_bf16 v[184:187], v[32:35], a[168:171], v[184:187]\n\t"
  "ds_read_b64_tr_b16 v[52:53], v28 offset:24736\n\t"
  "v_mfma_f32_16x16x32_bf16 v[184:187], v[36:39], a[184:187], v[184:187]\n\t"
  "ds_read_b64_tr_b16 v[54:55], v28 offset:28832\n\t"
  "v_mfma_f32_16x16x32_bf16 v[188:191], v[36:39], a[188:191], v[188:191]\n\t"
  "buffer_load_dwordx4 a[124:127], v57, s[40:43], s71 offen offset:1024 sc1\n\t"
  "v_mfma_f32_16x16x32_bf16 v[188:191], v[32:35], a[172:175], v[188:191]\n\t"
  "s_waitcnt lgkmcnt(4)\n\t"
  "v_mfma_f32_16x16x32_bf16 v[64:67], v[40:43], a[160:163], v[64:67]\n\t"
  "ds_read_b64_tr_b16 v[32:33], v28 offset:16576\n\t"
  "v_mfma_f32_16x16x32_bf16 v[64:67], v[44:47], a[176:179], v[64:67]\n\t"
  "ds_read_b64_tr_b16 v[34:35], v28 offset:20672\n\t"
  "v_mfma_f32_16x16x32_bf16 v[68:71], v[44:47], a[180:183], v[68:71]\n\t"
  "s_bfe_u32 s71, s85, 0x40008\n\t"
  "s_lshl_b32 s71, s71, 11\n\t"
  "v_mfma_f32_16x16x32_bf16 v[68:71], v[40:43], a[164:167], v[68:71]\n\t"
  "buffer_load_dwordx4 a[132:135], v11, s[40:43], s71 offen offset:0 sc1\n\t"
  "v_mfma_f32_16x16x32_bf16 v[72:75], v[40:43], a[168:171], v[72:75]\n\t"
  "ds_read_b64_tr_b16 v[36:37], v28 offset:24768\n\t"
  "v_mfma_f32_16x16x32_bf16 v[72:75], v[44:47], a[184:187], v[72:75]\n\t"
  "ds_read_b64_tr_b16 v[38:39], v28 offset:28864\n\t"
  "v_mfma_f32_16x16x32_bf16 v[76:79], v[44:47], a[188:191], v[76:79]\n\t"
  "v_mfma_f32_16x16x32_bf16 v[76:79], v[40:43], a[172:175], v[76:79]\n\t"
  "s_waitcnt lgkmcnt(4)\n\t"
  "v_mfma_f32_16x16x32_bf16 v[96:99], v[48:51], a[160:163], v[96:99]\n\t"
  "ds_read_b64_tr_b16 v[40:41], v29 offset:32768\n\t"
  "v_mfma_f32_16x16x32_bf16 v[96:99], v[52:55], a[176:179], v[96:99]\n\t"
  "ds_read_b64_tr_b16 v[42:43], v29 offset:36864\n\t"
  "v_mfma_f32_16x16x32_bf16 v[100:103], v[52:55], a[180:183], v[100:103]\n\t"
  "buffer_load_dwordx4 a[136:139], v56, s[40:43], s71 offen offset:0 sc1\n\t"
  "v_mfma_f32_16x16x32_bf16 v[100:103], v[48:51], a[164:167], v[100:103]\n\t"
  "v_mfma_f32_16x16x32_bf16 v[104:107], v[48:51], a[168:171], v[104:107]\n\t"
  "ds_read_b64_tr_b16 v[44:45], v29 offset:40960\n\t"
  "v_mfma_f32_16x16x32_bf16 v[104:107], v[52:55], a[184:187], v[104:107]\n\t"
  "ds_read_b64_tr_b16 v[46:47], v29 offset:45056\n\t"
  "v_mfma_f32_16x16x32_bf16 v[108:111], v[52:55], a[188:191], v[108:111]\n\t"
  "v_mfma_f32_16x16x32_bf16 v[108:111], v[48:51], a[172:175], v[108:111]\n\t"
  "buffer_load_dwordx4 a[140:143], v57, s[40:43], s71 offen offset:0 sc1\n\t"
  "s_waitcnt lgkmcnt(4)\n\t"
  "v_mfma_f32_16x16x32_bf16 v[128:131], v[32:35], a[160:163], v[128:131]\n\t"
  "ds_read_b64_tr_b16 v[48:49], v29 offset:32800\n\t"
  "v_mfma_f32_16x16x32_bf16 v[128:131], v[36:39], a[176:179], v[128:131]\n\t"
  "ds_read_b64_tr_b16 v[50:51], v29 offset:36896\n\t"
  "v_mfma_f32_16x16x32_bf16 v[132:135], v[36:39], a[180:183], v[132:135]\n\t"
  "v_mfma_f32_16x16x32_bf16 v[132:135], v[32:35], a[164:167], v[132:135]\n\t"
  "buffer_load_dwordx4 a[148:151], v11, s[40:43], s71 offen offset:1024 sc1\n\t"
  "v_mfma_f32_16x16x32_bf16 v[136:139], v[32:35], a[168:171], v[136:139]\n\t"
  "ds_read_b64_tr_b16 v[52:53], v29 offset:40992\n\t"
  "v_mfma_f32_16x16x32_bf16 v[136:139], v[36:39], a[184:187], v[136:139]\n\t"
  "ds_read_b64_tr_b16 v[54:55], v29 offset:45088\n\t"
  "v_mfma_f32_16x16x32_bf16 v[140:143], v[36:39], a[188:191], v[140:143]\n\t"
  "v_mfma_f32_16x16x32_bf16 v[140:143], v[32:35], a[172:175], v[140:143]\n\t"
  "buffer_load_dwordx4 a[152:155], v56, s[40:43], s71 offen offset:1024 sc1\n\t"
  "s_waitcnt lgkmcnt(4)\n\t"
  "v_mfma_f32_16x16x32_bf16 v[212:215], v[40:43], a[68:71], v[212:215]\n\t"
  "ds_read_b64_tr_b16 v[32:33], v29 offset:32832\n\t"
  "s_waitcnt vmcnt(19)\n\t"
  "v_subrev_u32 v26, s74, v26\n\t"
  "v_mfma_f32_16x16x32_bf16 v[212:215], v[44:47], a[84:87], v[212:215]\n\t"
  "ds_read_b64_tr_b16 v[34:35], v29 offset:36928\n\t"
  "v_cmp_lt_u32 vcc, 1, v26\n\t"
  "v_mfma_f32_16x16x32_bf16 v[216:219], v[44:47], a[88:91], v[216:219]\n\t"
  "s_nop 4\n\t"
  "s_cbranch_vccnz L_k1_sp3\n\t"
  "L_k1_rs3:\n\t"
  "v_mfma_f32_16x16x32_bf16 v[216:219], v[40:43], a[72:75], v[216:219]\n\t"
  "ds_read_b64_tr_b16 v[36:37], v29 offset:41024\n\t"
  "v_mfma_f32_16x16x32_bf16 v[220:223], v[40:43], a[76:79], v[220:223]\n\t"
  "ds_read_b64_tr_b16 v[38:39], v29 offset:45120\n\t"
  "v_mfma_f32_16x16x32_bf16 v[220:223], v[44:47], a[92:95], v[220:223]\n\t"
  "buffer_load_dwordx4 a[156:159], v57, s[40:43], s71 offen offset:1024 sc1\n\t"
  "s_waitcnt lgkmcnt(4)\n\t"
  "v_mfma_f32_16x16x32_bf16 v[244:247], v[48:51], a[68:71], v[244:247]\n\t"
  "ds_read_b64_tr_b16 v[40:41], v29 offset:32864\n\t"
  "v_mfma_f32_16x16x32_bf16 v[244:247], v[52:55], a[84:87], v[244:247]\n\t"
  "ds_read_b64_tr_b16 v[42:43], v29 offset:36960\n\t"
  "v_mfma_f32_16x16x32_bf16 v[248:251], v[52:55], a[88:91], v[248:251]\n\t"
  "v_mfma_f32_16x16x32_bf16 v[248:251], v[48:51], a[72:75], v[248:251]\n\t"
  "ds_read_b64_tr_b16 v[44:45], v29 offset:41056\n\t"
  "v_mfma_f32_16x16x32_bf16 v[252:255], v[48:51], a[76:79], v[252:255]\n\t"
  "ds_read_b64_tr_b16 v[46:47], v29 offset:45152\n\t"
  "s_bfe_u32 s71, s85, 0x4000c\n\t"
  "s_lshl_b32 s71, s71, 11\n\t"
  "v_mfma_f32_16x16x32_bf16 v[252:255], v[52:55], a[92:95], v[252:255]\n\t"
  "buffer_load_dwordx4 a[164:167], v11, s[40:43], s71 offen offset:0 sc1\n\t"
  "s_waitcnt lgkmcnt(4)\n\t"
  "v_mfma_f32_16x16x32_bf16 v[116:119], v[32:35], a[68:71], v[116:119]\n\t"
  "ds_read_b64_tr_b16 v[48:49], v29 offset:32896\n\t"
  "v_mfma_f32_16x16x32_bf16 v[116:119], v[36:39], a[84:87], v[116:119]\n\t"
  "ds_read_b64_tr_b16 v[50:51], v29 offset:36992\n\t"
  "v_mfma_f32_16x16x32_bf16 v[120:123], v[36:39], a[88:91], v[120:123]\n\t"
  "v_mfma_f32_16x16x32_bf16 v[120:123], v[32:35], a[72:75], v[120:123]\n\t"
  "ds_read_b64_tr_b16 v[52:53], v29 offset:41088\n\t"
  "v_mfma_f32_16x16x32_bf16 v[124:127], v[32:35], a[76:79], v[124:127]\n\t"
  "ds_read_b64_tr_b16 v[54:55], v29 offset:45184\n\t"
  "v_mfma_f32_16x16x32_bf16 v[124:127], v[36:39], a[92:95], v[124:127]\n\t"
  "buffer_load_dwordx4 a[168:171], v56, s[40:43], s71 offen offset:0 sc1\n\t"
  "s_add_u32 m0, s69, 81920\n\t"
  "s_waitcnt lgkmcnt(4)\n\t"
  "v_mfma_f32_16x16x32_bf16 v[180:183], v[40:43], a[68:71], v[180:183]\n\t"
  "ds_read_b64_tr_b16 v[32:33], v29 offset:32928\n\t"
  "s_bfe_u32 s70, s85, 0x40014\n\t"
  "v_mfma_f32_16x16x32_bf16 v[180:183], v[44:47], a[84:87], v[180:183]\n\t"
  "ds_read_b64_tr_b16 v[34:35], v29 offset:37024\n\t"
  "s_mul_i32 s70, s70, 57344\n\t"
  "v_mfma_f32_16x16x32_bf16 v[184:187], v[44:47], a[88:91], v[184:187]\n\t"
  "s_waitcnt vmcnt(14)\n\t"
  "s_barrier\n\t"
  "v_mfma_f32_16x16x32_bf16 v[184:187], v[40:43], a[72:75], v[184:187]\n\t"
  "ds_read_b64_tr_b16 v[36:37], v29 offset:41120\n\t"
  "v_mfma_f32_16x16x32_bf16 v[188:191], v[40:43], a[76:79], v[188:191]\n\t"
  "ds_read_b64_tr_b16 v[38:39], v29 offset:45216\n\t"
  "v_mov_b32 v27, s91\n\t"
  "v_mfma_f32_16x16x32_bf16 v[188:191], v[44:47], a[92:95], v[188:191]\n\t"
  "buffer_load_dwordx4 v6, s[60:63], s70 offen offset:0  lds\n\t"
  "s_waitcnt lgkmcnt(4)\n\t"
  "v_mfma_f32_16x16x32_bf16 v[68:71], v[48:51], a[68:71], v[68:71]\n\t"
  "ds_read_b64_tr_b16 v[40:41], v29 offset:32960\n\t"
  "v_mfma_f32_16x16x32_bf16 v[68:71], v[52:55], a[84:87], v[68:71]\n\t"
  "ds_read_b64_tr_b16 v[42:43], v29 offset:37056\n\t"
  "v_mfma_f32_16x16x32_bf16 v[72:75], v[52:55], a[88:91], v[72:75]\n\t"
  "v_mfma_f32_16x16x32_bf16 v[72:75], v[48:51], a[72:75], v[72:75]\n\t"
  "ds_read_b64_tr_b16 v[44:45], v29 offset:41152\n\t"
  "v_mfma_f32_16x16x32_bf16 v[76:79], v[48:51], a[76:79], v[76:79]\n\t"
  "ds_read_b64_tr_b16 v[46:47], v29 offset:45248\n\t"
  "v_mfma_f32_16x16x32_bf16 v[76:79], v[52:55], a[92:95], v[76:79]\n\t"
  "buffer_load_dwordx4 v7, s[60:63], s70 offen offset:1024  lds\n\t"
  "s_waitcnt lgkmcnt(4)\n\t"
  "v_mfma_f32_16x16x32_bf16 v[100:103], v[32:35], a[68:71], v[100:103]\n\t"
  "ds_read_b64_tr_b16 v[48:49], v29 offset:49152\n\t"
  "v_mfma_f32_16x16x32_bf16 v[100:103], v[36:39], a[84:87], v[100:103]\n\t"
  "ds_read_b64_tr_b16 v[50:51], v29 offset:53248\n\t"
  "v_mfma_f32_16x16x32_bf16 v[104:107], v[36:39], a[88:91], v[104:107]\n\t"
  "buffer_store_dword v27, v21, s[92:95], 0 offen offset:2048 sc1\n\t"
  "v_mfma_f32_16x16x32_bf16 v[104:107], v[32:35], a[72:75], v[104:107]\n\t"
  "ds_read_b64_tr_b16 v[52:53], v29 offset:57344\n\t"
  "s_and_b32 s87, s96, 3\n\t"
  "v_mfma_f32_16x16x32_bf16 v[108:111], v[32:35], a[76:79], v[108:111]\n\t"
  "ds_read_b64_tr_b16 v[54:55], v29 offset:61440\n\t"
  "s_lshl_b32 s87, s87, 13\n\t"
  "v_mfma_f32_16x16x32_bf16 v[108:111], v[36:39], a[92:95], v[108:111]\n\t"
  "buffer_load_dwordx4 v8, s[60:63], s70 offen offset:2048  lds\n\t"
  "s_waitcnt lgkmcnt(4)\n\t"
  "v_mfma_f32_16x16x32_bf16 v[132:135], v[40:43], a[68:71], v[132:135]\n\t"
  "ds_read_b64_tr_b16 v[32:33], v29 offset:49184\n\t"
  "v_add_u32 v12, s87, v11\n\t"
  "v_mfma_f32_16x16x32_bf16 v[132:135], v[44:47], a[84:87], v[132:135]\n\t"
  "ds_read_b64_tr_b16 v[34:35], v29 offset:53280\n\t"
  "v_add_u32 v13, s87, v56\n\t"
  "v_mfma_f32_16x16x32_bf16 v[136:139], v[44:47], a[88:91], v[136:139]\n\t"
  "v_add_u32 v14, s87, v57\n\t"
  "s_and_b32 s86, s96, 12\n\t"
  "v_mfma_f32_16x16x32_bf16 v[136:139], v[40:43], a[72:75], v[136:139]\n\t"
  "ds_read_b64_tr_b16 v[36:37], v29 offset:57376\n\t"
  "s_lshl_b32 s86, s86, 13\n\t"
  "v_mfma_f32_16x16x32_bf16 v[140:143], v[40:43], a[76:79], v[140:143]\n\t"
  "ds_read_b64_tr_b16 v[38:39], v29 offset:61472\n\t"
  "s_sub_u32 s87, s87, s86\n\t"
  "v_mfma_f32_16x16x32_bf16 v[140:143], v[44:47], a[92:95], v[140:143]\n\t"
  "buffer_load_dwordx4 v9, s[60:63], s70 offen offset:3072  lds\n\t"
  "s_waitcnt vmcnt(13) lgkmcnt(4)\n\t"
  "v_mfma_f32_16x16x32_bf16 v[212:215], v[48:51], a[100:103], v[212:215]\n\t"
  "ds_read_b64_tr_b16 v[40:41], v29 offset:49216\n\t"
  "s_add_u32 m0, s69, 65536\n\t"
  "v_mfma_f32_16x16x32_bf16 v[212:215], v[52:55], a[116:119], v[212:215]\n\t"
  "ds_read_b64_tr_b16 v[42:43], v29 offset:53312\n\t"
  "s_bfe_u32 s70, s85, 0x40010\n\t"
  "v_mfma_f32_16x16x32_bf16 v[216:219], v[52:55], a[120:123], v[216:219]\n\t"
  "s_mul_i32 s70, s70, 57344\n\t"
  "v_add_u32 v15, s87, v10\n\t"
  "v_mfma_f32_16x16x32_bf16 v[216:219], v[48:51], a[104:107], v[216:219]\n\t"
  "ds_read_b64_tr_b16 v[44:45], v29 offset:57408\n\t"
  "s_lshr_b32 s87, s96, 2\n\t"
  "v_mfma_f32_16x16x32_bf16 v[220:223], v[48:51], a[108:111], v[220:223]\n\t"
  "ds_read_b64_tr_b16 v[46:47], v29 offset:61504\n\t"
  "s_add_u32 s87, s87, 1\n\t"
  "v_mfma_f32_16x16x32_bf16 v[220:223], v[52:55], a[124:127], v[220:223]\n\t"
  "buffer_load_dwordx4 v6, s[60:63], s70 offen offset:0  lds\n\t"
  "s_waitcnt lgkmcnt(4)\n\t"
  "v_mfma_f32_16x16x32_bf16 v[244:247], v[32:35], a[100:103], v[244:247]\n\t"
  "ds_read_b64_tr_b16 v[48:49], v29 offset:49248\n\t"
  "s_and_b32 s87, s87, 3\n\t"
  "v_mfma_f32_16x16x32_bf16 v[244:247], v[36:39], a[116:119], v[244:247]\n\t"
  "ds_read_b64_tr_b16 v[50:51], v29 offset:53344\n\t"
  "s_lshl_b32 s87, s87, 15\n\t"
  "v_mfma_f32_16x16x32_bf16 v[248:251], v[36:39], a[120:123], v[248:251]\n\t"
  "s_add_u32 s52, s87, s79\n\t"
  "s_add_u32 s53, s87, s80\n\t"
  "v_mfma_f32_16x16x32_bf16 v[248:251], v[32:35], a[104:107], v[248:251]\n\t"
  "ds_read_b64_tr_b16 v[52:53], v29 offset:57440\n\t"
  "s_lshr_b32 s87, s96, 2\n\t"
  "v_mfma_f32_16x16x32_bf16 v[252:255], v[32:35], a[108:111], v[252:255]\n\t"
  "ds_read_b64_tr_b16 v[54:55], v29 offset:61536\n\t"
  "s_add_u32 s87, s87, 2\n\t"
  "v_mfma_f32_16x16x32_bf16 v[252:255], v[36:39], a[124:127], v[252:255]\n\t"
  "buffer_load_dwordx4 v7, s[60:63], s70 offen offset:1024  lds\n\t"
  "s_waitcnt lgkmcnt(4)\n\t"
  "v_mfma_f32_16x16x32_bf16 v[116:119], v[40:43], a[100:103], v[116:119]\n\t"
  "ds_read_b64_tr_b16 v[32:33], v29 offset:49280\n\t"
  "s_and_b32 s87, s87, 3\n\t"
  "v_mfma_f32_16x16x32_bf16 v[116:119], v[44:47], a[116:119], v[116:119]\n\t"
  "ds_read_b64_tr_b16 v[34:35], v29 offset:53376\n\t"
  "s_lshl_b32 s87, s87, 15\n\t"
  "v_mfma_f32_16x16x32_bf16 v[120:123], v[44:47], a[120:123], v[120:123]\n\t"
  "s_add_u32 s54, s87, s79\n\t"
  "s_add_u32 s55, s87, s80\n\t"
  "v_mfma_f32_16x16x32_bf16 v[120:123], v[40:43], a[104:107], v[120:123]\n\t"
  "ds_read_b64_tr_b16 v[36:37], v29 offset:57472\n\t"
  "s_lshr_b32 s87, s96, 2\n\t"
  "v_mfma_f32_16x16x32_bf16 v[124:127], v[40:43], a[108:111], v[124:127]\n\t"
  "ds_read_b64_tr_b16 v[38:39], v29 offset:61568\n\t"
  "s_add_u32 s87, s87, 3\n\t"
  "v_mfma_f32_16x16x32_bf16 v[124:127], v[44:47], a[124:127], v[124:127]\n\t"
  "buffer_load_dwordx4 v8, s[60:63], s70 offen offset:2048  lds\n\t"
  "s_waitcnt lgkmcnt(4)\n\t"
  "v_mfma_f32_16x16x32_bf16 v[180:183], v[48:51], a[100:103], v[180:183]\n\t"
  "ds_read_b64_tr_b16 v[40:41], v29 offset:49312\n\t"
  "s_and_b32 s87, s87, 3\n\t"
  "v_mfma_f32_16x16x32_bf16 v[180:183], v[52:55], a[116:119], v[180:183]\n\t"
  "ds_read_b64_tr_b16 v[42:43], v29 offset:53408\n\t"
  "s_lshl_b32 s87, s87, 15\n\t"
  "v_mfma_f32_16x16x32_bf16 v[184:187], v[52:55], a[120:123], v[184:187]\n\t"
  "s_add_u32 s48, s87, s79\n\t"
  "s_add_u32 s49, s87, s80\n\t"
  "v_mfma_f32_16x16x32_bf16 v[184:187], v[48:51], a[104:107], v[184:187]\n\t"
  "ds_read_b64_tr_b16 v[44:45], v29 offset:57504\n\t"
  "s_lshl_b32 s87, s97, 2\n\t"
  "v_mfma_f32_16x16x32_bf16 v[188:191], v[48:51], a[108:111], v[188:191]\n\t"
  "ds_read_b64_tr_b16 v[46:47], v29 offset:61600\n\t"
  "s_and_b32 s86, s96, 3\n\t"
  "v_mfma_f32_16x16x32_bf16 v[188:191], v[52:55], a[124:127], v[188:191]\n\t"
  "buffer_load_dwordx4 v9, s[60:63], s70 offen offset:3072  lds\n\t"
  "s_waitcnt lgkmcnt(4)\n\t"
  "v_mfma_f32_16x16x32_bf16 v[68:71], v[32:35], a[100:103], v[68:71]\n\t"
  "ds_read_b64_tr_b16 v[48:49], v29 offset:49344\n\t"
  "s_add_u32 s87, s87, s86\n\t"
  "v_mfma_f32_16x16x32_bf16 v[68:71], v[36:39], a[116:119], v[68:71]\n\t"
  "ds_read_b64_tr_b16 v[50:51], v29 offset:53440\n\t"
  "s_lshl_b32 s87, s87, 2\n\t"
  "v_mfma_f32_16x16x32_bf16 v[72:75], v[36:39], a[120:123], v[72:75]\n\t"
  "s_add_u32 s87, s87, s90\n\t"
  "s_lshl_b32 s87, s87, 4\n\t"
  "v_mfma_f32_16x16x32_bf16 v[72:75], v[32:35], a[104:107], v[72:75]\n\t"
  "ds_read_b64_tr_b16 v[52:53], v29 offset:57536\n\t"
  "s_add_u32 s87, s87, 8192\n\t"
  "v_mfma_f32_16x16x32_bf16 v[76:79], v[32:35], a[108:111], v[76:79]\n\t"
  "ds_read_b64_tr_b16 v[54:55], v29 offset:61632\n\t"
  "v_bfe_u32 v61, v20, 4, 2\n\t"
  "v_mfma_f32_16x16x32_bf16 v[76:79], v[36:39], a[124:127], v[76:79]\n\t"
  "buffer_load_dwordx4 a[172:175], v57, s[40:43], s71 offen offset:0 sc1\n\t"
  "s_waitcnt lgkmcnt(4)\n\t"
  "v_mfma_f32_16x16x32_bf16 v[100:103], v[40:43], a[100:103], v[100:103]\n\t"
  "ds_read_b64_tr_b16 v[32:33], v58 offset:0\n\t"
  "v_lshlrev_b32 v61, 2, v61\n\t"
  "v_mfma_f32_16x16x32_bf16 v[100:103], v[44:47], a[116:119], v[100:103]\n\t"
  "ds_read_b64_tr_b16 v[34:35], v58 offset:4096\n\t"
  "v_add_u32 v61, s87, v61\n\t"
  "v_mfma_f32_16x16x32_bf16 v[104:107], v[44:47], a[120:123], v[104:107]\n\t"
  "s_and_b32 s86, s96, 12\n\t"
  "s_add_u32 s87, s87, s86\n\t"
  "v_mfma_f32_16x16x32_bf16 v[104:107], v[40:43], a[104:107], v[104:107]\n\t"
  "ds_read_b64_tr_b16 v[36:37], v58 offset:8192\n\t"
  "v_lshlrev_b32 v60, 12, v20\n\t"
  "v_mfma_f32_16x16x32_bf16 v[108:111], v[40:43], a[108:111], v[108:111]\n\t"
  "ds_read_b64_tr_b16 v[38:39], v58 offset:12288\n\t"
  "v_add_u32 v60, s87, v60\n\t"
  "v_mfma_f32_16x16x32_bf16 v[108:111], v[44:47], a[124:127], v[108:111]\n\t"
  "buffer_load_dwordx4 a[180:183], v11, s[40:43], s71 offen offset:1024 sc1\n\t"
  "s_waitcnt lgkmcnt(4)\n\t"
  "v_mfma_f32_16x16x32_bf16 v[132:135], v[48:51], a[100:103], v[132:135]\n\t"
  "ds_read_b64_tr_b16 v[40:41], v58 offset:32\n\t"
  "v_mov_b32 v27, s72\n\t"
  "v_mfma_f32_16x16x32_bf16 v[132:135], v[52:55], a[116:119], v[132:135]\n\t"
  "ds_read_b64_tr_b16 v[42:43], v58 offset:4128\n\t"
  "v_lshrrev_b32 v0, 6, v20\n\t"
  "v_mfma_f32_16x16x32_bf16 v[136:139], v[52:55], a[120:123], v[136:139]\n\t"
  "v_lshlrev_b32 v62, 2, v0\n\t"
  "v_mul_u32_u24 v0, 896, v0\n\t"
  "v_mfma_f32_16x16x32_bf16 v[136:139], v[48:51], a[104:107], v[136:139]\n\t"
  "ds_read_b64_tr_b16 v[44:45], v58 offset:8224\n\t"
  "s_lshr_b32 s87, s96, 2\n\t"
  "v_mfma_f32_16x16x32_bf16 v[140:143], v[48:51], a[108:111], v[140:143]\n\t"
  "ds_read_b64_tr_b16 v[46:47], v58 offset:12320\n\t"
  "s_lshl_b32 s86, s90, 2\n\t"
  "v_mfma_f32_16x16x32_bf16 v[140:143], v[52:55], a[124:127], v[140:143]\n\t"
  "buffer_load_dwordx4 a[184:187], v56, s[40:43], s71 offen offset:1024 sc1\n\t"
  "s_waitcnt vmcnt(14) lgkmcnt(4)\n\t"
  "v_mfma_f32_16x16x32_bf16 v[212:215], v[32:35], a[132:135], v[212:215]\n\t"
  "ds_read_b64_tr_b16 v[48:49], v58 offset:64\n\t"
  "s_add_u32 s87, s87, s86\n\t"
  "v_mfma_f32_16x16x32_bf16 v[212:215], v[36:39], a[148:151], v[212:215]\n\t"
  "ds_read_b64_tr_b16 v[50:51], v58 offset:4160\n\t"
  "s_mul_i32 s87, s87, 14336\n\t"
  "v_mfma_f32_16x16x32_bf16 v[216:219], v[36:39], a[152:155], v[216:219]\n\t"
  "buffer_load_dwordx4 a[188:191], v57, s[40:43], s71 offen offset:1024 sc1\n\t"
  "v_add_u32 v0, s87, v0\n\t"
  "v_mfma_f32_16x16x32_bf16 v[216:219], v[32:35], a[136:139], v[216:219]\n\t"
  "ds_read_b64_tr_b16 v[52:53], v58 offset:8256\n\t"
  "s_and_b32 s87, s96, 3\n\t"
  "v_mfma_f32_16x16x32_bf16 v[220:223], v[32:35], a[140:143], v[220:223]\n\t"
  "ds_read_b64_tr_b16 v[54:55], v58 offset:12352\n\t"
  "s_mul_i32 s87, s87, 224\n\t"
  "v_mfma_f32_16x16x32_bf16 v[220:223], v[36:39], a[156:159], v[220:223]\n\t"
  "v_add_u32 v0, s87, v0\n\t"
  "s_waitcnt lgkmcnt(4)\n\t"
  "v_mfma_f32_16x16x32_bf16 v[244:247], v[40:43], a[132:135], v[244:247]\n\t"
  "ds_read_b64_tr_b16 v[32:33], v58 offset:96\n\t"
  "v_bfe_u32 v1, v20, 4, 2\n\t"
  "v_mfma_f32_16x16x32_bf16 v[244:247], v[44:47], a[148:151], v[244:247]\n\t"
  "ds_read_b64_tr_b16 v[34:35], v58 offset:4192\n\t"
  "s_bfe_u32 s71, s85, 0x40014\n\t"
  "s_lshl_b32 s71, s71, 11\n\t"
  "v_mfma_f32_16x16x32_bf16 v[248:251], v[44:47], a[152:155], v[248:251]\n\t"
  "buffer_load_dwordx4 a[100:103], v11, s[40:43], s71 offen offset:0 sc1\n\t"
  "v_lshl_add_u32 v4, v1, 4, v0\n\t"
  "v_mfma_f32_16x16x32_bf16 v[248:251], v[40:43], a[136:139], v[248:251]\n\t"
  "ds_read_b64_tr_b16 v[36:37], v58 offset:8288\n\t"
  "v_lshl_add_u32 v5, v1, 3, v0\n\t"
  "v_mfma_f32_16x16x32_bf16 v[252:255], v[40:43], a[140:143], v[252:255]\n\t"
  "ds_read_b64_tr_b16 v[38:39], v58 offset:12384\n\t"
  "v_bfe_u32 v0, v20, 4, 1\n\t"
  "v_mfma_f32_16x16x32_bf16 v[252:255], v[44:47], a[156:159], v[252:255]\n\t"
  "v_bfe_u32 v2, v20, 5, 1\n\t"
  "s_waitcnt lgkmcnt(4)\n\t"
  "v_mfma_f32_16x16x32_bf16 v[116:119], v[48:51], a[132:135], v[116:119]\n\t"
  "ds_read_b64_tr_b16 v[40:41], v58 offset:128\n\t"
  "v_lshl_add_u32 v0, v0, 1, v2\n\t"
  "v_mfma_f32_16x16x32_bf16 v[116:119], v[52:55], a[148:151], v[116:119]\n\t"
  "ds_read_b64_tr_b16 v[42:43], v58 offset:4224\n\t"
  "v_lshl_add_u32 v59, v0, 6, v62\n\t"
  "v_mfma_f32_16x16x32_bf16 v[120:123], v[52:55], a[152:155], v[120:123]\n\t"
  "buffer_load_dwordx4 a[104:107], v56, s[40:43], s71 offen offset:0 sc1\n\t"
  "v_lshl_add_u32 v62, v1, 6, v62\n\t"
  "v_mfma_f32_16x16x32_bf16 v[120:123], v[48:51], a[136:139], v[120:123]\n\t"
  "ds_read_b64_tr_b16 v[44:45], v58 offset:8320\n\t"
  "v_mfma_f32_16x16x32_bf16 v[124:127], v[48:51], a[140:143], v[124:127]\n\t"
  "ds_read_b64_tr_b16 v[46:47], v58 offset:12416\n\t"
  "v_mfma_f32_16x16x32_bf16 v[124:127], v[52:55], a[156:159], v[124:127]\n\t"
  "s_add_u32 m0, s69, 49152\n\t"
  "s_waitcnt lgkmcnt(4)\n\t"
  "v_mfma_f32_16x16x32_bf16 v[180:183], v[32:35], a[132:135], v[180:183]\n\t"
  "ds_read_b64_tr_b16 v[48:49], v58 offset:160\n\t"
  "s_bfe_u32 s70, s85, 0x4001c\n\t"
  "v_mfma_f32_16x16x32_bf16 v[180:183], v[36:39], a[148:151], v[180:183]\n\t"
  "ds_read_b64_tr_b16 v[50:51], v58 offset:4256\n\t"
  "s_mul_i32 s70, s70, 57344\n\t"
  "v_mfma_f32_16x16x32_bf16 v[184:187], v[36:39], a[152:155], v[184:187]\n\t"
  "s_waitcnt vmcnt(6)\n\t"
  "s_barrier\n\t"
  "v_mfma_f32_16x16x32_bf16 v[184:187], v[32:35], a[136:139], v[184:187]\n\t"
  "ds_read_b64_tr_b16 v[52:53], v58 offset:8352\n\t"
  "v_mfma_f32_16x16x32_bf16 v[188:191], v[32:35], a[140:143], v[188:191]\n\t"
  "ds_read_b64_tr_b16 v[54:55], v58 offset:12448\n\t"
  "v_mfma_f32_16x16x32_bf16 v[188:191], v[36:39], a[156:159], v[188:191]\n\t"
  "buffer_load_dwordx4 v6, s[60:63], s70 offen offset:0  lds\n\t"
  "s_waitcnt lgkmcnt(4)\n\t"
  "v_mfma_f32_16x16x32_bf16 v[68:71], v[40:43], a[132:135], v[68:71]\n\t"
  "ds_read_b64_tr_b16 v[32:33], v58 offset:192\n\t"
  "v_mfma_f32_16x16x32_bf16 v[68:71], v[44:47], a[148:151], v[68:71]\n\t"
  "ds_read_b64_tr_b16 v[34:35], v58 offset:4288\n\t"
  "v_mfma_f32_16x16x32_bf16 v[72:75], v[44:47], a[152:155], v[72:75]\n\t"
  "v_mfma_f32_16x16x32_bf16 v[72:75], v[40:43], a[136:139], v[72:75]\n\t"
  "ds_read_b64_tr_b16 v[36:37], v58 offset:8384\n\t"
  "v_mfma_f32_16x16x32_bf16 v[76:79], v[40:43], a[140:143], v[76:79]\n\t"
  "ds_read_b64_tr_b16 v[38:39], v58 offset:12480\n\t"
  "v_mfma_f32_16x16x32_bf16 v[76:79], v[44:47], a[156:159], v[76:79]\n\t"
  "buffer_load_dwordx4 v7, s[60:63], s70 offen offset:1024  lds\n\t"
  "s_waitcnt lgkmcnt(4)\n\t"
  "v_mfma_f32_16x16x32_bf16 v[100:103], v[48:51], a[132:135], v[100:103]\n\t"
  "ds_read_b64_tr_b16 v[40:41], v58 offset:16384\n\t"
  "v_mfma_f32_16x16x32_bf16 v[100:103], v[52:55], a[148:151], v[100:103]\n\t"
  "ds_read_b64_tr_b16 v[42:43], v58 offset:20480\n\t"
  "v_mfma_f32_16x16x32_bf16 v[104:107], v[52:55], a[152:155], v[104:107]\n\t"
  "v_mfma_f32_16x16x32_bf16 v[104:107], v[48:51], a[136:139], v[104:107]\n\t"
  "ds_read_b64_tr_b16 v[44:45], v58 offset:24576\n\t"
  "v_mfma_f32_16x16x32_bf16 v[108:111], v[48:51], a[140:143], v[108:111]\n\t"
  "ds_read_b64_tr_b16 v[46:47], v58 offset:28672\n\t"
  "v_mfma_f32_16x16x32_bf16 v[108:111], v[52:55], a[156:159], v[108:111]\n\t"
  "buffer_load_dwordx4 v8, s[60:63], s70 offen offset:2048  lds\n\t"
  "s_waitcnt lgkmcnt(4)\n\t"
  "v_mfma_f32_16x16x32_bf16 v[132:135], v[32:35], a[132:135], v[132:135]\n\t"
  "ds_read_b64_tr_b16 v[48:49], v58 offset:16416\n\t"
  "v_mfma_f32_16x16x32_bf16 v[132:135], v[36:39], a[148:151], v[132:135]\n\t"
  "ds_read_b64_tr_b16 v[50:51], v58 offset:20512\n\t"
  "v_mfma_f32_16x16x32_bf16 v[136:139], v[36:39], a[152:155], v[136:139]\n\t"
  "v_mfma_f32_16x16x32_bf16 v[136:139], v[32:35], a[136:139], v[136:139]\n\t"
  "ds_read_b64_tr_b16 v[52:53], v58 offset:24608\n\t"
  "v_mfma_f32_16x16x32_bf16 v[140:143], v[32:35], a[140:143], v[140:143]\n\t"
  "ds_read_b64_tr_b16 v[54:55], v58 offset:28704\n\t"
  "v_mfma_f32_16x16x32_bf16 v[140:143], v[36:39], a[156:159], v[140:143]\n\t"
  "buffer_load_dwordx4 v9, s[60:63], s70 offen offset:3072  lds\n\t"
  "s_waitcnt vmcnt(6) lgkmcnt(4)\n\t"
  "v_mfma_f32_16x16x32_bf16 v[212:215], v[40:43], a[164:167], v[212:215]\n\t"
  "ds_read_b64_tr_b16 v[32:33], v58 offset:16448\n\t"
  "s_add_u32 m0, s69, 32768\n\t"
  "v_mfma_f32_16x16x32_bf16 v[212:215], v[44:47], a[180:183], v[212:215]\n\t"
  "ds_read_b64_tr_b16 v[34:35], v58 offset:20544\n\t"
  "s_bfe_u32 s70, s85, 0x40018\n\t"
  "v_mfma_f32_16x16x32_bf16 v[216:219], v[44:47], a[184:187], v[216:219]\n\t"
  "buffer_load_dword v26, v30, s[92:95], 0 offen offset:2048 sc1\n\t"
  "s_mul_i32 s70, s70, 57344\n\t"
  "v_mfma_f32_16x16x32_bf16 v[216:219], v[40:43], a[168:171], v[216:219]\n\t"
  "ds_read_b64_tr_b16 v[36:37], v58 offset:24640\n\t"
  "v_mfma_f32_16x16x32_bf16 v[220:223], v[40:43], a[172:175], v[220:223]\n\t"
  "ds_read_b64_tr_b16 v[38:39], v58 offset:28736\n\t"
  "v_mfma_f32_16x16x32_bf16 v[220:223], v[44:47], a[188:191], v[220:223]\n\t"
  "buffer_load_dwordx4 v6, s[60:63], s70 offen offset:0  lds\n\t"
  "s_waitcnt lgkmcnt(4)\n\t"
  "v_mfma_f32_16x16x32_bf16 v[244:247], v[48:51], a[164:167], v[244:247]\n\t"
  "ds_read_b64_tr_b16 v[40:41], v58 offset:16480\n\t"
  "v_mfma_f32_16x16x32_bf16 v[244:247], v[52:55], a[180:183], v[244:247]\n\t"
  "ds_read_b64_tr_b16 v[42:43], v58 offset:20576\n\t"
  "v_mfma_f32_16x16x32_bf16 v[248:251], v[52:55], a[184:187], v[248:251]\n\t"
  "v_mfma_f32_16x16x32_bf16 v[248:251], v[48:51], a[168:171], v[248:251]\n\t"
  "ds_read_b64_tr_b16 v[44:45], v58 offset:24672\n\t"
  "v_mfma_f32_16x16x32_bf16 v[252:255], v[48:51], a[172:175], v[252:255]\n\t"
  "ds_read_b64_tr_b16 v[46:47], v58 offset:28768\n\t"
  "v_mfma_f32_16x16x32_bf16 v[252:255], v[52:55], a[188:191], v[252:255]\n\t"
  "buffer_load_dwordx4 v7, s[60:63], s70 offen offset:1024  lds\n\t"
  "s_waitcnt lgkmcnt(4)\n\t"
  "v_mfma_f32_16x16x32_bf16 v[116:119], v[32:35], a[164:167], v[116:119]\n\t"
  "ds_read_b64_tr_b16 v[48:49], v58 offset:16512\n\t"
  "v_mfma_f32_16x16x32_bf16 v[116:119], v[36:39], a[180:183], v[116:119]\n\t"
  "ds_read_b64_tr_b16 v[50:51], v58 offset:20608\n\t"
  "v_mfma_f32_16x16x32_bf16 v[120:123], v[36:39], a[184:187], v[120:123]\n\t"
  "v_mfma_f32_16x16x32_bf16 v[120:123], v[32:35], a[168:171], v[120:123]\n\t"
  "ds_read_b64_tr_b16 v[52:53], v58 offset:24704\n\t"
  "v_mfma_f32_16x16x32_bf16 v[124:127], v[32:35], a[172:175], v[124:127]\n\t"
  "ds_read_b64_tr_b16 v[54:55], v58 offset:28800\n\t"
  "v_mfma_f32_16x16x32_bf16 v[124:127], v[36:39], a[188:191], v[124:127]\n\t"
  "buffer_load_dwordx4 v8, s[60:63], s70 offen offset:2048  lds\n\t"
  "s_waitcnt lgkmcnt(4)\n\t"
  "v_mfma_f32_16x16x32_bf16 v[180:183], v[40:43], a[164:167], v[180:183]\n\t"
  "ds_read_b64_tr_b16 v[32:33], v58 offset:16544\n\t"
  "v_mfma_f32_16x16x32_bf16 v[180:183], v[44:47], a[180:183], v[180:183]\n\t"
  "ds_read_b64_tr_b16 v[34:35], v58 offset:20640\n\t"
  "v_mfma_f32_16x16x32_bf16 v[184:187], v[44:47], a[184:187], v[184:187]\n\t"
  "v_mfma_f32_16x16x32_bf16 v[184:187], v[40:43], a[168:171], v[184:187]\n\t"
  "ds_read_b64_tr_b16 v[36:37], v58 offset:24736\n\t"
  "v_mfma_f32_16x16x32_bf16 v[188:191], v[40:43], a[172:175], v[188:191]\n\t"
  "ds_read_b64_tr_b16 v[38:39], v58 offset:28832\n\t"
  "v_mfma_f32_16x16x32_bf16 v[188:191], v[44:47], a[188:191], v[188:191]\n\t"
  "buffer_load_dwordx4 v9, s[60:63], s70 offen offset:3072  lds\n\t"
  "s_waitcnt lgkmcnt(4)\n\t"
  "v_mfma_f32_16x16x32_bf16 v[68:71], v[48:51], a[164:167], v[68:71]\n\t"
  "ds_read_b64_tr_b16 v[40:41], v58 offset:16576\n\t"
  "v_mfma_f32_16x16x32_bf16 v[68:71], v[52:55], a[180:183], v[68:71]\n\t"
  "ds_read_b64_tr_b16 v[42:43], v58 offset:20672\n\t"
  "v_mfma_f32_16x16x32_bf16 v[72:75], v[52:55], a[184:187], v[72:75]\n\t"
  "v_mfma_f32_16x16x32_bf16 v[72:75], v[48:51], a[168:171], v[72:75]\n\t"
  "ds_read_b64_tr_b16 v[44:45], v58 offset:24768\n\t"
  "v_mfma_f32_16x16x32_bf16 v[76:79], v[48:51], a[172:175], v[76:79]\n\t"
  "ds_read_b64_tr_b16 v[46:47], v58 offset:28864\n\t"
  "v_mfma_f32_16x16x32_bf16 v[76:79], v[52:55], a[188:191], v[76:79]\n\t"
  "buffer_load_dwordx4 a[108:111], v57, s[40:43], s71 offen offset:0 sc1\n\t"
  "s_waitcnt lgkmcnt(4)\n\t"
  "v_mfma_f32_16x16x32_bf16 v[100:103], v[32:35], a[164:167], v[100:103]\n\t"
  "ds_read_b64_tr_b16 v[48:49], v29 offset:0\n\t"
  "v_mfma_f32_16x16x32_bf16 v[100:103], v[36:39], a[180:183], v[100:103]\n\t"
  "ds_read_b64_tr_b16 v[50:51], v29 offset:4096\n\t"
  "v_mfma_f32_16x16x32_bf16 v[104:107], v[36:39], a[184:187], v[104:107]\n\t"
  "v_mfma_f32_16x16x32_bf16 v[104:107], v[32:35], a[168:171], v[104:107]\n\t"
  "ds_read_b64_tr_b16 v[52:53], v29 offset:8192\n\t"
  "v_mfma_f32_16x16x32_bf16 v[108:111], v[32:35], a[172:175], v[108:111]\n\t"
  "ds_read_b64_tr_b16 v[54:55], v29 offset:12288\n\t"
  "v_mfma_f32_16x16x32_bf16 v[108:111], v[36:39], a[188:191], v[108:111]\n\t"
  "buffer_load_dwordx4 a[116:119], v11, s[40:43], s71 offen offset:1024 sc1\n\t"
  "s_waitcnt lgkmcnt(4)\n\t"
  "v_mfma_f32_16x16x32_bf16 v[132:135], v[40:43], a[164:167], v[132:135]\n\t"
  "ds_read_b64_tr_b16 v[32:33], v29 offset:32\n\t"
  "v_mfma_f32_16x16x32_bf16 v[132:135], v[44:47], a[180:183], v[132:135]\n\t"
  "ds_read_b64_tr_b16 v[34:35], v29 offset:4128\n\t"
  "v_mfma_f32_16x16x32_bf16 v[136:139], v[44:47], a[184:187], v[136:139]\n\t"
  "v_mfma_f32_16x16x32_bf16 v[136:139], v[40:43], a[168:171], v[136:139]\n\t"
  "ds_read_b64_tr_b16 v[36:37], v29 offset:8224\n\t"
  "v_mfma_f32_16x16x32_bf16 v[140:143], v[40:43], a[172:175], v[140:143]\n\t"
  "ds_read_b64_tr_b16 v[38:39], v29 offset:12320\n\t"
  "v_mfma_f32_16x16x32_bf16 v[140:143], v[44:47], a[188:191], v[140:143]\n\t"
  "buffer_load_dwordx4 a[120:123], v56, s[40:43], s71 offen offset:1024 sc1\n\t"
  "s_waitcnt lgkmcnt(4)\n\t"
  "v_mfma_f32_16x16x32_bf16 v[212:215], v[48:51], a[4:7], v[212:215]\n\t"
  "ds_read_b64_tr_b16 v[40:41], v29 offset:64\n\t"
  "v_mfma_f32_16x16x32_bf16 v[212:215], v[52:55], a[20:23], v[212:215]\n\t"
  "ds_read_b64_tr_b16 v[42:43], v29 offset:4160\n\t"
  "v_mfma_f32_16x16x32_bf16 v[216:219], v[52:55], a[24:27], v[216:219]\n\t"
  "buffer_load_dwordx4 a[124:127], v57, s[40:43], s71 offen offset:1024 sc1\n\t"
  "v_mfma_f32_16x16x32_bf16 v[216:219], v[48:51], a[8:11], v[216:219]\n\t"
  "ds_read_b64_tr_b16 v[44:45], v29 offset:8256\n\t"
  "v_mfma_f32_16x16x32_bf16 v[220:223], v[48:51], a[12:15], v[220:223]\n\t"
  "ds_read_b64_tr_b16 v[46:47], v29 offset:12352\n\t"
  "v_mfma_f32_16x16x32_bf16 v[220:223], v[52:55], a[28:31], v[220:223]\n\t"
  "s_waitcnt lgkmcnt(4)\n\t"
  "v_mfma_f32_16x16x32_bf16 v[244:247], v[32:35], a[4:7], v[244:247]\n\t"
  "ds_read_b64_tr_b16 v[48:49], v29 offset:96\n\t"
  "v_mfma_f32_16x16x32_bf16 v[244:247], v[36:39], a[20:23], v[244:247]\n\t"
  "ds_read_b64_tr_b16 v[50:51], v29 offset:4192\n\t"
  "s_bfe_u32 s71, s85, 0x40018\n\t"
  "s_lshl_b32 s71, s71, 11\n\t"
  "v_mfma_f32_16x16x32_bf16 v[248:251], v[36:39], a[24:27], v[248:251]\n\t"
  "buffer_load_dwordx4 a[132:135], v11, s[40:43], s71 offen offset:0 sc1\n\t"
  "v_mfma_f32_16x16x32_bf16 v[248:251], v[32:35], a[8:11], v[248:251]\n\t"
  "ds_read_b64_tr_b16 v[52:53], v29 offset:8288\n\t"
  "v_mfma_f32_16x16x32_bf16 v[252:255], v[32:35], a[12:15], v[252:255]\n\t"
  "ds_read_b64_tr_b16 v[54:55], v29 offset:12384\n\t"
  "v_mfma_f32_16x16x32_bf16 v[252:255], v[36:39], a[28:31], v[252:255]\n\t"
  "s_waitcnt lgkmcnt(4)\n\t"
  "v_mfma_f32_16x16x32_bf16 v[116:119], v[40:43], a[4:7], v[116:119]\n\t"
  "ds_read_b64_tr_b16 v[32:33], v29 offset:128\n\t"
  "v_mfma_f32_16x16x32_bf16 v[116:119], v[44:47], a[20:23], v[116:119]\n\t"
  "ds_read_b64_tr_b16 v[34:35], v29 offset:4224\n\t"
  "v_mfma_f32_16x16x32_bf16 v[120:123], v[44:47], a[24:27], v[120:123]\n\t"
  "buffer_load_dwordx4 a[136:139], v56, s[40:43], s71 offen offset:0 sc1\n\t"
  "v_mfma_f32_16x16x32_bf16 v[120:123], v[40:43], a[8:11], v[120:123]\n\t"
  "ds_read_b64_tr_b16 v[36:37], v29 offset:8320\n\t"
  "v_mfma_f32_16x16x32_bf16 v[124:127], v[40:43], a[12:15], v[124:127]\n\t"
  "ds_read_b64_tr_b16 v[38:39], v29 offset:12416\n\t"
  "v_mfma_f32_16x16x32_bf16 v[124:127], v[44:47], a[28:31], v[124:127]\n\t"
  "s_waitcnt lgkmcnt(4)\n\t"
  "v_mfma_f32_16x16x32_bf16 v[180:183], v[48:51], a[4:7], v[180:183]\n\t"
  "ds_read_b64_tr_b16 v[40:41], v29 offset:160\n\t"
  "v_mfma_f32_16x16x32_bf16 v[180:183], v[52:55], a[20:23], v[180:183]\n\t"
  "ds_read_b64_tr_b16 v[42:43], v29 offset:4256\n\t"
  "v_mfma_f32_16x16x32_bf16 v[184:187], v[52:55], a[24:27], v[184:187]\n\t"
  "buffer_load_dwordx4 a[140:143], v57, s[40:43], s71 offen offset:0 sc1\n\t"
  "s_waitcnt vmcnt(7)\n\t"
  "s_barrier\n\t"
  "v_mfma_f32_16x16x32_bf16 v[184:187], v[48:51], a[8:11], v[184:187]\n\t"
  "ds_read_b64_tr_b16 v[44:45], v29 offset:8352\n\t"
  "v_mfma_f32_16x16x32_bf16 v[188:191], v[48:51], a[12:15], v[188:191]\n\t"
  "ds_read_b64_tr_b16 v[46:47], v29 offset:12448\n\t"
  "v_mfma_f32_16x16x32_bf16 v[188:191], v[52:55], a[28:31], v[188:191]\n\t"
  "s_waitcnt lgkmcnt(4)\n\t"
  "v_mfma_f32_16x16x32_bf16 v[68:71], v[32:35], a[4:7], v[68:71]\n\t"
  "ds_read_b64_tr_b16 v[48:49], v29 offset:192\n\t"
  "v_mfma_f32_16x16x32_bf16 v[68:71], v[36:39], a[20:23], v[68:71]\n\t"
  "ds_read_b64_tr_b16 v[50:51], v29 offset:4288\n\t"
  "v_mfma_f32_16x16x32_bf16 v[72:75], v[36:39], a[24:27], v[72:75]\n\t"
  "buffer_load_dwordx4 a[148:151], v11, s[40:43], s71 offen offset:1024 sc1\n\t"
  "v_mfma_f32_16x16x32_bf16 v[72:75], v[32:35], a[8:11], v[72:75]\n\t"
  "ds_read_b64_tr_b16 v[52:53], v29 offset:8384\n\t"
  "v_mfma_f32_16x16x32_bf16 v[76:79], v[32:35], a[12:15], v[76:79]\n\t"
  "ds_read_b64_tr_b16 v[54:55], v29 offset:12480\n\t"
  "v_mfma_f32_16x16x32_bf16 v[76:79], v[36:39], a[28:31], v[76:79]\n\t"
  "s_waitcnt lgkmcnt(4)\n\t"
  "v_mfma_f32_16x16x32_bf16 v[100:103], v[40:43], a[4:7], v[100:103]\n\t"
  "ds_read_b64_tr_b16 v[32:33], v29 offset:16384\n\t"
  "v_mfma_f32_16x16x32_bf16 v[100:103], v[44:47], a[20:23], v[100:103]\n\t"
  "ds_read_b64_tr_b16 v[34:35], v29 offset:20480\n\t"
  "v_mfma_f32_16x16x32_bf16 v[104:107], v[44:47], a[24:27], v[104:107]\n\t"
  "buffer_load_dwordx4 a[152:155], v56, s[40:43], s71 offen offset:1024 sc1\n\t"
  "v_mfma_f32_16x16x32_bf16 v[104:107], v[40:43], a[8:11], v[104:107]\n\t"
  "ds_read_b64_tr_b16 v[36:37], v29 offset:24576\n\t"
  "v_mfma_f32_16x16x32_bf16 v[108:111], v[40:43], a[12:15], v[108:111]\n\t"
  "ds_read_b64_tr_b16 v[38:39], v29 offset:28672\n\t"
  "v_mfma_f32_16x16x32_bf16 v[108:111], v[44:47], a[28:31], v[108:111]\n\t"
  "s_waitcnt lgkmcnt(4)\n\t"
  "v_mfma_f32_16x16x32_bf16 v[132:135], v[48:51], a[4:7], v[132:135]\n\t"
  "ds_read_b64_tr_b16 v[40:41], v29 offset:16416\n\t"
  "v_mfma_f32_16x16x32_bf16 v[132:135], v[52:55], a[20:23], v[132:135]\n\t"
  "ds_read_b64_tr_b16 v[42:43], v29 offset:20512\n\t"
  "v_mfma_f32_16x16x32_bf16 v[136:139], v[52:55], a[24:27], v[136:139]\n\t"
  "buffer_load_dwordx4 a[156:159], v57, s[40:43], s71 offen offset:1024 sc1\n\t"
  "v_mfma_f32_16x16x32_bf16 v[136:139], v[48:51], a[8:11], v[136:139]\n\t"
  "ds_read_b64_tr_b16 v[44:45], v29 offset:24608\n\t"
  "v_mfma_f32_16x16x32_bf16 v[140:143], v[48:51], a[12:15], v[140:143]\n\t"
  "ds_read_b64_tr_b16 v[46:47], v29 offset:28704\n\t"
  "v_mfma_f32_16x16x32_bf16 v[140:143], v[52:55], a[28:31], v[140:143]\n\t"
  "s_waitcnt vmcnt(6) lgkmcnt(4)\n\t"
  "v_mfma_f32_16x16x32_bf16 v[212:215], v[32:35], a[100:103], v[212:215]\n\t"
  "ds_read_b64_tr_b16 v[48:49], v29 offset:16448\n\t"
  "v_mfma_f32_16x16x32_bf16 v[212:215], v[36:39], a[116:119], v[212:215]\n\t"
  "ds_read_b64_tr_b16 v[50:51], v29 offset:20544\n\t"
  "s_bfe_u32 s71, s85, 0x4001c\n\t"
  "s_lshl_b32 s71, s71, 11\n\t"
  "v_mfma_f32_16x16x32_bf16 v[216:219], v[36:39], a[120:123], v[216:219]\n\t"
  "buffer_load_dwordx4 a[164:167], v11, s[40:43], s71 offen offset:0 sc1\n\t"
  "v_mfma_f32_16x16x32_bf16 v[216:219], v[32:35], a[104:107], v[216:219]\n\t"
  "ds_read_b64_tr_b16 v[52:53], v29 offset:24640\n\t"
  "v_mfma_f32_16x16x32_bf16 v[220:223], v[32:35], a[108:111], v[220:223]\n\t"
  "ds_read_b64_tr_b16 v[54:55], v29 offset:28736\n\t"
  "v_mfma_f32_16x16x32_bf16 v[220:223], v[36:39], a[124:127], v[220:223]\n\t"
  "buffer_load_dwordx4 a[168:171], v56, s[40:43], s71 offen offset:0 sc1\n\t"
  "s_waitcnt lgkmcnt(4)\n\t"
  "v_mfma_f32_16x16x32_bf16 v[244:247], v[40:43], a[100:103], v[244:247]\n\t"
  "ds_read_b64_tr_b16 v[32:33], v29 offset:16480\n\t"
  "v_mfma_f32_16x16x32_bf16 v[244:247], v[44:47], a[116:119], v[244:247]\n\t"
  "ds_read_b64_tr_b16 v[34:35], v29 offset:20576\n\t"
  "v_mfma_f32_16x16x32_bf16 v[248:251], v[44:47], a[120:123], v[248:251]\n\t"
  "buffer_load_dwordx4 a[172:175], v57, s[40:43], s71 offen offset:0 sc1\n\t"
  "v_mfma_f32_16x16x32_bf16 v[248:251], v[40:43], a[104:107], v[248:251]\n\t"
  "ds_read_b64_tr_b16 v[36:37], v29 offset:24672\n\t"
  "v_mfma_f32_16x16x32_bf16 v[252:255], v[40:43], a[108:111], v[252:255]\n\t"
  "ds_read_b64_tr_b16 v[38:39], v29 offset:28768\n\t"
  "v_mfma_f32_16x16x32_bf16 v[252:255], v[44:47], a[124:127], v[252:255]\n\t"
  "buffer_load_dwordx4 a[180:183], v11, s[40:43], s71 offen offset:1024 sc1\n\t"
  "s_waitcnt lgkmcnt(4)\n\t"
  "v_mfma_f32_16x16x32_bf16 v[116:119], v[48:51], a[100:103], v[116:119]\n\t"
  "ds_read_b64_tr_b16 v[40:41], v29 offset:16512\n\t"
  "v_mfma_f32_16x16x32_bf16 v[116:119], v[52:55], a[116:119], v[116:119]\n\t"
  "ds_read_b64_tr_b16 v[42:43], v29 offset:20608\n\t"
  "v_mfma_f32_16x16x32_bf16 v[120:123], v[52:55], a[120:123], v[120:123]\n\t"
  "buffer_load_dwordx4 a[184:187], v56, s[40:43], s71 offen offset:1024 sc1\n\t"
  "v_mfma_f32_16x16x32_bf16 v[120:123], v[48:51], a[104:107], v[120:123]\n\t"
  "ds_read_b64_tr_b16 v[44:45], v29 offset:24704\n\t"
  "v_mfma_f32_16x16x32_bf16 v[124:127], v[48:51], a[108:111], v[124:127]\n\t"
  "ds_read_b64_tr_b16 v[46:47], v29 offset:28800\n\t"
  "v_mfma_f32_16x16x32_bf16 v[124:127], v[52:55], a[124:127], v[124:127]\n\t"
  "buffer_load_dwordx4 a[188:191], v57, s[40:43], s71 offen offset:1024 sc1\n\t"
  "s_waitcnt lgkmcnt(4)\n\t"
  "v_mfma_f32_16x16x32_bf16 v[180:183], v[32:35], a[100:103], v[180:183]\n\t"
  "ds_read_b64_tr_b16 v[48:49], v29 offset:16544\n\t"
  "v_mfma_f32_16x16x32_bf16 v[180:183], v[36:39], a[116:119], v[180:183]\n\t"
  "ds_read_b64_tr_b16 v[50:51], v29 offset:20640\n\t"
  "v_mfma_f32_16x16x32_bf16 v[184:187], v[36:39], a[120:123], v[184:187]\n\t"
  "v_mfma_f32_16x16x32_bf16 v[184:187], v[32:35], a[104:107], v[184:187]\n\t"
  "ds_read_b64_tr_b16 v[52:53], v29 offset:24736\n\t"
  "v_mfma_f32_16x16x32_bf16 v[188:191], v[32:35], a[108:111], v[188:191]\n\t"
  "ds_read_b64_tr_b16 v[54:55], v29 offset:28832\n\t"
  "s_bfe_u32 s71, s85, 0x40004\n\t"
  "s_lshl_b32 s71, s71, 11\n\t"
  "v_mfma_f32_16x16x32_bf16 v[188:191], v[36:39], a[124:127], v[188:191]\n\t"
  "buffer_load_dwordx4 a[192:195], v10, s[40:43], s71 offen offset:0 sc1\n\t"
  "v_subrev_u32 v26, s91, v26\n\t"
  "s_waitcnt lgkmcnt(4)\n\t"
  "v_mfma_f32_16x16x32_bf16 v[68:71], v[40:43], a[100:103], v[68:71]\n\t"
  "ds_read_b64_tr_b16 v[32:33], v29 offset:16576\n\t"
  "v_cmp_lt_u32 vcc, 0, v26\n\t"
  "v_mfma_f32_16x16x32_bf16 v[68:71], v[44:47], a[116:119], v[68:71]\n\t"
  "ds_read_b64_tr_b16 v[34:35], v29 offset:20672\n\t"
  "s_nop 4\n\t"
  "v_mfma_f32_16x16x32_bf16 v[72:75], v[44:47], a[120:123], v[72:75]\n\t"
  "s_cbranch_vccnz L_k1_sp4\n\t"
  "L_k1_rs4:\n\t"
  "v_mfma_f32_16x16x32_bf16 v[72:75], v[40:43], a[104:107], v[72:75]\n\t"
  "ds_read_b64_tr_b16 v[36:37], v29 offset:24768\n\t"
  "v_mfma_f32_16x16x32_bf16 v[76:79], v[40:43], a[108:111], v[76:79]\n\t"
  "ds_read_b64_tr_b16 v[38:39], v29 offset:28864\n\t"
  "v_mfma_f32_16x16x32_bf16 v[76:79], v[44:47], a[124:127], v[76:79]\n\t"
  "buffer_load_dwordx4 a[196:199], v10, s[40:43], s71 offen offset:1024 sc1\n\t"
  "s_waitcnt lgkmcnt(4)\n\t"
  "v_mfma_f32_16x16x32_bf16 v[100:103], v[48:51], a[100:103], v[100:103]\n\t"
  "ds_read_b64_tr_b16 v[40:41], v28 offset:32768\n\t"
  "v_mfma_f32_16x16x32_bf16 v[100:103], v[52:55], a[116:119], v[100:103]\n\t"
  "ds_read_b64_tr_b16 v[42:43], v28 offset:36864\n\t"
  "v_mfma_f32_16x16x32_bf16 v[104:107], v[52:55], a[120:123], v[104:107]\n\t"
  "v_mfma_f32_16x16x32_bf16 v[104:107], v[48:51], a[104:107], v[104:107]\n\t"
  "ds_read_b64_tr_b16 v[44:45], v28 offset:40960\n\t"
  "v_mfma_f32_16x16x32_bf16 v[108:111], v[48:51], a[108:111], v[108:111]\n\t"
  "ds_read_b64_tr_b16 v[46:47], v28 offset:45056\n\t"
  "s_bfe_u32 s71, s85, 0x40008\n\t"
  "s_lshl_b32 s71, s71, 11\n\t"
  "v_mfma_f32_16x16x32_bf16 v[108:111], v[52:55], a[124:127], v[108:111]\n\t"
  "buffer_load_dwordx4 a[200:203], v10, s[40:43], s71 offen offset:0 sc1\n\t"
  "s_waitcnt lgkmcnt(4)\n\t"
  "v_mfma_f32_16x16x32_bf16 v[132:135], v[32:35], a[100:103], v[132:135]\n\t"
  "ds_read_b64_tr_b16 v[48:49], v28 offset:49152\n\t"
  "v_mfma_f32_16x16x32_bf16 v[132:135], v[36:39], a[116:119], v[132:135]\n\t"
  "ds_read_b64_tr_b16 v[50:51], v28 offset:53248\n\t"
  "v_mfma_f32_16x16x32_bf16 v[136:139], v[36:39], a[120:123], v[136:139]\n\t"
  "v_mfma_f32_16x16x32_bf16 v[136:139], v[32:35], a[104:107], v[136:139]\n\t"
  "ds_read_b64_tr_b16 v[52:53], v28 offset:57344\n\t"
  "v_mfma_f32_16x16x32_bf16 v[140:143], v[32:35], a[108:111], v[140:143]\n\t"
  "ds_read_b64_tr_b16 v[54:55], v28 offset:61440\n\t"
  "v_mfma_f32_16x16x32_bf16 v[140:143], v[36:39], a[124:127], v[140:143]\n\t"
  "buffer_load_dwordx4 a[204:207], v10, s[40:43], s71 offen offset:1024 sc1\n\t"
  "s_waitcnt vmcnt(10) lgkmcnt(4)\n\t"
  "v_mfma_f32_16x16x32_bf16 v[212:215], v[40:43], a[132:135], v[212:215]\n\t"
  "ds_read_b64_tr_b16 v[32:33], v28 offset:32800\n\t"
  "v_mfma_f32_16x16x32_bf16 v[212:215], v[44:47], a[148:151], v[212:215]\n\t"
  "ds_read_b64_tr_b16 v[34:35], v28 offset:36896\n\t"
  "v_mfma_f32_16x16x32_bf16 v[216:219], v[44:47], a[152:155], v[216:219]\n\t"
  "v_mfma_f32_16x16x32_bf16 v[216:219], v[40:43], a[136:139], v[216:219]\n\t"
  "ds_read_b64_tr_b16 v[36:37], v28 offset:40992\n\t"
  "v_mfma_f32_16x16x32_bf16 v[220:223], v[40:43], a[140:143], v[220:223]\n\t"
  "ds_read_b64_tr_b16 v[38:39], v28 offset:45088\n\t"
  "s_bfe_u32 s71, s85, 0x4000c\n\t"
  "s_lshl_b32 s71, s71, 11\n\t"
  "v_mfma_f32_16x16x32_bf16 v[220:223], v[44:47], a[156:159], v[220:223]\n\t"
  "buffer_load_dwordx4 a[208:211], v10, s[40:43], s71 offen offset:0 sc1\n\t"
  "s_waitcnt vmcnt(5) lgkmcnt(4)\n\t"
  "v_mfma_f32_16x16x32_bf16 v[212:215], v[48:51], a[164:167], v[212:215]\n\t"
  "ds_read_b64_tr_b16 v[40:41], v28 offset:49184\n\t"
  "v_mfma_f32_16x16x32_bf16 v[212:215], v[52:55], a[180:183], v[212:215]\n\t"
  "ds_read_b64_tr_b16 v[42:43], v28 offset:53280\n\t"
  "v_mfma_f32_16x16x32_bf16 v[216:219], v[52:55], a[184:187], v[216:219]\n\t"
  "v_mfma_f32_16x16x32_bf16 v[216:219], v[48:51], a[168:171], v[216:219]\n\t"
  "ds_read_b64_tr_b16 v[44:45], v28 offset:57376\n\t"
  "v_mfma_f32_16x16x32_bf16 v[220:223], v[48:51], a[172:175], v[220:223]\n\t"
  "ds_read_b64_tr_b16 v[46:47], v28 offset:61472\n\t"
  "v_mfma_f32_16x16x32_bf16 v[220:223], v[52:55], a[188:191], v[220:223]\n\t"
  "buffer_load_dwordx4 a[212:215], v10, s[40:43], s71 offen offset:1024 sc1\n\t"
  "s_waitcnt lgkmcnt(4)\n\t"
  "v_mfma_f32_16x16x32_bf16 v[244:247], v[32:35], a[132:135], v[244:247]\n\t"
  "ds_read_b64_tr_b16 v[48:49], v28 offset:32832\n\t"
  "v_mfma_f32_16x16x32_bf16 v[244:247], v[36:39], a[148:151], v[244:247]\n\t"
  "ds_read_b64_tr_b16 v[50:51], v28 offset:36928\n\t"
  "v_mfma_f32_16x16x32_bf16 v[248:251], v[36:39], a[152:155], v[248:251]\n\t"
  "v_mfma_f32_16x16x32_bf16 v[248:251], v[32:35], a[136:139], v[248:251]\n\t"
  "ds_read_b64_tr_b16 v[52:53], v28 offset:41024\n\t"
  "v_mfma_f32_16x16x32_bf16 v[252:255], v[32:35], a[140:143], v[252:255]\n\t"
  "ds_read_b64_tr_b16 v[54:55], v28 offset:45120\n\t"
  "s_bfe_u32 s71, s85, 0x40014\n\t"
  "s_lshl_b32 s71, s71, 11\n\t"
  "v_mfma_f32_16x16x32_bf16 v[252:255], v[36:39], a[156:159], v[252:255]\n\t"
  "buffer_load_dwordx4 a[216:219], v10, s[40:43], s71 offen offset:0 sc1\n\t"
  "s_waitcnt lgkmcnt(4)\n\t"
  "v_mfma_f32_16x16x32_bf16 v[244:247], v[40:43], a[164:167], v[244:247]\n\t"
  "ds_read_b64_tr_b16 v[32:33], v28 offset:49216\n\t"
  "v_mfma_f32_16x16x32_bf16 v[244:247], v[44:47], a[180:183], v[244:247]\n\t"
  "ds_read_b64_tr_b16 v[34:35], v28 offset:53312\n\t"
  "v_mfma_f32_16x16x32_bf16 v[248:251], v[44:47], a[184:187], v[248:251]\n\t"
  "v_mfma_f32_16x16x32_bf16 v[248:251], v[40:43], a[168:171], v[248:251]\n\t"
  "ds_read_b64_tr_b16 v[36:37], v28 offset:57408\n\t"
  "v_mfma_f32_16x16x32_bf16 v[252:255], v[40:43], a[172:175], v[252:255]\n\t"
  "ds_read_b64_tr_b16 v[38:39], v28 offset:61504\n\t"
  "v_mfma_f32_16x16x32_bf16 v[252:255], v[44:47], a[188:191], v[252:255]\n\t"
  "buffer_load_dwordx4 a[220:223], v10, s[40:43], s71 offen offset:1024 sc1\n\t"
  "s_waitcnt lgkmcnt(4)\n\t"
  "v_mfma_f32_16x16x32_bf16 v[116:119], v[48:51], a[132:135], v[116:119]\n\t"
  "ds_read_b64_tr_b16 v[40:41], v28 offset:32864\n\t"
  "v_mfma_f32_16x16x32_bf16 v[116:119], v[52:55], a[148:151], v[116:119]\n\t"
  "ds_read_b64_tr_b16 v[42:43], v28 offset:36960\n\t"
  "v_mfma_f32_16x16x32_bf16 v[120:123], v[52:55], a[152:155], v[120:123]\n\t"
  "v_cvt_pk_bf16_f32 v212, v212, v213\n\t"
  "v_cvt_pk_bf16_f32 v213, v214, v215\n\t"
  "v_mfma_f32_16x16x32_bf16 v[120:123], v[48:51], a[136:139], v[120:123]\n\t"
  "ds_read_b64_tr_b16 v[44:45], v28 offset:41056\n\t"
  "v_cvt_pk_bf16_f32 v214, v244, v245\n\t"
  "v_mfma_f32_16x16x32_bf16 v[124:127], v[48:51], a[140:143], v[124:127]\n\t"
  "ds_read_b64_tr_b16 v[46:47], v28 offset:45152\n\t"
  "s_bfe_u32 s71, s85, 0x40018\n\t"
  "s_lshl_b32 s71, s71, 11\n\t"
  "v_mfma_f32_16x16x32_bf16 v[124:127], v[52:55], a[156:159], v[124:127]\n\t"
  "buffer_load_dwordx4 a[224:227], v10, s[40:43], s71 offen offset:0 sc1\n\t"
  "s_waitcnt lgkmcnt(4)\n\t"
  "v_mfma_f32_16x16x32_bf16 v[116:119], v[32:35], a[164:167], v[116:119]\n\t"
  "ds_read_b64_tr_b16 v[48:49], v28 offset:49248\n\t"
  "v_cvt_pk_bf16_f32 v215, v246, v247\n\t"
  "v_mfma_f32_16x16x32_bf16 v[116:119], v[36:39], a[180:183], v[116:119]\n\t"
  "ds_read_b64_tr_b16 v[50:51], v28 offset:53344\n\t"
  "v_mfma_f32_16x16x32_bf16 v[120:123], v[36:39], a[184:187], v[120:123]\n\t"
  "buffer_store_dwordx4 v[212:215], v12, s[40:43], s79 offen offset:0 sc1\n\t"
  "v_mfma_f32_16x16x32_bf16 v[120:123], v[32:35], a[168:171], v[120:123]\n\t"
  "ds_read_b64_tr_b16 v[52:53], v28 offset:57440\n\t"
  "v_cvt_pk_bf16_f32 v216, v216, v217\n\t"
  "v_mfma_f32_16x16x32_bf16 v[124:127], v[32:35], a[172:175], v[124:127]\n\t"
  "ds_read_b64_tr_b16 v[54:55], v28 offset:61536\n\t"
  "v_cvt_pk_bf16_f32 v217, v218, v219\n\t"
  "v_mfma_f32_16x16x32_bf16 v[124:127], v[36:39], a[188:191], v[124:127]\n\t"
  "buffer_load_dwordx4 a[228:231], v10, s[40:43], s71 offen offset:1024 sc1\n\t"
  "s_waitcnt lgkmcnt(4)\n\t"
  "v_mfma_f32_16x16x32_bf16 v[180:183], v[40:43], a[132:135], v[180:183]\n\t"
  "ds_read_b64_tr_b16 v[32:33], v28 offset:32896\n\t"
  "v_cvt_pk_bf16_f32 v218, v248, v249\n\t"
  "v_mfma_f32_16x16x32_bf16 v[180:183], v[44:47], a[148:151], v[180:183]\n\t"
  "ds_read_b64_tr_b16 v[34:35], v28 offset:36992\n\t"
  "v_cvt_pk_bf16_f32 v219, v250, v251\n\t"
  "v_mfma_f32_16x16x32_bf16 v[184:187], v[44:47], a[152:155], v[184:187]\n\t"
  "buffer_store_dwordx4 v[216:219], v13, s[40:43], s79 offen offset:0 sc1\n\t"
  "v_mfma_f32_16x16x32_bf16 v[184:187], v[40:43], a[136:139], v[184:187]\n\t"
  "ds_read_b64_tr_b16 v[36:37], v28 offset:41088\n\t"
  "v_cvt_pk_bf16_f32 v220, v220, v221\n\t"
  "v_mfma_f32_16x16x32_bf16 v[188:191], v[40:43], a[140:143], v[188:191]\n\t"
  "ds_read_b64_tr_b16 v[38:39], v28 offset:45184\n\t"
  "s_bfe_u32 s71, s85, 0x4001c\n\t"
  "s_lshl_b32 s71, s71, 11\n\t"
  "v_mfma_f32_16x16x32_bf16 v[188:191], v[44:47], a[156:159], v[188:191]\n\t"
  "buffer_load_dwordx4 a[232:235], v10, s[40:43], s71 offen offset:0 sc1\n\t"
  "s_waitcnt lgkmcnt(4)\n\t"
  "v_mfma_f32_16x16x32_bf16 v[180:183], v[48:51], a[164:167], v[180:183]\n\t"
  "ds_read_b64_tr_b16 v[40:41], v28 offset:49280\n\t"
  "v_cvt_pk_bf16_f32 v221, v222, v223\n\t"
  "v_mfma_f32_16x16x32_bf16 v[180:183], v[52:55], a[180:183], v[180:183]\n\t"
  "ds_read_b64_tr_b16 v[42:43], v28 offset:53376\n\t"
  "v_cvt_pk_bf16_f32 v222, v252, v253\n\t"
  "v_mfma_f32_16x16x32_bf16 v[184:187], v[52:55], a[184:187], v[184:187]\n\t"
  "v_cvt_pk_bf16_f32 v223, v254, v255\n\t"
  "v_mfma_f32_16x16x32_bf16 v[184:187], v[48:51], a[168:171], v[184:187]\n\t"
  "ds_read_b64_tr_b16 v[44:45], v28 offset:57472\n\t"
  "v_mfma_f32_16x16x32_bf16 v[188:191], v[48:51], a[172:175], v[188:191]\n\t"
  "ds_read_b64_tr_b16 v[46:47], v28 offset:61568\n\t"
  "v_mfma_f32_16x16x32_bf16 v[188:191], v[52:55], a[188:191], v[188:191]\n\t"
  "buffer_load_dwordx4 a[236:239], v10, s[40:43], s71 offen offset:1024 sc1\n\t"
  "s_waitcnt lgkmcnt(4)\n\t"
  "v_mfma_f32_16x16x32_bf16 v[68:71], v[32:35], a[132:135], v[68:71]\n\t"
  "ds_read_b64_tr_b16 v[48:49], v28 offset:32928\n\t"
  "v_mfma_f32_16x16x32_bf16 v[68:71], v[36:39], a[148:151], v[68:71]\n\t"
  "ds_read_b64_tr_b16 v[50:51], v28 offset:37024\n\t"
  "v_mfma_f32_16x16x32_bf16 v[72:75], v[36:39], a[152:155], v[72:75]\n\t"
  "buffer_store_dwordx4 v[220:223], v14, s[40:43], s79 offen offset:0 sc1\n\t"
  "v_mfma_f32_16x16x32_bf16 v[72:75], v[32:35], a[136:139], v[72:75]\n\t"
  "ds_read_b64_tr_b16 v[52:53], v28 offset:41120\n\t"
  "v_cvt_pk_bf16_f32 v116, v116, v117\n\t"
  "v_mfma_f32_16x16x32_bf16 v[76:79], v[32:35], a[140:143], v[76:79]\n\t"
  "ds_read_b64_tr_b16 v[54:55], v28 offset:45216\n\t"
  "v_cvt_pk_bf16_f32 v117, v118, v119\n\t"
  "v_mfma_f32_16x16x32_bf16 v[76:79], v[36:39], a[156:159], v[76:79]\n\t"
  "v_cvt_pk_bf16_f32 v118, v180, v181\n\t"
  "s_waitcnt lgkmcnt(4)\n\t"
  "v_mfma_f32_16x16x32_bf16 v[68:71], v[40:43], a[164:167], v[68:71]\n\t"
  "ds_read_b64_tr_b16 v[32:33], v28 offset:49312\n\t"
  "v_cvt_pk_bf16_f32 v119, v182, v183\n\t"
  "v_mfma_f32_16x16x32_bf16 v[68:71], v[44:47], a[180:183], v[68:71]\n\t"
  "ds_read_b64_tr_b16 v[34:35], v28 offset:53408\n\t"
  "v_mfma_f32_16x16x32_bf16 v[72:75], v[44:47], a[184:187], v[72:75]\n\t"
  "buffer_store_dwordx4 v[116:119], v12, s[40:43], s79 offen offset:1024 sc1\n\t"
  "v_mfma_f32_16x16x32_bf16 v[72:75], v[40:43], a[168:171], v[72:75]\n\t"
  "ds_read_b64_tr_b16 v[36:37], v28 offset:57504\n\t"
  "v_cvt_pk_bf16_f32 v120, v120, v121\n\t"
  "v_mfma_f32_16x16x32_bf16 v[76:79], v[40:43], a[172:175], v[76:79]\n\t"
  "ds_read_b64_tr_b16 v[38:39], v28 offset:61600\n\t"
  "v_cvt_pk_bf16_f32 v121, v122, v123\n\t"
  "v_mfma_f32_16x16x32_bf16 v[76:79], v[44:47], a[188:191], v[76:79]\n\t"
  "v_cvt_pk_bf16_f32 v122, v184, v185\n\t"
  "s_waitcnt lgkmcnt(4)\n\t"
  "v_mfma_f32_16x16x32_bf16 v[100:103], v[48:51], a[132:135], v[100:103]\n\t"
  "ds_read_b64_tr_b16 v[40:41], v28 offset:32960\n\t"
  "v_cvt_pk_bf16_f32 v123, v186, v187\n\t"
  "v_mfma_f32_16x16x32_bf16 v[100:103], v[52:55], a[148:151], v[100:103]\n\t"
  "ds_read_b64_tr_b16 v[42:43], v28 offset:37056\n\t"
  "v_mfma_f32_16x16x32_bf16 v[104:107], v[52:55], a[152:155], v[104:107]\n\t"
  "ds_read_b64_tr_b16 v[192:193], v29 offset:32768\n\t"
  "ds_read_b64_tr_b16 v[194:195], v29 offset:36864\n\t"
  "v_mfma_f32_16x16x32_bf16 v[104:107], v[48:51], a[136:139], v[104:107]\n\t"
  "ds_read_b64_tr_b16 v[44:45], v28 offset:41152\n\t"
  "v_mfma_f32_16x16x32_bf16 v[108:111], v[48:51], a[140:143], v[108:111]\n\t"
  "ds_read_b64_tr_b16 v[46:47], v28 offset:45248\n\t"
  "v_mfma_f32_16x16x32_bf16 v[108:111], v[52:55], a[156:159], v[108:111]\n\t"
  "ds_read_b64_tr_b16 v[196:197], v29 offset:40960\n\t"
  "ds_read_b64_tr_b16 v[198:199], v29 offset:45056\n\t"
  "s_waitcnt lgkmcnt(8)\n\t"
  "v_mfma_f32_16x16x32_bf16 v[100:103], v[32:35], a[164:167], v[100:103]\n\t"
  "ds_read_b64_tr_b16 v[48:49], v28 offset:49344\n\t"
  "v_mfma_f32_16x16x32_bf16 v[100:103], v[36:39], a[180:183], v[100:103]\n\t"
  "ds_read_b64_tr_b16 v[50:51], v28 offset:53440\n\t"
  "v_mfma_f32_16x16x32_bf16 v[104:107], v[36:39], a[184:187], v[104:107]\n\t"
  "ds_read_b64_tr_b16 v[200:201], v29 offset:49152\n\t"
  "ds_read_b64_tr_b16 v[202:203], v29 offset:53248\n\t"
  "v_mfma_f32_16x16x32_bf16 v[104:107], v[32:35], a[168:171], v[104:107]\n\t"
  "ds_read_b64_tr_b16 v[52:53], v28 offset:57536\n\t"
  "v_mfma_f32_16x16x32_bf16 v[108:111], v[32:35], a[172:175], v[108:111]\n\t"
  "ds_read_b64_tr_b16 v[54:55], v28 offset:61632\n\t"
  "v_mfma_f32_16x16x32_bf16 v[108:111], v[36:39], a[188:191], v[108:111]\n\t"
  "ds_read_b64_tr_b16 v[204:205], v29 offset:57344\n\t"
  "ds_read_b64_tr_b16 v[206:207], v29 offset:61440\n\t"
  "s_waitcnt lgkmcnt(10)\n\t"
  "v_mfma_f32_16x16x32_bf16 v[132:135], v[40:43], a[132:135], v[132:135]\n\t"
  "buffer_store_dwordx4 v[120:123], v13, s[40:43], s79 offen offset:1024 sc1\n\t"
  "v_mfma_f32_16x16x32_bf16 v[132:135], v[44:47], a[148:151], v[132:135]\n\t"
  "v_cvt_pk_bf16_f32 v124, v124, v125\n\t"
  "v_cvt_pk_bf16_f32 v125, v126, v127\n\t"
  "v_mfma_f32_16x16x32_bf16 v[136:139], v[44:47], a[152:155], v[136:139]\n\t"
  "ds_read_b64_tr_b16 v[224:225], v29 offset:32800\n\t"
  "ds_read_b64_tr_b16 v[226:227], v29 offset:36896\n\t"
  "v_mfma_f32_16x16x32_bf16 v[136:139], v[40:43], a[136:139], v[136:139]\n\t"
  "v_cvt_pk_bf16_f32 v126, v188, v189\n\t"
  "v_cvt_pk_bf16_f32 v127, v190, v191\n\t"
  "v_mfma_f32_16x16x32_bf16 v[140:143], v[40:43], a[140:143], v[140:143]\n\t"
  "buffer_store_dwordx4 v[124:127], v14, s[40:43], s79 offen offset:1024 sc1\n\t"
  "v_mfma_f32_16x16x32_bf16 v[140:143], v[44:47], a[156:159], v[140:143]\n\t"
  "ds_read_b64_tr_b16 v[228:229], v29 offset:40992\n\t"
  "ds_read_b64_tr_b16 v[230:231], v29 offset:45088\n\t"
  "s_waitcnt lgkmcnt(6)\n\t"
  "v_mfma_f32_16x16x32_bf16 v[132:135], v[48:51], a[164:167], v[132:135]\n\t"
  "v_cvt_pk_bf16_f32 v68, v68, v69\n\t"
  "v_cvt_pk_bf16_f32 v69, v70, v71\n\t"
  "v_mfma_f32_16x16x32_bf16 v[132:135], v[52:55], a[180:183], v[132:135]\n\t"
  "v_cvt_pk_bf16_f32 v70, v100, v101\n\t"
  "v_cvt_pk_bf16_f32 v71, v102, v103\n\t"
  "v_mfma_f32_16x16x32_bf16 v[136:139], v[52:55], a[184:187], v[136:139]\n\t"
  "ds_read_b64_tr_b16 v[232:233], v29 offset:49184\n\t"
  "ds_read_b64_tr_b16 v[234:235], v29 offset:53280\n\t"
  "buffer_store_dwordx4 v[68:71], v12, s[40:43], s80 offen offset:0 sc1\n\t"
  "v_mfma_f32_16x16x32_bf16 v[136:139], v[48:51], a[168:171], v[136:139]\n\t"
  "v_cvt_pk_bf16_f32 v72, v72, v73\n\t"
  "v_mfma_f32_16x16x32_bf16 v[140:143], v[48:51], a[172:175], v[140:143]\n\t"
  "v_cvt_pk_bf16_f32 v73, v74, v75\n\t"
  "v_mfma_f32_16x16x32_bf16 v[140:143], v[52:55], a[188:191], v[140:143]\n\t"
  "v_cvt_pk_bf16_f32 v74, v104, v105\n\t"
  "v_mfma_f32_16x16x32_bf16 v[208:211], v[192:195], a[64:67], v[208:211]\n\t"
  "ds_read_b64_tr_b16 v[236:237], v29 offset:57376\n\t"
  "ds_read_b64_tr_b16 v[238:239], v29 offset:61472\n\t"
  "v_cvt_pk_bf16_f32 v75, v106, v107\n\t"
  "v_mfma_f32_16x16x32_bf16 v[208:211], v[196:199], a[80:83], v[208:211]\n\t"
  "ds_read_b64_tr_b16 v[80:81], v29 offset:32832\n\t"
  "ds_read_b64_tr_b16 v[82:83], v29 offset:36928\n\t"
  "buffer_store_dwordx4 v[72:75], v13, s[40:43], s80 offen offset:0 sc1\n\t"
  "s_waitcnt vmcnt(19)\n\t"
  "v_mfma_f32_16x16x32_bf16 v[208:211], v[200:203], a[192:195], v[208:211]\n\t"
  "ds_read_b64_tr_b16 v[84:85], v29 offset:41024\n\t"
  "ds_read_b64_tr_b16 v[86:87], v29 offset:45120\n\t"
  "v_cvt_pk_bf16_f32 v76, v76, v77\n\t"
  "s_waitcnt vmcnt(18) lgkmcnt(12)\n\t"
  "v_mfma_f32_16x16x32_bf16 v[208:211], v[204:207], a[196:199], v[208:211]\n\t"
  "ds_read_b64_tr_b16 v[88:89], v29 offset:49216\n\t"
  "ds_read_b64_tr_b16 v[90:91], v29 offset:53312\n\t"
  "v_cvt_pk_bf16_f32 v77, v78, v79\n\t"
  "s_waitcnt lgkmcnt(10)\n\t"
  "v_mfma_f32_16x16x32_bf16 v[240:243], v[224:227], a[64:67], v[240:243]\n\t"
  "ds_read_b64_tr_b16 v[92:93], v29 offset:57408\n\t"
  "ds_read_b64_tr_b16 v[94:95], v29 offset:61504\n\t"
  "v_cvt_pk_bf16_f32 v78, v108, v109\n\t"
  "v_mfma_f32_16x16x32_bf16 v[240:243], v[228:231], a[80:83], v[240:243]\n\t"
  "ds_read_b64_tr_b16 v[192:193], v29 offset:32864\n\t"
  "ds_read_b64_tr_b16 v[194:195], v29 offset:36960\n\t"
  "v_cvt_pk_bf16_f32 v79, v110, v111\n\t"
  "s_waitcnt lgkmcnt(10)\n\t"
  "v_mfma_f32_16x16x32_bf16 v[240:243], v[232:235], a[192:195], v[240:243]\n\t"
  "ds_read_b64_tr_b16 v[196:197], v29 offset:41056\n\t"
  "ds_read_b64_tr_b16 v[198:199], v29 offset:45152\n\t"
  "buffer_store_dwordx4 v[76:79], v14, s[40:43], s80 offen offset:0 sc1\n\t"
  "v_mfma_f32_16x16x32_bf16 v[240:243], v[236:239], a[196:199], v[240:243]\n\t"
  "ds_read_b64_tr_b16 v[200:201], v29 offset:49248\n\t"
  "ds_read_b64_tr_b16 v[202:203], v29 offset:53344\n\t"
  "v_cvt_pk_bf16_f32 v132, v132, v133\n\t"
  "s_waitcnt lgkmcnt(10)\n\t"
  "v_mfma_f32_16x16x32_bf16 v[112:115], v[80:83], a[64:67], v[112:115]\n\t"
  "ds_read_b64_tr_b16 v[204:205], v29 offset:57440\n\t"
  "ds_read_b64_tr_b16 v[206:207], v29 offset:61536\n\t"
  "v_cvt_pk_bf16_f32 v133, v134, v135\n\t"
  "v_mfma_f32_16x16x32_bf16 v[112:115], v[84:87], a[80:83], v[112:115]\n\t"
  "ds_read_b64_tr_b16 v[224:225], v29 offset:32896\n\t"
  "ds_read_b64_tr_b16 v[226:227], v29 offset:36992\n\t"
  "buffer_store_dwordx2 v[132:133], v12, s[40:43], s80 offen offset:1024 sc1\n\t"
  "s_waitcnt lgkmcnt(10)\n\t"
  "v_mfma_f32_16x16x32_bf16 v[112:115], v[88:91], a[192:195], v[112:115]\n\t"
  "ds_read_b64_tr_b16 v[228:229], v29 offset:41088\n\t"
  "ds_read_b64_tr_b16 v[230:231], v29 offset:45184\n\t"
  "v_cvt_pk_bf16_f32 v136, v136, v137\n\t"
  "v_mfma_f32_16x16x32_bf16 v[112:115], v[92:95], a[196:199], v[112:115]\n\t"
  "ds_read_b64_tr_b16 v[232:233], v29 offset:49280\n\t"
  "ds_read_b64_tr_b16 v[234:235], v29 offset:53376\n\t"
  "v_cvt_pk_bf16_f32 v137, v138, v139\n\t"
  "s_waitcnt lgkmcnt(10)\n\t"
  "v_mfma_f32_16x16x32_bf16 v[176:179], v[192:195], a[64:67], v[176:179]\n\t"
  "ds_read_b64_tr_b16 v[236:237], v29 offset:57472\n\t"
  "ds_read_b64_tr_b16 v[238:239], v29 offset:61568\n\t"
  "buffer_store_dwordx2 v[136:137], v13, s[40:43], s80 offen offset:1024 sc1\n\t"
  "v_mfma_f32_16x16x32_bf16 v[176:179], v[196:199], a[80:83], v[176:179]\n\t"
  "ds_read_b64_tr_b16 v[80:81], v29 offset:32928\n\t"
  "ds_read_b64_tr_b16 v[82:83], v29 offset:37024\n\t"
  "v_cvt_pk_bf16_f32 v140, v140, v141\n\t"
  "s_waitcnt lgkmcnt(10)\n\t"
  "v_mfma_f32_16x16x32_bf16 v[176:179], v[200:203], a[192:195], v[176:179]\n\t"
  "ds_read_b64_tr_b16 v[84:85], v29 offset:41120\n\t"
  "ds_read_b64_tr_b16 v[86:87], v29 offset:45216\n\t"
  "v_cvt_pk_bf16_f32 v141, v142, v143\n\t"
  "v_mfma_f32_16x16x32_bf16 v[176:179], v[204:207], a[196:199], v[176:179]\n\t"
  "ds_read_b64_tr_b16 v[88:89], v29 offset:49312\n\t"
  "ds_read_b64_tr_b16 v[90:91], v29 offset:53408\n\t"
  "buffer_store_dwordx2 v[140:141], v14, s[40:43], s80 offen offset:1024 sc1\n\t"
  "s_waitcnt lgkmcnt(10)\n\t"
  "v_mfma_f32_16x16x32_bf16 v[64:67], v[224:227], a[64:67], v[64:67]\n\t"
  "ds_read_b64_tr_b16 v[92:93], v29 offset:57504\n\t"
  "ds_read_b64_tr_b16 v[94:95], v29 offset:61600\n\t"
  "v_mfma_f32_16x16x32_bf16 v[64:67], v[228:231], a[80:83], v[64:67]\n\t"
  "ds_read_b64_tr_b16 v[192:193], v29 offset:32960\n\t"
  "ds_read_b64_tr_b16 v[194:195], v29 offset:37056\n\t"
  "s_waitcnt lgkmcnt(10)\n\t"
  "v_mfma_f32_16x16x32_bf16 v[64:67], v[232:235], a[192:195], v[64:67]\n\t"
  "ds_read_b64_tr_b16 v[196:197], v29 offset:41152\n\t"
  "ds_read_b64_tr_b16 v[198:199], v29 offset:45248\n\t"
  "v_mfma_f32_16x16x32_bf16 v[64:67], v[236:239], a[196:199], v[64:67]\n\t"
  "ds_read_b64_tr_b16 v[200:201], v29 offset:49344\n\t"
  "ds_read_b64_tr_b16 v[202:203], v29 offset:53440\n\t"
  "s_waitcnt lgkmcnt(10)\n\t"
  "v_mfma_f32_16x16x32_bf16 v[96:99], v[80:83], a[64:67], v[96:99]\n\t"
  "ds_read_b64_tr_b16 v[204:205], v29 offset:57536\n\t"
  "ds_read_b64_tr_b16 v[206:207], v29 offset:61632\n\t"
  "v_mfma_f32_16x16x32_bf16 v[96:99], v[84:87], a[80:83], v[96:99]\n\t"
  "ds_read_b64_tr_b16 v[224:225], v58 offset:0\n\t"
  "ds_read_b64_tr_b16 v[226:227], v58 offset:4096\n\t"
  "s_waitcnt lgkmcnt(10)\n\t"
  "v_mfma_f32_16x16x32_bf16 v[96:99], v[88:91], a[192:195], v[96:99]\n\t"
  "ds_read_b64_tr_b16 v[228:229], v58 offset:8192\n\t"
  "ds_read_b64_tr_b16 v[230:231], v58 offset:12288\n\t"
  "v_mfma_f32_16x16x32_bf16 v[96:99], v[92:95], a[196:199], v[96:99]\n\t"
  "ds_read_b64_tr_b16 v[232:233], v58 offset:16384\n\t"
  "ds_read_b64_tr_b16 v[234:235], v58 offset:20480\n\t"
  "s_waitcnt lgkmcnt(10)\n\t"
  "v_mfma_f32_16x16x32_bf16 v[128:131], v[192:195], a[64:67], v[128:131]\n\t"
  "ds_read_b64_tr_b16 v[236:237], v58 offset:24576\n\t"
  "ds_read_b64_tr_b16 v[238:239], v58 offset:28672\n\t"
  "v_mfma_f32_16x16x32_bf16 v[128:131], v[196:199], a[80:83], v[128:131]\n\t"
  "ds_read_b64_tr_b16 v[80:81], v58 offset:32\n\t"
  "ds_read_b64_tr_b16 v[82:83], v58 offset:4128\n\t"
  "s_waitcnt lgkmcnt(10)\n\t"
  "v_mfma_f32_16x16x32_bf16 v[128:131], v[200:203], a[192:195], v[128:131]\n\t"
  "ds_read_b64_tr_b16 v[84:85], v58 offset:8224\n\t"
  "ds_read_b64_tr_b16 v[86:87], v58 offset:12320\n\t"
  "v_mfma_f32_16x16x32_bf16 v[128:131], v[204:207], a[196:199], v[128:131]\n\t"
  "ds_read_b64_tr_b16 v[88:89], v58 offset:16416\n\t"
  "ds_read_b64_tr_b16 v[90:91], v58 offset:20512\n\t"
  "s_waitcnt vmcnt(21) lgkmcnt(10)\n\t"
  "v_mfma_f32_16x16x32_bf16 v[208:211], v[224:227], a[200:203], v[208:211]\n\t"
  "ds_read_b64_tr_b16 v[92:93], v58 offset:24608\n\t"
  "ds_read_b64_tr_b16 v[94:95], v58 offset:28704\n\t"
  "s_waitcnt vmcnt(20)\n\t"
  "v_mfma_f32_16x16x32_bf16 v[208:211], v[228:231], a[204:207], v[208:211]\n\t"
  "ds_read_b64_tr_b16 v[192:193], v58 offset:64\n\t"
  "ds_read_b64_tr_b16 v[194:195], v58 offset:4160\n\t"
  "s_waitcnt vmcnt(19) lgkmcnt(10)\n\t"
  "v_mfma_f32_16x16x32_bf16 v[208:211], v[232:235], a[208:211], v[208:211]\n\t"
  "ds_read_b64_tr_b16 v[196:197], v58 offset:8256\n\t"
  "ds_read_b64_tr_b16 v[198:199], v58 offset:12352\n\t"
  "s_waitcnt vmcnt(18)\n\t"
  "v_mfma_f32_16x16x32_bf16 v[208:211], v[236:239], a[212:215], v[208:211]\n\t"
  "ds_read_b64_tr_b16 v[200:201], v58 offset:16448\n\t"
  "ds_read_b64_tr_b16 v[202:203], v58 offset:20544\n\t"
  "s_waitcnt lgkmcnt(10)\n\t"
  "v_mfma_f32_16x16x32_bf16 v[240:243], v[80:83], a[200:203], v[240:243]\n\t"
  "ds_read_b64_tr_b16 v[204:205], v58 offset:24640\n\t"
  "ds_read_b64_tr_b16 v[206:207], v58 offset:28736\n\t"
  "v_mfma_f32_16x16x32_bf16 v[240:243], v[84:87], a[204:207], v[240:243]\n\t"
  "ds_read_b64_tr_b16 v[224:225], v58 offset:96\n\t"
  "ds_read_b64_tr_b16 v[226:227], v58 offset:4192\n\t"
  "s_waitcnt lgkmcnt(10)\n\t"
  "v_mfma_f32_16x16x32_bf16 v[240:243], v[88:91], a[208:211], v[240:243]\n\t"
  "ds_read_b64_tr_b16 v[228:229], v58 offset:8288\n\t"
  "ds_read_b64_tr_b16 v[230:231], v58 offset:12384\n\t"
  "v_mfma_f32_16x16x32_bf16 v[240:243], v[92:95], a[212:215], v[240:243]\n\t"
  "ds_read_b64_tr_b16 v[232:233], v58 offset:16480\n\t"
  "ds_read_b64_tr_b16 v[234:235], v58 offset:20576\n\t"
  "s_waitcnt lgkmcnt(10)\n\t"
  "v_mfma_f32_16x16x32_bf16 v[112:115], v[192:195], a[200:203], v[112:115]\n\t"
  "ds_read_b64_tr_b16 v[236:237], v58 offset:24672\n\t"
  "ds_read_b64_tr_b16 v[238:239], v58 offset:28768\n\t"
  "v_mfma_f32_16x16x32_bf16 v[112:115], v[196:199], a[204:207], v[112:115]\n\t"
  "ds_read_b64_tr_b16 v[80:81], v58 offset:128\n\t"
  "ds_read_b64_tr_b16 v[82:83], v58 offset:4224\n\t"
  "s_waitcnt lgkmcnt(10)\n\t"
  "v_mfma_f32_16x16x32_bf16 v[112:115], v[200:203], a[208:211], v[112:115]\n\t"
  "ds_read_b64_tr_b16 v[84:85], v58 offset:8320\n\t"
  "ds_read_b64_tr_b16 v[86:87], v58 offset:12416\n\t"
  "v_mfma_f32_16x16x32_bf16 v[112:115], v[204:207], a[212:215], v[112:115]\n\t"
  "ds_read_b64_tr_b16 v[88:89], v58 offset:16512\n\t"
  "ds_read_b64_tr_b16 v[90:91], v58 offset:20608\n\t"
  "s_waitcnt lgkmcnt(10)\n\t"
  "v_mfma_f32_16x16x32_bf16 v[176:179], v[224:227], a[200:203], v[176:179]\n\t"
  "ds_read_b64_tr_b16 v[92:93], v58 offset:24704\n\t"
  "ds_read_b64_tr_b16 v[94:95], v58 offset:28800\n\t"
  "v_mfma_f32_16x16x32_bf16 v[176:179], v[228:231], a[204:207], v[176:179]\n\t"
  "ds_read_b64_tr_b16 v[192:193], v58 offset:160\n\t"
  "ds_read_b64_tr_b16 v[194:195], v58 offset:4256\n\t"
  "s_waitcnt lgkmcnt(10)\n\t"
  "v_mfma_f32_16x16x32_bf16 v[176:179], v[232:235], a[208:211], v[176:179]\n\t"
  "ds_read_b64_tr_b16 v[196:197], v58 offset:8352\n\t"
  "ds_read_b64_tr_b16 v[198:199], v58 offset:12448\n\t"
  "v_mfma_f32_16x16x32_bf16 v[176:179], v[236:239], a[212:215], v[176:179]\n\t"
  "ds_read_b64_tr_b16 v[200:201], v58 offset:16544\n\t"
  "ds_read_b64_tr_b16 v[202:203], v58 offset:20640\n\t"
  "s_waitcnt lgkmcnt(10)\n\t"
  "v_mfma_f32_16x16x32_bf16 v[64:67], v[80:83], a[200:203], v[64:67]\n\t"
  "ds_read_b64_tr_b16 v[204:205], v58 offset:24736\n\t"
  "ds_read_b64_tr_b16 v[206:207], v58 offset:28832\n\t"
  "v_mfma_f32_16x16x32_bf16 v[64:67], v[84:87], a[204:207], v[64:67]\n\t"
  "ds_read_b64_tr_b16 v[224:225], v58 offset:192\n\t"
  "ds_read_b64_tr_b16 v[226:227], v58 offset:4288\n\t"
  "s_waitcnt lgkmcnt(10)\n\t"
  "v_mfma_f32_16x16x32_bf16 v[64:67], v[88:91], a[208:211], v[64:67]\n\t"
  "ds_read_b64_tr_b16 v[228:229], v58 offset:8384\n\t"
  "ds_read_b64_tr_b16 v[230:231], v58 offset:12480\n\t"
  "v_mfma_f32_16x16x32_bf16 v[64:67], v[92:95], a[212:215], v[64:67]\n\t"
  "ds_read_b64_tr_b16 v[232:233], v58 offset:16576\n\t"
  "ds_read_b64_tr_b16 v[234:235], v58 offset:20672\n\t"
  "s_waitcnt lgkmcnt(10)\n\t"
  "v_mfma_f32_16x16x32_bf16 v[96:99], v[192:195], a[200:203], v[96:99]\n\t"
  "ds_read_b64_tr_b16 v[236:237], v58 offset:24768\n\t"
  "ds_read_b64_tr_b16 v[238:239], v58 offset:28864\n\t"
  "v_mfma_f32_16x16x32_bf16 v[96:99], v[196:199], a[204:207], v[96:99]\n\t"
  "ds_read_b64_tr_b16 v[80:81], v29 offset:0\n\t"
  "ds_read_b64_tr_b16 v[82:83], v29 offset:4096\n\t"
  "s_waitcnt lgkmcnt(10)\n\t"
  "v_mfma_f32_16x16x32_bf16 v[96:99], v[200:203], a[208:211], v[96:99]\n\t"
  "ds_read_b64_tr_b16 v[84:85], v29 offset:8192\n\t"
  "ds_read_b64_tr_b16 v[86:87], v29 offset:12288\n\t"
  "v_mfma_f32_16x16x32_bf16 v[96:99], v[204:207], a[212:215], v[96:99]\n\t"
  "ds_read_b64_tr_b16 v[88:89], v29 offset:16384\n\t"
  "ds_read_b64_tr_b16 v[90:91], v29 offset:20480\n\t"
  "s_waitcnt lgkmcnt(10)\n\t"
  "v_mfma_f32_16x16x32_bf16 v[128:131], v[224:227], a[200:203], v[128:131]\n\t"
  "ds_read_b64_tr_b16 v[92:93], v29 offset:24576\n\t"
  "ds_read_b64_tr_b16 v[94:95], v29 offset:28672\n\t"
  "v_mfma_f32_16x16x32_bf16 v[128:131], v[228:231], a[204:207], v[128:131]\n\t"
  "ds_read_b64_tr_b16 v[192:193], v29 offset:32\n\t"
  "ds_read_b64_tr_b16 v[194:195], v29 offset:4128\n\t"
  "s_waitcnt lgkmcnt(10)\n\t"
  "v_mfma_f32_16x16x32_bf16 v[128:131], v[232:235], a[208:211], v[128:131]\n\t"
  "ds_read_b64_tr_b16 v[196:197], v29 offset:8224\n\t"
  "ds_read_b64_tr_b16 v[198:199], v29 offset:12320\n\t"
  "v_mfma_f32_16x16x32_bf16 v[128:131], v[236:239], a[212:215], v[128:131]\n\t"
  "ds_read_b64_tr_b16 v[200:201], v29 offset:16416\n\t"
  "ds_read_b64_tr_b16 v[202:203], v29 offset:20512\n\t"
  "s_waitcnt vmcnt(0)\n\t"
  "s_waitcnt lgkmcnt(10)\n\t"
  "v_mfma_f32_16x16x32_bf16 v[208:211], v[80:83], a[0:3], v[208:211]\n\t"
  "ds_read_b64_tr_b16 v[204:205], v29 offset:24608\n\t"
  "ds_read_b64_tr_b16 v[206:207], v29 offset:28704\n\t"
  "buffer_store_dword v27, v60, s[92:95], 0 offen sc1\n\t"
  "v_mfma_f32_16x16x32_bf16 v[208:211], v[84:87], a[16:19], v[208:211]\n\t"
  "ds_read_b64_tr_b16 v[224:225], v29 offset:64\n\t"
  "ds_read_b64_tr_b16 v[226:227], v29 offset:4160\n\t"
  "s_waitcnt lgkmcnt(10)\n\t"
  "v_mfma_f32_16x16x32_bf16 v[208:211], v[88:91], a[216:219], v[208:211]\n\t"
  "ds_read_b64_tr_b16 v[228:229], v29 offset:8256\n\t"
  "ds_read_b64_tr_b16 v[230:231], v29 offset:12352\n\t"
  "v_mfma_f32_16x16x32_bf16 v[208:211], v[92:95], a[220:223], v[208:211]\n\t"
  "ds_read_b64_tr_b16 v[232:233], v29 offset:16448\n\t"
  "ds_read_b64_tr_b16 v[234:235], v29 offset:20544\n\t"
  "s_waitcnt lgkmcnt(10)\n\t"
  "v_mfma_f32_16x16x32_bf16 v[240:243], v[192:195], a[0:3], v[240:243]\n\t"
  "ds_read_b64_tr_b16 v[236:237], v29 offset:24640\n\t"
  "ds_read_b64_tr_b16 v[238:239], v29 offset:28736\n\t"
  "v_mfma_f32_16x16x32_bf16 v[240:243], v[196:199], a[16:19], v[240:243]\n\t"
  "ds_read_b64_tr_b16 v[80:81], v29 offset:96\n\t"
  "ds_read_b64_tr_b16 v[82:83], v29 offset:4192\n\t"
  "s_waitcnt lgkmcnt(10)\n\t"
  "v_mfma_f32_16x16x32_bf16 v[240:243], v[200:203], a[216:219], v[240:243]\n\t"
  "ds_read_b64_tr_b16 v[84:85], v29 offset:8288\n\t"
  "ds_read_b64_tr_b16 v[86:87], v29 offset:12384\n\t"
  "v_mfma_f32_16x16x32_bf16 v[240:243], v[204:207], a[220:223], v[240:243]\n\t"
  "ds_read_b64_tr_b16 v[88:89], v29 offset:16480\n\t"
  "ds_read_b64_tr_b16 v[90:91], v29 offset:20576\n\t"
  "s_waitcnt lgkmcnt(10)\n\t"
  "v_mfma_f32_16x16x32_bf16 v[112:115], v[224:227], a[0:3], v[112:115]\n\t"
  "ds_read_b64_tr_b16 v[92:93], v29 offset:24672\n\t"
  "ds_read_b64_tr_b16 v[94:95], v29 offset:28768\n\t"
  "v_mfma_f32_16x16x32_bf16 v[112:115], v[228:231], a[16:19], v[112:115]\n\t"
  "ds_read_b64_tr_b16 v[192:193], v29 offset:128\n\t"
  "ds_read_b64_tr_b16 v[194:195], v29 offset:4224\n\t"
  "s_waitcnt lgkmcnt(10)\n\t"
  "v_mfma_f32_16x16x32_bf16 v[112:115], v[232:235], a[216:219], v[112:115]\n\t"
  "ds_read_b64_tr_b16 v[196:197], v29 offset:8320\n\t"
  "ds_read_b64_tr_b16 v[198:199], v29 offset:12416\n\t"
  "v_mfma_f32_16x16x32_bf16 v[112:115], v[236:239], a[220:223], v[112:115]\n\t"
  "ds_read_b64_tr_b16 v[200:201], v29 offset:16512\n\t"
  "ds_read_b64_tr_b16 v[202:203], v29 offset:20608\n\t"
  "s_waitcnt lgkmcnt(10)\n\t"
  "v_mfma_f32_16x16x32_bf16 v[176:179], v[80:83], a[0:3], v[176:179]\n\t"
  "ds_read_b64_tr_b16 v[204:205], v29 offset:24704\n\t"
  "ds_read_b64_tr_b16 v[206:207], v29 offset:28800\n\t"
  "v_mfma_f32_16x16x32_bf16 v[176:179], v[84:87], a[16:19], v[176:179]\n\t"
  "ds_read_b64_tr_b16 v[224:225], v29 offset:160\n\t"
  "ds_read_b64_tr_b16 v[226:227], v29 offset:4256\n\t"
  "s_waitcnt lgkmcnt(10)\n\t"
  "v_mfma_f32_16x16x32_bf16 v[176:179], v[88:91], a[216:219], v[176:179]\n\t"
  "ds_read_b64_tr_b16 v[228:229], v29 offset:8352\n\t"
  "ds_read_b64_tr_b16 v[230:231], v29 offset:12448\n\t"
  "v_mfma_f32_16x16x32_bf16 v[176:179], v[92:95], a[220:223], v[176:179]\n\t"
  "ds_read_b64_tr_b16 v[232:233], v29 offset:16544\n\t"
  "ds_read_b64_tr_b16 v[234:235], v29 offset:20640\n\t"
  "s_waitcnt lgkmcnt(10)\n\t"
  "v_mfma_f32_16x16x32_bf16 v[64:67], v[192:195], a[0:3], v[64:67]\n\t"
  "ds_read_b64_tr_b16 v[236:237], v29 offset:24736\n\t"
  "ds_read_b64_tr_b16 v[238:239], v29 offset:28832\n\t"
  "v_mfma_f32_16x16x32_bf16 v[64:67], v[196:199], a[16:19], v[64:67]\n\t"
  "ds_read_b64_tr_b16 v[80:81], v29 offset:192\n\t"
  "ds_read_b64_tr_b16 v[82:83], v29 offset:4288\n\t"
  "s_waitcnt lgkmcnt(10)\n\t"
  "v_mfma_f32_16x16x32_bf16 v[64:67], v[200:203], a[216:219], v[64:67]\n\t"
  "ds_read_b64_tr_b16 v[84:85], v29 offset:8384\n\t"
  "ds_read_b64_tr_b16 v[86:87], v29 offset:12480\n\t"
  "v_mfma_f32_16x16x32_bf16 v[64:67], v[204:207], a[220:223], v[64:67]\n\t"
  "ds_read_b64_tr_b16 v[88:89], v29 offset:16576\n\t"
  "ds_read_b64_tr_b16 v[90:91], v29 offset:20672\n\t"
  "buffer_load_dword v0, v61, s[92:95], 0 offen sc1\n\t"
  "s_waitcnt lgkmcnt(10)\n\t"
  "v_mfma_f32_16x16x32_bf16 v[96:99], v[224:227], a[0:3], v[96:99]\n\t"
  "ds_read_b64_tr_b16 v[92:93], v29 offset:24768\n\t"
  "ds_read_b64_tr_b16 v[94:95], v29 offset:28864\n\t"
  "v_mfma_f32_16x16x32_bf16 v[96:99], v[228:231], a[16:19], v[96:99]\n\t"
  "ds_read_b64_tr_b16 v[192:193], v28 offset:32768\n\t"
  "ds_read_b64_tr_b16 v[194:195], v28 offset:36864\n\t"
  "s_waitcnt lgkmcnt(10)\n\t"
  "v_mfma_f32_16x16x32_bf16 v[96:99], v[232:235], a[216:219], v[96:99]\n\t"
  "ds_read_b64_tr_b16 v[196:197], v28 offset:40960\n\t"
  "ds_read_b64_tr_b16 v[198:199], v28 offset:45056\n\t"
  "v_mfma_f32_16x16x32_bf16 v[96:99], v[236:239], a[220:223], v[96:99]\n\t"
  "ds_read_b64_tr_b16 v[200:201], v28 offset:49152\n\t"
  "ds_read_b64_tr_b16 v[202:203], v28 offset:53248\n\t"
  "s_waitcnt lgkmcnt(10)\n\t"
  "v_mfma_f32_16x16x32_bf16 v[128:131], v[80:83], a[0:3], v[128:131]\n\t"
  "ds_read_b64_tr_b16 v[204:205], v28 offset:57344\n\t"
  "ds_read_b64_tr_b16 v[206:207], v28 offset:61440\n\t"
  "v_mfma_f32_16x16x32_bf16 v[128:131], v[84:87], a[16:19], v[128:131]\n\t"
  "ds_read_b64_tr_b16 v[224:225], v28 offset:32800\n\t"
  "ds_read_b64_tr_b16 v[226:227], v28 offset:36896\n\t"
  "s_waitcnt lgkmcnt(10)\n\t"
  "v_mfma_f32_16x16x32_bf16 v[128:131], v[88:91], a[216:219], v[128:131]\n\t"
  "ds_read_b64_tr_b16 v[228:229], v28 offset:40992\n\t"
  "ds_read_b64_tr_b16 v[230:231], v28 offset:45088\n\t"
  "v_mfma_f32_16x16x32_bf16 v[128:131], v[92:95], a[220:223], v[128:131]\n\t"
  "ds_read_b64_tr_b16 v[232:233], v28 offset:49184\n\t"
  "ds_read_b64_tr_b16 v[234:235], v28 offset:53280\n\t"
  "s_waitcnt lgkmcnt(10)\n\t"
  "v_mfma_f32_16x16x32_bf16 v[208:211], v[192:195], a[224:227], v[208:211]\n\t"
  "ds_read_b64_tr_b16 v[236:237], v28 offset:57376\n\t"
  "ds_read_b64_tr_b16 v[238:239], v28 offset:61472\n\t"
  "v_mfma_f32_16x16x32_bf16 v[208:211], v[196:199], a[228:231], v[208:211]\n\t"
  "ds_read_b64_tr_b16 v[80:81], v28 offset:32832\n\t"
  "ds_read_b64_tr_b16 v[82:83], v28 offset:36928\n\t"
  "s_waitcnt lgkmcnt(10)\n\t"
  "v_mfma_f32_16x16x32_bf16 v[208:211], v[200:203], a[232:235], v[208:211]\n\t"
  "ds_read_b64_tr_b16 v[84:85], v28 offset:41024\n\t"
  "ds_read_b64_tr_b16 v[86:87], v28 offset:45120\n\t"
  "v_mfma_f32_16x16x32_bf16 v[208:211], v[204:207], a[236:239], v[208:211]\n\t"
  "ds_read_b64_tr_b16 v[88:89], v28 offset:49216\n\t"
  "ds_read_b64_tr_b16 v[90:91], v28 offset:53312\n\t"
  "buffer_load_dword v1, v61, s[92:95], 0 offen sc1\n\t"
  "s_waitcnt lgkmcnt(10)\n\t"
  "v_mfma_f32_16x16x32_bf16 v[240:243], v[224:227], a[224:227], v[240:243]\n\t"
  "ds_read_b64_tr_b16 v[92:93], v28 offset:57408\n\t"
  "ds_read_b64_tr_b16 v[94:95], v28 offset:61504\n\t"
  "v_mfma_f32_16x16x32_bf16 v[240:243], v[228:231], a[228:231], v[240:243]\n\t"
  "ds_read_b64_tr_b16 v[192:193], v28 offset:32864\n\t"
  "ds_read_b64_tr_b16 v[194:195], v28 offset:36960\n\t"
  "s_waitcnt lgkmcnt(10)\n\t"
  "v_mfma_f32_16x16x32_bf16 v[240:243], v[232:235], a[232:235], v[240:243]\n\t"
  "ds_read_b64_tr_b16 v[196:197], v28 offset:41056\n\t"
  "ds_read_b64_tr_b16 v[198:199], v28 offset:45152\n\t"
  "v_mfma_f32_16x16x32_bf16 v[240:243], v[236:239], a[236:239], v[240:243]\n\t"
  "ds_read_b64_tr_b16 v[200:201], v28 offset:49248\n\t"
  "ds_read_b64_tr_b16 v[202:203], v28 offset:53344\n\t"
  "s_waitcnt lgkmcnt(10)\n\t"
  "v_mfma_f32_16x16x32_bf16 v[112:115], v[80:83], a[224:227], v[112:115]\n\t"
  "ds_read_b64_tr_b16 v[204:205], v28 offset:57440\n\t"
  "ds_read_b64_tr_b16 v[206:207], v28 offset:61536\n\t"
  "v_mfma_f32_16x16x32_bf16 v[112:115], v[84:87], a[228:231], v[112:115]\n\t"
  "ds_read_b64_tr_b16 v[224:225], v28 offset:32896\n\t"
  "ds_read_b64_tr_b16 v[226:227], v28 offset:36992\n\t"
  "s_waitcnt lgkmcnt(10)\n\t"
  "v_mfma_f32_16x16x32_bf16 v[112:115], v[88:91], a[232:235], v[112:115]\n\t"
  "ds_read_b64_tr_b16 v[228:229], v28 offset:41088\n\t"
  "ds_read_b64_tr_b16 v[230:231], v28 offset:45184\n\t"
  "v_mfma_f32_16x16x32_bf16 v[112:115], v[92:95], a[236:239], v[112:115]\n\t"
  "ds_read_b64_tr_b16 v[232:233], v28 offset:49280\n\t"
  "ds_read_b64_tr_b16 v[234:235], v28 offset:53376\n\t"
  "s_waitcnt lgkmcnt(10)\n\t"
  "v_mfma_f32_16x16x32_bf16 v[176:179], v[192:195], a[224:227], v[176:179]\n\t"
  "ds_read_b64_tr_b16 v[236:237], v28 offset:57472\n\t"
  "ds_read_b64_tr_b16 v[238:239], v28 offset:61568\n\t"
  "v_mfma_f32_16x16x32_bf16 v[176:179], v[196:199], a[228:231], v[176:179]\n\t"
  "ds_read_b64_tr_b16 v[80:81], v28 offset:32928\n\t"
  "ds_read_b64_tr_b16 v[82:83], v28 offset:37024\n\t"
  "s_waitcnt lgkmcnt(10)\n\t"
  "v_mfma_f32_16x16x32_bf16 v[176:179], v[200:203], a[232:235], v[176:179]\n\t"
  "ds_read_b64_tr_b16 v[84:85], v28 offset:41120\n\t"
  "ds_read_b64_tr_b16 v[86:87], v28 offset:45216\n\t"
  "v_mfma_f32_16x16x32_bf16 v[176:179], v[204:207], a[236:239], v[176:179]\n\t"
  "ds_read_b64_tr_b16 v[88:89], v28 offset:49312\n\t"
  "ds_read_b64_tr_b16 v[90:91], v28 offset:53408\n\t"
  "buffer_load_dword v2, v61, s[92:95], 0 offen sc1\n\t"
  "s_waitcnt lgkmcnt(10)\n\t"
  "v_mfma_f32_16x16x32_bf16 v[64:67], v[224:227], a[224:227], v[64:67]\n\t"
  "ds_read_b64_tr_b16 v[92:93], v28 offset:57504\n\t"
  "ds_read_b64_tr_b16 v[94:95], v28 offset:61600\n\t"
  "v_mfma_f32_16x16x32_bf16 v[64:67], v[228:231], a[228:231], v[64:67]\n\t"
  "ds_read_b64_tr_b16 v[192:193], v28 offset:32960\n\t"
  "ds_read_b64_tr_b16 v[194:195], v28 offset:37056\n\t"
  "s_waitcnt lgkmcnt(10)\n\t"
  "v_mfma_f32_16x16x32_bf16 v[64:67], v[232:235], a[232:235], v[64:67]\n\t"
  "ds_read_b64_tr_b16 v[196:197], v28 offset:41152\n\t"
  "ds_read_b64_tr_b16 v[198:199], v28 offset:45248\n\t"
  "v_mfma_f32_16x16x32_bf16 v[64:67], v[236:239], a[236:239], v[64:67]\n\t"
  "ds_read_b64_tr_b16 v[200:201], v28 offset:49344\n\t"
  "ds_read_b64_tr_b16 v[202:203], v28 offset:53440\n\t"
  "s_waitcnt lgkmcnt(10)\n\t"
  "v_mfma_f32_16x16x32_bf16 v[96:99], v[80:83], a[224:227], v[96:99]\n\t"
  "ds_read_b64_tr_b16 v[204:205], v28 offset:57536\n\t"
  "ds_read_b64_tr_b16 v[206:207], v28 offset:61632\n\t"
  "v_mfma_f32_16x16x32_bf16 v[96:99], v[84:87], a[228:231], v[96:99]\n\t"
  "s_waitcnt lgkmcnt(8)\n\t"
  "v_mfma_f32_16x16x32_bf16 v[96:99], v[88:91], a[232:235], v[96:99]\n\t"
  "v_mfma_f32_16x16x32_bf16 v[96:99], v[92:95], a[236:239], v[96:99]\n\t"
  "s_waitcnt lgkmcnt(4)\n\t"
  "v_mfma_f32_16x16x32_bf16 v[128:131], v[192:195], a[224:227], v[128:131]\n\t"
  "v_mfma_f32_16x16x32_bf16 v[128:131], v[196:199], a[228:231], v[128:131]\n\t"
  "s_waitcnt lgkmcnt(0)\n\t"
  "v_mfma_f32_16x16x32_bf16 v[128:131], v[200:203], a[232:235], v[128:131]\n\t"
  "v_mfma_f32_16x16x32_bf16 v[128:131], v[204:207], a[236:239], v[128:131]\n\t"
  "buffer_load_dword v3, v61, s[92:95], 0 offen sc1\n\t"
  "s_waitcnt vmcnt(3)\n\t"
  "v_cmp_ne_u32 vcc, s72, v0\n\t"
  "s_nop 4\n\t"
  "s_cbranch_vccz L_k1_xok\n\t"
  "s_waitcnt vmcnt(2)\n\t"
  "v_cmp_ne_u32 vcc, s72, v1\n\t"
  "s_nop 4\n\t"
  "s_cbranch_vccz L_k1_xok\n\t"
  "s_waitcnt vmcnt(1)\n\t"
  "v_cmp_ne_u32 vcc, s72, v2\n\t"
  "s_nop 4\n\t"
  "s_cbranch_vccz L_k1_xok\n\t"
  "s_waitcnt vmcnt(0)\n\t"
  "v_cmp_ne_u32 vcc, s72, v3\n\t"
  "s_nop 4\n\t"
  "s_cbranch_vccz L_k1_xok\n\t"
  "L_k1_xs:\n\t"
  "s_mov_b32 s88, 0x80000\n\t"
  "L_k1_xs_l:\n\t"
  "s_sleep 0\n\t"
  "buffer_load_dword v26, v61, s[92:95], 0 offen sc1\n\t"
  "s_waitcnt vmcnt(0)\n\t"
  "v_cmp_ne_u32 vcc, s72, v26\n\t"
  "s_nop 4\n\t"
  "s_cbranch_vccz L_k1_xok\n\t"
  "s_sub_u32 s88, s88, 1\n\t"
  "s_cmp_lg_u32 s88, 0\n\t"
  "s_cbranch_scc1 L_k1_xs_l\n\t"
  "s_branch L_k1_xok\n\t"
  "L_k1_xok:\n\t"
  "buffer_load_dwordx4 v[212:215], v15, s[40:43], s52 offen offset:0 sc1\n\t"
  "buffer_load_dwordx4 v[248:251], v15, s[40:43], s54 offen offset:0 sc1\n\t"
  "buffer_load_dwordx4 v[124:127], v15, s[40:43], s48 offen offset:0 sc1\n\t"
  "buffer_load_dwordx4 v[216:219], v15, s[40:43], s52 offen offset:1024 sc1\n\t"
  "buffer_load_dwordx4 v[252:255], v15, s[40:43], s54 offen offset:1024 sc1\n\t"
  "buffer_load_dwordx4 v[180:183], v15, s[40:43], s48 offen offset:1024 sc1\n\t"
  "buffer_load_dwordx4 v[220:223], v15, s[40:43], s53 offen offset:0 sc1\n\t"
  "buffer_load_dwordx4 v[116:119], v15, s[40:43], s55 offen offset:0 sc1\n\t"
  "buffer_load_dwordx4 v[184:187], v15, s[40:43], s49 offen offset:0 sc1\n\t"
  "buffer_load_dwordx2 v[244:245], v15, s[40:43], s53 offen offset:1024 sc1\n\t"
  "buffer_load_dwordx2 v[120:121], v15, s[40:43], s55 offen offset:1024 sc1\n\t"
  "buffer_load_dwordx2 v[188:189], v15, s[40:43], s49 offen offset:1024 sc1\n\t"
  "s_waitcnt vmcnt(11)\n\t"
  "v_lshlrev_b32 v32, 16, v212\n\t"
  "v_and_b32 v33, 0xffff0000, v212\n\t"
  "v_lshlrev_b32 v34, 16, v213\n\t"
  "v_and_b32 v35, 0xffff0000, v213\n\t"
  "v_pk_add_f32 v[208:209], v[208:209], v[32:33]\n\t"
  "v_pk_add_f32 v[210:211], v[210:211], v[34:35]\n\t"
  "v_lshlrev_b32 v36, 16, v214\n\t"
  "v_and_b32 v37, 0xffff0000, v214\n\t"
  "v_lshlrev_b32 v38, 16, v215\n\t"
  "v_and_b32 v39, 0xffff0000, v215\n\t"
  "v_pk_add_f32 v[240:241], v[240:241], v[36:37]\n\t"
  "v_pk_add_f32 v[242:243], v[242:243], v[38:39]\n\t"
  "s_waitcnt vmcnt(10)\n\t"
  "v_lshlrev_b32 v40, 16, v248\n\t"
  "v_and_b32 v41, 0xffff0000, v248\n\t"
  "v_lshlrev_b32 v42, 16, v249\n\t"
  "v_and_b32 v43, 0xffff0000, v249\n\t"
  "v_pk_add_f32 v[208:209], v[208:209], v[40:41]\n\t"
  "v_pk_add_f32 v[210:211], v[210:211], v[42:43]\n\t"
  "v_lshlrev_b32 v44, 16, v250\n\t"
  "v_and_b32 v45, 0xffff0000, v250\n\t"
  "v_lshlrev_b32 v46, 16, v251\n\t"
  "v_and_b32 v47, 0xffff0000, v251\n\t"
  "v_pk_add_f32 v[240:241], v[240:241], v[44:45]\n\t"
  "v_pk_add_f32 v[242:243], v[242:243], v[46:47]\n\t"
  "s_waitcnt vmcnt(9)\n\t"
  "v_lshlrev_b32 v48, 16, v124\n\t"
  "v_and_b32 v49, 0xffff0000, v124\n\t"
  "v_lshlrev_b32 v50, 16, v125\n\t"
  "v_and_b32 v51, 0xffff0000, v125\n\t"
  "v_pk_add_f32 v[208:209], v[208:209], v[48:49]\n\t"
  "v_pk_add_f32 v[210:211], v[210:211], v[50:51]\n\t"
  "v_lshlrev_b32 v52, 16, v126\n\t"
  "v_and_b32 v53, 0xffff0000, v126\n\t"
  "v_lshlrev_b32 v54, 16, v127\n\t"
  "v_and_b32 v55, 0xffff0000, v127\n\t"
  "v_pk_add_f32 v[240:241], v[240:241], v[52:53]\n\t"
  "v_pk_add_f32 v[242:243], v[242:243], v[54:55]\n\t"
  "v_cvt_pk_bf16_f32 v212, v208, v209\n\t"
  "v_cvt_pk_bf16_f32 v213, v210, v211\n\t"
  "v_cvt_pk_bf16_f32 v214, v240, v241\n\t"
  "v_cvt_pk_bf16_f32 v215, v242, v243\n\t"
  "s_nop 0\n\t"
  "v_permlane16_swap_b32 v212, v214\n\t"
  "v_permlane16_swap_b32 v213, v215\n\t"
  "ds_bpermute_b32 v248, v59, v212\n\t"
  "ds_bpermute_b32 v249, v59, v213\n\t"
  "ds_bpermute_b32 v250, v59, v214\n\t"
  "ds_bpermute_b32 v251, v59, v215\n\t"
  "s_waitcnt vmcnt(8)\n\t"
  "v_lshlrev_b32 v32, 16, v216\n\t"
  "v_and_b32 v33, 0xffff0000, v216\n\t"
  "v_lshlrev_b32 v34, 16, v217\n\t"
  "v_and_b32 v35, 0xffff0000, v217\n\t"
  "v_pk_add_f32 v[112:113], v[112:113], v[32:33]\n\t"
  "v_pk_add_f32 v[114:115], v[114:115], v[34:35]\n\t"
  "v_lshlrev_b32 v36, 16, v218\n\t"
  "v_and_b32 v37, 0xffff0000, v218\n\t"
  "v_lshlrev_b32 v38, 16, v219\n\t"
  "v_and_b32 v39, 0xffff0000, v219\n\t"
  "v_pk_add_f32 v[176:177], v[176:177], v[36:37]\n\t"
  "v_pk_add_f32 v[178:179], v[178:179], v[38:39]\n\t"
  "s_waitcnt vmcnt(7)\n\t"
  "v_lshlrev_b32 v40, 16, v252\n\t"
  "v_and_b32 v41, 0xffff0000, v252\n\t"
  "v_lshlrev_b32 v42, 16, v253\n\t"
  "v_and_b32 v43, 0xffff0000, v253\n\t"
  "v_pk_add_f32 v[112:113], v[112:113], v[40:41]\n\t"
  "v_pk_add_f32 v[114:115], v[114:115], v[42:43]\n\t"
  "v_lshlrev_b32 v44, 16, v254\n\t"
  "v_and_b32 v45, 0xffff0000, v254\n\t"
  "v_lshlrev_b32 v46, 16, v255\n\t"
  "v_and_b32 v47, 0xffff0000, v255\n\t"
  "v_pk_add_f32 v[176:177], v[176:177], v[44:45]\n\t"
  "v_pk_add_f32 v[178:179], v[178:179], v[46:47]\n\t"
  "s_waitcnt vmcnt(6)\n\t"
  "v_lshlrev_b32 v48, 16, v180\n\t"
  "v_and_b32 v49, 0xffff0000, v180\n\t"
  "v_lshlrev_b32 v50, 16, v181\n\t"
  "v_and_b32 v51, 0xffff0000, v181\n\t"
  "v_pk_add_f32 v[112:113], v[112:113], v[48:49]\n\t"
  "v_pk_add_f32 v[114:115], v[114:115], v[50:51]\n\t"
  "v_lshlrev_b32 v52, 16, v182\n\t"
  "v_and_b32 v53, 0xffff0000, v182\n\t"
  "v_lshlrev_b32 v54, 16, v183\n\t"
  "v_and_b32 v55, 0xffff0000, v183\n\t"
  "v_pk_add_f32 v[176:177], v[176:177], v[52:53]\n\t"
  "v_pk_add_f32 v[178:179], v[178:179], v[54:55]\n\t"
  "v_cvt_pk_bf16_f32 v216, v112, v113\n\t"
  "v_cvt_pk_bf16_f32 v217, v114, v115\n\t"
  "v_cvt_pk_bf16_f32 v218, v176, v177\n\t"
  "v_cvt_pk_bf16_f32 v219, v178, v179\n\t"
  "s_nop 0\n\t"
  "v_permlane16_swap_b32 v216, v218\n\t"
  "v_permlane16_swap_b32 v217, v219\n\t"
  "ds_bpermute_b32 v252, v59, v216\n\t"
  "ds_bpermute_b32 v253, v59, v217\n\t"
  "ds_bpermute_b32 v254, v59, v218\n\t"
  "ds_bpermute_b32 v255, v59, v219\n\t"
  "s_waitcnt lgkmcnt(4)\n\t"
  "buffer_store_dwordx4 v[248:251], v4, s[44:47], 0 offen offset:0\n\t"
  "s_waitcnt vmcnt(6)\n\t"
  "v_lshlrev_b32 v32, 16, v220\n\t"
  "v_and_b32 v33, 0xffff0000, v220\n\t"
  "v_lshlrev_b32 v34, 16, v221\n\t"
  "v_and_b32 v35, 0xffff0000, v221\n\t"
  "v_pk_add_f32 v[64:65], v[64:65], v[32:33]\n\t"
  "v_pk_add_f32 v[66:67], v[66:67], v[34:35]\n\t"
  "v_lshlrev_b32 v36, 16, v222\n\t"
  "v_and_b32 v37, 0xffff0000, v222\n\t"
  "v_lshlrev_b32 v38, 16, v223\n\t"
  "v_and_b32 v39, 0xffff0000, v223\n\t"
  "v_pk_add_f32 v[96:97], v[96:97], v[36:37]\n\t"
  "v_pk_add_f32 v[98:99], v[98:99], v[38:39]\n\t"
  "s_waitcnt vmcnt(5)\n\t"
  "v_lshlrev_b32 v40, 16, v116\n\t"
  "v_and_b32 v41, 0xffff0000, v116\n\t"
  "v_lshlrev_b32 v42, 16, v117\n\t"
  "v_and_b32 v43, 0xffff0000, v117\n\t"
  "v_pk_add_f32 v[64:65], v[64:65], v[40:41]\n\t"
  "v_pk_add_f32 v[66:67], v[66:67], v[42:43]\n\t"
  "v_lshlrev_b32 v44, 16, v118\n\t"
  "v_and_b32 v45, 0xffff0000, v118\n\t"
  "v_lshlrev_b32 v46, 16, v119\n\t"
  "v_and_b32 v47, 0xffff0000, v119\n\t"
  "v_pk_add_f32 v[96:97], v[96:97], v[44:45]\n\t"
  "v_pk_add_f32 v[98:99], v[98:99], v[46:47]\n\t"
  "s_waitcnt vmcnt(4)\n\t"
  "v_lshlrev_b32 v48, 16, v184\n\t"
  "v_and_b32 v49, 0xffff0000, v184\n\t"
  "v_lshlrev_b32 v50, 16, v185\n\t"
  "v_and_b32 v51, 0xffff0000, v185\n\t"
  "v_pk_add_f32 v[64:65], v[64:65], v[48:49]\n\t"
  "v_pk_add_f32 v[66:67], v[66:67], v[50:51]\n\t"
  "v_lshlrev_b32 v52, 16, v186\n\t"
  "v_and_b32 v53, 0xffff0000, v186\n\t"
  "v_lshlrev_b32 v54, 16, v187\n\t"
  "v_and_b32 v55, 0xffff0000, v187\n\t"
  "v_pk_add_f32 v[96:97], v[96:97], v[52:53]\n\t"
  "v_pk_add_f32 v[98:99], v[98:99], v[54:55]\n\t"
  "v_cvt_pk_bf16_f32 v220, v64, v65\n\t"
  "v_cvt_pk_bf16_f32 v221, v66, v67\n\t"
  "v_cvt_pk_bf16_f32 v222, v96, v97\n\t"
  "v_cvt_pk_bf16_f32 v223, v98, v99\n\t"
  "s_nop 0\n\t"
  "v_permlane16_swap_b32 v220, v222\n\t"
  "v_permlane16_swap_b32 v221, v223\n\t"
  "ds_bpermute_b32 v116, v59, v220\n\t"
  "ds_bpermute_b32 v117, v59, v221\n\t"
  "ds_bpermute_b32 v118, v59, v222\n\t"
  "ds_bpermute_b32 v119, v59, v223\n\t"
  "s_waitcnt lgkmcnt(4)\n\t"
  "buffer_store_dwordx4 v[252:255], v4, s[44:47], 0 offen offset:64\n\t"
  "s_waitcnt vmcnt(4)\n\t"
  "v_lshlrev_b32 v32, 16, v244\n\t"
  "v_and_b32 v33, 0xffff0000, v244\n\t"
  "v_lshlrev_b32 v34, 16, v245\n\t"
  "v_and_b32 v35, 0xffff0000, v245\n\t"
  "v_pk_add_f32 v[128:129], v[128:129], v[32:33]\n\t"
  "v_pk_add_f32 v[130:131], v[130:131], v[34:35]\n\t"
  "s_waitcnt vmcnt(3)\n\t"
  "v_lshlrev_b32 v40, 16, v120\n\t"
  "v_and_b32 v41, 0xffff0000, v120\n\t"
  "v_lshlrev_b32 v42, 16, v121\n\t"
  "v_and_b32 v43, 0xffff0000, v121\n\t"
  "v_pk_add_f32 v[128:129], v[128:129], v[40:41]\n\t"
  "v_pk_add_f32 v[130:131], v[130:131], v[42:43]\n\t"
  "s_waitcnt vmcnt(2)\n\t"
  "v_lshlrev_b32 v48, 16, v188\n\t"
  "v_and_b32 v49, 0xffff0000, v188\n\t"
  "v_lshlrev_b32 v50, 16, v189\n\t"
  "v_and_b32 v51, 0xffff0000, v189\n\t"
  "v_pk_add_f32 v[128:129], v[128:129], v[48:49]\n\t"
  "v_pk_add_f32 v[130:131], v[130:131], v[50:51]\n\t"
  "v_cvt_pk_bf16_f32 v244, v128, v129\n\t"
  "v_cvt_pk_bf16_f32 v245, v130, v131\n\t"
  "ds_bpermute_b32 v120, v62, v244\n\t"
  "ds_bpermute_b32 v121, v62, v245\n\t"
  "s_waitcnt lgkmcnt(2)\n\t"
  "buffer_store_dwordx4 v[116:119], v4, s[44:47], 0 offen offset:128\n\t"
  "s_waitcnt lgkmcnt(0)\n\t"
  "buffer_store_dwordx2 v[120:121], v5, s[44:47], 0 offen offset:192\n\t"
  "s_lshl_b32 s87, s90, 20\n\t"
  "s_lshl_b32 s70, s96, 14\n\t"
  "s_add_u32 s87, s87, s70\n\t"
  "s_lshl_b32 s70, s97, 7\n\t"
  "s_add_u32 s87, s87, s70\n\t"
  "s_lshr_b32 s70, s72, 2\n\t"
  "v_lshlrev_b32 v0, 19, v20\n\t"
  "v_add_u32 v0, s87, v0\n\t"
  "v_mov_b32 v1, s70\n\t"
  "buffer_store_dword v1, v0, s[92:95], 0 offen sc1\n\t"
  "s_branch L_k1_done\n\t"
  "L_k1_sp1:\n\t"
  "s_mov_b32 s88, 0x80000\n\t"
  "L_k1_sp1_l:\n\t"
  "s_sleep 2\n\t"
  "buffer_load_dword v26, v30, s[92:95], 0 offen offset:2048 sc1\n\t"
  "s_waitcnt vmcnt(0)\n\t"
  "v_subrev_u32 v26, s72, v26\n\t"
  "v_cmp_lt_u32 vcc, 3, v26\n\t"
  "s_nop 4\n\t"
  "s_cbranch_vccz L_k1_rs1\n\t"
  "s_sub_u32 s88, s88, 1\n\t"
  "s_cmp_lg_u32 s88, 0\n\t"
  "s_cbranch_scc1 L_k1_sp1_l\n\t"
  "s_branch L_k1_rs1\n\t"
  "L_k1_sp2:\n\t"
  "s_mov_b32 s88, 0x80000\n\t"
  "L_k1_sp2_l:\n\t"
  "s_sleep 2\n\t"
  "buffer_load_dword v26, v30, s[92:95], 0 offen offset:2048 sc1\n\t"
  "s_waitcnt vmcnt(0)\n\t"
  "v_subrev_u32 v26, s73, v26\n\t"
  "v_cmp_lt_u32 vcc, 2, v26\n\t"
  "s_nop 4\n\t"
  "s_cbranch_vccz L_k1_rs2\n\t"
  "s_sub_u32 s88, s88, 1\n\t"
  "s_cmp_lg_u32 s88, 0\n\t"
  "s_cbranch_scc1 L_k1_sp2_l\n\t"
  "s_branch L_k1_rs2\n\t"
  "L_k1_sp3:\n\t"
  "s_mov_b32 s88, 0x80000\n\t"
  "L_k1_sp3_l:\n\t"
  "s_sleep 2\n\t"
  "buffer_load_dword v26, v30, s[92:95], 0 offen offset:2048 sc1\n\t"
  "s_waitcnt vmcnt(0)\n\t"
  "v_subrev_u32 v26, s74, v26\n\t"
  "v_cmp_lt_u32 vcc, 1, v26\n\t"
  "s_nop 4\n\t"
  "s_cbranch_vccz L_k1_rs3\n\t"
  "s_sub_u32 s88, s88, 1\n\t"
  "s_cmp_lg_u32 s88, 0\n\t"
  "s_cbranch_scc1 L_k1_sp3_l\n\t"
  "s_branch L_k1_rs3\n\t"
  "L_k1_sp4:\n\t"
  "s_mov_b32 s88, 0x80000\n\t"
  "L_k1_sp4_l:\n\t"
  "s_sleep 2\n\t"
  "buffer_load_dword v26, v30, s[92:95], 0 offen offset:2048 sc1\n\t"
  "s_waitcnt vmcnt(0)\n\t"
  "v_subrev_u32 v26, s91, v26\n\t"
  "v_cmp_lt_u32 vcc, 0, v26\n\t"
  "s_nop 4\n\t"
  "s_cbranch_vccz L_k1_rs4\n\t"
  "s_sub_u32 s88, s88, 1\n\t"
  "s_cmp_lg_u32 s88, 0\n\t"
  "s_cbranch_scc1 L_k1_sp4_l\n\t"
  "s_branch L_k1_rs4\n\t"
  "L_k1_exit:\n\t"
  "s_waitcnt vmcnt(0) lgkmcnt(0)\n\t"
  "s_barrier\n\t"
  "L_k1_done:\n\t"
  "s_mov_b32 %0, s98\n\t"
  "s_mov_b32 %1, s72\n\t"
  "L_k1_end_all:\n\t"
    : "=s"(state), "=s"(tag)
    : "s"((const char*)x + (size_t)b * 215040), "s"((const char*)wq + h * 512), "s"((const char*)kc + (size_t)(b * 16 + h) * 262144), "s"((const char*)vc + (size_t)(b * 16 + h) * 262144),
      "s"(ws), "s"(wo), "s"((char*)out + (size_t)b * 215040), "s"(w), "s"((unsigned)(size_t)lds), "s"(blk)
    : "v0","v1","v2","v3","v4","v5","v6","v7","v8","v9","v10","v11","v12","v13","v14","v15","v16","v17","v18","v19","v20","v21","v22","v23","v24","v25","v26","v27","v28","v29","v30","v31","v32","v33","v34","v35","v36","v37","v38","v39","v40","v41","v42","v43","v44","v45","v46","v47","v48","v49","v50","v51","v52","v53","v54","v55","v56","v57","v58","v59","v60","v61","v62","v63","v64","v65","v66","v67","v68","v69","v70","v71","v72","v73","v74","v75","v76","v77","v78","v79","v80","v81","v82","v83","v84","v85","v86","v87","v88","v89","v90","v91","v92","v93","v94","v95","v96","v97","v98","v99","v100","v101","v102","v103","v104","v105","v106","v107","v108","v109","v110","v111","v112","v113","v114","v115","v116","v117","v118","v119","v120","v121","v122","v123","v124","v125","v126","v127","v128","v129","v130","v131","v132","v133","v134","v135","v136","v137","v138","v139","v140","v141","v142","v143","v144","v145","v146","v147","v148","v149","v150","v151","v152","v153","v154","v155","v156","v157","v158","v159","v160","v161","v162","v163","v164","v165","v166","v167","v168","v169","v170","v171","v172","v173","v174","v175","v176","v177","v178","v179","v180","v181","v182","v183","v184","v185","v186","v187","v188","v189","v190","v191","v192","v193","v194","v195","v196","v197","v198","v199","v200","v201","v202","v203","v204","v205","v206","v207","v208","v209","v210","v211","v212","v213","v214","v215","v216","v217","v218","v219","v220","v221","v222","v223","v224","v225","v226","v227","v228","v229","v230","v231","v232","v233","v234","v235","v236","v237","v238","v239","v240","v241","v242","v243","v244","v245","v246","v247","v248","v249","v250","v251","v252","v253","v254","v255","a0","a1","a2","a3","a4","a5","a6","a7","a8","a9","a10","a11","a12","a13","a14","a15","a16","a17","a18","a19","a20","a21","a22","a23","a24","a25","a26","a27","a28","a29","a30","a31","a32","a33","a34","a35","a36","a37","a38","a39","a40","a41","a42","a43","a44","a45","a46","a47","a48","a49","a50","a51","a52","a53","a54","a55","a56","a57","a58","a59","a60","a61","a62","a63","a64","a65","a66","a67","a68","a69","a70","a71","a72","a73","a74","a75","a76","a77","a78","a79","a80","a81","a82","a83","a84","a85","a86","a87","a88","a89","a90","a91","a92","a93","a94","a95","a96","a97","a98","a99","a100","a101","a102","a103","a104","a105","a106","a107","a108","a109","a110","a111","a112","a113","a114","a115","a116","a117","a118","a119","a120","a121","a122","a123","a124","a125","a126","a127","a128","a129","a130","a131","a132","a133","a134","a135","a136","a137","a138","a139","a140","a141","a142","a143","a144","a145","a146","a147","a148","a149","a150","a151","a152","a153","a154","a155","a156","a157","a158","a159","a160","a161","a162","a163","a164","a165","a166","a167","a168","a169","a170","a171","a172","a173","a174","a175","a176","a177","a178","a179","a180","a181","a182","a183","a184","a185","a186","a187","a188","a189","a190","a191","a192","a193","a194","a195","a196","a197","a198","a199","a200","a201","a202","a203","a204","a205","a206","a207","a208","a209","a210","a211","a212","a213","a214","a215","a216","a217","a218","a219","a220","a221","a222","a223","a224","a225","a226","a227","a228","a229","a230","a231","a232","a233","a234","a235","a236","a237","a238","a239","a240","a241","a242","a243","a244","a245","a246","a247","a248","a249","a250","a251","a252","a253","a254","a255","s40","s41","s42","s43","s44","s45","s46","s47","s48","s49","s50","s51","s52","s53","s54","s55","s56","s57","s58","s59","s60","s61","s62","s63","s64","s65","s66","s67","s68","s69","s70","s71","s72","s73","s74","s75","s76","s77","s78","s79","s80","s81","s82","s83","s84","s85","s86","s87","s88","s89","s90","s91","s92","s93","s94","s95","s96","s97","s98","s99","vcc","scc","memory");
  }
  if (state) {
    kptr ka = (kptr)__builtin_amdgcn_kernarg_segment_ptr();
    asm volatile("" : "+s"(ka));
    unsigned blk0 = blockIdx.x; asm volatile("" : "+s"(blk0));
    const unsigned blk = item_of(blk0), b = blk >> 4, h = blk & 15;
    char* ws2 = (char*)ka[5];
    {
      slow_rows((const u16*)ka[0], (const u16*)ka[1], (const u16*)ka[2], (const u16*)ka[3], ws2 + WS_O, (float*)(lds + 0 + 4096 * w), b, h, w);
      asm volatile("s_waitcnt vmcnt(0)" ::: "memory");
    }
    asm volatile(
  "s_mov_b32 s90, %2\n\t"
  "s_mov_b32 s89, %3\n\t"
  "s_mov_b32 s99, %4\n\t"
  "s_mov_b32 s72, %5\n\t"
  "s_add_u32 s74, s72, 2\n\t"
  "s_add_u32 s91, s72, 3\n\t"
  "s_and_b32 s96, s99, 15\n\t"
  "s_lshr_b32 s97, s99, 4\n\t"
  "s_lshl_b32 s69, s90, 12\n\t"
  "s_add_u32 s69, s69, s89\n\t"
  "v_mbcnt_lo_u32_b32 v0, -1, 0\n\t"
  "v_mbcnt_hi_u32_b32 v0, -1, v0\n\t"
  "v_lshlrev_b32 v20, 4, v0\n\t"
  "s_add_u32 s70, s96, 0\n\t"
  "s_and_b32 s70, s70, 3\n\t"
  "s_lshl_b32 s79, s70, 11\n\t"
  "s_add_u32 s70, s96, 1\n\t"
  "s_and_b32 s70, s70, 3\n\t"
  "s_lshl_b32 s80, s70, 11\n\t"
  "s_mov_b64 s[92:93], %0\n\t"
  "s_add_u32 s92, s92, 0x1e00000\n\ts_addc_u32 s93, s93, 0\n\t"
  "s_mov_b32 s94, 16384\n\t"
  "s_mov_b32 s95, 0x20000\n\t"
  "s_and_b32 s87, s96, 3\n\t"
  "s_mul_i32 s87, s87, 224\n\t"
  "s_lshr_b32 s86, s96, 2\n\t"
  "s_mul_i32 s86, s86, 917504\n\t"
  "s_add_u32 s87, s87, s86\n\t"
  "s_sub_u32 s86, 3670016, s87\n\t"
  "s_mov_b64 s[60:61], %6\n\t"
  "s_add_u32 s60, s60, s87\n\ts_addc_u32 s61, s61, 0\n\t"
  "s_mov_b32 s62, s86\n\t"
  "s_mov_b32 s63, 0x20000\n\t"
  "s_and_b32 s86, s96, 3\n\t"
  "s_mov_b32 s84, 0xcb61fa50\n\t"
  "s_cmp_eq_u32 s86, 1\n\ts_cselect_b32 s84, 0x1cb60fa5, s84\n\t"
  "s_cmp_eq_u32 s86, 2\n\ts_cselect_b32 s84, 0x61cb50fa, s84\n\t"
  "s_cmp_eq_u32 s86, 3\n\ts_cselect_b32 s84, 0xb61ca50f, s84\n\t"
  "s_mov_b32 s85, 0xe943d872\n\t"
  "s_cmp_eq_u32 s86, 1\n\ts_cselect_b32 s85, 0x3e942d87, s85\n\t"
  "s_cmp_eq_u32 s86, 2\n\ts_cselect_b32 s85, 0x43e972d8, s85\n\t"
  "s_cmp_eq_u32 s86, 3\n\ts_cselect_b32 s85, 0x943e872d, s85\n\t"
  "s_mul_i32 s87, s97, 1966080\n\t"
  "s_mov_b64 s[40:41], %0\n\t"
  "s_add_u32 s40, s40, s87\n\ts_addc_u32 s41, s41, 0\n\t"
  "s_mov_b32 s42, 1966080\n\t"
  "s_mov_b32 s43, 0x20000\n\t"
  "s_mov_b64 s[44:45], %1\n\t"
  "s_mov_b32 s46, 215040\n\t"
  "s_mov_b32 s47, 0x20000\n\t"
  "v_lshrrev_b32 v59, 4, v20\n\t"
  "v_subrev_u32 v59, 0, v59\n\t"
  "v_mul_u32_u24 v62, 4682, v59\n\t"
  "v_lshrrev_b32 v62, 16, v62\n\t"
  "v_mul_u32_u24 v63, 672, v62\n\t"
  "v_lshl_add_u32 v63, v59, 4, v63\n\t"
  "v_cmp_lt_u32 vcc, 55, v59\n\t"
  "v_mov_b32 v62, 0x40000000\n\t"
  "s_nop 0\n\t"
  "v_cndmask_b32 v63, v63, v62, vcc\n\t"
  "s_mul_i32 s87, s90, 14336\n\t"
  "v_add_u32 v63, s87, v63\n\t"
  "v_add_u32 v6, 0, v63\n\t"
  "v_add_u32 v8, 5120, v63\n\t"
  "v_lshrrev_b32 v59, 4, v20\n\t"
  "v_subrev_u32 v59, 8, v59\n\t"
  "v_mul_u32_u24 v62, 4682, v59\n\t"
  "v_lshrrev_b32 v62, 16, v62\n\t"
  "v_mul_u32_u24 v63, 672, v62\n\t"
  "v_lshl_add_u32 v63, v59, 4, v63\n\t"
  "v_cmp_lt_u32 vcc, 55, v59\n\t"
  "v_mov_b32 v62, 0x40000000\n\t"
  "s_nop 0\n\t"
  "v_cndmask_b32 v63, v63, v62, vcc\n\t"
  "s_mul_i32 s87, s90, 14336\n\t"
  "v_add_u32 v63, s87, v63\n\t"
  "v_add_u32 v7, 2560, v63\n\t"
  "v_add_u32 v9, 7680, v63\n\t"
  "v_lshrrev_b32 v59, 8, v20\n\t"
  "v_lshlrev_b32 v62, 10, v59\n\t"
  "v_and_b32 v59, 1, v59\n\t"
  "v_lshl_add_u32 v62, v59, 7, v62\n\t"
  "v_bfe_u32 v59, v20, 6, 2\n\t"
  "v_mul_u32_u24 v59, 224, v59\n\t"
  "v_add_u32 v62, v59, v62\n\t"
  "v_bfe_u32 v59, v20, 4, 2\n\t"
  "v_lshl_add_u32 v62, v59, 3, v62\n\t"
  "v_add_u32 v28, s89, v62\n\t"
  "v_add_u32 v29, 0x10000, v28\n\t"
  "v_add_u32 v58, 0x20000, v28\n\t"
  "s_lshr_b32 s86, s96, 2\n\t"
  "s_add_u32 s87, s86, 0\n\t"
  "s_and_b32 s87, s87, 3\n\t"
  "s_lshl_b32 s87, s87, 17\n\t"
  "s_lshl_b32 s86, s86, 15\n\t"
  "s_add_u32 s87, s87, s86\n\t"
  "s_lshl_b32 s86, s90, 19\n\t"
  "s_add_u32 s87, s87, s86\n\t"
  "v_add_u32 v10, s87, v20\n\t"
  "s_lshr_b32 s86, s96, 2\n\t"
  "s_add_u32 s87, s86, 1\n\t"
  "s_and_b32 s87, s87, 3\n\t"
  "s_lshl_b32 s87, s87, 17\n\t"
  "s_lshl_b32 s86, s86, 15\n\t"
  "s_add_u32 s87, s87, s86\n\t"
  "s_lshl_b32 s86, s90, 19\n\t"
  "s_add_u32 s87, s87, s86\n\t"
  "v_add_u32 v11, s87, v20\n\t"
  "s_lshr_b32 s86, s96, 2\n\t"
  "s_add_u32 s87, s86, 2\n\t"
  "s_and_b32 s87, s87, 3\n\t"
  "s_lshl_b32 s87, s87, 17\n\t"
  "s_lshl_b32 s86, s86, 15\n\t"
  "s_add_u32 s87, s87, s86\n\t"
  "s_lshl_b32 s86, s90, 19\n\t"
  "s_add_u32 s87, s87, s86\n\t"
  "v_add_u32 v56, s87, v20\n\t"
  "s_lshr_b32 s86, s96, 2\n\t"
  "s_add_u32 s87, s86, 3\n\t"
  "s_and_b32 s87, s87, 3\n\t"
  "s_lshl_b32 s87, s87, 17\n\t"
  "s_lshl_b32 s86, s86, 15\n\t"
  "s_add_u32 s87, s87, s86\n\t"
  "s_lshl_b32 s86, s90, 19\n\t"
  "s_add_u32 s87, s87, s86\n\t"
  "v_add_u32 v57, s87, v20\n\t"
  "s_lshl_b32 s87, s97, 2\n\t"
  "s_add_u32 s87, s87, s90\n\t"
  "s_lshl_b32 s87, s87, 4\n\t"
  "s_add_u32 s86, s87, s96\n\t"
  "s_lshl_b32 s86, s86, 2\n\t"
  "v_lshlrev_b32 v21, 12, v20\n\t"
  "v_add_u32 v21, s86, v21\n\t"
  "s_and_b32 s86, s86, 0xfffffff0\n\t"
  "v_bfe_u32 v30, v20, 4, 2\n\t"
  "v_lshlrev_b32 v30, 2, v30\n\t"
  "v_add_u32 v30, s86, v30\n\t"
  "v_mov_b32 v27, s74\n\t"
  "buffer_store_dword v27, v21, s[92:95], 0 offen offset:2048 sc1\n\t"
  "L_k1c_o:\n\t"
  "s_mov_b32 s88, 0x80000\n\t"
  "L_k1c_o_l:\n\t"
  "s_sleep 2\n\t"
  "buffer_load_dword v26, v30, s[92:95], 0 offen offset:2048 sc1\n\t"
  "s_waitcnt vmcnt(0)\n\t"
  "v_subrev_u32 v26, s74, v26\n\t"
  "v_cmp_lt_u32 vcc, 1, v26\n\t"
  "s_nop 4\n\t"
  "s_cbranch_vccz L_k1c_o_ok\n\t"
  "s_sub_u32 s88, s88, 1\n\t"
  "s_cmp_lg_u32 s88, 0\n\t"
  "s_cbranch_scc1 L_k1c_o_l\n\t"
  "s_branch L_k1c_o_ok\n\t"
  "L_k1c_o_ok:\n\t"
  "s_waitcnt vmcnt(0) lgkmcnt(0)\n\t"
  "s_waitcnt vmcnt(0) lgkmcnt(0)\n\t"
  "s_barrier\n\t"
  "s_add_u32 m0, s69, 16384\n\t"
  "s_bfe_u32 s70, s84, 0x40000\n\t"
  "s_mul_i32 s70, s70, 57344\n\t"
  "buffer_load_dwordx4 v6, s[60:63], s70 offen offset:0  lds\n\t"
  "buffer_load_dwordx4 v7, s[60:63], s70 offen offset:1024  lds\n\t"
  "buffer_load_dwordx4 v8, s[60:63], s70 offen offset:2048  lds\n\t"
  "buffer_load_dwordx4 v9, s[60:63], s70 offen offset:3072  lds\n\t"
  "s_bfe_u32 s71, s84, 0x40000\n\t"
  "s_lshl_b32 s71, s71, 11\n\t"
  "buffer_load_dwordx4 a[0:3], v10, s[40:43], s71 offen offset:0 sc1\n\t"
  "buffer_load_dwordx4 a[4:7], v11, s[40:43], s71 offen offset:0 sc1\n\t"
  "buffer_load_dwordx4 a[8:11], v56, s[40:43], s71 offen offset:0 sc1\n\t"
  "buffer_load_dwordx4 a[12:15], v57, s[40:43], s71 offen offset:0 sc1\n\t"
  "buffer_load_dwordx4 a[16:19], v10, s[40:43], s71 offen offset:1024 sc1\n\t"
  "buffer_load_dwordx4 a[20:23], v11, s[40:43], s71 offen offset:1024 sc1\n\t"
  "buffer_load_dwordx4 a[24:27], v56, s[40:43], s71 offen offset:1024 sc1\n\t"
  "buffer_load_dwordx4 a[28:31], v57, s[40:43], s71 offen offset:1024 sc1\n\t"
  "s_waitcnt vmcnt(0) lgkmcnt(0)\n\t"
  "s_barrier\n\t"
  "ds_read_b64_tr_b16 v[32:33], v28 offset:16384\n\t"
  "ds_read_b64_tr_b16 v[34:35], v28 offset:20480\n\t"
  "s_waitcnt lgkmcnt(0)\n\t"
  "v_mfma_f32_16x16x32_bf16 v[208:211], v[32:35], a[0:3], 0\n\t"
  "v_mfma_f32_16x16x32_bf16 v[212:215], v[32:35], a[4:7], 0\n\t"
  "v_mfma_f32_16x16x32_bf16 v[216:219], v[32:35], a[8:11], 0\n\t"
  "v_mfma_f32_16x16x32_bf16 v[220:223], v[32:35], a[12:15], 0\n\t"
  "ds_read_b64_tr_b16 v[32:33], v28 offset:16416\n\t"
  "ds_read_b64_tr_b16 v[34:35], v28 offset:20512\n\t"
  "s_waitcnt lgkmcnt(0)\n\t"
  "v_mfma_f32_16x16x32_bf16 v[240:243], v[32:35], a[0:3], 0\n\t"
  "v_mfma_f32_16x16x32_bf16 v[244:247], v[32:35], a[4:7], 0\n\t"
  "v_mfma_f32_16x16x32_bf16 v[248:251], v[32:35], a[8:11], 0\n\t"
  "v_mfma_f32_16x16x32_bf16 v[252:255], v[32:35], a[12:15], 0\n\t"
  "ds_read_b64_tr_b16 v[32:33], v28 offset:16448\n\t"
  "ds_read_b64_tr_b16 v[34:35], v28 offset:20544\n\t"
  "s_waitcnt lgkmcnt(0)\n\t"
  "v_mfma_f32_16x16x32_bf16 v[112:115], v[32:35], a[0:3], 0\n\t"
  "v_mfma_f32_16x16x32_bf16 v[116:119], v[32:35], a[4:7], 0\n\t"
  "v_mfma_f32_16x16x32_bf16 v[120:123], v[32:35], a[8:11], 0\n\t"
  "v_mfma_f32_16x16x32_bf16 v[124:127], v[32:35], a[12:15], 0\n\t"
  "ds_read_b64_tr_b16 v[32:33], v28 offset:16480\n\t"
  "ds_read_b64_tr_b16 v[34:35], v28 offset:20576\n\t"
  "s_waitcnt lgkmcnt(0)\n\t"
  "v_mfma_f32_16x16x32_bf16 v[176:179], v[32:35], a[0:3], 0\n\t"
  "v_mfma_f32_16x16x32_bf16 v[180:183], v[32:35], a[4:7], 0\n\t"
  "v_mfma_f32_16x16x32_bf16 v[184:187], v[32:35], a[8:11], 0\n\t"
  "v_mfma_f32_16x16x32_bf16 v[188:191], v[32:35], a[12:15], 0\n\t"
  "ds_read_b64_tr_b16 v[32:33], v28 offset:16512\n\t"
  "ds_read_b64_tr_b16 v[34:35], v28 offset:20608\n\t"
  "s_waitcnt lgkmcnt(0)\n\t"
  "v_mfma_f32_16x16x32_bf16 v[64:67], v[32:35], a[0:3], 0\n\t"
  "v_mfma_f32_16x16x32_bf16 v[68:71], v[32:35], a[4:7], 0\n\t"
  "v_mfma_f32_16x16x32_bf16 v[72:75], v[32:35], a[8:11], 0\n\t"
  "v_mfma_f32_16x16x32_bf16 v[76:79], v[32:35], a[12:15], 0\n\t"
  "ds_read_b64_tr_b16 v[32:33], v28 offset:16544\n\t"
  "ds_read_b64_tr_b16 v[34:35], v28 offset:20640\n\t"
  "s_waitcnt lgkmcnt(0)\n\t"
  "v_mfma_f32_16x16x32_bf16 v[96:99], v[32:35], a[0:3], 0\n\t"
  "v_mfma_f32_16x16x32_bf16 v[100:103], v[32:35], a[4:7], 0\n\t"
  "v_mfma_f32_16x16x32_bf16 v[104:107], v[32:35], a[8:11], 0\n\t"
  "v_mfma_f32_16x16x32_bf16 v[108:111], v[32:35], a[12:15], 0\n\t"
  "ds_read_b64_tr_b16 v[32:33], v28 offset:16576\n\t"
  "ds_read_b64_tr_b16 v[34:35], v28 offset:20672\n\t"
  "s_waitcnt lgkmcnt(0)\n\t"
  "v_mfma_f32_16x16x32_bf16 v[128:131], v[32:35], a[0:3], 0\n\t"
  "v_mfma_f32_16x16x32_bf16 v[132:135], v[32:35], a[4:7], 0\n\t"
  "v_mfma_f32_16x16x32_bf16 v[136:139], v[32:35], a[8:11], 0\n\t"
  "v_mfma_f32_16x16x32_bf16 v[140:143], v[32:35], a[12:15], 0\n\t"
  "ds_read_b64_tr_b16 v[32:33], v28 offset:24576\n\t"
  "ds_read_b64_tr_b16 v[34:35], v28 offset:28672\n\t"
  "s_waitcnt lgkmcnt(0)\n\t"
  "v_mfma_f32_16x16x32_bf16 v[208:211], v[32:35], a[16:19], v[208:211]\n\t"
  "v_mfma_f32_16x16x32_bf16 v[212:215], v[32:35], a[20:23], v[212:215]\n\t"
  "v_mfma_f32_16x16x32_bf16 v[216:219], v[32:35], a[24:27], v[216:219]\n\t"
  "v_mfma_f32_16x16x32_bf16 v[220:223], v[32:35], a[28:31], v[220:223]\n\t"
  "ds_read_b64_tr_b16 v[32:33], v28 offset:24608\n\t"
  "ds_read_b64_tr_b16 v[34:35], v28 offset:28704\n\t"
  "s_waitcnt lgkmcnt(0)\n\t"
  "v_mfma_f32_16x16x32_bf16 v[240:243], v[32:35], a[16:19], v[240:243]\n\t"
  "v_mfma_f32_16x16x32_bf16 v[244:247], v[32:35], a[20:23], v[244:247]\n\t"
  "v_mfma_f32_16x16x32_bf16 v[248:251], v[32:35], a[24:27], v[248:251]\n\t"
  "v_mfma_f32_16x16x32_bf16 v[252:255], v[32:35], a[28:31], v[252:255]\n\t"
  "ds_read_b64_tr_b16 v[32:33], v28 offset:24640\n\t"
  "ds_read_b64_tr_b16 v[34:35], v28 offset:28736\n\t"
  "s_waitcnt lgkmcnt(0)\n\t"
  "v_mfma_f32_16x16x32_bf16 v[112:115], v[32:35], a[16:19], v[112:115]\n\t"
  "v_mfma_f32_16x16x32_bf16 v[116:119], v[32:35], a[20:23], v[116:119]\n\t"
  "v_mfma_f32_16x16x32_bf16 v[120:123], v[32:35], a[24:27], v[120:123]\n\t"
  "v_mfma_f32_16x16x32_bf16 v[124:127], v[32:35], a[28:31], v[124:127]\n\t"
  "ds_read_b64_tr_b16 v[32:33], v28 offset:24672\n\t"
  "ds_read_b64_tr_b16 v[34:35], v28 offset:28768\n\t"
  "s_waitcnt lgkmcnt(0)\n\t"
  "v_mfma_f32_16x16x32_bf16 v[176:179], v[32:35], a[16:19], v[176:179]\n\t"
  "v_mfma_f32_16x16x32_bf16 v[180:183], v[32:35], a[20:23], v[180:183]\n\t"
  "v_mfma_f32_16x16x32_bf16 v[184:187], v[32:35], a[24:27], v[184:187]\n\t"
  "v_mfma_f32_16x16x32_bf16 v[188:191], v[32:35], a[28:31], v[188:191]\n\t"
  "ds_read_b64_tr_b16 v[32:33], v28 offset:24704\n\t"
  "ds_read_b64_tr_b16 v[34:35], v28 offset:28800\n\t"
  "s_waitcnt lgkmcnt(0)\n\t"
  "v_mfma_f32_16x16x32_bf16 v[64:67], v[32:35], a[16:19], v[64:67]\n\t"
  "v_mfma_f32_16x16x32_bf16 v[68:71], v[32:35], a[20:23], v[68:71]\n\t"
  "v_mfma_f32_16x16x32_bf16 v[72:75], v[32:35], a[24:27], v[72:75]\n\t"
  "v_mfma_f32_16x16x32_bf16 v[76:79], v[32:35], a[28:31], v[76:79]\n\t"
  "ds_read_b64_tr_b16 v[32:33], v28 offset:24736\n\t"
  "ds_read_b64_tr_b16 v[34:35], v28 offset:28832\n\t"
  "s_waitcnt lgkmcnt(0)\n\t"
  "v_mfma_f32_16x16x32_bf16 v[96:99], v[32:35], a[16:19], v[96:99]\n\t"
  "v_mfma_f32_16x16x32_bf16 v[100:103], v[32:35], a[20:23], v[100:103]\n\t"
  "v_mfma_f32_16x16x32_bf16 v[104:107], v[32:35], a[24:27], v[104:107]\n\t"
  "v_mfma_f32_16x16x32_bf16 v[108:111], v[32:35], a[28:31], v[108:111]\n\t"
  "ds_read_b64_tr_b16 v[32:33], v28 offset:24768\n\t"
  "ds_read_b64_tr_b16 v[34:35], v28 offset:28864\n\t"
  "s_waitcnt lgkmcnt(0)\n\t"
  "v_mfma_f32_16x16x32_bf16 v[128:131], v[32:35], a[16:19], v[128:131]\n\t"
  "v_mfma_f32_16x16x32_bf16 v[132:135], v[32:35], a[20:23], v[132:135]\n\t"
  "v_mfma_f32_16x16x32_bf16 v[136:139], v[32:35], a[24:27], v[136:139]\n\t"
  "v_mfma_f32_16x16x32_bf16 v[140:143], v[32:35], a[28:31], v[140:143]\n\t"
  "s_waitcnt vmcnt(0) lgkmcnt(0)\n\t"
  "s_barrier\n\t"
  "s_add_u32 m0, s69, 16384\n\t"
  "s_bfe_u32 s70, s84, 0x40004\n\t"
  "s_mul_i32 s70, s70, 57344\n\t"
  "buffer_load_dwordx4 v6, s[60:63], s70 offen offset:0  lds\n\t"
  "buffer_load_dwordx4 v7, s[60:63], s70 offen offset:1024  lds\n\t"
  "buffer_load_dwordx4 v8, s[60:63], s70 offen offset:2048  lds\n\t"
  "buffer_load_dwordx4 v9, s[60:63], s70 offen offset:3072  lds\n\t"
  "s_bfe_u32 s71, s84, 0x40004\n\t"
  "s_lshl_b32 s71, s71, 11\n\t"
  "buffer_load_dwordx4 a[96:99], v10, s[40:43], s71 offen offset:0 sc1\n\t"
  "buffer_load_dwordx4 a[100:103], v11, s[40:43], s71 offen offset:0 sc1\n\t"
  "buffer_load_dwordx4 a[104:107], v56, s[40:43], s71 offen offset:0 sc1\n\t"
  "buffer_load_dwordx4 a[108:111], v57, s[40:43], s71 offen offset:0 sc1\n\t"
  "buffer_load_dwordx4 a[112:115], v10, s[40:43], s71 offen offset:1024 sc1\n\t"
  "buffer_load_dwordx4 a[116:119], v11, s[40:43], s71 offen offset:1024 sc1\n\t"
  "buffer_load_dwordx4 a[120:123], v56, s[40:43], s71 offen offset:1024 sc1\n\t"
  "buffer_load_dwordx4 a[124:127], v57, s[40:43], s71 offen offset:1024 sc1\n\t"
  "s_waitcnt vmcnt(0) lgkmcnt(0)\n\t"
  "s_barrier\n\t"
  "ds_read_b64_tr_b16 v[32:33], v28 offset:16384\n\t"
  "ds_read_b64_tr_b16 v[34:35], v28 offset:20480\n\t"
  "s_waitcnt lgkmcnt(0)\n\t"
  "v_mfma_f32_16x16x32_bf16 v[208:211], v[32:35], a[96:99], v[208:211]\n\t"
  "v_mfma_f32_16x16x32_bf16 v[212:215], v[32:35], a[100:103], v[212:215]\n\t"
  "v_mfma_f32_16x16x32_bf16 v[216:219], v[32:35], a[104:107], v[216:219]\n\t"
  "v_mfma_f32_16x16x32_bf16 v[220:223], v[32:35], a[108:111], v[220:223]\n\t"
  "ds_read_b64_tr_b16 v[32:33], v28 offset:16416\n\t"
  "ds_read_b64_tr_b16 v[34:35], v28 offset:20512\n\t"
  "s_waitcnt lgkmcnt(0)\n\t"
  "v_mfma_f32_16x16x32_bf16 v[240:243], v[32:35], a[96:99], v[240:243]\n\t"
  "v_mfma_f32_16x16x32_bf16 v[244:247], v[32:35], a[100:103], v[244:247]\n\t"
  "v_mfma_f32_16x16x32_bf16 v[248:251], v[32:35], a[104:107], v[248:251]\n\t"
  "v_mfma_f32_16x16x32_bf16 v[252:255], v[32:35], a[108:111], v[252:255]\n\t"
  "ds_read_b64_tr_b16 v[32:33], v28 offset:16448\n\t"
  "ds_read_b64_tr_b16 v[34:35], v28 offset:20544\n\t"
  "s_waitcnt lgkmcnt(0)\n\t"
  "v_mfma_f32_16x16x32_bf16 v[112:115], v[32:35], a[96:99], v[112:115]\n\t"
  "v_mfma_f32_16x16x32_bf16 v[116:119], v[32:35], a[100:103], v[116:119]\n\t"
  "v_mfma_f32_16x16x32_bf16 v[120:123], v[32:35], a[104:107], v[120:123]\n\t"
  "v_mfma_f32_16x16x32_bf16 v[124:127], v[32:35], a[108:111], v[124:127]\n\t"
  "ds_read_b64_tr_b16 v[32:33], v28 offset:16480\n\t"
  "ds_read_b64_tr_b16 v[34:35], v28 offset:20576\n\t"
  "s_waitcnt lgkmcnt(0)\n\t"
  "v_mfma_f32_16x16x32_bf16 v[176:179], v[32:35], a[96:99], v[176:179]\n\t"
  "v_mfma_f32_16x16x32_bf16 v[180:183], v[32:35], a[100:103], v[180:183]\n\t"
  "v_mfma_f32_16x16x32_bf16 v[184:187], v[32:35], a[104:107], v[184:187]\n\t"
  "v_mfma_f32_16x16x32_bf16 v[188:191], v[32:35], a[108:111], v[188:191]\n\t"
  "ds_read_b64_tr_b16 v[32:33], v28 offset:16512\n\t"
  "ds_read_b64_tr_b16 v[34:35], v28 offset:20608\n\t"
  "s_waitcnt lgkmcnt(0)\n\t"
  "v_mfma_f32_16x16x32_bf16 v[64:67], v[32:35], a[96:99], v[64:67]\n\t"
  "v_mfma_f32_16x16x32_bf16 v[68:71], v[32:35], a[100:103], v[68:71]\n\t"
  "v_mfma_f32_16x16x32_bf16 v[72:75], v[32:35], a[104:107], v[72:75]\n\t"
  "v_mfma_f32_16x16x32_bf16 v[76:79], v[32:35], a[108:111], v[76:79]\n\t"
  "ds_read_b64_tr_b16 v[32:33], v28 offset:16544\n\t"
  "ds_read_b64_tr_b16 v[34:35], v28 offset:20640\n\t"
  "s_waitcnt lgkmcnt(0)\n\t"
  "v_mfma_f32_16x16x32_bf16 v[96:99], v[32:35], a[96:99], v[96:99]\n\t"
  "v_mfma_f32_16x16x32_bf16 v[100:103], v[32:35], a[100:103], v[100:103]\n\t"
  "v_mfma_f32_16x16x32_bf16 v[104:107], v[32:35], a[104:107], v[104:107]\n\t"
  "v_mfma_f32_16x16x32_bf16 v[108:111], v[32:35], a[108:111], v[108:111]\n\t"
  "ds_read_b64_tr_b16 v[32:33], v28 offset:16576\n\t"
  "ds_read_b64_tr_b16 v[34:35], v28 offset:20672\n\t"
  "s_waitcnt lgkmcnt(0)\n\t"
  "v_mfma_f32_16x16x32_bf16 v[128:131], v[32:35], a[96:99], v[128:131]\n\t"
  "v_mfma_f32_16x16x32_bf16 v[132:135], v[32:35], a[100:103], v[132:135]\n\t"
  "v_mfma_f32_16x16x32_bf16 v[136:139], v[32:35], a[104:107], v[136:139]\n\t"
  "v_mfma_f32_16x16x32_bf16 v[140:143], v[32:35], a[108:111], v[140:143]\n\t"
  "ds_read_b64_tr_b16 v[32:33], v28 offset:24576\n\t"
  "ds_read_b64_tr_b16 v[34:35], v28 offset:28672\n\t"
  "s_waitcnt lgkmcnt(0)\n\t"
  "v_mfma_f32_16x16x32_bf16 v[208:211], v[32:35], a[112:115], v[208:211]\n\t"
  "v_mfma_f32_16x16x32_bf16 v[212:215], v[32:35], a[116:119], v[212:215]\n\t"
  "v_mfma_f32_16x16x32_bf16 v[216:219], v[32:35], a[120:123], v[216:219]\n\t"
  "v_mfma_f32_16x16x32_bf16 v[220:223], v[32:35], a[124:127], v[220:223]\n\t"
  "ds_read_b64_tr_b16 v[32:33], v28 offset:24608\n\t"
  "ds_read_b64_tr_b16 v[34:35], v28 offset:28704\n\t"
  "s_waitcnt lgkmcnt(0)\n\t"
  "v_mfma_f32_16x16x32_bf16 v[240:243], v[32:35], a[112:115], v[240:243]\n\t"
  "v_mfma_f32_16x16x32_bf16 v[244:247], v[32:35], a[116:119], v[244:247]\n\t"
  "v_mfma_f32_16x16x32_bf16 v[248:251], v[32:35], a[120:123], v[248:251]\n\t"
  "v_mfma_f32_16x16x32_bf16 v[252:255], v[32:35], a[124:127], v[252:255]\n\t"
  "ds_read_b64_tr_b16 v[32:33], v28 offset:24640\n\t"
  "ds_read_b64_tr_b16 v[34:35], v28 offset:28736\n\t"
  "s_waitcnt lgkmcnt(0)\n\t"
  "v_mfma_f32_16x16x32_bf16 v[112:115], v[32:35], a[112:115], v[112:115]\n\t"
  "v_mfma_f32_16x16x32_bf16 v[116:119], v[32:35], a[116:119], v[116:119]\n\t"
  "v_mfma_f32_16x16x32_bf16 v[120:123], v[32:35], a[120:123], v[120:123]\n\t"
  "v_mfma_f32_16x16x32_bf16 v[124:127], v[32:35], a[124:127], v[124:127]\n\t"
  "ds_read_b64_tr_b16 v[32:33], v28 offset:24672\n\t"
  "ds_read_b64_tr_b16 v[34:35], v28 offset:28768\n\t"
  "s_waitcnt lgkmcnt(0)\n\t"
  "v_mfma_f32_16x16x32_bf16 v[176:179], v[32:35], a[112:115], v[176:179]\n\t"
  "v_mfma_f32_16x16x32_bf16 v[180:183], v[32:35], a[116:119], v[180:183]\n\t"
  "v_mfma_f32_16x16x32_bf16 v[184:187], v[32:35], a[120:123], v[184:187]\n\t"
  "v_mfma_f32_16x16x32_bf16 v[188:191], v[32:35], a[124:127], v[188:191]\n\t"
  "ds_read_b64_tr_b16 v[32:33], v28 offset:24704\n\t"
  "ds_read_b64_tr_b16 v[34:35], v28 offset:28800\n\t"
  "s_waitcnt lgkmcnt(0)\n\t"
  "v_mfma_f32_16x16x32_bf16 v[64:67], v[32:35], a[112:115], v[64:67]\n\t"
  "v_mfma_f32_16x16x32_bf16 v[68:71], v[32:35], a[116:119], v[68:71]\n\t"
  "v_mfma_f32_16x16x32_bf16 v[72:75], v[32:35], a[120:123], v[72:75]\n\t"
  "v_mfma_f32_16x16x32_bf16 v[76:79], v[32:35], a[124:127], v[76:79]\n\t"
  "ds_read_b64_tr_b16 v[32:33], v28 offset:24736\n\t"
  "ds_read_b64_tr_b16 v[34:35], v28 offset:28832\n\t"
  "s_waitcnt lgkmcnt(0)\n\t"
  "v_mfma_f32_16x16x32_bf16 v[96:99], v[32:35], a[112:115], v[96:99]\n\t"
  "v_mfma_f32_16x16x32_bf16 v[100:103], v[32:35], a[116:119], v[100:103]\n\t"
  "v_mfma_f32_16x16x32_bf16 v[104:107], v[32:35], a[120:123], v[104:107]\n\t"
  "v_mfma_f32_16x16x32_bf16 v[108:111], v[32:35], a[124:127], v[108:111]\n\t"
  "ds_read_b64_tr_b16 v[32:33], v28 offset:24768\n\t"
  "ds_read_b64_tr_b16 v[34:35], v28 offset:28864\n\t"
  "s_waitcnt lgkmcnt(0)\n\t"
  "v_mfma_f32_16x16x32_bf16 v[128:131], v[32:35], a[112:115], v[128:131]\n\t"
  "v_mfma_f32_16x16x32_bf16 v[132:135], v[32:35], a[116:119], v[132:135]\n\t"
  "v_mfma_f32_16x16x32_bf16 v[136:139], v[32:35], a[120:123], v[136:139]\n\t"
  "v_mfma_f32_16x16x32_bf16 v[140:143], v[32:35], a[124:127], v[140:143]\n\t"
  "s_waitcnt vmcnt(0) lgkmcnt(0)\n\t"
  "s_barrier\n\t"
  "s_add_u32 m0, s69, 16384\n\t"
  "s_bfe_u32 s70, s84, 0x40008\n\t"
  "s_mul_i32 s70, s70, 57344\n\t"
  "buffer_load_dwordx4 v6, s[60:63], s70 offen offset:0  lds\n\t"
  "buffer_load_dwordx4 v7, s[60:63], s70 offen offset:1024  lds\n\t"
  "buffer_load_dwordx4 v8, s[60:63], s70 offen offset:2048  lds\n\t"
  "buffer_load_dwordx4 v9, s[60:63], s70 offen offset:3072  lds\n\t"
  "s_bfe_u32 s71, s84, 0x40008\n\t"
  "s_lshl_b32 s71, s71, 11\n\t"
  "buffer_load_dwordx4 a[128:131], v10, s[40:43], s71 offen offset:0 sc1\n\t"
  "buffer_load_dwordx4 a[132:135], v11, s[40:43], s71 offen offset:0 sc1\n\t"
  "buffer_load_dwordx4 a[136:139], v56, s[40:43], s71 offen offset:0 sc1\n\t"
  "buffer_load_dwordx4 a[140:143], v57, s[40:43], s71 offen offset:0 sc1\n\t"
  "buffer_load_dwordx4 a[144:147], v10, s[40:43], s71 offen offset:1024 sc1\n\t"
  "buffer_load_dwordx4 a[148:151], v11, s[40:43], s71 offen offset:1024 sc1\n\t"
  "buffer_load_dwordx4 a[152:155], v56, s[40:43], s71 offen offset:1024 sc1\n\t"
  "buffer_load_dwordx4 a[156:159], v57, s[40:43], s71 offen offset:1024 sc1\n\t"
  "s_waitcnt vmcnt(0) lgkmcnt(0)\n\t"
  "s_barrier\n\t"
  "ds_read_b64_tr_b16 v[32:33], v28 offset:16384\n\t"
  "ds_read_b64_tr_b16 v[34:35], v28 offset:20480\n\t"
  "s_waitcnt lgkmcnt(0)\n\t"
  "v_mfma_f32_16x16x32_bf16 v[208:211], v[32:35], a[128:131], v[208:211]\n\t"
  "v_mfma_f32_16x16x32_bf16 v[212:215], v[32:35], a[132:135], v[212:215]\n\t"
  "v_mfma_f32_16x16x32_bf16 v[216:219], v[32:35], a[136:139], v[216:219]\n\t"
  "v_mfma_f32_16x16x32_bf16 v[220:223], v[32:35], a[140:143], v[220:223]\n\t"
  "ds_read_b64_tr_b16 v[32:33], v28 offset:16416\n\t"
  "ds_read_b64_tr_b16 v[34:35], v28 offset:20512\n\t"
  "s_waitcnt lgkmcnt(0)\n\t"
  "v_mfma_f32_16x16x32_bf16 v[240:243], v[32:35], a[128:131], v[240:243]\n\t"
  "v_mfma_f32_16x16x32_bf16 v[244:247], v[32:35], a[132:135], v[244:247]\n\t"
  "v_mfma_f32_16x16x32_bf16 v[248:251], v[32:35], a[136:139], v[248:251]\n\t"
  "v_mfma_f32_16x16x32_bf16 v[252:255], v[32:35], a[140:143], v[252:255]\n\t"
  "ds_read_b64_tr_b16 v[32:33], v28 offset:16448\n\t"
  "ds_read_b64_tr_b16 v[34:35], v28 offset:20544\n\t"
  "s_waitcnt lgkmcnt(0)\n\t"
  "v_mfma_f32_16x16x32_bf16 v[112:115], v[32:35], a[128:131], v[112:115]\n\t"
  "v_mfma_f32_16x16x32_bf16 v[116:119], v[32:35], a[132:135], v[116:119]\n\t"
  "v_mfma_f32_16x16x32_bf16 v[120:123], v[32:35], a[136:139], v[120:123]\n\t"
  "v_mfma_f32_16x16x32_bf16 v[124:127], v[32:35], a[140:143], v[124:127]\n\t"
  "ds_read_b64_tr_b16 v[32:33], v28 offset:16480\n\t"
  "ds_read_b64_tr_b16 v[34:35], v28 offset:20576\n\t"
  "s_waitcnt lgkmcnt(0)\n\t"
  "v_mfma_f32_16x16x32_bf16 v[176:179], v[32:35], a[128:131], v[176:179]\n\t"
  "v_mfma_f32_16x16x32_bf16 v[180:183], v[32:35], a[132:135], v[180:183]\n\t"
  "v_mfma_f32_16x16x32_bf16 v[184:187], v[32:35], a[136:139], v[184:187]\n\t"
  "v_mfma_f32_16x16x32_bf16 v[188:191], v[32:35], a[140:143], v[188:191]\n\t"
  "ds_read_b64_tr_b16 v[32:33], v28 offset:16512\n\t"
  "ds_read_b64_tr_b16 v[34:35], v28 offset:20608\n\t"
  "s_waitcnt lgkmcnt(0)\n\t"
  "v_mfma_f32_16x16x32_bf16 v[64:67], v[32:35], a[128:131], v[64:67]\n\t"
  "v_mfma_f32_16x16x32_bf16 v[68:71], v[32:35], a[132:135], v[68:71]\n\t"
  "v_mfma_f32_16x16x32_bf16 v[72:75], v[32:35], a[136:139], v[72:75]\n\t"
  "v_mfma_f32_16x16x32_bf16 v[76:79], v[32:35], a[140:143], v[76:79]\n\t"
  "ds_read_b64_tr_b16 v[32:33], v28 offset:16544\n\t"
  "ds_read_b64_tr_b16 v[34:35], v28 offset:20640\n\t"
  "s_waitcnt lgkmcnt(0)\n\t"
  "v_mfma_f32_16x16x32_bf16 v[96:99], v[32:35], a[128:131], v[96:99]\n\t"
  "v_mfma_f32_16x16x32_bf16 v[100:103], v[32:35], a[132:135], v[100:103]\n\t"
  "v_mfma_f32_16x16x32_bf16 v[104:107], v[32:35], a[136:139], v[104:107]\n\t"
  "v_mfma_f32_16x16x32_bf16 v[108:111], v[32:35], a[140:143], v[108:111]\n\t"
  "ds_read_b64_tr_b16 v[32:33], v28 offset:16576\n\t"
  "ds_read_b64_tr_b16 v[34:35], v28 offset:20672\n\t"
  "s_waitcnt lgkmcnt(0)\n\t"
  "v_mfma_f32_16x16x32_bf16 v[128:131], v[32:35], a[128:131], v[128:131]\n\t"
  "v_mfma_f32_16x16x32_bf16 v[132:135], v[32:35], a[132:135], v[132:135]\n\t"
  "v_mfma_f32_16x16x32_bf16 v[136:139], v[32:35], a[136:139], v[136:139]\n\t"
  "v_mfma_f32_16x16x32_bf16 v[140:143], v[32:35], a[140:143], v[140:143]\n\t"
  "ds_read_b64_tr_b16 v[32:33], v28 offset:24576\n\t"
  "ds_read_b64_tr_b16 v[34:35], v28 offset:28672\n\t"
  "s_waitcnt lgkmcnt(0)\n\t"
  "v_mfma_f32_16x16x32_bf16 v[208:211], v[32:35], a[144:147], v[208:211]\n\t"
  "v_mfma_f32_16x16x32_bf16 v[212:215], v[32:35], a[148:151], v[212:215]\n\t"
  "v_mfma_f32_16x16x32_bf16 v[216:219], v[32:35], a[152:155], v[216:219]\n\t"
  "v_mfma_f32_16x16x32_bf16 v[220:223], v[32:35], a[156:159], v[220:223]\n\t"
  "ds_read_b64_tr_b16 v[32:33], v28 offset:24608\n\t"
  "ds_read_b64_tr_b16 v[34:35], v28 offset:28704\n\t"
  "s_waitcnt lgkmcnt(0)\n\t"
  "v_mfma_f32_16x16x32_bf16 v[240:243], v[32:35], a[144:147], v[240:243]\n\t"
  "v_mfma_f32_16x16x32_bf16 v[244:247], v[32:35], a[148:151], v[244:247]\n\t"
  "v_mfma_f32_16x16x32_bf16 v[248:251], v[32:35], a[152:155], v[248:251]\n\t"
  "v_mfma_f32_16x16x32_bf16 v[252:255], v[32:35], a[156:159], v[252:255]\n\t"
  "ds_read_b64_tr_b16 v[32:33], v28 offset:24640\n\t"
  "ds_read_b64_tr_b16 v[34:35], v28 offset:28736\n\t"
  "s_waitcnt lgkmcnt(0)\n\t"
  "v_mfma_f32_16x16x32_bf16 v[112:115], v[32:35], a[144:147], v[112:115]\n\t"
  "v_mfma_f32_16x16x32_bf16 v[116:119], v[32:35], a[148:151], v[116:119]\n\t"
  "v_mfma_f32_16x16x32_bf16 v[120:123], v[32:35], a[152:155], v[120:123]\n\t"
  "v_mfma_f32_16x16x32_bf16 v[124:127], v[32:35], a[156:159], v[124:127]\n\t"
  "ds_read_b64_tr_b16 v[32:33], v28 offset:24672\n\t"
  "ds_read_b64_tr_b16 v[34:35], v28 offset:28768\n\t"
  "s_waitcnt lgkmcnt(0)\n\t"
  "v_mfma_f32_16x16x32_bf16 v[176:179], v[32:35], a[144:147], v[176:179]\n\t"
  "v_mfma_f32_16x16x32_bf16 v[180:183], v[32:35], a[148:151], v[180:183]\n\t"
  "v_mfma_f32_16x16x32_bf16 v[184:187], v[32:35], a[152:155], v[184:187]\n\t"
  "v_mfma_f32_16x16x32_bf16 v[188:191], v[32:35], a[156:159], v[188:191]\n\t"
  "ds_read_b64_tr_b16 v[32:33], v28 offset:24704\n\t"
  "ds_read_b64_tr_b16 v[34:35], v28 offset:28800\n\t"
  "s_waitcnt lgkmcnt(0)\n\t"
  "v_mfma_f32_16x16x32_bf16 v[64:67], v[32:35], a[144:147], v[64:67]\n\t"
  "v_mfma_f32_16x16x32_bf16 v[68:71], v[32:35], a[148:151], v[68:71]\n\t"
  "v_mfma_f32_16x16x32_bf16 v[72:75], v[32:35], a[152:155], v[72:75]\n\t"
  "v_mfma_f32_16x16x32_bf16 v[76:79], v[32:35], a[156:159], v[76:79]\n\t"
  "ds_read_b64_tr_b16 v[32:33], v28 offset:24736\n\t"
  "ds_read_b64_tr_b16 v[34:35], v28 offset:28832\n\t"
  "s_waitcnt lgkmcnt(0)\n\t"
  "v_mfma_f32_16x16x32_bf16 v[96:99], v[32:35], a[144:147], v[96:99]\n\t"
  "v_mfma_f32_16x16x32_bf16 v[100:103], v[32:35], a[148:151], v[100:103]\n\t"
  "v_mfma_f32_16x16x32_bf16 v[104:107], v[32:35], a[152:155], v[104:107]\n\t"
  "v_mfma_f32_16x16x32_bf16 v[108:111], v[32:35], a[156:159], v[108:111]\n\t"
  "ds_read_b64_tr_b16 v[32:33], v28 offset:24768\n\t"
  "ds_read_b64_tr_b16 v[34:35], v28 offset:28864\n\t"
  "s_waitcnt lgkmcnt(0)\n\t"
  "v_mfma_f32_16x16x32_bf16 v[128:131], v[32:35], a[144:147], v[128:131]\n\t"
  "v_mfma_f32_16x16x32_bf16 v[132:135], v[32:35], a[148:151], v[132:135]\n\t"
  "v_mfma_f32_16x16x32_bf16 v[136:139], v[32:35], a[152:155], v[136:139]\n\t"
  "v_mfma_f32_16x16x32_bf16 v[140:143], v[32:35], a[156:159], v[140:143]\n\t"
  "s_waitcnt vmcnt(0) lgkmcnt(0)\n\t"
  "s_barrier\n\t"
  "s_add_u32 m0, s69, 16384\n\t"
  "s_bfe_u32 s70, s84, 0x4000c\n\t"
  "s_mul_i32 s70, s70, 57344\n\t"
  "buffer_load_dwordx4 v6, s[60:63], s70 offen offset:0  lds\n\t"
  "buffer_load_dwordx4 v7, s[60:63], s70 offen offset:1024  lds\n\t"
  "buffer_load_dwordx4 v8, s[60:63], s70 offen offset:2048  lds\n\t"
  "buffer_load_dwordx4 v9, s[60:63], s70 offen offset:3072  lds\n\t"
  "s_bfe_u32 s71, s84, 0x4000c\n\t"
  "s_lshl_b32 s71, s71, 11\n\t"
  "buffer_load_dwordx4 a[160:163], v10, s[40:43], s71 offen offset:0 sc1\n\t"
  "buffer_load_dwordx4 a[164:167], v11, s[40:43], s71 offen offset:0 sc1\n\t"
  "buffer_load_dwordx4 a[168:171], v56, s[40:43], s71 offen offset:0 sc1\n\t"
  "buffer_load_dwordx4 a[172:175], v57, s[40:43], s71 offen offset:0 sc1\n\t"
  "buffer_load_dwordx4 a[176:179], v10, s[40:43], s71 offen offset:1024 sc1\n\t"
  "buffer_load_dwordx4 a[180:183], v11, s[40:43], s71 offen offset:1024 sc1\n\t"
  "buffer_load_dwordx4 a[184:187], v56, s[40:43], s71 offen offset:1024 sc1\n\t"
  "buffer_load_dwordx4 a[188:191], v57, s[40:43], s71 offen offset:1024 sc1\n\t"
  "s_waitcnt vmcnt(0) lgkmcnt(0)\n\t"
  "s_barrier\n\t"
  "ds_read_b64_tr_b16 v[32:33], v28 offset:16384\n\t"
  "ds_read_b64_tr_b16 v[34:35], v28 offset:20480\n\t"
  "s_waitcnt lgkmcnt(0)\n\t"
  "v_mfma_f32_16x16x32_bf16 v[208:211], v[32:35], a[160:163], v[208:211]\n\t"
  "v_mfma_f32_16x16x32_bf16 v[212:215], v[32:35], a[164:167], v[212:215]\n\t"
  "v_mfma_f32_16x16x32_bf16 v[216:219], v[32:35], a[168:171], v[216:219]\n\t"
  "v_mfma_f32_16x16x32_bf16 v[220:223], v[32:35], a[172:175], v[220:223]\n\t"
  "ds_read_b64_tr_b16 v[32:33], v28 offset:16416\n\t"
  "ds_read_b64_tr_b16 v[34:35], v28 offset:20512\n\t"
  "s_waitcnt lgkmcnt(0)\n\t"
  "v_mfma_f32_16x16x32_bf16 v[240:243], v[32:35], a[160:163], v[240:243]\n\t"
  "v_mfma_f32_16x16x32_bf16 v[244:247], v[32:35], a[164:167], v[244:247]\n\t"
  "v_mfma_f32_16x16x32_bf16 v[248:251], v[32:35], a[168:171], v[248:251]\n\t"
  "v_mfma_f32_16x16x32_bf16 v[252:255], v[32:35], a[172:175], v[252:255]\n\t"
  "ds_read_b64_tr_b16 v[32:33], v28 offset:16448\n\t"
  "ds_read_b64_tr_b16 v[34:35], v28 offset:20544\n\t"
  "s_waitcnt lgkmcnt(0)\n\t"
  "v_mfma_f32_16x16x32_bf16 v[112:115], v[32:35], a[160:163], v[112:115]\n\t"
  "v_mfma_f32_16x16x32_bf16 v[116:119], v[32:35], a[164:167], v[116:119]\n\t"
  "v_mfma_f32_16x16x32_bf16 v[120:123], v[32:35], a[168:171], v[120:123]\n\t"
  "v_mfma_f32_16x16x32_bf16 v[124:127], v[32:35], a[172:175], v[124:127]\n\t"
  "ds_read_b64_tr_b16 v[32:33], v28 offset:16480\n\t"
  "ds_read_b64_tr_b16 v[34:35], v28 offset:20576\n\t"
  "s_waitcnt lgkmcnt(0)\n\t"
  "v_mfma_f32_16x16x32_bf16 v[176:179], v[32:35], a[160:163], v[176:179]\n\t"
  "v_mfma_f32_16x16x32_bf16 v[180:183], v[32:35], a[164:167], v[180:183]\n\t"
  "v_mfma_f32_16x16x32_bf16 v[184:187], v[32:35], a[168:171], v[184:187]\n\t"
  "v_mfma_f32_16x16x32_bf16 v[188:191], v[32:35], a[172:175], v[188:191]\n\t"
  "ds_read_b64_tr_b16 v[32:33], v28 offset:16512\n\t"
  "ds_read_b64_tr_b16 v[34:35], v28 offset:20608\n\t"
  "s_waitcnt lgkmcnt(0)\n\t"
  "v_mfma_f32_16x16x32_bf16 v[64:67], v[32:35], a[160:163], v[64:67]\n\t"
  "v_mfma_f32_16x16x32_bf16 v[68:71], v[32:35], a[164:167], v[68:71]\n\t"
  "v_mfma_f32_16x16x32_bf16 v[72:75], v[32:35], a[168:171], v[72:75]\n\t"
  "v_mfma_f32_16x16x32_bf16 v[76:79], v[32:35], a[172:175], v[76:79]\n\t"
  "ds_read_b64_tr_b16 v[32:33], v28 offset:16544\n\t"
  "ds_read_b64_tr_b16 v[34:35], v28 offset:20640\n\t"
  "s_waitcnt lgkmcnt(0)\n\t"
  "v_mfma_f32_16x16x32_bf16 v[96:99], v[32:35], a[160:163], v[96:99]\n\t"
  "v_mfma_f32_16x16x32_bf16 v[100:103], v[32:35], a[164:167], v[100:103]\n\t"
  "v_mfma_f32_16x16x32_bf16 v[104:107], v[32:35], a[168:171], v[104:107]\n\t"
  "v_mfma_f32_16x16x32_bf16 v[108:111], v[32:35], a[172:175], v[108:111]\n\t"
  "ds_read_b64_tr_b16 v[32:33], v28 offset:16576\n\t"
  "ds_read_b64_tr_b16 v[34:35], v28 offset:20672\n\t"
  "s_waitcnt lgkmcnt(0)\n\t"
  "v_mfma_f32_16x16x32_bf16 v[128:131], v[32:35], a[160:163], v[128:131]\n\t"
  "v_mfma_f32_16x16x32_bf16 v[132:135], v[32:35], a[164:167], v[132:135]\n\t"
  "v_mfma_f32_16x16x32_bf16 v[136:139], v[32:35], a[168:171], v[136:139]\n\t"
  "v_mfma_f32_16x16x32_bf16 v[140:143], v[32:35], a[172:175], v[140:143]\n\t"
  "ds_read_b64_tr_b16 v[32:33], v28 offset:24576\n\t"
  "ds_read_b64_tr_b16 v[34:35], v28 offset:28672\n\t"
  "s_waitcnt lgkmcnt(0)\n\t"
  "v_mfma_f32_16x16x32_bf16 v[208:211], v[32:35], a[176:179], v[208:211]\n\t"
  "v_mfma_f32_16x16x32_bf16 v[212:215], v[32:35], a[180:183], v[212:215]\n\t"
  "v_mfma_f32_16x16x32_bf16 v[216:219], v[32:35], a[184:187], v[216:219]\n\t"
  "v_mfma_f32_16x16x32_bf16 v[220:223], v[32:35], a[188:191], v[220:223]\n\t"
  "ds_read_b64_tr_b16 v[32:33], v28 offset:24608\n\t"
  "ds_read_b64_tr_b16 v[34:35], v28 offset:28704\n\t"
  "s_waitcnt lgkmcnt(0)\n\t"
  "v_mfma_f32_16x16x32_bf16 v[240:243], v[32:35], a[176:179], v[240:243]\n\t"
  "v_mfma_f32_16x16x32_bf16 v[244:247], v[32:35], a[180:183], v[244:247]\n\t"
  "v_mfma_f32_16x16x32_bf16 v[248:251], v[32:35], a[184:187], v[248:251]\n\t"
  "v_mfma_f32_16x16x32_bf16 v[252:255], v[32:35], a[188:191], v[252:255]\n\t"
  "ds_read_b64_tr_b16 v[32:33], v28 offset:24640\n\t"
  "ds_read_b64_tr_b16 v[34:35], v28 offset:28736\n\t"
  "s_waitcnt lgkmcnt(0)\n\t"
  "v_mfma_f32_16x16x32_bf16 v[112:115], v[32:35], a[176:179], v[112:115]\n\t"
  "v_mfma_f32_16x16x32_bf16 v[116:119], v[32:35], a[180:183], v[116:119]\n\t"
  "v_mfma_f32_16x16x32_bf16 v[120:123], v[32:35], a[184:187], v[120:123]\n\t"
  "v_mfma_f32_16x16x32_bf16 v[124:127], v[32:35], a[188:191], v[124:127]\n\t"
  "ds_read_b64_tr_b16 v[32:33], v28 offset:24672\n\t"
  "ds_read_b64_tr_b16 v[34:35], v28 offset:28768\n\t"
  "s_waitcnt lgkmcnt(0)\n\t"
  "v_mfma_f32_16x16x32_bf16 v[176:179], v[32:35], a[176:179], v[176:179]\n\t"
  "v_mfma_f32_16x16x32_bf16 v[180:183], v[32:35], a[180:183], v[180:183]\n\t"
  "v_mfma_f32_16x16x32_bf16 v[184:187], v[32:35], a[184:187], v[184:187]\n\t"
  "v_mfma_f32_16x16x32_bf16 v[188:191], v[32:35], a[188:191], v[188:191]\n\t"
  "ds_read_b64_tr_b16 v[32:33], v28 offset:24704\n\t"
  "ds_read_b64_tr_b16 v[34:35], v28 offset:28800\n\t"
  "s_waitcnt lgkmcnt(0)\n\t"
  "v_mfma_f32_16x16x32_bf16 v[64:67], v[32:35], a[176:179], v[64:67]\n\t"
  "v_mfma_f32_16x16x32_bf16 v[68:71], v[32:35], a[180:183], v[68:71]\n\t"
  "v_mfma_f32_16x16x32_bf16 v[72:75], v[32:35], a[184:187], v[72:75]\n\t"
  "v_mfma_f32_16x16x32_bf16 v[76:79], v[32:35], a[188:191], v[76:79]\n\t"
  "ds_read_b64_tr_b16 v[32:33], v28 offset:24736\n\t"
  "ds_read_b64_tr_b16 v[34:35], v28 offset:28832\n\t"
  "s_waitcnt lgkmcnt(0)\n\t"
  "v_mfma_f32_16x16x32_bf16 v[96:99], v[32:35], a[176:179], v[96:99]\n\t"
  "v_mfma_f32_16x16x32_bf16 v[100:103], v[32:35], a[180:183], v[100:103]\n\t"
  "v_mfma_f32_16x16x32_bf16 v[104:107], v[32:35], a[184:187], v[104:107]\n\t"
  "v_mfma_f32_16x16x32_bf16 v[108:111], v[32:35], a[188:191], v[108:111]\n\t"
  "ds_read_b64_tr_b16 v[32:33], v28 offset:24768\n\t"
  "ds_read_b64_tr_b16 v[34:35], v28 offset:28864\n\t"
  "s_waitcnt lgkmcnt(0)\n\t"
  "v_mfma_f32_16x16x32_bf16 v[128:131], v[32:35], a[176:179], v[128:131]\n\t"
  "v_mfma_f32_16x16x32_bf16 v[132:135], v[32:35], a[180:183], v[132:135]\n\t"
  "v_mfma_f32_16x16x32_bf16 v[136:139], v[32:35], a[184:187], v[136:139]\n\t"
  "v_mfma_f32_16x16x32_bf16 v[140:143], v[32:35], a[188:191], v[140:143]\n\t"
  "s_waitcnt vmcnt(0) lgkmcnt(0)\n\t"
  "s_barrier\n\t"
  "s_add_u32 m0, s69, 16384\n\t"
  "s_bfe_u32 s70, s84, 0x40010\n\t"
  "s_mul_i32 s70, s70, 57344\n\t"
  "buffer_load_dwordx4 v6, s[60:63], s70 offen offset:0  lds\n\t"
  "buffer_load_dwordx4 v7, s[60:63], s70 offen offset:1024  lds\n\t"
  "buffer_load_dwordx4 v8, s[60:63], s70 offen offset:2048  lds\n\t"
  "buffer_load_dwordx4 v9, s[60:63], s70 offen offset:3072  lds\n\t"
  "s_bfe_u32 s71, s84, 0x40010\n\t"
  "s_lshl_b32 s71, s71, 11\n\t"
  "buffer_load_dwordx4 a[32:35], v10, s[40:43], s71 offen offset:0 sc1\n\t"
  "buffer_load_dwordx4 a[36:39], v11, s[40:43], s71 offen offset:0 sc1\n\t"
  "buffer_load_dwordx4 a[40:43], v56, s[40:43], s71 offen offset:0 sc1\n\t"
  "buffer_load_dwordx4 a[44:47], v57, s[40:43], s71 offen offset:0 sc1\n\t"
  "buffer_load_dwordx4 a[48:51], v10, s[40:43], s71 offen offset:1024 sc1\n\t"
  "buffer_load_dwordx4 a[52:55], v11, s[40:43], s71 offen offset:1024 sc1\n\t"
  "buffer_load_dwordx4 a[56:59], v56, s[40:43], s71 offen offset:1024 sc1\n\t"
  "buffer_load_dwordx4 a[60:63], v57, s[40:43], s71 offen offset:1024 sc1\n\t"
  "s_waitcnt vmcnt(0) lgkmcnt(0)\n\t"
  "s_barrier\n\t"
  "ds_read_b64_tr_b16 v[32:33], v28 offset:16384\n\t"
  "ds_read_b64_tr_b16 v[34:35], v28 offset:20480\n\t"
  "s_waitcnt lgkmcnt(0)\n\t"
  "v_mfma_f32_16x16x32_bf16 v[208:211], v[32:35], a[32:35], v[208:211]\n\t"
  "v_mfma_f32_16x16x32_bf16 v[212:215], v[32:35], a[36:39], v[212:215]\n\t"
  "v_mfma_f32_16x16x32_bf16 v[216:219], v[32:35], a[40:43], v[216:219]\n\t"
  "v_mfma_f32_16x16x32_bf16 v[220:223], v[32:35], a[44:47], v[220:223]\n\t"
  "ds_read_b64_tr_b16 v[32:33], v28 offset:16416\n\t"
  "ds_read_b64_tr_b16 v[34:35], v28 offset:20512\n\t"
  "s_waitcnt lgkmcnt(0)\n\t"
  "v_mfma_f32_16x16x32_bf16 v[240:243], v[32:35], a[32:35], v[240:243]\n\t"
  "v_mfma_f32_16x16x32_bf16 v[244:247], v[32:35], a[36:39], v[244:247]\n\t"
  "v_mfma_f32_16x16x32_bf16 v[248:251], v[32:35], a[40:43], v[248:251]\n\t"
  "v_mfma_f32_16x16x32_bf16 v[252:255], v[32:35], a[44:47], v[252:255]\n\t"
  "ds_read_b64_tr_b16 v[32:33], v28 offset:16448\n\t"
  "ds_read_b64_tr_b16 v[34:35], v28 offset:20544\n\t"
  "s_waitcnt lgkmcnt(0)\n\t"
  "v_mfma_f32_16x16x32_bf16 v[112:115], v[32:35], a[32:35], v[112:115]\n\t"
  "v_mfma_f32_16x16x32_bf16 v[116:119], v[32:35], a[36:39], v[116:119]\n\t"
  "v_mfma_f32_16x16x32_bf16 v[120:123], v[32:35], a[40:43], v[120:123]\n\t"
  "v_mfma_f32_16x16x32_bf16 v[124:127], v[32:35], a[44:47], v[124:127]\n\t"
  "ds_read_b64_tr_b16 v[32:33], v28 offset:16480\n\t"
  "ds_read_b64_tr_b16 v[34:35], v28 offset:20576\n\t"
  "s_waitcnt lgkmcnt(0)\n\t"
  "v_mfma_f32_16x16x32_bf16 v[176:179], v[32:35], a[32:35], v[176:179]\n\t"
  "v_mfma_f32_16x16x32_bf16 v[180:183], v[32:35], a[36:39], v[180:183]\n\t"
  "v_mfma_f32_16x16x32_bf16 v[184:187], v[32:35], a[40:43], v[184:187]\n\t"
  "v_mfma_f32_16x16x32_bf16 v[188:191], v[32:35], a[44:47], v[188:191]\n\t"
  "ds_read_b64_tr_b16 v[32:33], v28 offset:16512\n\t"
  "ds_read_b64_tr_b16 v[34:35], v28 offset:20608\n\t"
  "s_waitcnt lgkmcnt(0)\n\t"
  "v_mfma_f32_16x16x32_bf16 v[64:67], v[32:35], a[32:35], v[64:67]\n\t"
  "v_mfma_f32_16x16x32_bf16 v[68:71], v[32:35], a[36:39], v[68:71]\n\t"
  "v_mfma_f32_16x16x32_bf16 v[72:75], v[32:35], a[40:43], v[72:75]\n\t"
  "v_mfma_f32_16x16x32_bf16 v[76:79], v[32:35], a[44:47], v[76:79]\n\t"
  "ds_read_b64_tr_b16 v[32:33], v28 offset:16544\n\t"
  "ds_read_b64_tr_b16 v[34:35], v28 offset:20640\n\t"
  "s_waitcnt lgkmcnt(0)\n\t"
  "v_mfma_f32_16x16x32_bf16 v[96:99], v[32:35], a[32:35], v[96:99]\n\t"
  "v_mfma_f32_16x16x32_bf16 v[100:103], v[32:35], a[36:39], v[100:103]\n\t"
  "v_mfma_f32_16x16x32_bf16 v[104:107], v[32:35], a[40:43], v[104:107]\n\t"
  "v_mfma_f32_16x16x32_bf16 v[108:111], v[32:35], a[44:47], v[108:111]\n\t"
  "ds_read_b64_tr_b16 v[32:33], v28 offset:16576\n\t"
  "ds_read_b64_tr_b16 v[34:35], v28 offset:20672\n\t"
  "s_waitcnt lgkmcnt(0)\n\t"
  "v_mfma_f32_16x16x32_bf16 v[128:131], v[32:35], a[32:35], v[128:131]\n\t"
  "v_mfma_f32_16x16x32_bf16 v[132:135], v[32:35], a[36:39], v[132:135]\n\t"
  "v_mfma_f32_16x16x32_bf16 v[136:139], v[32:35], a[40:43], v[136:139]\n\t"
  "v_mfma_f32_16x16x32_bf16 v[140:143], v[32:35], a[44:47], v[140:143]\n\t"
  "ds_read_b64_tr_b16 v[32:33], v28 offset:24576\n\t"
  "ds_read_b64_tr_b16 v[34:35], v28 offset:28672\n\t"
  "s_waitcnt lgkmcnt(0)\n\t"
  "v_mfma_f32_16x16x32_bf16 v[208:211], v[32:35], a[48:51], v[208:211]\n\t"
  "v_mfma_f32_16x16x32_bf16 v[212:215], v[32:35], a[52:55], v[212:215]\n\t"
  "v_mfma_f32_16x16x32_bf16 v[216:219], v[32:35], a[56:59], v[216:219]\n\t"
  "v_mfma_f32_16x16x32_bf16 v[220:223], v[32:35], a[60:63], v[220:223]\n\t"
  "ds_read_b64_tr_b16 v[32:33], v28 offset:24608\n\t"
  "ds_read_b64_tr_b16 v[34:35], v28 offset:28704\n\t"
  "s_waitcnt lgkmcnt(0)\n\t"
  "v_mfma_f32_16x16x32_bf16 v[240:243], v[32:35], a[48:51], v[240:243]\n\t"
  "v_mfma_f32_16x16x32_bf16 v[244:247], v[32:35], a[52:55], v[244:247]\n\t"
  "v_mfma_f32_16x16x32_bf16 v[248:251], v[32:35], a[56:59], v[248:251]\n\t"
  "v_mfma_f32_16x16x32_bf16 v[252:255], v[32:35], a[60:63], v[252:255]\n\t"
  "ds_read_b64_tr_b16 v[32:33], v28 offset:24640\n\t"
  "ds_read_b64_tr_b16 v[34:35], v28 offset:28736\n\t"
  "s_waitcnt lgkmcnt(0)\n\t"
  "v_mfma_f32_16x16x32_bf16 v[112:115], v[32:35], a[48:51], v[112:115]\n\t"
  "v_mfma_f32_16x16x32_bf16 v[116:119], v[32:35], a[52:55], v[116:119]\n\t"
  "v_mfma_f32_16x16x32_bf16 v[120:123], v[32:35], a[56:59], v[120:123]\n\t"
  "v_mfma_f32_16x16x32_bf16 v[124:127], v[32:35], a[60:63], v[124:127]\n\t"
  "ds_read_b64_tr_b16 v[32:33], v28 offset:24672\n\t"
  "ds_read_b64_tr_b16 v[34:35], v28 offset:28768\n\t"
  "s_waitcnt lgkmcnt(0)\n\t"
  "v_mfma_f32_16x16x32_bf16 v[176:179], v[32:35], a[48:51], v[176:179]\n\t"
  "v_mfma_f32_16x16x32_bf16 v[180:183], v[32:35], a[52:55], v[180:183]\n\t"
  "v_mfma_f32_16x16x32_bf16 v[184:187], v[32:35], a[56:59], v[184:187]\n\t"
  "v_mfma_f32_16x16x32_bf16 v[188:191], v[32:35], a[60:63], v[188:191]\n\t"
  "ds_read_b64_tr_b16 v[32:33], v28 offset:24704\n\t"
  "ds_read_b64_tr_b16 v[34:35], v28 offset:28800\n\t"
  "s_waitcnt lgkmcnt(0)\n\t"
  "v_mfma_f32_16x16x32_bf16 v[64:67], v[32:35], a[48:51], v[64:67]\n\t"
  "v_mfma_f32_16x16x32_bf16 v[68:71], v[32:35], a[52:55], v[68:71]\n\t"
  "v_mfma_f32_16x16x32_bf16 v[72:75], v[32:35], a[56:59], v[72:75]\n\t"
  "v_mfma_f32_16x16x32_bf16 v[76:79], v[32:35], a[60:63], v[76:79]\n\t"
  "ds_read_b64_tr_b16 v[32:33], v28 offset:24736\n\t"
  "ds_read_b64_tr_b16 v[34:35], v28 offset:28832\n\t"
  "s_waitcnt lgkmcnt(0)\n\t"
  "v_mfma_f32_16x16x32_bf16 v[96:99], v[32:35], a[48:51], v[96:99]\n\t"
  "v_mfma_f32_16x16x32_bf16 v[100:103], v[32:35], a[52:55], v[100:103]\n\t"
  "v_mfma_f32_16x16x32_bf16 v[104:107], v[32:35], a[56:59], v[104:107]\n\t"
  "v_mfma_f32_16x16x32_bf16 v[108:111], v[32:35], a[60:63], v[108:111]\n\t"
  "ds_read_b64_tr_b16 v[32:33], v28 offset:24768\n\t"
  "ds_read_b64_tr_b16 v[34:35], v28 offset:28864\n\t"
  "s_waitcnt lgkmcnt(0)\n\t"
  "v_mfma_f32_16x16x32_bf16 v[128:131], v[32:35], a[48:51], v[128:131]\n\t"
  "v_mfma_f32_16x16x32_bf16 v[132:135], v[32:35], a[52:55], v[132:135]\n\t"
  "v_mfma_f32_16x16x32_bf16 v[136:139], v[32:35], a[56:59], v[136:139]\n\t"
  "v_mfma_f32_16x16x32_bf16 v[140:143], v[32:35], a[60:63], v[140:143]\n\t"
  "s_waitcnt vmcnt(0) lgkmcnt(0)\n\t"
  "s_barrier\n\t"
  "s_add_u32 m0, s69, 16384\n\t"
  "s_bfe_u32 s70, s84, 0x40014\n\t"
  "s_mul_i32 s70, s70, 57344\n\t"
  "buffer_load_dwordx4 v6, s[60:63], s70 offen offset:0  lds\n\t"
  "buffer_load_dwordx4 v7, s[60:63], s70 offen offset:1024  lds\n\t"
  "buffer_load_dwordx4 v8, s[60:63], s70 offen offset:2048  lds\n\t"
  "buffer_load_dwordx4 v9, s[60:63], s70 offen offset:3072  lds\n\t"
  "s_bfe_u32 s71, s84, 0x40014\n\t"
  "s_lshl_b32 s71, s71, 11\n\t"
  "buffer_load_dwordx4 a[96:99], v10, s[40:43], s71 offen offset:0 sc1\n\t"
  "buffer_load_dwordx4 a[100:103], v11, s[40:43], s71 offen offset:0 sc1\n\t"
  "buffer_load_dwordx4 a[104:107], v56, s[40:43], s71 offen offset:0 sc1\n\t"
  "buffer_load_dwordx4 a[108:111], v57, s[40:43], s71 offen offset:0 sc1\n\t"
  "buffer_load_dwordx4 a[112:115], v10, s[40:43], s71 offen offset:1024 sc1\n\t"
  "buffer_load_dwordx4 a[116:119], v11, s[40:43], s71 offen offset:1024 sc1\n\t"
  "buffer_load_dwordx4 a[120:123], v56, s[40:43], s71 offen offset:1024 sc1\n\t"
  "buffer_load_dwordx4 a[124:127], v57, s[40:43], s71 offen offset:1024 sc1\n\t"
  "s_waitcnt vmcnt(0) lgkmcnt(0)\n\t"
  "s_barrier\n\t"
  "ds_read_b64_tr_b16 v[32:33], v28 offset:16384\n\t"
  "ds_read_b64_tr_b16 v[34:35], v28 offset:20480\n\t"
  "s_waitcnt lgkmcnt(0)\n\t"
  "v_mfma_f32_16x16x32_bf16 v[208:211], v[32:35], a[96:99], v[208:211]\n\t"
  "v_mfma_f32_16x16x32_bf16 v[212:215], v[32:35], a[100:103], v[212:215]\n\t"
  "v_mfma_f32_16x16x32_bf16 v[216:219], v[32:35], a[104:107], v[216:219]\n\t"
  "v_mfma_f32_16x16x32_bf16 v[220:223], v[32:35], a[108:111], v[220:223]\n\t"
  "ds_read_b64_tr_b16 v[32:33], v28 offset:16416\n\t"
  "ds_read_b64_tr_b16 v[34:35], v28 offset:20512\n\t"
  "s_waitcnt lgkmcnt(0)\n\t"
  "v_mfma_f32_16x16x32_bf16 v[240:243], v[32:35], a[96:99], v[240:243]\n\t"
  "v_mfma_f32_16x16x32_bf16 v[244:247], v[32:35], a[100:103], v[244:247]\n\t"
  "v_mfma_f32_16x16x32_bf16 v[248:251], v[32:35], a[104:107], v[248:251]\n\t"
  "v_mfma_f32_16x16x32_bf16 v[252:255], v[32:35], a[108:111], v[252:255]\n\t"
  "ds_read_b64_tr_b16 v[32:33], v28 offset:16448\n\t"
  "ds_read_b64_tr_b16 v[34:35], v28 offset:20544\n\t"
  "s_waitcnt lgkmcnt(0)\n\t"
  "v_mfma_f32_16x16x32_bf16 v[112:115], v[32:35], a[96:99], v[112:115]\n\t"
  "v_mfma_f32_16x16x32_bf16 v[116:119], v[32:35], a[100:103], v[116:119]\n\t"
  "v_mfma_f32_16x16x32_bf16 v[120:123], v[32:35], a[104:107], v[120:123]\n\t"
  "v_mfma_f32_16x16x32_bf16 v[124:127], v[32:35], a[108:111], v[124:127]\n\t"
  "ds_read_b64_tr_b16 v[32:33], v28 offset:16480\n\t"
  "ds_read_b64_tr_b16 v[34:35], v28 offset:20576\n\t"
  "s_waitcnt lgkmcnt(0)\n\t"
  "v_mfma_f32_16x16x32_bf16 v[176:179], v[32:35], a[96:99], v[176:179]\n\t"
  "v_mfma_f32_16x16x32_bf16 v[180:183], v[32:35], a[100:103], v[180:183]\n\t"
  "v_mfma_f32_16x16x32_bf16 v[184:187], v[32:35], a[104:107], v[184:187]\n\t"
  "v_mfma_f32_16x16x32_bf16 v[188:191], v[32:35], a[108:111], v[188:191]\n\t"
  "ds_read_b64_tr_b16 v[32:33], v28 offset:16512\n\t"
  "ds_read_b64_tr_b16 v[34:35], v28 offset:20608\n\t"
  "s_waitcnt lgkmcnt(0)\n\t"
  "v_mfma_f32_16x16x32_bf16 v[64:67], v[32:35], a[96:99], v[64:67]\n\t"
  "v_mfma_f32_16x16x32_bf16 v[68:71], v[32:35], a[100:103], v[68:71]\n\t"
  "v_mfma_f32_16x16x32_bf16 v[72:75], v[32:35], a[104:107], v[72:75]\n\t"
  "v_mfma_f32_16x16x32_bf16 v[76:79], v[32:35], a[108:111], v[76:79]\n\t"
  "ds_read_b64_tr_b16 v[32:33], v28 offset:16544\n\t"
  "ds_read_b64_tr_b16 v[34:35], v28 offset:20640\n\t"
  "s_waitcnt lgkmcnt(0)\n\t"
  "v_mfma_f32_16x16x32_bf16 v[96:99], v[32:35], a[96:99], v[96:99]\n\t"
  "v_mfma_f32_16x16x32_bf16 v[100:103], v[32:35], a[100:103], v[100:103]\n\t"
  "v_mfma_f32_16x16x32_bf16 v[104:107], v[32:35], a[104:107], v[104:107]\n\t"
  "v_mfma_f32_16x16x32_bf16 v[108:111], v[32:35], a[108:111], v[108:111]\n\t"
  "ds_read_b64_tr_b16 v[32:33], v28 offset:16576\n\t"
  "ds_read_b64_tr_b16 v[34:35], v28 offset:20672\n\t"
  "s_waitcnt lgkmcnt(0)\n\t"
  "v_mfma_f32_16x16x32_bf16 v[128:131], v[32:35], a[96:99], v[128:131]\n\t"
  "v_mfma_f32_16x16x32_bf16 v[132:135], v[32:35], a[100:103], v[132:135]\n\t"
  "v_mfma_f32_16x16x32_bf16 v[136:139], v[32:35], a[104:107], v[136:139]\n\t"
  "v_mfma_f32_16x16x32_bf16 v[140:143], v[32:35], a[108:111], v[140:143]\n\t"
  "ds_read_b64_tr_b16 v[32:33], v28 offset:24576\n\t"
  "ds_read_b64_tr_b16 v[34:35], v28 offset:28672\n\t"
  "s_waitcnt lgkmcnt(0)\n\t"
  "v_mfma_f32_16x16x32_bf16 v[208:211], v[32:35], a[112:115], v[208:211]\n\t"
  "v_mfma_f32_16x16x32_bf16 v[212:215], v[32:35], a[116:119], v[212:215]\n\t"
  "v_mfma_f32_16x16x32_bf16 v[216:219], v[32:35], a[120:123], v[216:219]\n\t"
  "v_mfma_f32_16x16x32_bf16 v[220:223], v[32:35], a[124:127], v[220:223]\n\t"
  "ds_read_b64_tr_b16 v[32:33], v28 offset:24608\n\t"
  "ds_read_b64_tr_b16 v[34:35], v28 offset:28704\n\t"
  "s_waitcnt lgkmcnt(0)\n\t"
  "v_mfma_f32_16x16x32_bf16 v[240:243], v[32:35], a[112:115], v[240:243]\n\t"
  "v_mfma_f32_16x16x32_bf16 v[244:247], v[32:35], a[116:119], v[244:247]\n\t"
  "v_mfma_f32_16x16x32_bf16 v[248:251], v[32:35], a[120:123], v[248:251]\n\t"
  "v_mfma_f32_16x16x32_bf16 v[252:255], v[32:35], a[124:127], v[252:255]\n\t"
  "ds_read_b64_tr_b16 v[32:33], v28 offset:24640\n\t"
  "ds_read_b64_tr_b16 v[34:35], v28 offset:28736\n\t"
  "s_waitcnt lgkmcnt(0)\n\t"
  "v_mfma_f32_16x16x32_bf16 v[112:115], v[32:35], a[112:115], v[112:115]\n\t"
  "v_mfma_f32_16x16x32_bf16 v[116:119], v[32:35], a[116:119], v[116:119]\n\t"
  "v_mfma_f32_16x16x32_bf16 v[120:123], v[32:35], a[120:123], v[120:123]\n\t"
  "v_mfma_f32_16x16x32_bf16 v[124:127], v[32:35], a[124:127], v[124:127]\n\t"
  "ds_read_b64_tr_b16 v[32:33], v28 offset:24672\n\t"
  "ds_read_b64_tr_b16 v[34:35], v28 offset:28768\n\t"
  "s_waitcnt lgkmcnt(0)\n\t"
  "v_mfma_f32_16x16x32_bf16 v[176:179], v[32:35], a[112:115], v[176:179]\n\t"
  "v_mfma_f32_16x16x32_bf16 v[180:183], v[32:35], a[116:119], v[180:183]\n\t"
  "v_mfma_f32_16x16x32_bf16 v[184:187], v[32:35], a[120:123], v[184:187]\n\t"
  "v_mfma_f32_16x16x32_bf16 v[188:191], v[32:35], a[124:127], v[188:191]\n\t"
  "ds_read_b64_tr_b16 v[32:33], v28 offset:24704\n\t"
  "ds_read_b64_tr_b16 v[34:35], v28 offset:28800\n\t"
  "s_waitcnt lgkmcnt(0)\n\t"
  "v_mfma_f32_16x16x32_bf16 v[64:67], v[32:35], a[112:115], v[64:67]\n\t"
  "v_mfma_f32_16x16x32_bf16 v[68:71], v[32:35], a[116:119], v[68:71]\n\t"
  "v_mfma_f32_16x16x32_bf16 v[72:75], v[32:35], a[120:123], v[72:75]\n\t"
  "v_mfma_f32_16x16x32_bf16 v[76:79], v[32:35], a[124:127], v[76:79]\n\t"
  "ds_read_b64_tr_b16 v[32:33], v28 offset:24736\n\t"
  "ds_read_b64_tr_b16 v[34:35], v28 offset:28832\n\t"
  "s_waitcnt lgkmcnt(0)\n\t"
  "v_mfma_f32_16x16x32_bf16 v[96:99], v[32:35], a[112:115], v[96:99]\n\t"
  "v_mfma_f32_16x16x32_bf16 v[100:103], v[32:35], a[116:119], v[100:103]\n\t"
  "v_mfma_f32_16x16x32_bf16 v[104:107], v[32:35], a[120:123], v[104:107]\n\t"
  "v_mfma_f32_16x16x32_bf16 v[108:111], v[32:35], a[124:127], v[108:111]\n\t"
  "ds_read_b64_tr_b16 v[32:33], v28 offset:24768\n\t"
  "ds_read_b64_tr_b16 v[34:35], v28 offset:28864\n\t"
  "s_waitcnt lgkmcnt(0)\n\t"
  "v_mfma_f32_16x16x32_bf16 v[128:131], v[32:35], a[112:115], v[128:131]\n\t"
  "v_mfma_f32_16x16x32_bf16 v[132:135], v[32:35], a[116:119], v[132:135]\n\t"
  "v_mfma_f32_16x16x32_bf16 v[136:139], v[32:35], a[120:123], v[136:139]\n\t"
  "v_mfma_f32_16x16x32_bf16 v[140:143], v[32:35], a[124:127], v[140:143]\n\t"
  "s_waitcnt vmcnt(0) lgkmcnt(0)\n\t"
  "s_barrier\n\t"
  "s_add_u32 m0, s69, 16384\n\t"
  "s_bfe_u32 s70, s84, 0x40018\n\t"
  "s_mul_i32 s70, s70, 57344\n\t"
  "buffer_load_dwordx4 v6, s[60:63], s70 offen offset:0  lds\n\t"
  "buffer_load_dwordx4 v7, s[60:63], s70 offen offset:1024  lds\n\t"
  "buffer_load_dwordx4 v8, s[60:63], s70 offen offset:2048  lds\n\t"
  "buffer_load_dwordx4 v9, s[60:63], s70 offen offset:3072  lds\n\t"
  "s_bfe_u32 s71, s84, 0x40018\n\t"
  "s_lshl_b32 s71, s71, 11\n\t"
  "buffer_load_dwordx4 a[128:131], v10, s[40:43], s71 offen offset:0 sc1\n\t"
  "buffer_load_dwordx4 a[132:135], v11, s[40:43], s71 offen offset:0 sc1\n\t"
  "buffer_load_dwordx4 a[136:139], v56, s[40:43], s71 offen offset:0 sc1\n\t"
  "buffer_load_dwordx4 a[140:143], v57, s[40:43], s71 offen offset:0 sc1\n\t"
  "buffer_load_dwordx4 a[144:147], v10, s[40:43], s71 offen offset:1024 sc1\n\t"
  "buffer_load_dwordx4 a[148:151], v11, s[40:43], s71 offen offset:1024 sc1\n\t"
  "buffer_load_dwordx4 a[152:155], v56, s[40:43], s71 offen offset:1024 sc1\n\t"
  "buffer_load_dwordx4 a[156:159], v57, s[40:43], s71 offen offset:1024 sc1\n\t"
  "s_waitcnt vmcnt(0) lgkmcnt(0)\n\t"
  "s_barrier\n\t"
  "ds_read_b64_tr_b16 v[32:33], v28 offset:16384\n\t"
  "ds_read_b64_tr_b16 v[34:35], v28 offset:20480\n\t"
  "s_waitcnt lgkmcnt(0)\n\t"
  "v_mfma_f32_16x16x32_bf16 v[208:211], v[32:35], a[128:131], v[208:211]\n\t"
  "v_mfma_f32_16x16x32_bf16 v[212:215], v[32:35], a[132:135], v[212:215]\n\t"
  "v_mfma_f32_16x16x32_bf16 v[216:219], v[32:35], a[136:139], v[216:219]\n\t"
  "v_mfma_f32_16x16x32_bf16 v[220:223], v[32:35], a[140:143], v[220:223]\n\t"
  "ds_read_b64_tr_b16 v[32:33], v28 offset:16416\n\t"
  "ds_read_b64_tr_b16 v[34:35], v28 offset:20512\n\t"
  "s_waitcnt lgkmcnt(0)\n\t"
  "v_mfma_f32_16x16x32_bf16 v[240:243], v[32:35], a[128:131], v[240:243]\n\t"
  "v_mfma_f32_16x16x32_bf16 v[244:247], v[32:35], a[132:135], v[244:247]\n\t"
  "v_mfma_f32_16x16x32_bf16 v[248:251], v[32:35], a[136:139], v[248:251]\n\t"
  "v_mfma_f32_16x16x32_bf16 v[252:255], v[32:35], a[140:143], v[252:255]\n\t"
  "ds_read_b64_tr_b16 v[32:33], v28 offset:16448\n\t"
  "ds_read_b64_tr_b16 v[34:35], v28 offset:20544\n\t"
  "s_waitcnt lgkmcnt(0)\n\t"
  "v_mfma_f32_16x16x32_bf16 v[112:115], v[32:35], a[128:131], v[112:115]\n\t"
  "v_mfma_f32_16x16x32_bf16 v[116:119], v[32:35], a[132:135], v[116:119]\n\t"
  "v_mfma_f32_16x16x32_bf16 v[120:123], v[32:35], a[136:139], v[120:123]\n\t"
  "v_mfma_f32_16x16x32_bf16 v[124:127], v[32:35], a[140:143], v[124:127]\n\t"
  "ds_read_b64_tr_b16 v[32:33], v28 offset:16480\n\t"
  "ds_read_b64_tr_b16 v[34:35], v28 offset:20576\n\t"
  "s_waitcnt lgkmcnt(0)\n\t"
  "v_mfma_f32_16x16x32_bf16 v[176:179], v[32:35], a[128:131], v[176:179]\n\t"
  "v_mfma_f32_16x16x32_bf16 v[180:183], v[32:35], a[132:135], v[180:183]\n\t"
  "v_mfma_f32_16x16x32_bf16 v[184:187], v[32:35], a[136:139], v[184:187]\n\t"
  "v_mfma_f32_16x16x32_bf16 v[188:191], v[32:35], a[140:143], v[188:191]\n\t"
  "ds_read_b64_tr_b16 v[32:33], v28 offset:16512\n\t"
  "ds_read_b64_tr_b16 v[34:35], v28 offset:20608\n\t"
  "s_waitcnt lgkmcnt(0)\n\t"
  "v_mfma_f32_16x16x32_bf16 v[64:67], v[32:35], a[128:131], v[64:67]\n\t"
  "v_mfma_f32_16x16x32_bf16 v[68:71], v[32:35], a[132:135], v[68:71]\n\t"
  "v_mfma_f32_16x16x32_bf16 v[72:75], v[32:35], a[136:139], v[72:75]\n\t"
  "v_mfma_f32_16x16x32_bf16 v[76:79], v[32:35], a[140:143], v[76:79]\n\t"
  "ds_read_b64_tr_b16 v[32:33], v28 offset:16544\n\t"
  "ds_read_b64_tr_b16 v[34:35], v28 offset:20640\n\t"
  "s_waitcnt lgkmcnt(0)\n\t"
  "v_mfma_f32_16x16x32_bf16 v[96:99], v[32:35], a[128:131], v[96:99]\n\t"
  "v_mfma_f32_16x16x32_bf16 v[100:103], v[32:35], a[132:135], v[100:103]\n\t"
  "v_mfma_f32_16x16x32_bf16 v[104:107], v[32:35], a[136:139], v[104:107]\n\t"
  "v_mfma_f32_16x16x32_bf16 v[108:111], v[32:35], a[140:143], v[108:111]\n\t"
  "ds_read_b64_tr_b16 v[32:33], v28 offset:16576\n\t"
  "ds_read_b64_tr_b16 v[34:35], v28 offset:20672\n\t"
  "s_waitcnt lgkmcnt(0)\n\t"
  "v_mfma_f32_16x16x32_bf16 v[128:131], v[32:35], a[128:131], v[128:131]\n\t"
  "v_mfma_f32_16x16x32_bf16 v[132:135], v[32:35], a[132:135], v[132:135]\n\t"
  "v_mfma_f32_16x16x32_bf16 v[136:139], v[32:35], a[136:139], v[136:139]\n\t"
  "v_mfma_f32_16x16x32_bf16 v[140:143], v[32:35], a[140:143], v[140:143]\n\t"
  "ds_read_b64_tr_b16 v[32:33], v28 offset:24576\n\t"
  "ds_read_b64_tr_b16 v[34:35], v28 offset:28672\n\t"
  "s_waitcnt lgkmcnt(0)\n\t"
  "v_mfma_f32_16x16x32_bf16 v[208:211], v[32:35], a[144:147], v[208:211]\n\t"
  "v_mfma_f32_16x16x32_bf16 v[212:215], v[32:35], a[148:151], v[212:215]\n\t"
  "v_mfma_f32_16x16x32_bf16 v[216:219], v[32:35], a[152:155], v[216:219]\n\t"
  "v_mfma_f32_16x16x32_bf16 v[220:223], v[32:35], a[156:159], v[220:223]\n\t"
  "ds_read_b64_tr_b16 v[32:33], v28 offset:24608\n\t"
  "ds_read_b64_tr_b16 v[34:35], v28 offset:28704\n\t"
  "s_waitcnt lgkmcnt(0)\n\t"
  "v_mfma_f32_16x16x32_bf16 v[240:243], v[32:35], a[144:147], v[240:243]\n\t"
  "v_mfma_f32_16x16x32_bf16 v[244:247], v[32:35], a[148:151], v[244:247]\n\t"
  "v_mfma_f32_16x16x32_bf16 v[248:251], v[32:35], a[152:155], v[248:251]\n\t"
  "v_mfma_f32_16x16x32_bf16 v[252:255], v[32:35], a[156:159], v[252:255]\n\t"
  "ds_read_b64_tr_b16 v[32:33], v28 offset:24640\n\t"
  "ds_read_b64_tr_b16 v[34:35], v28 offset:28736\n\t"
  "s_waitcnt lgkmcnt(0)\n\t"
  "v_mfma_f32_16x16x32_bf16 v[112:115], v[32:35], a[144:147], v[112:115]\n\t"
  "v_mfma_f32_16x16x32_bf16 v[116:119], v[32:35], a[148:151], v[116:119]\n\t"
  "v_mfma_f32_16x16x32_bf16 v[120:123], v[32:35], a[152:155], v[120:123]\n\t"
  "v_mfma_f32_16x16x32_bf16 v[124:127], v[32:35], a[156:159], v[124:127]\n\t"
  "ds_read_b64_tr_b16 v[32:33], v28 offset:24672\n\t"
  "ds_read_b64_tr_b16 v[34:35], v28 offset:28768\n\t"
  "s_waitcnt lgkmcnt(0)\n\t"
  "v_mfma_f32_16x16x32_bf16 v[176:179], v[32:35], a[144:147], v[176:179]\n\t"
  "v_mfma_f32_16x16x32_bf16 v[180:183], v[32:35], a[148:151], v[180:183]\n\t"
  "v_mfma_f32_16x16x32_bf16 v[184:187], v[32:35], a[152:155], v[184:187]\n\t"
  "v_mfma_f32_16x16x32_bf16 v[188:191], v[32:35], a[156:159], v[188:191]\n\t"
  "ds_read_b64_tr_b16 v[32:33], v28 offset:24704\n\t"
  "ds_read_b64_tr_b16 v[34:35], v28 offset:28800\n\t"
  "s_waitcnt lgkmcnt(0)\n\t"
  "v_mfma_f32_16x16x32_bf16 v[64:67], v[32:35], a[144:147], v[64:67]\n\t"
  "v_mfma_f32_16x16x32_bf16 v[68:71], v[32:35], a[148:151], v[68:71]\n\t"
  "v_mfma_f32_16x16x32_bf16 v[72:75], v[32:35], a[152:155], v[72:75]\n\t"
  "v_mfma_f32_16x16x32_bf16 v[76:79], v[32:35], a[156:159], v[76:79]\n\t"
  "ds_read_b64_tr_b16 v[32:33], v28 offset:24736\n\t"
  "ds_read_b64_tr_b16 v[34:35], v28 offset:28832\n\t"
  "s_waitcnt lgkmcnt(0)\n\t"
  "v_mfma_f32_16x16x32_bf16 v[96:99], v[32:35], a[144:147], v[96:99]\n\t"
  "v_mfma_f32_16x16x32_bf16 v[100:103], v[32:35], a[148:151], v[100:103]\n\t"
  "v_mfma_f32_16x16x32_bf16 v[104:107], v[32:35], a[152:155], v[104:107]\n\t"
  "v_mfma_f32_16x16x32_bf16 v[108:111], v[32:35], a[156:159], v[108:111]\n\t"
  "ds_read_b64_tr_b16 v[32:33], v28 offset:24768\n\t"
  "ds_read_b64_tr_b16 v[34:35], v28 offset:28864\n\t"
  "s_waitcnt lgkmcnt(0)\n\t"
  "v_mfma_f32_16x16x32_bf16 v[128:131], v[32:35], a[144:147], v[128:131]\n\t"
  "v_mfma_f32_16x16x32_bf16 v[132:135], v[32:35], a[148:151], v[132:135]\n\t"
  "v_mfma_f32_16x16x32_bf16 v[136:139], v[32:35], a[152:155], v[136:139]\n\t"
  "v_mfma_f32_16x16x32_bf16 v[140:143], v[32:35], a[156:159], v[140:143]\n\t"
  "s_waitcnt vmcnt(0) lgkmcnt(0)\n\t"
  "s_barrier\n\t"
  "s_add_u32 m0, s69, 16384\n\t"
  "s_bfe_u32 s70, s84, 0x4001c\n\t"
  "s_mul_i32 s70, s70, 57344\n\t"
  "buffer_load_dwordx4 v6, s[60:63], s70 offen offset:0  lds\n\t"
  "buffer_load_dwordx4 v7, s[60:63], s70 offen offset:1024  lds\n\t"
  "buffer_load_dwordx4 v8, s[60:63], s70 offen offset:2048  lds\n\t"
  "buffer_load_dwordx4 v9, s[60:63], s70 offen offset:3072  lds\n\t"
  "s_bfe_u32 s71, s84, 0x4001c\n\t"
  "s_lshl_b32 s71, s71, 11\n\t"
  "buffer_load_dwordx4 a[160:163], v10, s[40:43], s71 offen offset:0 sc1\n\t"
  "buffer_load_dwordx4 a[164:167], v11, s[40:43], s71 offen offset:0 sc1\n\t"
  "buffer_load_dwordx4 a[168:171], v56, s[40:43], s71 offen offset:0 sc1\n\t"
  "buffer_load_dwordx4 a[172:175], v57, s[40:43], s71 offen offset:0 sc1\n\t"
  "buffer_load_dwordx4 a[176:179], v10, s[40:43], s71 offen offset:1024 sc1\n\t"
  "buffer_load_dwordx4 a[180:183], v11, s[40:43], s71 offen offset:1024 sc1\n\t"
  "buffer_load_dwordx4 a[184:187], v56, s[40:43], s71 offen offset:1024 sc1\n\t"
  "buffer_load_dwordx4 a[188:191], v57, s[40:43], s71 offen offset:1024 sc1\n\t"
  "s_waitcnt vmcnt(0) lgkmcnt(0)\n\t"
  "s_barrier\n\t"
  "ds_read_b64_tr_b16 v[32:33], v28 offset:16384\n\t"
  "ds_read_b64_tr_b16 v[34:35], v28 offset:20480\n\t"
  "s_waitcnt lgkmcnt(0)\n\t"
  "v_mfma_f32_16x16x32_bf16 v[208:211], v[32:35], a[160:163], v[208:211]\n\t"
  "v_mfma_f32_16x16x32_bf16 v[212:215], v[32:35], a[164:167], v[212:215]\n\t"
  "v_mfma_f32_16x16x32_bf16 v[216:219], v[32:35], a[168:171], v[216:219]\n\t"
  "v_mfma_f32_16x16x32_bf16 v[220:223], v[32:35], a[172:175], v[220:223]\n\t"
  "ds_read_b64_tr_b16 v[32:33], v28 offset:16416\n\t"
  "ds_read_b64_tr_b16 v[34:35], v28 offset:20512\n\t"
  "s_waitcnt lgkmcnt(0)\n\t"
  "v_mfma_f32_16x16x32_bf16 v[240:243], v[32:35], a[160:163], v[240:243]\n\t"
  "v_mfma_f32_16x16x32_bf16 v[244:247], v[32:35], a[164:167], v[244:247]\n\t"
  "v_mfma_f32_16x16x32_bf16 v[248:251], v[32:35], a[168:171], v[248:251]\n\t"
  "v_mfma_f32_16x16x32_bf16 v[252:255], v[32:35], a[172:175], v[252:255]\n\t"
  "ds_read_b64_tr_b16 v[32:33], v28 offset:16448\n\t"
  "ds_read_b64_tr_b16 v[34:35], v28 offset:20544\n\t"
  "s_waitcnt lgkmcnt(0)\n\t"
  "v_mfma_f32_16x16x32_bf16 v[112:115], v[32:35], a[160:163], v[112:115]\n\t"
  "v_mfma_f32_16x16x32_bf16 v[116:119], v[32:35], a[164:167], v[116:119]\n\t"
  "v_mfma_f32_16x16x32_bf16 v[120:123], v[32:35], a[168:171], v[120:123]\n\t"
  "v_mfma_f32_16x16x32_bf16 v[124:127], v[32:35], a[172:175], v[124:127]\n\t"
  "ds_read_b64_tr_b16 v[32:33], v28 offset:16480\n\t"
  "ds_read_b64_tr_b16 v[34:35], v28 offset:20576\n\t"
  "s_waitcnt lgkmcnt(0)\n\t"
  "v_mfma_f32_16x16x32_bf16 v[176:179], v[32:35], a[160:163], v[176:179]\n\t"
  "v_mfma_f32_16x16x32_bf16 v[180:183], v[32:35], a[164:167], v[180:183]\n\t"
  "v_mfma_f32_16x16x32_bf16 v[184:187], v[32:35], a[168:171], v[184:187]\n\t"
  "v_mfma_f32_16x16x32_bf16 v[188:191], v[32:35], a[172:175], v[188:191]\n\t"
  "ds_read_b64_tr_b16 v[32:33], v28 offset:16512\n\t"
  "ds_read_b64_tr_b16 v[34:35], v28 offset:20608\n\t"
  "s_waitcnt lgkmcnt(0)\n\t"
  "v_mfma_f32_16x16x32_bf16 v[64:67], v[32:35], a[160:163], v[64:67]\n\t"
  "v_mfma_f32_16x16x32_bf16 v[68:71], v[32:35], a[164:167], v[68:71]\n\t"
  "v_mfma_f32_16x16x32_bf16 v[72:75], v[32:35], a[168:171], v[72:75]\n\t"
  "v_mfma_f32_16x16x32_bf16 v[76:79], v[32:35], a[172:175], v[76:79]\n\t"
  "ds_read_b64_tr_b16 v[32:33], v28 offset:16544\n\t"
  "ds_read_b64_tr_b16 v[34:35], v28 offset:20640\n\t"
  "s_waitcnt lgkmcnt(0)\n\t"
  "v_mfma_f32_16x16x32_bf16 v[96:99], v[32:35], a[160:163], v[96:99]\n\t"
  "v_mfma_f32_16x16x32_bf16 v[100:103], v[32:35], a[164:167], v[100:103]\n\t"
  "v_mfma_f32_16x16x32_bf16 v[104:107], v[32:35], a[168:171], v[104:107]\n\t"
  "v_mfma_f32_16x16x32_bf16 v[108:111], v[32:35], a[172:175], v[108:111]\n\t"
  "ds_read_b64_tr_b16 v[32:33], v28 offset:16576\n\t"
  "ds_read_b64_tr_b16 v[34:35], v28 offset:20672\n\t"
  "s_waitcnt lgkmcnt(0)\n\t"
  "v_mfma_f32_16x16x32_bf16 v[128:131], v[32:35], a[160:163], v[128:131]\n\t"
  "v_mfma_f32_16x16x32_bf16 v[132:135], v[32:35], a[164:167], v[132:135]\n\t"
  "v_mfma_f32_16x16x32_bf16 v[136:139], v[32:35], a[168:171], v[136:139]\n\t"
  "v_mfma_f32_16x16x32_bf16 v[140:143], v[32:35], a[172:175], v[140:143]\n\t"
  "ds_read_b64_tr_b16 v[32:33], v28 offset:24576\n\t"
  "ds_read_b64_tr_b16 v[34:35], v28 offset:28672\n\t"
  "s_waitcnt lgkmcnt(0)\n\t"
  "v_mfma_f32_16x16x32_bf16 v[208:211], v[32:35], a[176:179], v[208:211]\n\t"
  "v_mfma_f32_16x16x32_bf16 v[212:215], v[32:35], a[180:183], v[212:215]\n\t"
  "v_mfma_f32_16x16x32_bf16 v[216:219], v[32:35], a[184:187], v[216:219]\n\t"
  "v_mfma_f32_16x16x32_bf16 v[220:223], v[32:35], a[188:191], v[220:223]\n\t"
  "ds_read_b64_tr_b16 v[32:33], v28 offset:24608\n\t"
  "ds_read_b64_tr_b16 v[34:35], v28 offset:28704\n\t"
  "s_waitcnt lgkmcnt(0)\n\t"
  "v_mfma_f32_16x16x32_bf16 v[240:243], v[32:35], a[176:179], v[240:243]\n\t"
  "v_mfma_f32_16x16x32_bf16 v[244:247], v[32:35], a[180:183], v[244:247]\n\t"
  "v_mfma_f32_16x16x32_bf16 v[248:251], v[32:35], a[184:187], v[248:251]\n\t"
  "v_mfma_f32_16x16x32_bf16 v[252:255], v[32:35], a[188:191], v[252:255]\n\t"
  "ds_read_b64_tr_b16 v[32:33], v28 offset:24640\n\t"
  "ds_read_b64_tr_b16 v[34:35], v28 offset:28736\n\t"
  "s_waitcnt lgkmcnt(0)\n\t"
  "v_mfma_f32_16x16x32_bf16 v[112:115], v[32:35], a[176:179], v[112:115]\n\t"
  "v_mfma_f32_16x16x32_bf16 v[116:119], v[32:35], a[180:183], v[116:119]\n\t"
  "v_mfma_f32_16x16x32_bf16 v[120:123], v[32:35], a[184:187], v[120:123]\n\t"
  "v_mfma_f32_16x16x32_bf16 v[124:127], v[32:35], a[188:191], v[124:127]\n\t"
  "ds_read_b64_tr_b16 v[32:33], v28 offset:24672\n\t"
  "ds_read_b64_tr_b16 v[34:35], v28 offset:28768\n\t"
  "s_waitcnt lgkmcnt(0)\n\t"
  "v_mfma_f32_16x16x32_bf16 v[176:179], v[32:35], a[176:179], v[176:179]\n\t"
  "v_mfma_f32_16x16x32_bf16 v[180:183], v[32:35], a[180:183], v[180:183]\n\t"
  "v_mfma_f32_16x16x32_bf16 v[184:187], v[32:35], a[184:187], v[184:187]\n\t"
  "v_mfma_f32_16x16x32_bf16 v[188:191], v[32:35], a[188:191], v[188:191]\n\t"
  "ds_read_b64_tr_b16 v[32:33], v28 offset:24704\n\t"
  "ds_read_b64_tr_b16 v[34:35], v28 offset:28800\n\t"
  "s_waitcnt lgkmcnt(0)\n\t"
  "v_mfma_f32_16x16x32_bf16 v[64:67], v[32:35], a[176:179], v[64:67]\n\t"
  "v_mfma_f32_16x16x32_bf16 v[68:71], v[32:35], a[180:183], v[68:71]\n\t"
  "v_mfma_f32_16x16x32_bf16 v[72:75], v[32:35], a[184:187], v[72:75]\n\t"
  "v_mfma_f32_16x16x32_bf16 v[76:79], v[32:35], a[188:191], v[76:79]\n\t"
  "ds_read_b64_tr_b16 v[32:33], v28 offset:24736\n\t"
  "ds_read_b64_tr_b16 v[34:35], v28 offset:28832\n\t"
  "s_waitcnt lgkmcnt(0)\n\t"
  "v_mfma_f32_16x16x32_bf16 v[96:99], v[32:35], a[176:179], v[96:99]\n\t"
  "v_mfma_f32_16x16x32_bf16 v[100:103], v[32:35], a[180:183], v[100:103]\n\t"
  "v_mfma_f32_16x16x32_bf16 v[104:107], v[32:35], a[184:187], v[104:107]\n\t"
  "v_mfma_f32_16x16x32_bf16 v[108:111], v[32:35], a[188:191], v[108:111]\n\t"
  "ds_read_b64_tr_b16 v[32:33], v28 offset:24768\n\t"
  "ds_read_b64_tr_b16 v[34:35], v28 offset:28864\n\t"
  "s_waitcnt lgkmcnt(0)\n\t"
  "v_mfma_f32_16x16x32_bf16 v[128:131], v[32:35], a[176:179], v[128:131]\n\t"
  "v_mfma_f32_16x16x32_bf16 v[132:135], v[32:35], a[180:183], v[132:135]\n\t"
  "v_mfma_f32_16x16x32_bf16 v[136:139], v[32:35], a[184:187], v[136:139]\n\t"
  "v_mfma_f32_16x16x32_bf16 v[140:143], v[32:35], a[188:191], v[140:143]\n\t"
  "s_waitcnt vmcnt(0) lgkmcnt(0)\n\t"
  "s_barrier\n\t"
  "s_add_u32 m0, s69, 16384\n\t"
  "s_bfe_u32 s70, s85, 0x40000\n\t"
  "s_mul_i32 s70, s70, 57344\n\t"
  "buffer_load_dwordx4 v6, s[60:63], s70 offen offset:0  lds\n\t"
  "buffer_load_dwordx4 v7, s[60:63], s70 offen offset:1024  lds\n\t"
  "buffer_load_dwordx4 v8, s[60:63], s70 offen offset:2048  lds\n\t"
  "buffer_load_dwordx4 v9, s[60:63], s70 offen offset:3072  lds\n\t"
  "s_bfe_u32 s71, s85, 0x40000\n\t"
  "s_lshl_b32 s71, s71, 11\n\t"
  "buffer_load_dwordx4 a[64:67], v10, s[40:43], s71 offen offset:0 sc1\n\t"
  "buffer_load_dwordx4 a[68:71], v11, s[40:43], s71 offen offset:0 sc1\n\t"
  "buffer_load_dwordx4 a[72:75], v56, s[40:43], s71 offen offset:0 sc1\n\t"
  "buffer_load_dwordx4 a[76:79], v57, s[40:43], s71 offen offset:0 sc1\n\t"
  "buffer_load_dwordx4 a[80:83], v10, s[40:43], s71 offen offset:1024 sc1\n\t"
  "buffer_load_dwordx4 a[84:87], v11, s[40:43], s71 offen offset:1024 sc1\n\t"
  "buffer_load_dwordx4 a[88:91], v56, s[40:43], s71 offen offset:1024 sc1\n\t"
  "buffer_load_dwordx4 a[92:95], v57, s[40:43], s71 offen offset:1024 sc1\n\t"
  "s_waitcnt vmcnt(0) lgkmcnt(0)\n\t"
  "s_barrier\n\t"
  "ds_read_b64_tr_b16 v[32:33], v28 offset:16384\n\t"
  "ds_read_b64_tr_b16 v[34:35], v28 offset:20480\n\t"
  "s_waitcnt lgkmcnt(0)\n\t"
  "v_mfma_f32_16x16x32_bf16 v[208:211], v[32:35], a[64:67], v[208:211]\n\t"
  "v_mfma_f32_16x16x32_bf16 v[212:215], v[32:35], a[68:71], v[212:215]\n\t"
  "v_mfma_f32_16x16x32_bf16 v[216:219], v[32:35], a[72:75], v[216:219]\n\t"
  "v_mfma_f32_16x16x32_bf16 v[220:223], v[32:35], a[76:79], v[220:223]\n\t"
  "ds_read_b64_tr_b16 v[32:33], v28 offset:16416\n\t"
  "ds_read_b64_tr_b16 v[34:35], v28 offset:20512\n\t"
  "s_waitcnt lgkmcnt(0)\n\t"
  "v_mfma_f32_16x16x32_bf16 v[240:243], v[32:35], a[64:67], v[240:243]\n\t"
  "v_mfma_f32_16x16x32_bf16 v[244:247], v[32:35], a[68:71], v[244:247]\n\t"
  "v_mfma_f32_16x16x32_bf16 v[248:251], v[32:35], a[72:75], v[248:251]\n\t"
  "v_mfma_f32_16x16x32_bf16 v[252:255], v[32:35], a[76:79], v[252:255]\n\t"
  "ds_read_b64_tr_b16 v[32:33], v28 offset:16448\n\t"
  "ds_read_b64_tr_b16 v[34:35], v28 offset:20544\n\t"
  "s_waitcnt lgkmcnt(0)\n\t"
  "v_mfma_f32_16x16x32_bf16 v[112:115], v[32:35], a[64:67], v[112:115]\n\t"
  "v_mfma_f32_16x16x32_bf16 v[116:119], v[32:35], a[68:71], v[116:119]\n\t"
  "v_mfma_f32_16x16x32_bf16 v[120:123], v[32:35], a[72:75], v[120:123]\n\t"
  "v_mfma_f32_16x16x32_bf16 v[124:127], v[32:35], a[76:79], v[124:127]\n\t"
  "ds_read_b64_tr_b16 v[32:33], v28 offset:16480\n\t"
  "ds_read_b64_tr_b16 v[34:35], v28 offset:20576\n\t"
  "s_waitcnt lgkmcnt(0)\n\t"
  "v_mfma_f32_16x16x32_bf16 v[176:179], v[32:35], a[64:67], v[176:179]\n\t"
  "v_mfma_f32_16x16x32_bf16 v[180:183], v[32:35], a[68:71], v[180:183]\n\t"
  "v_mfma_f32_16x16x32_bf16 v[184:187], v[32:35], a[72:75], v[184:187]\n\t"
  "v_mfma_f32_16x16x32_bf16 v[188:191], v[32:35], a[76:79], v[188:191]\n\t"
  "ds_read_b64_tr_b16 v[32:33], v28 offset:16512\n\t"
  "ds_read_b64_tr_b16 v[34:35], v28 offset:20608\n\t"
  "s_waitcnt lgkmcnt(0)\n\t"
  "v_mfma_f32_16x16x32_bf16 v[64:67], v[32:35], a[64:67], v[64:67]\n\t"
  "v_mfma_f32_16x16x32_bf16 v[68:71], v[32:35], a[68:71], v[68:71]\n\t"
  "v_mfma_f32_16x16x32_bf16 v[72:75], v[32:35], a[72:75], v[72:75]\n\t"
  "v_mfma_f32_16x16x32_bf16 v[76:79], v[32:35], a[76:79], v[76:79]\n\t"
  "ds_read_b64_tr_b16 v[32:33], v28 offset:16544\n\t"
  "ds_read_b64_tr_b16 v[34:35], v28 offset:20640\n\t"
  "s_waitcnt lgkmcnt(0)\n\t"
  "v_mfma_f32_16x16x32_bf16 v[96:99], v[32:35], a[64:67], v[96:99]\n\t"
  "v_mfma_f32_16x16x32_bf16 v[100:103], v[32:35], a[68:71], v[100:103]\n\t"
  "v_mfma_f32_16x16x32_bf16 v[104:107], v[32:35], a[72:75], v[104:107]\n\t"
  "v_mfma_f32_16x16x32_bf16 v[108:111], v[32:35], a[76:79], v[108:111]\n\t"
  "ds_read_b64_tr_b16 v[32:33], v28 offset:16576\n\t"
  "ds_read_b64_tr_b16 v[34:35], v28 offset:20672\n\t"
  "s_waitcnt lgkmcnt(0)\n\t"
  "v_mfma_f32_16x16x32_bf16 v[128:131], v[32:35], a[64:67], v[128:131]\n\t"
  "v_mfma_f32_16x16x32_bf16 v[132:135], v[32:35], a[68:71], v[132:135]\n\t"
  "v_mfma_f32_16x16x32_bf16 v[136:139], v[32:35], a[72:75], v[136:139]\n\t"
  "v_mfma_f32_16x16x32_bf16 v[140:143], v[32:35], a[76:79], v[140:143]\n\t"
  "ds_read_b64_tr_b16 v[32:33], v28 offset:24576\n\t"
  "ds_read_b64_tr_b16 v[34:35], v28 offset:28672\n\t"
  "s_waitcnt lgkmcnt(0)\n\t"
  "v_mfma_f32_16x16x32_bf16 v[208:211], v[32:35], a[80:83], v[208:211]\n\t"
  "v_mfma_f32_16x16x32_bf16 v[212:215], v[32:35], a[84:87], v[212:215]\n\t"
  "v_mfma_f32_16x16x32_bf16 v[216:219], v[32:35], a[88:91], v[216:219]\n\t"
  "v_mfma_f32_16x16x32_bf16 v[220:223], v[32:35], a[92:95], v[220:223]\n\t"
  "ds_read_b64_tr_b16 v[32:33], v28 offset:24608\n\t"
  "ds_read_b64_tr_b16 v[34:35], v28 offset:28704\n\t"
  "s_waitcnt lgkmcnt(0)\n\t"
  "v_mfma_f32_16x16x32_bf16 v[240:243], v[32:35], a[80:83], v[240:243]\n\t"
  "v_mfma_f32_16x16x32_bf16 v[244:247], v[32:35], a[84:87], v[244:247]\n\t"
  "v_mfma_f32_16x16x32_bf16 v[248:251], v[32:35], a[88:91], v[248:251]\n\t"
  "v_mfma_f32_16x16x32_bf16 v[252:255], v[32:35], a[92:95], v[252:255]\n\t"
  "ds_read_b64_tr_b16 v[32:33], v28 offset:24640\n\t"
  "ds_read_b64_tr_b16 v[34:35], v28 offset:28736\n\t"
  "s_waitcnt lgkmcnt(0)\n\t"
  "v_mfma_f32_16x16x32_bf16 v[112:115], v[32:35], a[80:83], v[112:115]\n\t"
  "v_mfma_f32_16x16x32_bf16 v[116:119], v[32:35], a[84:87], v[116:119]\n\t"
  "v_mfma_f32_16x16x32_bf16 v[120:123], v[32:35], a[88:91], v[120:123]\n\t"
  "v_mfma_f32_16x16x32_bf16 v[124:127], v[32:35], a[92:95], v[124:127]\n\t"
  "ds_read_b64_tr_b16 v[32:33], v28 offset:24672\n\t"
  "ds_read_b64_tr_b16 v[34:35], v28 offset:28768\n\t"
  "s_waitcnt lgkmcnt(0)\n\t"
  "v_mfma_f32_16x16x32_bf16 v[176:179], v[32:35], a[80:83], v[176:179]\n\t"
  "v_mfma_f32_16x16x32_bf16 v[180:183], v[32:35], a[84:87], v[180:183]\n\t"
  "v_mfma_f32_16x16x32_bf16 v[184:187], v[32:35], a[88:91], v[184:187]\n\t"
  "v_mfma_f32_16x16x32_bf16 v[188:191], v[32:35], a[92:95], v[188:191]\n\t"
  "ds_read_b64_tr_b16 v[32:33], v28 offset:24704\n\t"
  "ds_read_b64_tr_b16 v[34:35], v28 offset:28800\n\t"
  "s_waitcnt lgkmcnt(0)\n\t"
  "v_mfma_f32_16x16x32_bf16 v[64:67], v[32:35], a[80:83], v[64:67]\n\t"
  "v_mfma_f32_16x16x32_bf16 v[68:71], v[32:35], a[84:87], v[68:71]\n\t"
  "v_mfma_f32_16x16x32_bf16 v[72:75], v[32:35], a[88:91], v[72:75]\n\t"
  "v_mfma_f32_16x16x32_bf16 v[76:79], v[32:35], a[92:95], v[76:79]\n\t"
  "ds_read_b64_tr_b16 v[32:33], v28 offset:24736\n\t"
  "ds_read_b64_tr_b16 v[34:35], v28 offset:28832\n\t"
  "s_waitcnt lgkmcnt(0)\n\t"
  "v_mfma_f32_16x16x32_bf16 v[96:99], v[32:35], a[80:83], v[96:99]\n\t"
  "v_mfma_f32_16x16x32_bf16 v[100:103], v[32:35], a[84:87], v[100:103]\n\t"
  "v_mfma_f32_16x16x32_bf16 v[104:107], v[32:35], a[88:91], v[104:107]\n\t"
  "v_mfma_f32_16x16x32_bf16 v[108:111], v[32:35], a[92:95], v[108:111]\n\t"
  "ds_read_b64_tr_b16 v[32:33], v28 offset:24768\n\t"
  "ds_read_b64_tr_b16 v[34:35], v28 offset:28864\n\t"
  "s_waitcnt lgkmcnt(0)\n\t"
  "v_mfma_f32_16x16x32_bf16 v[128:131], v[32:35], a[80:83], v[128:131]\n\t"
  "v_mfma_f32_16x16x32_bf16 v[132:135], v[32:35], a[84:87], v[132:135]\n\t"
  "v_mfma_f32_16x16x32_bf16 v[136:139], v[32:35], a[88:91], v[136:139]\n\t"
  "v_mfma_f32_16x16x32_bf16 v[140:143], v[32:35], a[92:95], v[140:143]\n\t"
  "s_waitcnt vmcnt(0) lgkmcnt(0)\n\t"
  "s_barrier\n\t"
  "s_add_u32 m0, s69, 16384\n\t"
  "s_bfe_u32 s70, s85, 0x40004\n\t"
  "s_mul_i32 s70, s70, 57344\n\t"
  "buffer_load_dwordx4 v6, s[60:63], s70 offen offset:0  lds\n\t"
  "buffer_load_dwordx4 v7, s[60:63], s70 offen offset:1024  lds\n\t"
  "buffer_load_dwordx4 v8, s[60:63], s70 offen offset:2048  lds\n\t"
  "buffer_load_dwordx4 v9, s[60:63], s70 offen offset:3072  lds\n\t"
  "s_bfe_u32 s71, s85, 0x40004\n\t"
  "s_lshl_b32 s71, s71, 11\n\t"
  "buffer_load_dwordx4 a[192:195], v10, s[40:43], s71 offen offset:0 sc1\n\t"
  "buffer_load_dwordx4 a[100:103], v11, s[40:43], s71 offen offset:0 sc1\n\t"
  "buffer_load_dwordx4 a[104:107], v56, s[40:43], s71 offen offset:0 sc1\n\t"
  "buffer_load_dwordx4 a[108:111], v57, s[40:43], s71 offen offset:0 sc1\n\t"
  "buffer_load_dwordx4 a[196:199], v10, s[40:43], s71 offen offset:1024 sc1\n\t"
  "buffer_load_dwordx4 a[116:119], v11, s[40:43], s71 offen offset:1024 sc1\n\t"
  "buffer_load_dwordx4 a[120:123], v56, s[40:43], s71 offen offset:1024 sc1\n\t"
  "buffer_load_dwordx4 a[124:127], v57, s[40:43], s71 offen offset:1024 sc1\n\t"
  "s_waitcnt vmcnt(0) lgkmcnt(0)\n\t"
  "s_barrier\n\t"
  "ds_read_b64_tr_b16 v[32:33], v28 offset:16384\n\t"
  "ds_read_b64_tr_b16 v[34:35], v28 offset:20480\n\t"
  "s_waitcnt lgkmcnt(0)\n\t"
  "v_mfma_f32_16x16x32_bf16 v[208:211], v[32:35], a[192:195], v[208:211]\n\t"
  "v_mfma_f32_16x16x32_bf16 v[212:215], v[32:35], a[100:103], v[212:215]\n\t"
  "v_mfma_f32_16x16x32_bf16 v[216:219], v[32:35], a[104:107], v[216:219]\n\t"
  "v_mfma_f32_16x16x32_bf16 v[220:223], v[32:35], a[108:111], v[220:223]\n\t"
  "ds_read_b64_tr_b16 v[32:33], v28 offset:16416\n\t"
  "ds_read_b64_tr_b16 v[34:35], v28 offset:20512\n\t"
  "s_waitcnt lgkmcnt(0)\n\t"
  "v_mfma_f32_16x16x32_bf16 v[240:243], v[32:35], a[192:195], v[240:243]\n\t"
  "v_mfma_f32_16x16x32_bf16 v[244:247], v[32:35], a[100:103], v[244:247]\n\t"
  "v_mfma_f32_16x16x32_bf16 v[248:251], v[32:35], a[104:107], v[248:251]\n\t"
  "v_mfma_f32_16x16x32_bf16 v[252:255], v[32:35], a[108:111], v[252:255]\n\t"
  "ds_read_b64_tr_b16 v[32:33], v28 offset:16448\n\t"
  "ds_read_b64_tr_b16 v[34:35], v28 offset:20544\n\t"
  "s_waitcnt lgkmcnt(0)\n\t"
  "v_mfma_f32_16x16x32_bf16 v[112:115], v[32:35], a[192:195], v[112:115]\n\t"
  "v_mfma_f32_16x16x32_bf16 v[116:119], v[32:35], a[100:103], v[116:119]\n\t"
  "v_mfma_f32_16x16x32_bf16 v[120:123], v[32:35], a[104:107], v[120:123]\n\t"
  "v_mfma_f32_16x16x32_bf16 v[124:127], v[32:35], a[108:111], v[124:127]\n\t"
  "ds_read_b64_tr_b16 v[32:33], v28 offset:16480\n\t"
  "ds_read_b64_tr_b16 v[34:35], v28 offset:20576\n\t"
  "s_waitcnt lgkmcnt(0)\n\t"
  "v_mfma_f32_16x16x32_bf16 v[176:179], v[32:35], a[192:195], v[176:179]\n\t"
  "v_mfma_f32_16x16x32_bf16 v[180:183], v[32:35], a[100:103], v[180:183]\n\t"
  "v_mfma_f32_16x16x32_bf16 v[184:187], v[32:35], a[104:107], v[184:187]\n\t"
  "v_mfma_f32_16x16x32_bf16 v[188:191], v[32:35], a[108:111], v[188:191]\n\t"
  "ds_read_b64_tr_b16 v[32:33], v28 offset:16512\n\t"
  "ds_read_b64_tr_b16 v[34:35], v28 offset:20608\n\t"
  "s_waitcnt lgkmcnt(0)\n\t"
  "v_mfma_f32_16x16x32_bf16 v[64:67], v[32:35], a[192:195], v[64:67]\n\t"
  "v_mfma_f32_16x16x32_bf16 v[68:71], v[32:35], a[100:103], v[68:71]\n\t"
  "v_mfma_f32_16x16x32_bf16 v[72:75], v[32:35], a[104:107], v[72:75]\n\t"
  "v_mfma_f32_16x16x32_bf16 v[76:79], v[32:35], a[108:111], v[76:79]\n\t"
  "ds_read_b64_tr_b16 v[32:33], v28 offset:16544\n\t"
  "ds_read_b64_tr_b16 v[34:35], v28 offset:20640\n\t"
  "s_waitcnt lgkmcnt(0)\n\t"
  "v_mfma_f32_16x16x32_bf16 v[96:99], v[32:35], a[192:195], v[96:99]\n\t"
  "v_mfma_f32_16x16x32_bf16 v[100:103], v[32:35], a[100:103], v[100:103]\n\t"
  "v_mfma_f32_16x16x32_bf16 v[104:107], v[32:35], a[104:107], v[104:107]\n\t"
  "v_mfma_f32_16x16x32_bf16 v[108:111], v[32:35], a[108:111], v[108:111]\n\t"
  "ds_read_b64_tr_b16 v[32:33], v28 offset:16576\n\t"
  "ds_read_b64_tr_b16 v[34:35], v28 offset:20672\n\t"
  "s_waitcnt lgkmcnt(0)\n\t"
  "v_mfma_f32_16x16x32_bf16 v[128:131], v[32:35], a[192:195], v[128:131]\n\t"
  "v_mfma_f32_16x16x32_bf16 v[132:135], v[32:35], a[100:103], v[132:135]\n\t"
  "v_mfma_f32_16x16x32_bf16 v[136:139], v[32:35], a[104:107], v[136:139]\n\t"
  "v_mfma_f32_16x16x32_bf16 v[140:143], v[32:35], a[108:111], v[140:143]\n\t"
  "ds_read_b64_tr_b16 v[32:33], v28 offset:24576\n\t"
  "ds_read_b64_tr_b16 v[34:35], v28 offset:28672\n\t"
  "s_waitcnt lgkmcnt(0)\n\t"
  "v_mfma_f32_16x16x32_bf16 v[208:211], v[32:35], a[196:199], v[208:211]\n\t"
  "v_mfma_f32_16x16x32_bf16 v[212:215], v[32:35], a[116:119], v[212:215]\n\t"
  "v_mfma_f32_16x16x32_bf16 v[216:219], v[32:35], a[120:123], v[216:219]\n\t"
  "v_mfma_f32_16x16x32_bf16 v[220:223], v[32:35], a[124:127], v[220:223]\n\t"
  "ds_read_b64_tr_b16 v[32:33], v28 offset:24608\n\t"
  "ds_read_b64_tr_b16 v[34:35], v28 offset:28704\n\t"
  "s_waitcnt lgkmcnt(0)\n\t"
  "v_mfma_f32_16x16x32_bf16 v[240:243], v[32:35], a[196:199], v[240:243]\n\t"
  "v_mfma_f32_16x16x32_bf16 v[244:247], v[32:35], a[116:119], v[244:247]\n\t"
  "v_mfma_f32_16x16x32_bf16 v[248:251], v[32:35], a[120:123], v[248:251]\n\t"
  "v_mfma_f32_16x16x32_bf16 v[252:255], v[32:35], a[124:127], v[252:255]\n\t"
  "ds_read_b64_tr_b16 v[32:33], v28 offset:24640\n\t"
  "ds_read_b64_tr_b16 v[34:35], v28 offset:28736\n\t"
  "s_waitcnt lgkmcnt(0)\n\t"
  "v_mfma_f32_16x16x32_bf16 v[112:115], v[32:35], a[196:199], v[112:115]\n\t"
  "v_mfma_f32_16x16x32_bf16 v[116:119], v[32:35], a[116:119], v[116:119]\n\t"
  "v_mfma_f32_16x16x32_bf16 v[120:123], v[32:35], a[120:123], v[120:123]\n\t"
  "v_mfma_f32_16x16x32_bf16 v[124:127], v[32:35], a[124:127], v[124:127]\n\t"
  "ds_read_b64_tr_b16 v[32:33], v28 offset:24672\n\t"
  "ds_read_b64_tr_b16 v[34:35], v28 offset:28768\n\t"
  "s_waitcnt lgkmcnt(0)\n\t"
  "v_mfma_f32_16x16x32_bf16 v[176:179], v[32:35], a[196:199], v[176:179]\n\t"
  "v_mfma_f32_16x16x32_bf16 v[180:183], v[32:35], a[116:119], v[180:183]\n\t"
  "v_mfma_f32_16x16x32_bf16 v[184:187], v[32:35], a[120:123], v[184:187]\n\t"
  "v_mfma_f32_16x16x32_bf16 v[188:191], v[32:35], a[124:127], v[188:191]\n\t"
  "ds_read_b64_tr_b16 v[32:33], v28 offset:24704\n\t"
  "ds_read_b64_tr_b16 v[34:35], v28 offset:28800\n\t"
  "s_waitcnt lgkmcnt(0)\n\t"
  "v_mfma_f32_16x16x32_bf16 v[64:67], v[32:35], a[196:199], v[64:67]\n\t"
  "v_mfma_f32_16x16x32_bf16 v[68:71], v[32:35], a[116:119], v[68:71]\n\t"
  "v_mfma_f32_16x16x32_bf16 v[72:75], v[32:35], a[120:123], v[72:75]\n\t"
  "v_mfma_f32_16x16x32_bf16 v[76:79], v[32:35], a[124:127], v[76:79]\n\t"
  "ds_read_b64_tr_b16 v[32:33], v28 offset:24736\n\t"
  "ds_read_b64_tr_b16 v[34:35], v28 offset:28832\n\t"
  "s_waitcnt lgkmcnt(0)\n\t"
  "v_mfma_f32_16x16x32_bf16 v[96:99], v[32:35], a[196:199], v[96:99]\n\t"
  "v_mfma_f32_16x16x32_bf16 v[100:103], v[32:35], a[116:119], v[100:103]\n\t"
  "v_mfma_f32_16x16x32_bf16 v[104:107], v[32:35], a[120:123], v[104:107]\n\t"
  "v_mfma_f32_16x16x32_bf16 v[108:111], v[32:35], a[124:127], v[108:111]\n\t"
  "ds_read_b64_tr_b16 v[32:33], v28 offset:24768\n\t"
  "ds_read_b64_tr_b16 v[34:35], v28 offset:28864\n\t"
  "s_waitcnt lgkmcnt(0)\n\t"
  "v_mfma_f32_16x16x32_bf16 v[128:131], v[32:35], a[196:199], v[128:131]\n\t"
  "v_mfma_f32_16x16x32_bf16 v[132:135], v[32:35], a[116:119], v[132:135]\n\t"
  "v_mfma_f32_16x16x32_bf16 v[136:139], v[32:35], a[120:123], v[136:139]\n\t"
  "v_mfma_f32_16x16x32_bf16 v[140:143], v[32:35], a[124:127], v[140:143]\n\t"
  "s_waitcnt vmcnt(0) lgkmcnt(0)\n\t"
  "s_barrier\n\t"
  "s_add_u32 m0, s69, 16384\n\t"
  "s_bfe_u32 s70, s85, 0x40008\n\t"
  "s_mul_i32 s70, s70, 57344\n\t"
  "buffer_load_dwordx4 v6, s[60:63], s70 offen offset:0  lds\n\t"
  "buffer_load_dwordx4 v7, s[60:63], s70 offen offset:1024  lds\n\t"
  "buffer_load_dwordx4 v8, s[60:63], s70 offen offset:2048  lds\n\t"
  "buffer_load_dwordx4 v9, s[60:63], s70 offen offset:3072  lds\n\t"
  "s_bfe_u32 s71, s85, 0x40008\n\t"
  "s_lshl_b32 s71, s71, 11\n\t"
  "buffer_load_dwordx4 a[200:203], v10, s[40:43], s71 offen offset:0 sc1\n\t"
  "buffer_load_dwordx4 a[132:135], v11, s[40:43], s71 offen offset:0 sc1\n\t"
  "buffer_load_dwordx4 a[136:139], v56, s[40:43], s71 offen offset:0 sc1\n\t"
  "buffer_load_dwordx4 a[140:143], v57, s[40:43], s71 offen offset:0 sc1\n\t"
  "buffer_load_dwordx4 a[204:207], v10, s[40:43], s71 offen offset:1024 sc1\n\t"
  "buffer_load_dwordx4 a[148:151], v11, s[40:43], s71 offen offset:1024 sc1\n\t"
  "buffer_load_dwordx4 a[152:155], v56, s[40:43], s71 offen offset:1024 sc1\n\t"
  "buffer_load_dwordx4 a[156:159], v57, s[40:43], s71 offen offset:1024 sc1\n\t"
  "s_waitcnt vmcnt(0) lgkmcnt(0)\n\t"
  "s_barrier\n\t"
  "ds_read_b64_tr_b16 v[32:33], v28 offset:16384\n\t"
  "ds_read_b64_tr_b16 v[34:35], v28 offset:20480\n\t"
  "s_waitcnt lgkmcnt(0)\n\t"
  "v_mfma_f32_16x16x32_bf16 v[208:211], v[32:35], a[200:203], v[208:211]\n\t"
  "v_mfma_f32_16x16x32_bf16 v[212:215], v[32:35], a[132:135], v[212:215]\n\t"
  "v_mfma_f32_16x16x32_bf16 v[216:219], v[32:35], a[136:139], v[216:219]\n\t"
  "v_mfma_f32_16x16x32_bf16 v[220:223], v[32:35], a[140:143], v[220:223]\n\t"
  "ds_read_b64_tr_b16 v[32:33], v28 offset:16416\n\t"
  "ds_read_b64_tr_b16 v[34:35], v28 offset:20512\n\t"
  "s_waitcnt lgkmcnt(0)\n\t"
  "v_mfma_f32_16x16x32_bf16 v[240:243], v[32:35], a[200:203], v[240:243]\n\t"
  "v_mfma_f32_16x16x32_bf16 v[244:247], v[32:35], a[132:135], v[244:247]\n\t"
  "v_mfma_f32_16x16x32_bf16 v[248:251], v[32:35], a[136:139], v[248:251]\n\t"
  "v_mfma_f32_16x16x32_bf16 v[252:255], v[32:35], a[140:143], v[252:255]\n\t"
  "ds_read_b64_tr_b16 v[32:33], v28 offset:16448\n\t"
  "ds_read_b64_tr_b16 v[34:35], v28 offset:20544\n\t"
  "s_waitcnt lgkmcnt(0)\n\t"
  "v_mfma_f32_16x16x32_bf16 v[112:115], v[32:35], a[200:203], v[112:115]\n\t"
  "v_mfma_f32_16x16x32_bf16 v[116:119], v[32:35], a[132:135], v[116:119]\n\t"
  "v_mfma_f32_16x16x32_bf16 v[120:123], v[32:35], a[136:139], v[120:123]\n\t"
  "v_mfma_f32_16x16x32_bf16 v[124:127], v[32:35], a[140:143], v[124:127]\n\t"
  "ds_read_b64_tr_b16 v[32:33], v28 offset:16480\n\t"
  "ds_read_b64_tr_b16 v[34:35], v28 offset:20576\n\t"
  "s_waitcnt lgkmcnt(0)\n\t"
  "v_mfma_f32_16x16x32_bf16 v[176:179], v[32:35], a[200:203], v[176:179]\n\t"
  "v_mfma_f32_16x16x32_bf16 v[180:183], v[32:35], a[132:135], v[180:183]\n\t"
  "v_mfma_f32_16x16x32_bf16 v[184:187], v[32:35], a[136:139], v[184:187]\n\t"
  "v_mfma_f32_16x16x32_bf16 v[188:191], v[32:35], a[140:143], v[188:191]\n\t"
  "ds_read_b64_tr_b16 v[32:33], v28 offset:16512\n\t"
  "ds_read_b64_tr_b16 v[34:35], v28 offset:20608\n\t"
  "s_waitcnt lgkmcnt(0)\n\t"
  "v_mfma_f32_16x16x32_bf16 v[64:67], v[32:35], a[200:203], v[64:67]\n\t"
  "v_mfma_f32_16x16x32_bf16 v[68:71], v[32:35], a[132:135], v[68:71]\n\t"
  "v_mfma_f32_16x16x32_bf16 v[72:75], v[32:35], a[136:139], v[72:75]\n\t"
  "v_mfma_f32_16x16x32_bf16 v[76:79], v[32:35], a[140:143], v[76:79]\n\t"
  "ds_read_b64_tr_b16 v[32:33], v28 offset:16544\n\t"
  "ds_read_b64_tr_b16 v[34:35], v28 offset:20640\n\t"
  "s_waitcnt lgkmcnt(0)\n\t"
  "v_mfma_f32_16x16x32_bf16 v[96:99], v[32:35], a[200:203], v[96:99]\n\t"
  "v_mfma_f32_16x16x32_bf16 v[100:103], v[32:35], a[132:135], v[100:103]\n\t"
  "v_mfma_f32_16x16x32_bf16 v[104:107], v[32:35], a[136:139], v[104:107]\n\t"
  "v_mfma_f32_16x16x32_bf16 v[108:111], v[32:35], a[140:143], v[108:111]\n\t"
  "ds_read_b64_tr_b16 v[32:33], v28 offset:16576\n\t"
  "ds_read_b64_tr_b16 v[34:35], v28 offset:20672\n\t"
  "s_waitcnt lgkmcnt(0)\n\t"
  "v_mfma_f32_16x16x32_bf16 v[128:131], v[32:35], a[200:203], v[128:131]\n\t"
  "v_mfma_f32_16x16x32_bf16 v[132:135], v[32:35], a[132:135], v[132:135]\n\t"
  "v_mfma_f32_16x16x32_bf16 v[136:139], v[32:35], a[136:139], v[136:139]\n\t"
  "v_mfma_f32_16x16x32_bf16 v[140:143], v[32:35], a[140:143], v[140:143]\n\t"
  "ds_read_b64_tr_b16 v[32:33], v28 offset:24576\n\t"
  "ds_read_b64_tr_b16 v[34:35], v28 offset:28672\n\t"
  "s_waitcnt lgkmcnt(0)\n\t"
  "v_mfma_f32_16x16x32_bf16 v[208:211], v[32:35], a[204:207], v[208:211]\n\t"
  "v_mfma_f32_16x16x32_bf16 v[212:215], v[32:35], a[148:151], v[212:215]\n\t"
  "v_mfma_f32_16x16x32_bf16 v[216:219], v[32:35], a[152:155], v[216:219]\n\t"
  "v_mfma_f32_16x16x32_bf16 v[220:223], v[32:35], a[156:159], v[220:223]\n\t"
  "ds_read_b64_tr_b16 v[32:33], v28 offset:24608\n\t"
  "ds_read_b64_tr_b16 v[34:35], v28 offset:28704\n\t"
  "s_waitcnt lgkmcnt(0)\n\t"
  "v_mfma_f32_16x16x32_bf16 v[240:243], v[32:35], a[204:207], v[240:243]\n\t"
  "v_mfma_f32_16x16x32_bf16 v[244:247], v[32:35], a[148:151], v[244:247]\n\t"
  "v_mfma_f32_16x16x32_bf16 v[248:251], v[32:35], a[152:155], v[248:251]\n\t"
  "v_mfma_f32_16x16x32_bf16 v[252:255], v[32:35], a[156:159], v[252:255]\n\t"
  "ds_read_b64_tr_b16 v[32:33], v28 offset:24640\n\t"
  "ds_read_b64_tr_b16 v[34:35], v28 offset:28736\n\t"
  "s_waitcnt lgkmcnt(0)\n\t"
  "v_mfma_f32_16x16x32_bf16 v[112:115], v[32:35], a[204:207], v[112:115]\n\t"
  "v_mfma_f32_16x16x32_bf16 v[116:119], v[32:35], a[148:151], v[116:119]\n\t"
  "v_mfma_f32_16x16x32_bf16 v[120:123], v[32:35], a[152:155], v[120:123]\n\t"
  "v_mfma_f32_16x16x32_bf16 v[124:127], v[32:35], a[156:159], v[124:127]\n\t"
  "ds_read_b64_tr_b16 v[32:33], v28 offset:24672\n\t"
  "ds_read_b64_tr_b16 v[34:35], v28 offset:28768\n\t"
  "s_waitcnt lgkmcnt(0)\n\t"
  "v_mfma_f32_16x16x32_bf16 v[176:179], v[32:35], a[204:207], v[176:179]\n\t"
  "v_mfma_f32_16x16x32_bf16 v[180:183], v[32:35], a[148:151], v[180:183]\n\t"
  "v_mfma_f32_16x16x32_bf16 v[184:187], v[32:35], a[152:155], v[184:187]\n\t"
  "v_mfma_f32_16x16x32_bf16 v[188:191], v[32:35], a[156:159], v[188:191]\n\t"
  "ds_read_b64_tr_b16 v[32:33], v28 offset:24704\n\t"
  "ds_read_b64_tr_b16 v[34:35], v28 offset:28800\n\t"
  "s_waitcnt lgkmcnt(0)\n\t"
  "v_mfma_f32_16x16x32_bf16 v[64:67], v[32:35], a[204:207], v[64:67]\n\t"
  "v_mfma_f32_16x16x32_bf16 v[68:71], v[32:35], a[148:151], v[68:71]\n\t"
  "v_mfma_f32_16x16x32_bf16 v[72:75], v[32:35], a[152:155], v[72:75]\n\t"
  "v_mfma_f32_16x16x32_bf16 v[76:79], v[32:35], a[156:159], v[76:79]\n\t"
  "ds_read_b64_tr_b16 v[32:33], v28 offset:24736\n\t"
  "ds_read_b64_tr_b16 v[34:35], v28 offset:28832\n\t"
  "s_waitcnt lgkmcnt(0)\n\t"
  "v_mfma_f32_16x16x32_bf16 v[96:99], v[32:35], a[204:207], v[96:99]\n\t"
  "v_mfma_f32_16x16x32_bf16 v[100:103], v[32:35], a[148:151], v[100:103]\n\t"
  "v_mfma_f32_16x16x32_bf16 v[104:107], v[32:35], a[152:155], v[104:107]\n\t"
  "v_mfma_f32_16x16x32_bf16 v[108:111], v[32:35], a[156:159], v[108:111]\n\t"
  "ds_read_b64_tr_b16 v[32:33], v28 offset:24768\n\t"
  "ds_read_b64_tr_b16 v[34:35], v28 offset:28864\n\t"
  "s_waitcnt lgkmcnt(0)\n\t"
  "v_mfma_f32_16x16x32_bf16 v[128:131], v[32:35], a[204:207], v[128:131]\n\t"
  "v_mfma_f32_16x16x32_bf16 v[132:135], v[32:35], a[148:151], v[132:135]\n\t"
  "v_mfma_f32_16x16x32_bf16 v[136:139], v[32:35], a[152:155], v[136:139]\n\t"
  "v_mfma_f32_16x16x32_bf16 v[140:143], v[32:35], a[156:159], v[140:143]\n\t"
  "s_waitcnt vmcnt(0) lgkmcnt(0)\n\t"
  "s_barrier\n\t"
  "s_add_u32 m0, s69, 16384\n\t"
  "s_bfe_u32 s70, s85, 0x4000c\n\t"
  "s_mul_i32 s70, s70, 57344\n\t"
  "buffer_load_dwordx4 v6, s[60:63], s70 offen offset:0  lds\n\t"
  "buffer_load_dwordx4 v7, s[60:63], s70 offen offset:1024  lds\n\t"
  "buffer_load_dwordx4 v8, s[60:63], s70 offen offset:2048  lds\n\t"
  "buffer_load_dwordx4 v9, s[60:63], s70 offen offset:3072  lds\n\t"
  "s_bfe_u32 s71, s85, 0x4000c\n\t"
  "s_lshl_b32 s71, s71, 11\n\t"
  "buffer_load_dwordx4 a[208:211], v10, s[40:43], s71 offen offset:0 sc1\n\t"
  "buffer_load_dwordx4 a[164:167], v11, s[40:43], s71 offen offset:0 sc1\n\t"
  "buffer_load_dwordx4 a[168:171], v56, s[40:43], s71 offen offset:0 sc1\n\t"
  "buffer_load_dwordx4 a[172:175], v57, s[40:43], s71 offen offset:0 sc1\n\t"
  "buffer_load_dwordx4 a[212:215], v10, s[40:43], s71 offen offset:1024 sc1\n\t"
  "buffer_load_dwordx4 a[180:183], v11, s[40:43], s71 offen offset:1024 sc1\n\t"
  "buffer_load_dwordx4 a[184:187], v56, s[40:43], s71 offen offset:1024 sc1\n\t"
  "buffer_load_dwordx4 a[188:191], v57, s[40:43], s71 offen offset:1024 sc1\n\t"
  "s_waitcnt vmcnt(0) lgkmcnt(0)\n\t"
  "s_barrier\n\t"
  "ds_read_b64_tr_b16 v[32:33], v28 offset:16384\n\t"
  "ds_read_b64_tr_b16 v[34:35], v28 offset:20480\n\t"
  "s_waitcnt lgkmcnt(0)\n\t"
  "v_mfma_f32_16x16x32_bf16 v[208:211], v[32:35], a[208:211], v[208:211]\n\t"
  "v_mfma_f32_16x16x32_bf16 v[212:215], v[32:35], a[164:167], v[212:215]\n\t"
  "v_mfma_f32_16x16x32_bf16 v[216:219], v[32:35], a[168:171], v[216:219]\n\t"
  "v_mfma_f32_16x16x32_bf16 v[220:223], v[32:35], a[172:175], v[220:223]\n\t"
  "ds_read_b64_tr_b16 v[32:33], v28 offset:16416\n\t"
  "ds_read_b64_tr_b16 v[34:35], v28 offset:20512\n\t"
  "s_waitcnt lgkmcnt(0)\n\t"
  "v_mfma_f32_16x16x32_bf16 v[240:243], v[32:35], a[208:211], v[240:243]\n\t"
  "v_mfma_f32_16x16x32_bf16 v[244:247], v[32:35], a[164:167], v[244:247]\n\t"
  "v_mfma_f32_16x16x32_bf16 v[248:251], v[32:35], a[168:171], v[248:251]\n\t"
  "v_mfma_f32_16x16x32_bf16 v[252:255], v[32:35], a[172:175], v[252:255]\n\t"
  "ds_read_b64_tr_b16 v[32:33], v28 offset:16448\n\t"
  "ds_read_b64_tr_b16 v[34:35], v28 offset:20544\n\t"
  "s_waitcnt lgkmcnt(0)\n\t"
  "v_mfma_f32_16x16x32_bf16 v[112:115], v[32:35], a[208:211], v[112:115]\n\t"
  "v_mfma_f32_16x16x32_bf16 v[116:119], v[32:35], a[164:167], v[116:119]\n\t"
  "v_mfma_f32_16x16x32_bf16 v[120:123], v[32:35], a[168:171], v[120:123]\n\t"
  "v_mfma_f32_16x16x32_bf16 v[124:127], v[32:35], a[172:175], v[124:127]\n\t"
  "ds_read_b64_tr_b16 v[32:33], v28 offset:16480\n\t"
  "ds_read_b64_tr_b16 v[34:35], v28 offset:20576\n\t"
  "s_waitcnt lgkmcnt(0)\n\t"
  "v_mfma_f32_16x16x32_bf16 v[176:179], v[32:35], a[208:211], v[176:179]\n\t"
  "v_mfma_f32_16x16x32_bf16 v[180:183], v[32:35], a[164:167], v[180:183]\n\t"
  "v_mfma_f32_16x16x32_bf16 v[184:187], v[32:35], a[168:171], v[184:187]\n\t"
  "v_mfma_f32_16x16x32_bf16 v[188:191], v[32:35], a[172:175], v[188:191]\n\t"
  "ds_read_b64_tr_b16 v[32:33], v28 offset:16512\n\t"
  "ds_read_b64_tr_b16 v[34:35], v28 offset:20608\n\t"
  "s_waitcnt lgkmcnt(0)\n\t"
  "v_mfma_f32_16x16x32_bf16 v[64:67], v[32:35], a[208:211], v[64:67]\n\t"
  "v_mfma_f32_16x16x32_bf16 v[68:71], v[32:35], a[164:167], v[68:71]\n\t"
  "v_mfma_f32_16x16x32_bf16 v[72:75], v[32:35], a[168:171], v[72:75]\n\t"
  "v_mfma_f32_16x16x32_bf16 v[76:79], v[32:35], a[172:175], v[76:79]\n\t"
  "ds_read_b64_tr_b16 v[32:33], v28 offset:16544\n\t"
  "ds_read_b64_tr_b16 v[34:35], v28 offset:20640\n\t"
  "s_waitcnt lgkmcnt(0)\n\t"
  "v_mfma_f32_16x16x32_bf16 v[96:99], v[32:35], a[208:211], v[96:99]\n\t"
  "v_mfma_f32_16x16x32_bf16 v[100:103], v[32:35], a[164:167], v[100:103]\n\t"
  "v_mfma_f32_16x16x32_bf16 v[104:107], v[32:35], a[168:171], v[104:107]\n\t"
  "v_mfma_f32_16x16x32_bf16 v[108:111], v[32:35], a[172:175], v[108:111]\n\t"
  "ds_read_b64_tr_b16 v[32:33], v28 offset:16576\n\t"
  "ds_read_b64_tr_b16 v[34:35], v28 offset:20672\n\t"
  "s_waitcnt lgkmcnt(0)\n\t"
  "v_mfma_f32_16x16x32_bf16 v[128:131], v[32:35], a[208:211], v[128:131]\n\t"
  "v_mfma_f32_16x16x32_bf16 v[132:135], v[32:35], a[164:167], v[132:135]\n\t"
  "v_mfma_f32_16x16x32_bf16 v[136:139], v[32:35], a[168:171], v[136:139]\n\t"
  "v_mfma_f32_16x16x32_bf16 v[140:143], v[32:35], a[172:175], v[140:143]\n\t"
  "ds_read_b64_tr_b16 v[32:33], v28 offset:24576\n\t"
  "ds_read_b64_tr_b16 v[34:35], v28 offset:28672\n\t"
  "s_waitcnt lgkmcnt(0)\n\t"
  "v_mfma_f32_16x16x32_bf16 v[208:211], v[32:35], a[212:215], v[208:211]\n\t"
  "v_mfma_f32_16x16x32_bf16 v[212:215], v[32:35], a[180:183], v[212:215]\n\t"
  "v_mfma_f32_16x16x32_bf16 v[216:219], v[32:35], a[184:187], v[216:219]\n\t"
  "v_mfma_f32_16x16x32_bf16 v[220:223], v[32:35], a[188:191], v[220:223]\n\t"
  "ds_read_b64_tr_b16 v[32:33], v28 offset:24608\n\t"
  "ds_read_b64_tr_b16 v[34:35], v28 offset:28704\n\t"
  "s_waitcnt lgkmcnt(0)\n\t"
  "v_mfma_f32_16x16x32_bf16 v[240:243], v[32:35], a[212:215], v[240:243]\n\t"
  "v_mfma_f32_16x16x32_bf16 v[244:247], v[32:35], a[180:183], v[244:247]\n\t"
  "v_mfma_f32_16x16x32_bf16 v[248:251], v[32:35], a[184:187], v[248:251]\n\t"
  "v_mfma_f32_16x16x32_bf16 v[252:255], v[32:35], a[188:191], v[252:255]\n\t"
  "ds_read_b64_tr_b16 v[32:33], v28 offset:24640\n\t"
  "ds_read_b64_tr_b16 v[34:35], v28 offset:28736\n\t"
  "s_waitcnt lgkmcnt(0)\n\t"
  "v_mfma_f32_16x16x32_bf16 v[112:115], v[32:35], a[212:215], v[112:115]\n\t"
  "v_mfma_f32_16x16x32_bf16 v[116:119], v[32:35], a[180:183], v[116:119]\n\t"
  "v_mfma_f32_16x16x32_bf16 v[120:123], v[32:35], a[184:187], v[120:123]\n\t"
  "v_mfma_f32_16x16x32_bf16 v[124:127], v[32:35], a[188:191], v[124:127]\n\t"
  "ds_read_b64_tr_b16 v[32:33], v28 offset:24672\n\t"
  "ds_read_b64_tr_b16 v[34:35], v28 offset:28768\n\t"
  "s_waitcnt lgkmcnt(0)\n\t"
  "v_mfma_f32_16x16x32_bf16 v[176:179], v[32:35], a[212:215], v[176:179]\n\t"
  "v_mfma_f32_16x16x32_bf16 v[180:183], v[32:35], a[180:183], v[180:183]\n\t"
  "v_mfma_f32_16x16x32_bf16 v[184:187], v[32:35], a[184:187], v[184:187]\n\t"
  "v_mfma_f32_16x16x32_bf16 v[188:191], v[32:35], a[188:191], v[188:191]\n\t"
  "ds_read_b64_tr_b16 v[32:33], v28 offset:24704\n\t"
  "ds_read_b64_tr_b16 v[34:35], v28 offset:28800\n\t"
  "s_waitcnt lgkmcnt(0)\n\t"
  "v_mfma_f32_16x16x32_bf16 v[64:67], v[32:35], a[212:215], v[64:67]\n\t"
  "v_mfma_f32_16x16x32_bf16 v[68:71], v[32:35], a[180:183], v[68:71]\n\t"
  "v_mfma_f32_16x16x32_bf16 v[72:75], v[32:35], a[184:187], v[72:75]\n\t"
  "v_mfma_f32_16x16x32_bf16 v[76:79], v[32:35], a[188:191], v[76:79]\n\t"
  "ds_read_b64_tr_b16 v[32:33], v28 offset:24736\n\t"
  "ds_read_b64_tr_b16 v[34:35], v28 offset:28832\n\t"
  "s_waitcnt lgkmcnt(0)\n\t"
  "v_mfma_f32_16x16x32_bf16 v[96:99], v[32:35], a[212:215], v[96:99]\n\t"
  "v_mfma_f32_16x16x32_bf16 v[100:103], v[32:35], a[180:183], v[100:103]\n\t"
  "v_mfma_f32_16x16x32_bf16 v[104:107], v[32:35], a[184:187], v[104:107]\n\t"
  "v_mfma_f32_16x16x32_bf16 v[108:111], v[32:35], a[188:191], v[108:111]\n\t"
  "ds_read_b64_tr_b16 v[32:33], v28 offset:24768\n\t"
  "ds_read_b64_tr_b16 v[34:35], v28 offset:28864\n\t"
  "s_waitcnt lgkmcnt(0)\n\t"
  "v_mfma_f32_16x16x32_bf16 v[128:131], v[32:35], a[212:215], v[128:131]\n\t"
  "v_mfma_f32_16x16x32_bf16 v[132:135], v[32:35], a[180:183], v[132:135]\n\t"
  "v_mfma_f32_16x16x32_bf16 v[136:139], v[32:35], a[184:187], v[136:139]\n\t"
  "v_mfma_f32_16x16x32_bf16 v[140:143], v[32:35], a[188:191], v[140:143]\n\t"
  "s_waitcnt vmcnt(0) lgkmcnt(0)\n\t"
  "s_barrier\n\t"
  "s_add_u32 m0, s69, 16384\n\t"
  "s_bfe_u32 s70, s85, 0x40010\n\t"
  "s_mul_i32 s70, s70, 57344\n\t"
  "buffer_load_dwordx4 v6, s[60:63], s70 offen offset:0  lds\n\t"
  "buffer_load_dwordx4 v7, s[60:63], s70 offen offset:1024  lds\n\t"
  "buffer_load_dwordx4 v8, s[60:63], s70 offen offset:2048  lds\n\t"
  "buffer_load_dwordx4 v9, s[60:63], s70 offen offset:3072  lds\n\t"
  "s_bfe_u32 s71, s85, 0x40010\n\t"
  "s_lshl_b32 s71, s71, 11\n\t"
  "buffer_load_dwordx4 a[0:3], v10, s[40:43], s71 offen offset:0 sc1\n\t"
  "buffer_load_dwordx4 a[4:7], v11, s[40:43], s71 offen offset:0 sc1\n\t"
  "buffer_load_dwordx4 a[8:11], v56, s[40:43], s71 offen offset:0 sc1\n\t"
  "buffer_load_dwordx4 a[12:15], v57, s[40:43], s71 offen offset:0 sc1\n\t"
  "buffer_load_dwordx4 a[16:19], v10, s[40:43], s71 offen offset:1024 sc1\n\t"
  "buffer_load_dwordx4 a[20:23], v11, s[40:43], s71 offen offset:1024 sc1\n\t"
  "buffer_load_dwordx4 a[24:27], v56, s[40:43], s71 offen offset:1024 sc1\n\t"
  "buffer_load_dwordx4 a[28:31], v57, s[40:43], s71 offen offset:1024 sc1\n\t"
  "s_waitcnt vmcnt(0) lgkmcnt(0)\n\t"
  "s_barrier\n\t"
  "ds_read_b64_tr_b16 v[32:33], v28 offset:16384\n\t"
  "ds_read_b64_tr_b16 v[34:35], v28 offset:20480\n\t"
  "s_waitcnt lgkmcnt(0)\n\t"
  "v_mfma_f32_16x16x32_bf16 v[208:211], v[32:35], a[0:3], v[208:211]\n\t"
  "v_mfma_f32_16x16x32_bf16 v[212:215], v[32:35], a[4:7], v[212:215]\n\t"
  "v_mfma_f32_16x16x32_bf16 v[216:219], v[32:35], a[8:11], v[216:219]\n\t"
  "v_mfma_f32_16x16x32_bf16 v[220:223], v[32:35], a[12:15], v[220:223]\n\t"
  "ds_read_b64_tr_b16 v[32:33], v28 offset:16416\n\t"
  "ds_read_b64_tr_b16 v[34:35], v28 offset:20512\n\t"
  "s_waitcnt lgkmcnt(0)\n\t"
  "v_mfma_f32_16x16x32_bf16 v[240:243], v[32:35], a[0:3], v[240:243]\n\t"
  "v_mfma_f32_16x16x32_bf16 v[244:247], v[32:35], a[4:7], v[244:247]\n\t"
  "v_mfma_f32_16x16x32_bf16 v[248:251], v[32:35], a[8:11], v[248:251]\n\t"
  "v_mfma_f32_16x16x32_bf16 v[252:255], v[32:35], a[12:15], v[252:255]\n\t"
  "ds_read_b64_tr_b16 v[32:33], v28 offset:16448\n\t"
  "ds_read_b64_tr_b16 v[34:35], v28 offset:20544\n\t"
  "s_waitcnt lgkmcnt(0)\n\t"
  "v_mfma_f32_16x16x32_bf16 v[112:115], v[32:35], a[0:3], v[112:115]\n\t"
  "v_mfma_f32_16x16x32_bf16 v[116:119], v[32:35], a[4:7], v[116:119]\n\t"
  "v_mfma_f32_16x16x32_bf16 v[120:123], v[32:35], a[8:11], v[120:123]\n\t"
  "v_mfma_f32_16x16x32_bf16 v[124:127], v[32:35], a[12:15], v[124:127]\n\t"
  "ds_read_b64_tr_b16 v[32:33], v28 offset:16480\n\t"
  "ds_read_b64_tr_b16 v[34:35], v28 offset:20576\n\t"
  "s_waitcnt lgkmcnt(0)\n\t"
  "v_mfma_f32_16x16x32_bf16 v[176:179], v[32:35], a[0:3], v[176:179]\n\t"
  "v_mfma_f32_16x16x32_bf16 v[180:183], v[32:35], a[4:7], v[180:183]\n\t"
  "v_mfma_f32_16x16x32_bf16 v[184:187], v[32:35], a[8:11], v[184:187]\n\t"
  "v_mfma_f32_16x16x32_bf16 v[188:191], v[32:35], a[12:15], v[188:191]\n\t"
  "ds_read_b64_tr_b16 v[32:33], v28 offset:16512\n\t"
  "ds_read_b64_tr_b16 v[34:35], v28 offset:20608\n\t"
  "s_waitcnt lgkmcnt(0)\n\t"
  "v_mfma_f32_16x16x32_bf16 v[64:67], v[32:35], a[0:3], v[64:67]\n\t"
  "v_mfma_f32_16x16x32_bf16 v[68:71], v[32:35], a[4:7], v[68:71]\n\t"
  "v_mfma_f32_16x16x32_bf16 v[72:75], v[32:35], a[8:11], v[72:75]\n\t"
  "v_mfma_f32_16x16x32_bf16 v[76:79], v[32:35], a[12:15], v[76:79]\n\t"
  "ds_read_b64_tr_b16 v[32:33], v28 offset:16544\n\t"
  "ds_read_b64_tr_b16 v[34:35], v28 offset:20640\n\t"
  "s_waitcnt lgkmcnt(0)\n\t"
  "v_mfma_f32_16x16x32_bf16 v[96:99], v[32:35], a[0:3], v[96:99]\n\t"
  "v_mfma_f32_16x16x32_bf16 v[100:103], v[32:35], a[4:7], v[100:103]\n\t"
  "v_mfma_f32_16x16x32_bf16 v[104:107], v[32:35], a[8:11], v[104:107]\n\t"
  "v_mfma_f32_16x16x32_bf16 v[108:111], v[32:35], a[12:15], v[108:111]\n\t"
  "ds_read_b64_tr_b16 v[32:33], v28 offset:16576\n\t"
  "ds_read_b64_tr_b16 v[34:35], v28 offset:20672\n\t"
  "s_waitcnt lgkmcnt(0)\n\t"
  "v_mfma_f32_16x16x32_bf16 v[128:131], v[32:35], a[0:3], v[128:131]\n\t"
  "v_mfma_f32_16x16x32_bf16 v[132:135], v[32:35], a[4:7], v[132:135]\n\t"
  "v_mfma_f32_16x16x32_bf16 v[136:139], v[32:35], a[8:11], v[136:139]\n\t"
  "v_mfma_f32_16x16x32_bf16 v[140:143], v[32:35], a[12:15], v[140:143]\n\t"
  "ds_read_b64_tr_b16 v[32:33], v28 offset:24576\n\t"
  "ds_read_b64_tr_b16 v[34:35], v28 offset:28672\n\t"
  "s_waitcnt lgkmcnt(0)\n\t"
  "v_mfma_f32_16x16x32_bf16 v[208:211], v[32:35], a[16:19], v[208:211]\n\t"
  "v_mfma_f32_16x16x32_bf16 v[212:215], v[32:35], a[20:23], v[212:215]\n\t"
  "v_mfma_f32_16x16x32_bf16 v[216:219], v[32:35], a[24:27], v[216:219]\n\t"
  "v_mfma_f32_16x16x32_bf16 v[220:223], v[32:35], a[28:31], v[220:223]\n\t"
  "ds_read_b64_tr_b16 v[32:33], v28 offset:24608\n\t"
  "ds_read_b64_tr_b16 v[34:35], v28 offset:28704\n\t"
  "s_waitcnt lgkmcnt(0)\n\t"
  "v_mfma_f32_16x16x32_bf16 v[240:243], v[32:35], a[16:19], v[240:243]\n\t"
  "v_mfma_f32_16x16x32_bf16 v[244:247], v[32:35], a[20:23], v[244:247]\n\t"
  "v_mfma_f32_16x16x32_bf16 v[248:251], v[32:35], a[24:27], v[248:251]\n\t"
  "v_mfma_f32_16x16x32_bf16 v[252:255], v[32:35], a[28:31], v[252:255]\n\t"
  "ds_read_b64_tr_b16 v[32:33], v28 offset:24640\n\t"
  "ds_read_b64_tr_b16 v[34:35], v28 offset:28736\n\t"
  "s_waitcnt lgkmcnt(0)\n\t"
  "v_mfma_f32_16x16x32_bf16 v[112:115], v[32:35], a[16:19], v[112:115]\n\t"
  "v_mfma_f32_16x16x32_bf16 v[116:119], v[32:35], a[20:23], v[116:119]\n\t"
  "v_mfma_f32_16x16x32_bf16 v[120:123], v[32:35], a[24:27], v[120:123]\n\t"
  "v_mfma_f32_16x16x32_bf16 v[124:127], v[32:35], a[28:31], v[124:127]\n\t"
  "ds_read_b64_tr_b16 v[32:33], v28 offset:24672\n\t"
  "ds_read_b64_tr_b16 v[34:35], v28 offset:28768\n\t"
  "s_waitcnt lgkmcnt(0)\n\t"
  "v_mfma_f32_16x16x32_bf16 v[176:179], v[32:35], a[16:19], v[176:179]\n\t"
  "v_mfma_f32_16x16x32_bf16 v[180:183], v[32:35], a[20:23], v[180:183]\n\t"
  "v_mfma_f32_16x16x32_bf16 v[184:187], v[32:35], a[24:27], v[184:187]\n\t"
  "v_mfma_f32_16x16x32_bf16 v[188:191], v[32:35], a[28:31], v[188:191]\n\t"
  "ds_read_b64_tr_b16 v[32:33], v28 offset:24704\n\t"
  "ds_read_b64_tr_b16 v[34:35], v28 offset:28800\n\t"
  "s_waitcnt lgkmcnt(0)\n\t"
  "v_mfma_f32_16x16x32_bf16 v[64:67], v[32:35], a[16:19], v[64:67]\n\t"
  "v_mfma_f32_16x16x32_bf16 v[68:71], v[32:35], a[20:23], v[68:71]\n\t"
  "v_mfma_f32_16x16x32_bf16 v[72:75], v[32:35], a[24:27], v[72:75]\n\t"
  "v_mfma_f32_16x16x32_bf16 v[76:79], v[32:35], a[28:31], v[76:79]\n\t"
  "ds_read_b64_tr_b16 v[32:33], v28 offset:24736\n\t"
  "ds_read_b64_tr_b16 v[34:35], v28 offset:28832\n\t"
  "s_waitcnt lgkmcnt(0)\n\t"
  "v_mfma_f32_16x16x32_bf16 v[96:99], v[32:35], a[16:19], v[96:99]\n\t"
  "v_mfma_f32_16x16x32_bf16 v[100:103], v[32:35], a[20:23], v[100:103]\n\t"
  "v_mfma_f32_16x16x32_bf16 v[104:107], v[32:35], a[24:27], v[104:107]\n\t"
  "v_mfma_f32_16x16x32_bf16 v[108:111], v[32:35], a[28:31], v[108:111]\n\t"
  "ds_read_b64_tr_b16 v[32:33], v28 offset:24768\n\t"
  "ds_read_b64_tr_b16 v[34:35], v28 offset:28864\n\t"
  "s_waitcnt lgkmcnt(0)\n\t"
  "v_mfma_f32_16x16x32_bf16 v[128:131], v[32:35], a[16:19], v[128:131]\n\t"
  "v_mfma_f32_16x16x32_bf16 v[132:135], v[32:35], a[20:23], v[132:135]\n\t"
  "v_mfma_f32_16x16x32_bf16 v[136:139], v[32:35], a[24:27], v[136:139]\n\t"
  "v_mfma_f32_16x16x32_bf16 v[140:143], v[32:35], a[28:31], v[140:143]\n\t"
  "s_waitcnt vmcnt(0) lgkmcnt(0)\n\t"
  "s_barrier\n\t"
  "s_add_u32 m0, s69, 16384\n\t"
  "s_bfe_u32 s70, s85, 0x40014\n\t"
  "s_mul_i32 s70, s70, 57344\n\t"
  "buffer_load_dwordx4 v6, s[60:63], s70 offen offset:0  lds\n\t"
  "buffer_load_dwordx4 v7, s[60:63], s70 offen offset:1024  lds\n\t"
  "buffer_load_dwordx4 v8, s[60:63], s70 offen offset:2048  lds\n\t"
  "buffer_load_dwordx4 v9, s[60:63], s70 offen offset:3072  lds\n\t"
  "s_bfe_u32 s71, s85, 0x40014\n\t"
  "s_lshl_b32 s71, s71, 11\n\t"
  "buffer_load_dwordx4 a[216:219], v10, s[40:43], s71 offen offset:0 sc1\n\t"
  "buffer_load_dwordx4 a[100:103], v11, s[40:43], s71 offen offset:0 sc1\n\t"
  "buffer_load_dwordx4 a[104:107], v56, s[40:43], s71 offen offset:0 sc1\n\t"
  "buffer_load_dwordx4 a[108:111], v57, s[40:43], s71 offen offset:0 sc1\n\t"
  "buffer_load_dwordx4 a[220:223], v10, s[40:43], s71 offen offset:1024 sc1\n\t"
  "buffer_load_dwordx4 a[116:119], v11, s[40:43], s71 offen offset:1024 sc1\n\t"
  "buffer_load_dwordx4 a[120:123], v56, s[40:43], s71 offen offset:1024 sc1\n\t"
  "buffer_load_dwordx4 a[124:127], v57, s[40:43], s71 offen offset:1024 sc1\n\t"
  "s_waitcnt vmcnt(0) lgkmcnt(0)\n\t"
  "s_barrier\n\t"
  "ds_read_b64_tr_b16 v[32:33], v28 offset:16384\n\t"
  "ds_read_b64_tr_b16 v[34:35], v28 offset:20480\n\t"
  "s_waitcnt lgkmcnt(0)\n\t"
  "v_mfma_f32_16x16x32_bf16 v[208:211], v[32:35], a[216:219], v[208:211]\n\t"
  "v_mfma_f32_16x16x32_bf16 v[212:215], v[32:35], a[100:103], v[212:215]\n\t"
  "v_mfma_f32_16x16x32_bf16 v[216:219], v[32:35], a[104:107], v[216:219]\n\t"
  "v_mfma_f32_16x16x32_bf16 v[220:223], v[32:35], a[108:111], v[220:223]\n\t"
  "ds_read_b64_tr_b16 v[32:33], v28 offset:16416\n\t"
  "ds_read_b64_tr_b16 v[34:35], v28 offset:20512\n\t"
  "s_waitcnt lgkmcnt(0)\n\t"
  "v_mfma_f32_16x16x32_bf16 v[240:243], v[32:35], a[216:219], v[240:243]\n\t"
  "v_mfma_f32_16x16x32_bf16 v[244:247], v[32:35], a[100:103], v[244:247]\n\t"
  "v_mfma_f32_16x16x32_bf16 v[248:251], v[32:35], a[104:107], v[248:251]\n\t"
  "v_mfma_f32_16x16x32_bf16 v[252:255], v[32:35], a[108:111], v[252:255]\n\t"
  "ds_read_b64_tr_b16 v[32:33], v28 offset:16448\n\t"
  "ds_read_b64_tr_b16 v[34:35], v28 offset:20544\n\t"
  "s_waitcnt lgkmcnt(0)\n\t"
  "v_mfma_f32_16x16x32_bf16 v[112:115], v[32:35], a[216:219], v[112:115]\n\t"
  "v_mfma_f32_16x16x32_bf16 v[116:119], v[32:35], a[100:103], v[116:119]\n\t"
  "v_mfma_f32_16x16x32_bf16 v[120:123], v[32:35], a[104:107], v[120:123]\n\t"
  "v_mfma_f32_16x16x32_bf16 v[124:127], v[32:35], a[108:111], v[124:127]\n\t"
  "ds_read_b64_tr_b16 v[32:33], v28 offset:16480\n\t"
  "ds_read_b64_tr_b16 v[34:35], v28 offset:20576\n\t"
  "s_waitcnt lgkmcnt(0)\n\t"
  "v_mfma_f32_16x16x32_bf16 v[176:179], v[32:35], a[216:219], v[176:179]\n\t"
  "v_mfma_f32_16x16x32_bf16 v[180:183], v[32:35], a[100:103], v[180:183]\n\t"
  "v_mfma_f32_16x16x32_bf16 v[184:187], v[32:35], a[104:107], v[184:187]\n\t"
  "v_mfma_f32_16x16x32_bf16 v[188:191], v[32:35], a[108:111], v[188:191]\n\t"
  "ds_read_b64_tr_b16 v[32:33], v28 offset:16512\n\t"
  "ds_read_b64_tr_b16 v[34:35], v28 offset:20608\n\t"
  "s_waitcnt lgkmcnt(0)\n\t"
  "v_mfma_f32_16x16x32_bf16 v[64:67], v[32:35], a[216:219], v[64:67]\n\t"
  "v_mfma_f32_16x16x32_bf16 v[68:71], v[32:35], a[100:103], v[68:71]\n\t"
  "v_mfma_f32_16x16x32_bf16 v[72:75], v[32:35], a[104:107], v[72:75]\n\t"
  "v_mfma_f32_16x16x32_bf16 v[76:79], v[32:35], a[108:111], v[76:79]\n\t"
  "ds_read_b64_tr_b16 v[32:33], v28 offset:16544\n\t"
  "ds_read_b64_tr_b16 v[34:35], v28 offset:20640\n\t"
  "s_waitcnt lgkmcnt(0)\n\t"
  "v_mfma_f32_16x16x32_bf16 v[96:99], v[32:35], a[216:219], v[96:99]\n\t"
  "v_mfma_f32_16x16x32_bf16 v[100:103], v[32:35], a[100:103], v[100:103]\n\t"
  "v_mfma_f32_16x16x32_bf16 v[104:107], v[32:35], a[104:107], v[104:107]\n\t"
  "v_mfma_f32_16x16x32_bf16 v[108:111], v[32:35], a[108:111], v[108:111]\n\t"
  "ds_read_b64_tr_b16 v[32:33], v28 offset:16576\n\t"
  "ds_read_b64_tr_b16 v[34:35], v28 offset:20672\n\t"
  "s_waitcnt lgkmcnt(0)\n\t"
  "v_mfma_f32_16x16x32_bf16 v[128:131], v[32:35], a[216:219], v[128:131]\n\t"
  "v_mfma_f32_16x16x32_bf16 v[132:135], v[32:35], a[100:103], v[132:135]\n\t"
  "v_mfma_f32_16x16x32_bf16 v[136:139], v[32:35], a[104:107], v[136:139]\n\t"
  "v_mfma_f32_16x16x32_bf16 v[140:143], v[32:35], a[108:111], v[140:143]\n\t"
  "ds_read_b64_tr_b16 v[32:33], v28 offset:24576\n\t"
  "ds_read_b64_tr_b16 v[34:35], v28 offset:28672\n\t"
  "s_waitcnt lgkmcnt(0)\n\t"
  "v_mfma_f32_16x16x32_bf16 v[208:211], v[32:35], a[220:223], v[208:211]\n\t"
  "v_mfma_f32_16x16x32_bf16 v[212:215], v[32:35], a[116:119], v[212:215]\n\t"
  "v_mfma_f32_16x16x32_bf16 v[216:219], v[32:35], a[120:123], v[216:219]\n\t"
  "v_mfma_f32_16x16x32_bf16 v[220:223], v[32:35], a[124:127], v[220:223]\n\t"
  "ds_read_b64_tr_b16 v[32:33], v28 offset:24608\n\t"
  "ds_read_b64_tr_b16 v[34:35], v28 offset:28704\n\t"
  "s_waitcnt lgkmcnt(0)\n\t"
  "v_mfma_f32_16x16x32_bf16 v[240:243], v[32:35], a[220:223], v[240:243]\n\t"
  "v_mfma_f32_16x16x32_bf16 v[244:247], v[32:35], a[116:119], v[244:247]\n\t"
  "v_mfma_f32_16x16x32_bf16 v[248:251], v[32:35], a[120:123], v[248:251]\n\t"
  "v_mfma_f32_16x16x32_bf16 v[252:255], v[32:35], a[124:127], v[252:255]\n\t"
  "ds_read_b64_tr_b16 v[32:33], v28 offset:24640\n\t"
  "ds_read_b64_tr_b16 v[34:35], v28 offset:28736\n\t"
  "s_waitcnt lgkmcnt(0)\n\t"
  "v_mfma_f32_16x16x32_bf16 v[112:115], v[32:35], a[220:223], v[112:115]\n\t"
  "v_mfma_f32_16x16x32_bf16 v[116:119], v[32:35], a[116:119], v[116:119]\n\t"
  "v_mfma_f32_16x16x32_bf16 v[120:123], v[32:35], a[120:123], v[120:123]\n\t"
  "v_mfma_f32_16x16x32_bf16 v[124:127], v[32:35], a[124:127], v[124:127]\n\t"
  "ds_read_b64_tr_b16 v[32:33], v28 offset:24672\n\t"
  "ds_read_b64_tr_b16 v[34:35], v28 offset:28768\n\t"
  "s_waitcnt lgkmcnt(0)\n\t"
  "v_mfma_f32_16x16x32_bf16 v[176:179], v[32:35], a[220:223], v[176:179]\n\t"
  "v_mfma_f32_16x16x32_bf16 v[180:183], v[32:35], a[116:119], v[180:183]\n\t"
  "v_mfma_f32_16x16x32_bf16 v[184:187], v[32:35], a[120:123], v[184:187]\n\t"
  "v_mfma_f32_16x16x32_bf16 v[188:191], v[32:35], a[124:127], v[188:191]\n\t"
  "ds_read_b64_tr_b16 v[32:33], v28 offset:24704\n\t"
  "ds_read_b64_tr_b16 v[34:35], v28 offset:28800\n\t"
  "s_waitcnt lgkmcnt(0)\n\t"
  "v_mfma_f32_16x16x32_bf16 v[64:67], v[32:35], a[220:223], v[64:67]\n\t"
  "v_mfma_f32_16x16x32_bf16 v[68:71], v[32:35], a[116:119], v[68:71]\n\t"
  "v_mfma_f32_16x16x32_bf16 v[72:75], v[32:35], a[120:123], v[72:75]\n\t"
  "v_mfma_f32_16x16x32_bf16 v[76:79], v[32:35], a[124:127], v[76:79]\n\t"
  "ds_read_b64_tr_b16 v[32:33], v28 offset:24736\n\t"
  "ds_read_b64_tr_b16 v[34:35], v28 offset:28832\n\t"
  "s_waitcnt lgkmcnt(0)\n\t"
  "v_mfma_f32_16x16x32_bf16 v[96:99], v[32:35], a[220:223], v[96:99]\n\t"
  "v_mfma_f32_16x16x32_bf16 v[100:103], v[32:35], a[116:119], v[100:103]\n\t"
  "v_mfma_f32_16x16x32_bf16 v[104:107], v[32:35], a[120:123], v[104:107]\n\t"
  "v_mfma_f32_16x16x32_bf16 v[108:111], v[32:35], a[124:127], v[108:111]\n\t"
  "ds_read_b64_tr_b16 v[32:33], v28 offset:24768\n\t"
  "ds_read_b64_tr_b16 v[34:35], v28 offset:28864\n\t"
  "s_waitcnt lgkmcnt(0)\n\t"
  "v_mfma_f32_16x16x32_bf16 v[128:131], v[32:35], a[220:223], v[128:131]\n\t"
  "v_mfma_f32_16x16x32_bf16 v[132:135], v[32:35], a[116:119], v[132:135]\n\t"
  "v_mfma_f32_16x16x32_bf16 v[136:139], v[32:35], a[120:123], v[136:139]\n\t"
  "v_mfma_f32_16x16x32_bf16 v[140:143], v[32:35], a[124:127], v[140:143]\n\t"
  "s_waitcnt vmcnt(0) lgkmcnt(0)\n\t"
  "s_barrier\n\t"
  "s_add_u32 m0, s69, 16384\n\t"
  "s_bfe_u32 s70, s85, 0x40018\n\t"
  "s_mul_i32 s70, s70, 57344\n\t"
  "buffer_load_dwordx4 v6, s[60:63], s70 offen offset:0  lds\n\t"
  "buffer_load_dwordx4 v7, s[60:63], s70 offen offset:1024  lds\n\t"
  "buffer_load_dwordx4 v8, s[60:63], s70 offen offset:2048  lds\n\t"
  "buffer_load_dwordx4 v9, s[60:63], s70 offen offset:3072  lds\n\t"
  "s_bfe_u32 s71, s85, 0x40018\n\t"
  "s_lshl_b32 s71, s71, 11\n\t"
  "buffer_load_dwordx4 a[224:227], v10, s[40:43], s71 offen offset:0 sc1\n\t"
  "buffer_load_dwordx4 a[132:135], v11, s[40:43], s71 offen offset:0 sc1\n\t"
  "buffer_load_dwordx4 a[136:139], v56, s[40:43], s71 offen offset:0 sc1\n\t"
  "buffer_load_dwordx4 a[140:143], v57, s[40:43], s71 offen offset:0 sc1\n\t"
  "buffer_load_dwordx4 a[228:231], v10, s[40:43], s71 offen offset:1024 sc1\n\t"
  "buffer_load_dwordx4 a[148:151], v11, s[40:43], s71 offen offset:1024 sc1\n\t"
  "buffer_load_dwordx4 a[152:155], v56, s[40:43], s71 offen offset:1024 sc1\n\t"
  "buffer_load_dwordx4 a[156:159], v57, s[40:43], s71 offen offset:1024 sc1\n\t"
  "s_waitcnt vmcnt(0) lgkmcnt(0)\n\t"
  "s_barrier\n\t"
  "ds_read_b64_tr_b16 v[32:33], v28 offset:16384\n\t"
  "ds_read_b64_tr_b16 v[34:35], v28 offset:20480\n\t"
  "s_waitcnt lgkmcnt(0)\n\t"
  "v_mfma_f32_16x16x32_bf16 v[208:211], v[32:35], a[224:227], v[208:211]\n\t"
  "v_mfma_f32_16x16x32_bf16 v[212:215], v[32:35], a[132:135], v[212:215]\n\t"
  "v_mfma_f32_16x16x32_bf16 v[216:219], v[32:35], a[136:139], v[216:219]\n\t"
  "v_mfma_f32_16x16x32_bf16 v[220:223], v[32:35], a[140:143], v[220:223]\n\t"
  "ds_read_b64_tr_b16 v[32:33], v28 offset:16416\n\t"
  "ds_read_b64_tr_b16 v[34:35], v28 offset:20512\n\t"
  "s_waitcnt lgkmcnt(0)\n\t"
  "v_mfma_f32_16x16x32_bf16 v[240:243], v[32:35], a[224:227], v[240:243]\n\t"
  "v_mfma_f32_16x16x32_bf16 v[244:247], v[32:35], a[132:135], v[244:247]\n\t"
  "v_mfma_f32_16x16x32_bf16 v[248:251], v[32:35], a[136:139], v[248:251]\n\t"
  "v_mfma_f32_16x16x32_bf16 v[252:255], v[32:35], a[140:143], v[252:255]\n\t"
  "ds_read_b64_tr_b16 v[32:33], v28 offset:16448\n\t"
  "ds_read_b64_tr_b16 v[34:35], v28 offset:20544\n\t"
  "s_waitcnt lgkmcnt(0)\n\t"
  "v_mfma_f32_16x16x32_bf16 v[112:115], v[32:35], a[224:227], v[112:115]\n\t"
  "v_mfma_f32_16x16x32_bf16 v[116:119], v[32:35], a[132:135], v[116:119]\n\t"
  "v_mfma_f32_16x16x32_bf16 v[120:123], v[32:35], a[136:139], v[120:123]\n\t"
  "v_mfma_f32_16x16x32_bf16 v[124:127], v[32:35], a[140:143], v[124:127]\n\t"
  "ds_read_b64_tr_b16 v[32:33], v28 offset:16480\n\t"
  "ds_read_b64_tr_b16 v[34:35], v28 offset:20576\n\t"
  "s_waitcnt lgkmcnt(0)\n\t"
  "v_mfma_f32_16x16x32_bf16 v[176:179], v[32:35], a[224:227], v[176:179]\n\t"
  "v_mfma_f32_16x16x32_bf16 v[180:183], v[32:35], a[132:135], v[180:183]\n\t"
  "v_mfma_f32_16x16x32_bf16 v[184:187], v[32:35], a[136:139], v[184:187]\n\t"
  "v_mfma_f32_16x16x32_bf16 v[188:191], v[32:35], a[140:143], v[188:191]\n\t"
  "ds_read_b64_tr_b16 v[32:33], v28 offset:16512\n\t"
  "ds_read_b64_tr_b16 v[34:35], v28 offset:20608\n\t"
  "s_waitcnt lgkmcnt(0)\n\t"
  "v_mfma_f32_16x16x32_bf16 v[64:67], v[32:35], a[224:227], v[64:67]\n\t"
  "v_mfma_f32_16x16x32_bf16 v[68:71], v[32:35], a[132:135], v[68:71]\n\t"
  "v_mfma_f32_16x16x32_bf16 v[72:75], v[32:35], a[136:139], v[72:75]\n\t"
  "v_mfma_f32_16x16x32_bf16 v[76:79], v[32:35], a[140:143], v[76:79]\n\t"
  "ds_read_b64_tr_b16 v[32:33], v28 offset:16544\n\t"
  "ds_read_b64_tr_b16 v[34:35], v28 offset:20640\n\t"
  "s_waitcnt lgkmcnt(0)\n\t"
  "v_mfma_f32_16x16x32_bf16 v[96:99], v[32:35], a[224:227], v[96:99]\n\t"
  "v_mfma_f32_16x16x32_bf16 v[100:103], v[32:35], a[132:135], v[100:103]\n\t"
  "v_mfma_f32_16x16x32_bf16 v[104:107], v[32:35], a[136:139], v[104:107]\n\t"
  "v_mfma_f32_16x16x32_bf16 v[108:111], v[32:35], a[140:143], v[108:111]\n\t"
  "ds_read_b64_tr_b16 v[32:33], v28 offset:16576\n\t"
  "ds_read_b64_tr_b16 v[34:35], v28 offset:20672\n\t"
  "s_waitcnt lgkmcnt(0)\n\t"
  "v_mfma_f32_16x16x32_bf16 v[128:131], v[32:35], a[224:227], v[128:131]\n\t"
  "v_mfma_f32_16x16x32_bf16 v[132:135], v[32:35], a[132:135], v[132:135]\n\t"
  "v_mfma_f32_16x16x32_bf16 v[136:139], v[32:35], a[136:139], v[136:139]\n\t"
  "v_mfma_f32_16x16x32_bf16 v[140:143], v[32:35], a[140:143], v[140:143]\n\t"
  "ds_read_b64_tr_b16 v[32:33], v28 offset:24576\n\t"
  "ds_read_b64_tr_b16 v[34:35], v28 offset:28672\n\t"
  "s_waitcnt lgkmcnt(0)\n\t"
  "v_mfma_f32_16x16x32_bf16 v[208:211], v[32:35], a[228:231], v[208:211]\n\t"
  "v_mfma_f32_16x16x32_bf16 v[212:215], v[32:35], a[148:151], v[212:215]\n\t"
  "v_mfma_f32_16x16x32_bf16 v[216:219], v[32:35], a[152:155], v[216:219]\n\t"
  "v_mfma_f32_16x16x32_bf16 v[220:223], v[32:35], a[156:159], v[220:223]\n\t"
  "ds_read_b64_tr_b16 v[32:33], v28 offset:24608\n\t"
  "ds_read_b64_tr_b16 v[34:35], v28 offset:28704\n\t"
  "s_waitcnt lgkmcnt(0)\n\t"
  "v_mfma_f32_16x16x32_bf16 v[240:243], v[32:35], a[228:231], v[240:243]\n\t"
  "v_mfma_f32_16x16x32_bf16 v[244:247], v[32:35], a[148:151], v[244:247]\n\t"
  "v_mfma_f32_16x16x32_bf16 v[248:251], v[32:35], a[152:155], v[248:251]\n\t"
  "v_mfma_f32_16x16x32_bf16 v[252:255], v[32:35], a[156:159], v[252:255]\n\t"
  "ds_read_b64_tr_b16 v[32:33], v28 offset:24640\n\t"
  "ds_read_b64_tr_b16 v[34:35], v28 offset:28736\n\t"
  "s_waitcnt lgkmcnt(0)\n\t"
  "v_mfma_f32_16x16x32_bf16 v[112:115], v[32:35], a[228:231], v[112:115]\n\t"
  "v_mfma_f32_16x16x32_bf16 v[116:119], v[32:35], a[148:151], v[116:119]\n\t"
  "v_mfma_f32_16x16x32_bf16 v[120:123], v[32:35], a[152:155], v[120:123]\n\t"
  "v_mfma_f32_16x16x32_bf16 v[124:127], v[32:35], a[156:159], v[124:127]\n\t"
  "ds_read_b64_tr_b16 v[32:33], v28 offset:24672\n\t"
  "ds_read_b64_tr_b16 v[34:35], v28 offset:28768\n\t"
  "s_waitcnt lgkmcnt(0)\n\t"
  "v_mfma_f32_16x16x32_bf16 v[176:179], v[32:35], a[228:231], v[176:179]\n\t"
  "v_mfma_f32_16x16x32_bf16 v[180:183], v[32:35], a[148:151], v[180:183]\n\t"
  "v_mfma_f32_16x16x32_bf16 v[184:187], v[32:35], a[152:155], v[184:187]\n\t"
  "v_mfma_f32_16x16x32_bf16 v[188:191], v[32:35], a[156:159], v[188:191]\n\t"
  "ds_read_b64_tr_b16 v[32:33], v28 offset:24704\n\t"
  "ds_read_b64_tr_b16 v[34:35], v28 offset:28800\n\t"
  "s_waitcnt lgkmcnt(0)\n\t"
  "v_mfma_f32_16x16x32_bf16 v[64:67], v[32:35], a[228:231], v[64:67]\n\t"
  "v_mfma_f32_16x16x32_bf16 v[68:71], v[32:35], a[148:151], v[68:71]\n\t"
  "v_mfma_f32_16x16x32_bf16 v[72:75], v[32:35], a[152:155], v[72:75]\n\t"
  "v_mfma_f32_16x16x32_bf16 v[76:79], v[32:35], a[156:159], v[76:79]\n\t"
  "ds_read_b64_tr_b16 v[32:33], v28 offset:24736\n\t"
  "ds_read_b64_tr_b16 v[34:35], v28 offset:28832\n\t"
  "s_waitcnt lgkmcnt(0)\n\t"
  "v_mfma_f32_16x16x32_bf16 v[96:99], v[32:35], a[228:231], v[96:99]\n\t"
  "v_mfma_f32_16x16x32_bf16 v[100:103], v[32:35], a[148:151], v[100:103]\n\t"
  "v_mfma_f32_16x16x32_bf16 v[104:107], v[32:35], a[152:155], v[104:107]\n\t"
  "v_mfma_f32_16x16x32_bf16 v[108:111], v[32:35], a[156:159], v[108:111]\n\t"
  "ds_read_b64_tr_b16 v[32:33], v28 offset:24768\n\t"
  "ds_read_b64_tr_b16 v[34:35], v28 offset:28864\n\t"
  "s_waitcnt lgkmcnt(0)\n\t"
  "v_mfma_f32_16x16x32_bf16 v[128:131], v[32:35], a[228:231], v[128:131]\n\t"
  "v_mfma_f32_16x16x32_bf16 v[132:135], v[32:35], a[148:151], v[132:135]\n\t"
  "v_mfma_f32_16x16x32_bf16 v[136:139], v[32:35], a[152:155], v[136:139]\n\t"
  "v_mfma_f32_16x16x32_bf16 v[140:143], v[32:35], a[156:159], v[140:143]\n\t"
  "s_waitcnt vmcnt(0) lgkmcnt(0)\n\t"
  "s_barrier\n\t"
  "s_add_u32 m0, s69, 16384\n\t"
  "s_bfe_u32 s70, s85, 0x4001c\n\t"
  "s_mul_i32 s70, s70, 57344\n\t"
  "buffer_load_dwordx4 v6, s[60:63], s70 offen offset:0  lds\n\t"
  "buffer_load_dwordx4 v7, s[60:63], s70 offen offset:1024  lds\n\t"
  "buffer_load_dwordx4 v8, s[60:63], s70 offen offset:2048  lds\n\t"
  "buffer_load_dwordx4 v9, s[60:63], s70 offen offset:3072  lds\n\t"
  "s_bfe_u32 s71, s85, 0x4001c\n\t"
  "s_lshl_b32 s71, s71, 11\n\t"
  "buffer_load_dwordx4 a[232:235], v10, s[40:43], s71 offen offset:0 sc1\n\t"
  "buffer_load_dwordx4 a[164:167], v11, s[40:43], s71 offen offset:0 sc1\n\t"
  "buffer_load_dwordx4 a[168:171], v56, s[40:43], s71 offen offset:0 sc1\n\t"
  "buffer_load_dwordx4 a[172:175], v57, s[40:43], s71 offen offset:0 sc1\n\t"
  "buffer_load_dwordx4 a[236:239], v10, s[40:43], s71 offen offset:1024 sc1\n\t"
  "buffer_load_dwordx4 a[180:183], v11, s[40:43], s71 offen offset:1024 sc1\n\t"
  "buffer_load_dwordx4 a[184:187], v56, s[40:43], s71 offen offset:1024 sc1\n\t"
  "buffer_load_dwordx4 a[188:191], v57, s[40:43], s71 offen offset:1024 sc1\n\t"
  "s_waitcnt vmcnt(0) lgkmcnt(0)\n\t"
  "s_barrier\n\t"
  "ds_read_b64_tr_b16 v[32:33], v28 offset:16384\n\t"
  "ds_read_b64_tr_b16 v[34:35], v28 offset:20480\n\t"
  "s_waitcnt lgkmcnt(0)\n\t"
  "v_mfma_f32_16x16x32_bf16 v[208:211], v[32:35], a[232:235], v[208:211]\n\t"
  "v_mfma_f32_16x16x32_bf16 v[212:215], v[32:35], a[164:167], v[212:215]\n\t"
  "v_mfma_f32_16x16x32_bf16 v[216:219], v[32:35], a[168:171], v[216:219]\n\t"
  "v_mfma_f32_16x16x32_bf16 v[220:223], v[32:35], a[172:175], v[220:223]\n\t"
  "ds_read_b64_tr_b16 v[32:33], v28 offset:16416\n\t"
  "ds_read_b64_tr_b16 v[34:35], v28 offset:20512\n\t"
  "s_waitcnt lgkmcnt(0)\n\t"
  "v_mfma_f32_16x16x32_bf16 v[240:243], v[32:35], a[232:235], v[240:243]\n\t"
  "v_mfma_f32_16x16x32_bf16 v[244:247], v[32:35], a[164:167], v[244:247]\n\t"
  "v_mfma_f32_16x16x32_bf16 v[248:251], v[32:35], a[168:171], v[248:251]\n\t"
  "v_mfma_f32_16x16x32_bf16 v[252:255], v[32:35], a[172:175], v[252:255]\n\t"
  "ds_read_b64_tr_b16 v[32:33], v28 offset:16448\n\t"
  "ds_read_b64_tr_b16 v[34:35], v28 offset:20544\n\t"
  "s_waitcnt lgkmcnt(0)\n\t"
  "v_mfma_f32_16x16x32_bf16 v[112:115], v[32:35], a[232:235], v[112:115]\n\t"
  "v_mfma_f32_16x16x32_bf16 v[116:119], v[32:35], a[164:167], v[116:119]\n\t"
  "v_mfma_f32_16x16x32_bf16 v[120:123], v[32:35], a[168:171], v[120:123]\n\t"
  "v_mfma_f32_16x16x32_bf16 v[124:127], v[32:35], a[172:175], v[124:127]\n\t"
  "ds_read_b64_tr_b16 v[32:33], v28 offset:16480\n\t"
  "ds_read_b64_tr_b16 v[34:35], v28 offset:20576\n\t"
  "s_waitcnt lgkmcnt(0)\n\t"
  "v_mfma_f32_16x16x32_bf16 v[176:179], v[32:35], a[232:235], v[176:179]\n\t"
  "v_mfma_f32_16x16x32_bf16 v[180:183], v[32:35], a[164:167], v[180:183]\n\t"
  "v_mfma_f32_16x16x32_bf16 v[184:187], v[32:35], a[168:171], v[184:187]\n\t"
  "v_mfma_f32_16x16x32_bf16 v[188:191], v[32:35], a[172:175], v[188:191]\n\t"
  "ds_read_b64_tr_b16 v[32:33], v28 offset:16512\n\t"
  "ds_read_b64_tr_b16 v[34:35], v28 offset:20608\n\t"
  "s_waitcnt lgkmcnt(0)\n\t"
  "v_mfma_f32_16x16x32_bf16 v[64:67], v[32:35], a[232:235], v[64:67]\n\t"
  "v_mfma_f32_16x16x32_bf16 v[68:71], v[32:35], a[164:167], v[68:71]\n\t"
  "v_mfma_f32_16x16x32_bf16 v[72:75], v[32:35], a[168:171], v[72:75]\n\t"
  "v_mfma_f32_16x16x32_bf16 v[76:79], v[32:35], a[172:175], v[76:79]\n\t"
  "ds_read_b64_tr_b16 v[32:33], v28 offset:16544\n\t"
  "ds_read_b64_tr_b16 v[34:35], v28 offset:20640\n\t"
  "s_waitcnt lgkmcnt(0)\n\t"
  "v_mfma_f32_16x16x32_bf16 v[96:99], v[32:35], a[232:235], v[96:99]\n\t"
  "v_mfma_f32_16x16x32_bf16 v[100:103], v[32:35], a[164:167], v[100:103]\n\t"
  "v_mfma_f32_16x16x32_bf16 v[104:107], v[32:35], a[168:171], v[104:107]\n\t"
  "v_mfma_f32_16x16x32_bf16 v[108:111], v[32:35], a[172:175], v[108:111]\n\t"
  "ds_read_b64_tr_b16 v[32:33], v28 offset:16576\n\t"
  "ds_read_b64_tr_b16 v[34:35], v28 offset:20672\n\t"
  "s_waitcnt lgkmcnt(0)\n\t"
  "v_mfma_f32_16x16x32_bf16 v[128:131], v[32:35], a[232:235], v[128:131]\n\t"
  "v_mfma_f32_16x16x32_bf16 v[132:135], v[32:35], a[164:167], v[132:135]\n\t"
  "v_mfma_f32_16x16x32_bf16 v[136:139], v[32:35], a[168:171], v[136:139]\n\t"
  "v_mfma_f32_16x16x32_bf16 v[140:143], v[32:35], a[172:175], v[140:143]\n\t"
  "ds_read_b64_tr_b16 v[32:33], v28 offset:24576\n\t"
  "ds_read_b64_tr_b16 v[34:35], v28 offset:28672\n\t"
  "s_waitcnt lgkmcnt(0)\n\t"
  "v_mfma_f32_16x16x32_bf16 v[208:211], v[32:35], a[236:239], v[208:211]\n\t"
  "v_mfma_f32_16x16x32_bf16 v[212:215], v[32:35], a[180:183], v[212:215]\n\t"
  "v_mfma_f32_16x16x32_bf16 v[216:219], v[32:35], a[184:187], v[216:219]\n\t"
  "v_mfma_f32_16x16x32_bf16 v[220:223], v[32:35], a[188:191], v[220:223]\n\t"
  "ds_read_b64_tr_b16 v[32:33], v28 offset:24608\n\t"
  "ds_read_b64_tr_b16 v[34:35], v28 offset:28704\n\t"
  "s_waitcnt lgkmcnt(0)\n\t"
  "v_mfma_f32_16x16x32_bf16 v[240:243], v[32:35], a[236:239], v[240:243]\n\t"
  "v_mfma_f32_16x16x32_bf16 v[244:247], v[32:35], a[180:183], v[244:247]\n\t"
  "v_mfma_f32_16x16x32_bf16 v[248:251], v[32:35], a[184:187], v[248:251]\n\t"
  "v_mfma_f32_16x16x32_bf16 v[252:255], v[32:35], a[188:191], v[252:255]\n\t"
  "ds_read_b64_tr_b16 v[32:33], v28 offset:24640\n\t"
  "ds_read_b64_tr_b16 v[34:35], v28 offset:28736\n\t"
  "s_waitcnt lgkmcnt(0)\n\t"
  "v_mfma_f32_16x16x32_bf16 v[112:115], v[32:35], a[236:239], v[112:115]\n\t"
  "v_mfma_f32_16x16x32_bf16 v[116:119], v[32:35], a[180:183], v[116:119]\n\t"
  "v_mfma_f32_16x16x32_bf16 v[120:123], v[32:35], a[184:187], v[120:123]\n\t"
  "v_mfma_f32_16x16x32_bf16 v[124:127], v[32:35], a[188:191], v[124:127]\n\t"
  "ds_read_b64_tr_b16 v[32:33], v28 offset:24672\n\t"
  "ds_read_b64_tr_b16 v[34:35], v28 offset:28768\n\t"
  "s_waitcnt lgkmcnt(0)\n\t"
  "v_mfma_f32_16x16x32_bf16 v[176:179], v[32:35], a[236:239], v[176:179]\n\t"
  "v_mfma_f32_16x16x32_bf16 v[180:183], v[32:35], a[180:183], v[180:183]\n\t"
  "v_mfma_f32_16x16x32_bf16 v[184:187], v[32:35], a[184:187], v[184:187]\n\t"
  "v_mfma_f32_16x16x32_bf16 v[188:191], v[32:35], a[188:191], v[188:191]\n\t"
  "ds_read_b64_tr_b16 v[32:33], v28 offset:24704\n\t"
  "ds_read_b64_tr_b16 v[34:35], v28 offset:28800\n\t"
  "s_waitcnt lgkmcnt(0)\n\t"
  "v_mfma_f32_16x16x32_bf16 v[64:67], v[32:35], a[236:239], v[64:67]\n\t"
  "v_mfma_f32_16x16x32_bf16 v[68:71], v[32:35], a[180:183], v[68:71]\n\t"
  "v_mfma_f32_16x16x32_bf16 v[72:75], v[32:35], a[184:187], v[72:75]\n\t"
  "v_mfma_f32_16x16x32_bf16 v[76:79], v[32:35], a[188:191], v[76:79]\n\t"
  "ds_read_b64_tr_b16 v[32:33], v28 offset:24736\n\t"
  "ds_read_b64_tr_b16 v[34:35], v28 offset:28832\n\t"
  "s_waitcnt lgkmcnt(0)\n\t"
  "v_mfma_f32_16x16x32_bf16 v[96:99], v[32:35], a[236:239], v[96:99]\n\t"
  "v_mfma_f32_16x16x32_bf16 v[100:103], v[32:35], a[180:183], v[100:103]\n\t"
  "v_mfma_f32_16x16x32_bf16 v[104:107], v[32:35], a[184:187], v[104:107]\n\t"
  "v_mfma_f32_16x16x32_bf16 v[108:111], v[32:35], a[188:191], v[108:111]\n\t"
  "ds_read_b64_tr_b16 v[32:33], v28 offset:24768\n\t"
  "ds_read_b64_tr_b16 v[34:35], v28 offset:28864\n\t"
  "s_waitcnt lgkmcnt(0)\n\t"
  "v_mfma_f32_16x16x32_bf16 v[128:131], v[32:35], a[236:239], v[128:131]\n\t"
  "v_mfma_f32_16x16x32_bf16 v[132:135], v[32:35], a[180:183], v[132:135]\n\t"
  "v_mfma_f32_16x16x32_bf16 v[136:139], v[32:35], a[184:187], v[136:139]\n\t"
  "v_mfma_f32_16x16x32_bf16 v[140:143], v[32:35], a[188:191], v[140:143]\n\t"
  "v_mov_b32 v27, s91\n\t"
  "buffer_store_dword v27, v21, s[92:95], 0 offen offset:2048 sc1\n\t"
  "s_and_b32 s87, s96, 3\n\t"
  "s_lshl_b32 s87, s87, 13\n\t"
  "v_add_u32 v12, s87, v11\n\t"
  "v_add_u32 v13, s87, v56\n\t"
  "v_add_u32 v14, s87, v57\n\t"
  "s_and_b32 s86, s96, 12\n\t"
  "s_lshl_b32 s86, s86, 13\n\t"
  "s_sub_u32 s87, s87, s86\n\t"
  "v_add_u32 v15, s87, v10\n\t"
  "s_lshr_b32 s87, s96, 2\n\t"
  "s_add_u32 s87, s87, 1\n\t"
  "s_and_b32 s87, s87, 3\n\t"
  "s_lshl_b32 s87, s87, 15\n\t"
  "s_add_u32 s52, s87, s79\n\t"
  "s_add_u32 s53, s87, s80\n\t"
  "s_lshr_b32 s87, s96, 2\n\t"
  "s_add_u32 s87, s87, 2\n\t"
  "s_and_b32 s87, s87, 3\n\t"
  "s_lshl_b32 s87, s87, 15\n\t"
  "s_add_u32 s54, s87, s79\n\t"
  "s_add_u32 s55, s87, s80\n\t"
  "s_lshr_b32 s87, s96, 2\n\t"
  "s_add_u32 s87, s87, 3\n\t"
  "s_and_b32 s87, s87, 3\n\t"
  "s_lshl_b32 s87, s87, 15\n\t"
  "s_add_u32 s48, s87, s79\n\t"
  "s_add_u32 s49, s87, s80\n\t"
  "s_lshl_b32 s87, s97, 2\n\t"
  "s_and_b32 s86, s96, 3\n\t"
  "s_add_u32 s87, s87, s86\n\t"
  "s_lshl_b32 s87, s87, 2\n\t"
  "s_add_u32 s87, s87, s90\n\t"
  "s_lshl_b32 s87, s87, 4\n\t"
  "s_add_u32 s87, s87, 8192\n\t"
  "v_bfe_u32 v61, v20, 4, 2\n\t"
  "v_lshlrev_b32 v61, 2, v61\n\t"
  "v_add_u32 v61, s87, v61\n\t"
  "s_and_b32 s86, s96, 12\n\t"
  "s_add_u32 s87, s87, s86\n\t"
  "v_lshlrev_b32 v60, 12, v20\n\t"
  "v_add_u32 v60, s87, v60\n\t"
  "v_mov_b32 v27, s72\n\t"
  "v_lshrrev_b32 v0, 6, v20\n\t"
  "v_lshlrev_b32 v62, 2, v0\n\t"
  "v_mul_u32_u24 v0, 896, v0\n\t"
  "s_lshr_b32 s87, s96, 2\n\t"
  "s_lshl_b32 s86, s90, 2\n\t"
  "s_add_u32 s87, s87, s86\n\t"
  "s_mul_i32 s87, s87, 14336\n\t"
  "v_add_u32 v0, s87, v0\n\t"
  "s_and_b32 s87, s96, 3\n\t"
  "s_mul_i32 s87, s87, 224\n\t"
  "v_add_u32 v0, s87, v0\n\t"
  "v_bfe_u32 v1, v20, 4, 2\n\t"
  "v_lshl_add_u32 v4, v1, 4, v0\n\t"
  "v_lshl_add_u32 v5, v1, 3, v0\n\t"
  "v_bfe_u32 v0, v20, 4, 1\n\t"
  "v_bfe_u32 v2, v20, 5, 1\n\t"
  "v_lshl_add_u32 v0, v0, 1, v2\n\t"
  "v_lshl_add_u32 v59, v0, 6, v62\n\t"
  "v_lshl_add_u32 v62, v1, 6, v62\n\t"
  "L_k1c_4:\n\t"
  "s_mov_b32 s88, 0x80000\n\t"
  "L_k1c_4_l:\n\t"
  "s_sleep 2\n\t"
  "buffer_load_dword v26, v30, s[92:95], 0 offen offset:2048 sc1\n\t"
  "s_waitcnt vmcnt(0)\n\t"
  "v_subrev_u32 v26, s91, v26\n\t"
  "v_cmp_lt_u32 vcc, 0, v26\n\t"
  "s_nop 4\n\t"
  "s_cbranch_vccz L_k1c_4_ok\n\t"
  "s_sub_u32 s88, s88, 1\n\t"
  "s_cmp_lg_u32 s88, 0\n\t"
  "s_cbranch_scc1 L_k1c_4_l\n\t"
  "s_branch L_k1c_4_ok\n\t"
  "L_k1c_4_ok:\n\t"
  "s_waitcnt vmcnt(0) lgkmcnt(0)\n\t"
  "v_cvt_pk_bf16_f32 v212, v212, v213\n\t"
  "v_cvt_pk_bf16_f32 v213, v214, v215\n\t"
  "v_cvt_pk_bf16_f32 v214, v244, v245\n\t"
  "v_cvt_pk_bf16_f32 v215, v246, v247\n\t"
  "buffer_store_dwordx4 v[212:215], v12, s[40:43], s79 offen offset:0 sc1\n\t"
  "v_cvt_pk_bf16_f32 v216, v216, v217\n\t"
  "v_cvt_pk_bf16_f32 v217, v218, v219\n\t"
  "v_cvt_pk_bf16_f32 v218, v248, v249\n\t"
  "v_cvt_pk_bf16_f32 v219, v250, v251\n\t"
  "buffer_store_dwordx4 v[216:219], v13, s[40:43], s79 offen offset:0 sc1\n\t"
  "v_cvt_pk_bf16_f32 v220, v220, v221\n\t"
  "v_cvt_pk_bf16_f32 v221, v222, v223\n\t"
  "v_cvt_pk_bf16_f32 v222, v252, v253\n\t"
  "v_cvt_pk_bf16_f32 v223, v254, v255\n\t"
  "buffer_store_dwordx4 v[220:223], v14, s[40:43], s79 offen offset:0 sc1\n\t"
  "v_cvt_pk_bf16_f32 v116, v116, v117\n\t"
  "v_cvt_pk_bf16_f32 v117, v118, v119\n\t"
  "v_cvt_pk_bf16_f32 v118, v180, v181\n\t"
  "v_cvt_pk_bf16_f32 v119, v182, v183\n\t"
  "buffer_store_dwordx4 v[116:119], v12, s[40:43], s79 offen offset:1024 sc1\n\t"
  "v_cvt_pk_bf16_f32 v120, v120, v121\n\t"
  "v_cvt_pk_bf16_f32 v121, v122, v123\n\t"
  "v_cvt_pk_bf16_f32 v122, v184, v185\n\t"
  "v_cvt_pk_bf16_f32 v123, v186, v187\n\t"
  "buffer_store_dwordx4 v[120:123], v13, s[40:43], s79 offen offset:1024 sc1\n\t"
  "v_cvt_pk_bf16_f32 v124, v124, v125\n\t"
  "v_cvt_pk_bf16_f32 v125, v126, v127\n\t"
  "v_cvt_pk_bf16_f32 v126, v188, v189\n\t"
  "v_cvt_pk_bf16_f32 v127, v190, v191\n\t"
  "buffer_store_dwordx4 v[124:127], v14, s[40:43], s79 offen offset:1024 sc1\n\t"
  "v_cvt_pk_bf16_f32 v68, v68, v69\n\t"
  "v_cvt_pk_bf16_f32 v69, v70, v71\n\t"
  "v_cvt_pk_bf16_f32 v70, v100, v101\n\t"
  "v_cvt_pk_bf16_f32 v71, v102, v103\n\t"
  "buffer_store_dwordx4 v[68:71], v12, s[40:43], s80 offen offset:0 sc1\n\t"
  "v_cvt_pk_bf16_f32 v72, v72, v73\n\t"
  "v_cvt_pk_bf16_f32 v73, v74, v75\n\t"
  "v_cvt_pk_bf16_f32 v74, v104, v105\n\t"
  "v_cvt_pk_bf16_f32 v75, v106, v107\n\t"
  "buffer_store_dwordx4 v[72:75], v13, s[40:43], s80 offen offset:0 sc1\n\t"
  "v_cvt_pk_bf16_f32 v76, v76, v77\n\t"
  "v_cvt_pk_bf16_f32 v77, v78, v79\n\t"
  "v_cvt_pk_bf16_f32 v78, v108, v109\n\t"
  "v_cvt_pk_bf16_f32 v79, v110, v111\n\t"
  "buffer_store_dwordx4 v[76:79], v14, s[40:43], s80 offen offset:0 sc1\n\t"
  "v_cvt_pk_bf16_f32 v132, v132, v133\n\t"
  "v_cvt_pk_bf16_f32 v133, v134, v135\n\t"
  "buffer_store_dwordx2 v[132:133], v12, s[40:43], s80 offen offset:1024 sc1\n\t"
  "v_cvt_pk_bf16_f32 v136, v136, v137\n\t"
  "v_cvt_pk_bf16_f32 v137, v138, v139\n\t"
  "buffer_store_dwordx2 v[136:137], v13, s[40:43], s80 offen offset:1024 sc1\n\t"
  "v_cvt_pk_bf16_f32 v140, v140, v141\n\t"
  "v_cvt_pk_bf16_f32 v141, v142, v143\n\t"
  "buffer_store_dwordx2 v[140:141], v14, s[40:43], s80 offen offset:1024 sc1\n\t"
  "s_waitcnt vmcnt(0) lgkmcnt(0)\n\t"
  "buffer_store_dword v27, v60, s[92:95], 0 offen sc1\n\t"
  "L_k1c_xs:\n\t"
  "s_mov_b32 s88, 0x80000\n\t"
  "L_k1c_xs_l:\n\t"
  "s_sleep 0\n\t"
  "buffer_load_dword v26, v61, s[92:95], 0 offen sc1\n\t"
  "s_waitcnt vmcnt(0)\n\t"
  "v_cmp_ne_u32 vcc, s72, v26\n\t"
  "s_nop 4\n\t"
  "s_cbranch_vccz L_k1c_xok\n\t"
  "s_sub_u32 s88, s88, 1\n\t"
  "s_cmp_lg_u32 s88, 0\n\t"
  "s_cbranch_scc1 L_k1c_xs_l\n\t"
  "s_branch L_k1c_xok\n\t"
  "L_k1c_xok:\n\t"
  "buffer_load_dwordx4 v[212:215], v15, s[40:43], s52 offen offset:0 sc1\n\t"
  "buffer_load_dwordx4 v[248:251], v15, s[40:43], s54 offen offset:0 sc1\n\t"
  "buffer_load_dwordx4 v[124:127], v15, s[40:43], s48 offen offset:0 sc1\n\t"
  "buffer_load_dwordx4 v[216:219], v15, s[40:43], s52 offen offset:1024 sc1\n\t"
  "buffer_load_dwordx4 v[252:255], v15, s[40:43], s54 offen offset:1024 sc1\n\t"
  "buffer_load_dwordx4 v[180:183], v15, s[40:43], s48 offen offset:1024 sc1\n\t"
  "buffer_load_dwordx4 v[220:223], v15, s[40:43], s53 offen offset:0 sc1\n\t"
  "buffer_load_dwordx4 v[116:119], v15, s[40:43], s55 offen offset:0 sc1\n\t"
  "buffer_load_dwordx4 v[184:187], v15, s[40:43], s49 offen offset:0 sc1\n\t"
  "buffer_load_dwordx2 v[244:245], v15, s[40:43], s53 offen offset:1024 sc1\n\t"
  "buffer_load_dwordx2 v[120:121], v15, s[40:43], s55 offen offset:1024 sc1\n\t"
  "buffer_load_dwordx2 v[188:189], v15, s[40:43], s49 offen offset:1024 sc1\n\t"
  "s_waitcnt vmcnt(11)\n\t"
  "v_lshlrev_b32 v32, 16, v212\n\t"
  "v_and_b32 v33, 0xffff0000, v212\n\t"
  "v_lshlrev_b32 v34, 16, v213\n\t"
  "v_and_b32 v35, 0xffff0000, v213\n\t"
  "v_pk_add_f32 v[208:209], v[208:209], v[32:33]\n\t"
  "v_pk_add_f32 v[210:211], v[210:211], v[34:35]\n\t"
  "v_lshlrev_b32 v36, 16, v214\n\t"
  "v_and_b32 v37, 0xffff0000, v214\n\t"
  "v_lshlrev_b32 v38, 16, v215\n\t"
  "v_and_b32 v39, 0xffff0000, v215\n\t"
  "v_pk_add_f32 v[240:241], v[240:241], v[36:37]\n\t"
  "v_pk_add_f32 v[242:243], v[242:243], v[38:39]\n\t"
  "s_waitcnt vmcnt(10)\n\t"
  "v_lshlrev_b32 v40, 16, v248\n\t"
  "v_and_b32 v41, 0xffff0000, v248\n\t"
  "v_lshlrev_b32 v42, 16, v249\n\t"
  "v_and_b32 v43, 0xffff0000, v249\n\t"
  "v_pk_add_f32 v[208:209], v[208:209], v[40:41]\n\t"
  "v_pk_add_f32 v[210:211], v[210:211], v[42:43]\n\t"
  "v_lshlrev_b32 v44, 16, v250\n\t"
  "v_and_b32 v45, 0xffff0000, v250\n\t"
  "v_lshlrev_b32 v46, 16, v251\n\t"
  "v_and_b32 v47, 0xffff0000, v251\n\t"
  "v_pk_add_f32 v[240:241], v[240:241], v[44:45]\n\t"
  "v_pk_add_f32 v[242:243], v[242:243], v[46:47]\n\t"
  "s_waitcnt vmcnt(9)\n\t"
  "v_lshlrev_b32 v48, 16, v124\n\t"
  "v_and_b32 v49, 0xffff0000, v124\n\t"
  "v_lshlrev_b32 v50, 16, v125\n\t"
  "v_and_b32 v51, 0xffff0000, v125\n\t"
  "v_pk_add_f32 v[208:209], v[208:209], v[48:49]\n\t"
  "v_pk_add_f32 v[210:211], v[210:211], v[50:51]\n\t"
  "v_lshlrev_b32 v52, 16, v126\n\t"
  "v_and_b32 v53, 0xffff0000, v126\n\t"
  "v_lshlrev_b32 v54, 16, v127\n\t"
  "v_and_b32 v55, 0xffff0000, v127\n\t"
  "v_pk_add_f32 v[240:241], v[240:241], v[52:53]\n\t"
  "v_pk_add_f32 v[242:243], v[242:243], v[54:55]\n\t"
  "v_cvt_pk_bf16_f32 v212, v208, v209\n\t"
  "v_cvt_pk_bf16_f32 v213, v210, v211\n\t"
  "v_cvt_pk_bf16_f32 v214, v240, v241\n\t"
  "v_cvt_pk_bf16_f32 v215, v242, v243\n\t"
  "s_nop 0\n\t"
  "v_permlane16_swap_b32 v212, v214\n\t"
  "v_permlane16_swap_b32 v213, v215\n\t"
  "ds_bpermute_b32 v248, v59, v212\n\t"
  "ds_bpermute_b32 v249, v59, v213\n\t"
  "ds_bpermute_b32 v250, v59, v214\n\t"
  "ds_bpermute_b32 v251, v59, v215\n\t"
  "s_waitcnt vmcnt(8)\n\t"
  "v_lshlrev_b32 v32, 16, v216\n\t"
  "v_and_b32 v33, 0xffff0000, v216\n\t"
  "v_lshlrev_b32 v34, 16, v217\n\t"
  "v_and_b32 v35, 0xffff0000, v217\n\t"
  "v_pk_add_f32 v[112:113], v[112:113], v[32:33]\n\t"
  "v_pk_add_f32 v[114:115], v[114:115], v[34:35]\n\t"
  "v_lshlrev_b32 v36, 16, v218\n\t"
  "v_and_b32 v37, 0xffff0000, v218\n\t"
  "v_lshlrev_b32 v38, 16, v219\n\t"
  "v_and_b32 v39, 0xffff0000, v219\n\t"
  "v_pk_add_f32 v[176:177], v[176:177], v[36:37]\n\t"
  "v_pk_add_f32 v[178:179], v[178:179], v[38:39]\n\t"
  "s_waitcnt vmcnt(7)\n\t"
  "v_lshlrev_b32 v40, 16, v252\n\t"
  "v_and_b32 v41, 0xffff0000, v252\n\t"
  "v_lshlrev_b32 v42, 16, v253\n\t"
  "v_and_b32 v43, 0xffff0000, v253\n\t"
  "v_pk_add_f32 v[112:113], v[112:113], v[40:41]\n\t"
  "v_pk_add_f32 v[114:115], v[114:115], v[42:43]\n\t"
  "v_lshlrev_b32 v44, 16, v254\n\t"
  "v_and_b32 v45, 0xffff0000, v254\n\t"
  "v_lshlrev_b32 v46, 16, v255\n\t"
  "v_and_b32 v47, 0xffff0000, v255\n\t"
  "v_pk_add_f32 v[176:177], v[176:177], v[44:45]\n\t"
  "v_pk_add_f32 v[178:179], v[178:179], v[46:47]\n\t"
  "s_waitcnt vmcnt(6)\n\t"
  "v_lshlrev_b32 v48, 16, v180\n\t"
  "v_and_b32 v49, 0xffff0000, v180\n\t"
  "v_lshlrev_b32 v50, 16, v181\n\t"
  "v_and_b32 v51, 0xffff0000, v181\n\t"
  "v_pk_add_f32 v[112:113], v[112:113], v[48:49]\n\t"
  "v_pk_add_f32 v[114:115], v[114:115], v[50:51]\n\t"
  "v_lshlrev_b32 v52, 16, v182\n\t"
  "v_and_b32 v53, 0xffff0000, v182\n\t"
  "v_lshlrev_b32 v54, 16, v183\n\t"
  "v_and_b32 v55, 0xffff0000, v183\n\t"
  "v_pk_add_f32 v[176:177], v[176:177], v[52:53]\n\t"
  "v_pk_add_f32 v[178:179], v[178:179], v[54:55]\n\t"
  "v_cvt_pk_bf16_f32 v216, v112, v113\n\t"
  "v_cvt_pk_bf16_f32 v217, v114, v115\n\t"
  "v_cvt_pk_bf16_f32 v218, v176, v177\n\t"
  "v_cvt_pk_bf16_f32 v219, v178, v179\n\t"
  "s_nop 0\n\t"
  "v_permlane16_swap_b32 v216, v218\n\t"
  "v_permlane16_swap_b32 v217, v219\n\t"
  "ds_bpermute_b32 v252, v59, v216\n\t"
  "ds_bpermute_b32 v253, v59, v217\n\t"
  "ds_bpermute_b32 v254, v59, v218\n\t"
  "ds_bpermute_b32 v255, v59, v219\n\t"
  "s_waitcnt lgkmcnt(4)\n\t"
  "buffer_store_dwordx4 v[248:251], v4, s[44:47], 0 offen offset:0\n\t"
  "s_waitcnt vmcnt(6)\n\t"
  "v_lshlrev_b32 v32, 16, v220\n\t"
  "v_and_b32 v33, 0xffff0000, v220\n\t"
  "v_lshlrev_b32 v34, 16, v221\n\t"
  "v_and_b32 v35, 0xffff0000, v221\n\t"
  "v_pk_add_f32 v[64:65], v[64:65], v[32:33]\n\t"
  "v_pk_add_f32 v[66:67], v[66:67], v[34:35]\n\t"
  "v_lshlrev_b32 v36, 16, v222\n\t"
  "v_and_b32 v37, 0xffff0000, v222\n\t"
  "v_lshlrev_b32 v38, 16, v223\n\t"
  "v_and_b32 v39, 0xffff0000, v223\n\t"
  "v_pk_add_f32 v[96:97], v[96:97], v[36:37]\n\t"
  "v_pk_add_f32 v[98:99], v[98:99], v[38:39]\n\t"
  "s_waitcnt vmcnt(5)\n\t"
  "v_lshlrev_b32 v40, 16, v116\n\t"
  "v_and_b32 v41, 0xffff0000, v116\n\t"
  "v_lshlrev_b32 v42, 16, v117\n\t"
  "v_and_b32 v43, 0xffff0000, v117\n\t"
  "v_pk_add_f32 v[64:65], v[64:65], v[40:41]\n\t"
  "v_pk_add_f32 v[66:67], v[66:67], v[42:43]\n\t"
  "v_lshlrev_b32 v44, 16, v118\n\t"
  "v_and_b32 v45, 0xffff0000, v118\n\t"
  "v_lshlrev_b32 v46, 16, v119\n\t"
  "v_and_b32 v47, 0xffff0000, v119\n\t"
  "v_pk_add_f32 v[96:97], v[96:97], v[44:45]\n\t"
  "v_pk_add_f32 v[98:99], v[98:99], v[46:47]\n\t"
  "s_waitcnt vmcnt(4)\n\t"
  "v_lshlrev_b32 v48, 16, v184\n\t"
  "v_and_b32 v49, 0xffff0000, v184\n\t"
  "v_lshlrev_b32 v50, 16, v185\n\t"
  "v_and_b32 v51, 0xffff0000, v185\n\t"
  "v_pk_add_f32 v[64:65], v[64:65], v[48:49]\n\t"
  "v_pk_add_f32 v[66:67], v[66:67], v[50:51]\n\t"
  "v_lshlrev_b32 v52, 16, v186\n\t"
  "v_and_b32 v53, 0xffff0000, v186\n\t"
  "v_lshlrev_b32 v54, 16, v187\n\t"
  "v_and_b32 v55, 0xffff0000, v187\n\t"
  "v_pk_add_f32 v[96:97], v[96:97], v[52:53]\n\t"
  "v_pk_add_f32 v[98:99], v[98:99], v[54:55]\n\t"
  "v_cvt_pk_bf16_f32 v220, v64, v65\n\t"
  "v_cvt_pk_bf16_f32 v221, v66, v67\n\t"
  "v_cvt_pk_bf16_f32 v222, v96, v97\n\t"
  "v_cvt_pk_bf16_f32 v223, v98, v99\n\t"
  "s_nop 0\n\t"
  "v_permlane16_swap_b32 v220, v222\n\t"
  "v_permlane16_swap_b32 v221, v223\n\t"
  "ds_bpermute_b32 v116, v59, v220\n\t"
  "ds_bpermute_b32 v117, v59, v221\n\t"
  "ds_bpermute_b32 v118, v59, v222\n\t"
  "ds_bpermute_b32 v119, v59, v223\n\t"
  "s_waitcnt lgkmcnt(4)\n\t"
  "buffer_store_dwordx4 v[252:255], v4, s[44:47], 0 offen offset:64\n\t"
  "s_waitcnt vmcnt(4)\n\t"
  "v_lshlrev_b32 v32, 16, v244\n\t"
  "v_and_b32 v33, 0xffff0000, v244\n\t"
  "v_lshlrev_b32 v34, 16, v245\n\t"
  "v_and_b32 v35, 0xffff0000, v245\n\t"
  "v_pk_add_f32 v[128:129], v[128:129], v[32:33]\n\t"
  "v_pk_add_f32 v[130:131], v[130:131], v[34:35]\n\t"
  "s_waitcnt vmcnt(3)\n\t"
  "v_lshlrev_b32 v40, 16, v120\n\t"
  "v_and_b32 v41, 0xffff0000, v120\n\t"
  "v_lshlrev_b32 v42, 16, v121\n\t"
  "v_and_b32 v43, 0xffff0000, v121\n\t"
  "v_pk_add_f32 v[128:129], v[128:129], v[40:41]\n\t"
  "v_pk_add_f32 v[130:131], v[130:131], v[42:43]\n\t"
  "s_waitcnt vmcnt(2)\n\t"
  "v_lshlrev_b32 v48, 16, v188\n\t"
  "v_and_b32 v49, 0xffff0000, v188\n\t"
  "v_lshlrev_b32 v50, 16, v189\n\t"
  "v_and_b32 v51, 0xffff0000, v189\n\t"
  "v_pk_add_f32 v[128:129], v[128:129], v[48:49]\n\t"
  "v_pk_add_f32 v[130:131], v[130:131], v[50:51]\n\t"
  "v_cvt_pk_bf16_f32 v244, v128, v129\n\t"
  "v_cvt_pk_bf16_f32 v245, v130, v131\n\t"
  "ds_bpermute_b32 v120, v62, v244\n\t"
  "ds_bpermute_b32 v121, v62, v245\n\t"
  "s_waitcnt lgkmcnt(2)\n\t"
  "buffer_store_dwordx4 v[116:119], v4, s[44:47], 0 offen offset:128\n\t"
  "s_waitcnt lgkmcnt(0)\n\t"
  "buffer_store_dwordx2 v[120:121], v5, s[44:47], 0 offen offset:192\n\t"
  "s_lshl_b32 s87, s90, 20\n\t"
  "s_lshl_b32 s70, s96, 14\n\t"
  "s_add_u32 s87, s87, s70\n\t"
  "s_lshl_b32 s70, s97, 7\n\t"
  "s_add_u32 s87, s87, s70\n\t"
  "s_lshr_b32 s70, s72, 2\n\t"
  "v_lshlrev_b32 v0, 19, v20\n\t"
  "v_add_u32 v0, s87, v0\n\t"
  "v_mov_b32 v1, s70\n\t"
  "buffer_store_dword v1, v0, s[92:95], 0 offen sc1\n\t"
    :
    : "s"(ws2), "s"((char*)ka[6] + (size_t)b * 215040), "s"(w), "s"((unsigned)(size_t)lds), "s"(blk), "s"(tag), "s"(ka[4])
    : "v0","v1","v2","v3","v4","v5","v6","v7","v8","v9","v10","v11","v12","v13","v14","v15","v16","v17","v18","v19","v20","v21","v22","v23","v24","v25","v26","v27","v28","v29","v30","v31","v32","v33","v34","v35","v36","v37","v38","v39","v40","v41","v42","v43","v44","v45","v46","v47","v48","v49","v50","v51","v52","v53","v54","v55","v56","v57","v58","v59","v60","v61","v62","v63","v64","v65","v66","v67","v68","v69","v70","v71","v72","v73","v74","v75","v76","v77","v78","v79","v80","v81","v82","v83","v84","v85","v86","v87","v88","v89","v90","v91","v92","v93","v94","v95","v96","v97","v98","v99","v100","v101","v102","v103","v104","v105","v106","v107","v108","v109","v110","v111","v112","v113","v114","v115","v116","v117","v118","v119","v120","v121","v122","v123","v124","v125","v126","v127","v128","v129","v130","v131","v132","v133","v134","v135","v136","v137","v138","v139","v140","v141","v142","v143","v144","v145","v146","v147","v148","v149","v150","v151","v152","v153","v154","v155","v156","v157","v158","v159","v160","v161","v162","v163","v164","v165","v166","v167","v168","v169","v170","v171","v172","v173","v174","v175","v176","v177","v178","v179","v180","v181","v182","v183","v184","v185","v186","v187","v188","v189","v190","v191","v192","v193","v194","v195","v196","v197","v198","v199","v200","v201","v202","v203","v204","v205","v206","v207","v208","v209","v210","v211","v212","v213","v214","v215","v216","v217","v218","v219","v220","v221","v222","v223","v224","v225","v226","v227","v228","v229","v230","v231","v232","v233","v234","v235","v236","v237","v238","v239","v240","v241","v242","v243","v244","v245","v246","v247","v248","v249","v250","v251","v252","v253","v254","v255","a0","a1","a2","a3","a4","a5","a6","a7","a8","a9","a10","a11","a12","a13","a14","a15","a16","a17","a18","a19","a20","a21","a22","a23","a24","a25","a26","a27","a28","a29","a30","a31","a32","a33","a34","a35","a36","a37","a38","a39","a40","a41","a42","a43","a44","a45","a46","a47","a48","a49","a50","a51","a52","a53","a54","a55","a56","a57","a58","a59","a60","a61","a62","a63","a64","a65","a66","a67","a68","a69","a70","a71","a72","a73","a74","a75","a76","a77","a78","a79","a80","a81","a82","a83","a84","a85","a86","a87","a88","a89","a90","a91","a92","a93","a94","a95","a96","a97","a98","a99","a100","a101","a102","a103","a104","a105","a106","a107","a108","a109","a110","a111","a112","a113","a114","a115","a116","a117","a118","a119","a120","a121","a122","a123","a124","a125","a126","a127","a128","a129","a130","a131","a132","a133","a134","a135","a136","a137","a138","a139","a140","a141","a142","a143","a144","a145","a146","a147","a148","a149","a150","a151","a152","a153","a154","a155","a156","a157","a158","a159","a160","a161","a162","a163","a164","a165","a166","a167","a168","a169","a170","a171","a172","a173","a174","a175","a176","a177","a178","a179","a180","a181","a182","a183","a184","a185","a186","a187","a188","a189","a190","a191","a192","a193","a194","a195","a196","a197","a198","a199","a200","a201","a202","a203","a204","a205","a206","a207","a208","a209","a210","a211","a212","a213","a214","a215","a216","a217","a218","a219","a220","a221","a222","a223","a224","a225","a226","a227","a228","a229","a230","a231","a232","a233","a234","a235","a236","a237","a238","a239","a240","a241","a242","a243","a244","a245","a246","a247","a248","a249","a250","a251","a252","a253","a254","a255","s40","s41","s42","s43","s44","s45","s46","s47","s48","s49","s50","s51","s52","s53","s54","s55","s56","s57","s58","s59","s60","s61","s62","s63","s64","s65","s66","s67","s68","s69","s70","s71","s72","s73","s74","s75","s76","s77","s78","s79","s80","s81","s82","s83","s84","s85","s86","s87","s88","s89","s90","s91","s92","s93","s94","s95","s96","s97","s98","s99","vcc","scc","memory");
  }
}

extern "C" void kernel_launch(void* const* d_in, const int* in_sizes, int n_in, void* d_out, int out_size, void* d_ws, size_t ws_size, hipStream_t stream) {
  (void)hipFuncSetAttribute((const void*)k1_attn, hipFuncAttributeMaxDynamicSharedMemorySize, LDS_K1);
  k1_attn<<<256, 256, LDS_K1, stream>>>((const u16*)d_in[0], (const u16*)d_in[1], (const u16*)d_in[2], (const u16*)d_in[3], (const u16*)d_in[4], (char*)d_ws, (u16*)d_out);
}
```
